```python
import torch

def _softmax(x, axis=-1):
    return torch.softmax(x, dim=axis)
def _gelu(x):
    return torch.nn.functional.gelu(x, approximate="tanh")
def _silu(x):
    return torch.nn.functional.silu(x)
def _sigmoid(x):
    return torch.sigmoid(x)
def _layernorm(x, axis=-1, eps=1e-5):
    m = x.mean(dim=axis, keepdim=True)
    v = x.var(dim=axis, keepdim=True, unbiased=False)
    return (x - m) / torch.sqrt(v + eps)
def _scatter_add(tgt, idx, src, axis=0):
    return tgt.clone().index_add_(axis, idx, src)
def _segment_sum(src, seg, n):
    out = torch.zeros((n,) + src.shape[1:], dtype=src.dtype)
    return out.index_add_(0, seg, src)
def _pad(x, pw):
    flat = tuple(v for pair in reversed(pw) for v in pair)
    return torch.nn.functional.pad(x, flat)

kernel_name = 'recurrent_scan__compressed_kv_cla__moe_arch_mod__prompt'

def setup_inputs():

    _w = (lambda a: (lambda t: t.to(torch.bfloat16) if t.is_floating_point() else t)(torch.as_tensor(a)))
    def _A_setup():
        B, H, Sq, Sk, D, M = 1, 32, 2048, 2048, 128, 1024
        Dc = 4 * D
        return {
            "x": _w((lambda *s: torch.randn(*s, dtype=torch.bfloat16) * 0.05)(B, Sq, M)),
            "c_kv": _w((lambda *s: torch.randn(*s, dtype=torch.bfloat16) * 0.2)(B, Sk, Dc)),
            "w_q": _w((lambda *s: torch.randn(*s, dtype=torch.bfloat16) * 3.2)(M, H, D) * (M ** -0.5)),
            "w_uk": _w((lambda *s: torch.randn(*s, dtype=torch.bfloat16) * 3.2)(Dc, H, D) * (Dc ** -0.5)),
            "w_uv": _w((lambda *s: torch.randn(*s, dtype=torch.bfloat16) * 0.2)(Dc, H, D) * (Dc ** -0.5)),
            "w_o": _w((lambda *s: torch.randn(*s, dtype=torch.bfloat16) * 0.2)(H, D, M) * ((H * D) ** -0.5)),
        }
    _A_in = _A_setup()

    def _B_setup():
        B, S, D, F, C = 1, 2048, 1024, 3584, 2
        return {
            "x": _w((lambda *s: torch.randn(*s, dtype=torch.bfloat16) * 0.05)(B, S, D)),
            "w_r": _w((lambda *s: torch.randn(*s, dtype=torch.bfloat16) * 0.2)(D)),
            "w_gate": _w((lambda *s: torch.randn(*s, dtype=torch.bfloat16) * 0.1)(D, F)),
            "w_up": _w((lambda *s: torch.randn(*s, dtype=torch.bfloat16) * 0.1)(D, F)),
            "w_down": _w((lambda *s: torch.randn(*s, dtype=torch.bfloat16) * 0.1)(F, D)),
        }
    _B_in = _B_setup()

    return {
        "A_x": _A_in["x"],
        "A_c_kv": _A_in["c_kv"],
        "A_w_q": _A_in["w_q"],
        "A_w_uk": _A_in["w_uk"],
        "A_w_uv": _A_in["w_uv"],
        "A_w_o": _A_in["w_o"],
        "B_x": _B_in["x"],
        "B_w_r": _B_in["w_r"],
        "B_w_gate": _B_in["w_gate"],
        "B_w_up": _B_in["w_up"],
        "B_w_down": _B_in["w_down"],
    }

def reference(A_x, A_c_kv, A_w_q, A_w_uk, A_w_uv, A_w_o, B_x, B_w_r, B_w_gate, B_w_up, B_w_down):
    def _A_ref(x, c_kv, w_q, w_uk, w_uv, w_o):
        B, Sq, M = x.shape[0], x.shape[1], x.shape[2]
        Sk = c_kv.shape[1]
        H, D = w_q.shape[1], w_q.shape[2]
        q = torch.einsum("bqm,mhd->bhqd", x, w_q)
        k = torch.einsum("bkc,chd->bhkd", c_kv, w_uk)
        v = torch.einsum("bkc,chd->bhkd", c_kv, w_uv)
        scores = torch.einsum("bhqd,bhkd->bhqk", q, k) * (D ** -0.5)
        off = Sk - Sq
        qi = torch.arange(Sq)[:, None]
        kj = torch.arange(Sk)[None, :]
        cmask = kj <= (qi + off)
        scores = torch.where(cmask[None, None], scores, -1e9)
        p = _softmax(scores, axis=-1)
        ctx = torch.einsum("bhqk,bhkd->bhqd", p, v)
        return torch.einsum("bhqd,hdm->bqm", ctx, w_o)

    def _B_ref(x, w_r, w_gate, w_up, w_down):
        B, S, D = x.shape
        C = 2
        r = x @ w_r
        idx = torch.argsort(-r, axis=-1)[:, :C]
        idx_e = (lambda x, n, axis: torch.repeat_interleave(x, n, dim=axis))(idx.reshape(B, C, 1), D, axis=2)
        x_sel = (lambda x, i, axis: torch.take_along_dim(x, i, dim=axis))(x, idx_e, axis=1)
        g = _silu(x_sel @ w_gate)
        u = x_sel @ w_up
        y = (g * u) @ w_down
        r_sel = _sigmoid((lambda x, i, axis: torch.take_along_dim(x, i, dim=axis))(r, idx, axis=1))
        y = y * r_sel.reshape(B, C, 1)
        b_off = (torch.arange(B) * S).reshape(B, 1)
        flat_idx = (idx + b_off).reshape(B * C)
        zero = (x * 0.0).reshape(B * S, D)
        upd = _scatter_add(zero, flat_idx, y.reshape(B * C, D), axis=0)
        return x + upd.reshape(B, S, D)

    x = A_x
    xn = _layernorm(x, axis=-1)
    s = _A_ref(xn, A_c_kv, A_w_q, A_w_uk, A_w_uv, A_w_o)
    g = _silu(_B_ref(xn, B_w_r, B_w_gate, B_w_up, B_w_down))
    return x + s * g
```

```cpp
#include <hip/hip_runtime.h>
#include <hip/hip_bf16.h>
#include <stdint.h>

typedef unsigned short u16;
typedef unsigned long long u64;
typedef short    bf16x8 __attribute__((ext_vector_type(8)));
typedef short    bf16x4 __attribute__((ext_vector_type(4)));
typedef float    f32x4  __attribute__((ext_vector_type(4)));
typedef float    f32x2  __attribute__((ext_vector_type(2)));
typedef __bf16   bfx2   __attribute__((ext_vector_type(2)));
typedef unsigned u32x4  __attribute__((ext_vector_type(4)));
typedef unsigned u32x2  __attribute__((ext_vector_type(2)));

#define DI __device__ __forceinline__

constexpr int SQ = 2048, DM = 1024, NH = 32, HD = 128, DC = 512, FF = 3584;

#define WSOFF 0
constexpr size_t WS_R    = 4u << 20;
constexpr size_t WS_FLAG = (4u << 20) + 65536;
constexpr size_t WS_GP   = 6u << 20;
constexpr size_t WS_YP   = 7u << 20;
constexpr size_t WS_PART = 96u << 20;
constexpr size_t WS_XF   = 16u << 20;
#define HSTR 1310720
constexpr size_t WS_KV   = 24u << 20;
constexpr size_t WS_CTX  = 72u << 20;
constexpr int F_EPOCH = 0, F_TOP = 1024, F_LN = 4096, F_KV = 8192, F_GU = 12288  , F_Y = 20480  , F_PARK = 524288  ,
              F_CTX = 65536  ;

#define PH_STAMP(i)
#define SST(i)
#define S2T(i)
#define SST_OUT()
#define ZST(i)
#define ZST_OUT()
DI unsigned cvtpk(float lo, float hi) { f32x2 v = {lo, hi}; bfx2 b = __builtin_convertvector(v, bfx2); return __builtin_bit_cast(unsigned, b); }
DI float rbf(float x) { return __uint_as_float(cvtpk(x, 0.f) << 16); }
#define DPP_ADD(v, ctrl, rmask) v += __uint_as_float(__builtin_amdgcn_update_dpp(0u, __float_as_uint(v), ctrl, rmask, 0xf, false))
DI unsigned tkey(unsigned fbits, int i) { unsigned u = fbits >> 16; u ^= (u & 0x8000u) ? 0xffffu : 0x8000u; return (u << 16) | (unsigned)(2047 - i); }
DI void merge_top2(unsigned& a1, unsigned& a2, unsigned b1, unsigned b2) { const unsigned lo = min(a1, b1); a1 = max(a1, b1); a2 = max(lo, max(a2, b2)); }
#define DPP_TOP2(a1, a2, ctrl, rmask) merge_top2(a1, a2, __builtin_amdgcn_update_dpp(0u, a1, ctrl, rmask, 0xf, false), __builtin_amdgcn_update_dpp(0u, a2, ctrl, rmask, 0xf, false))

#define GLDS_S(voff, sbase, ldsoff, imm)                                                                        \
    asm volatile("s_mov_b32 m0, %2\n\ts_nop 0\n\tglobal_load_lds_dwordx4 %0, %1 offset:%3"                       \
                 :: "v"(voff), "s"(sbase), "s"(ldsoff), "i"(imm) : "memory")
#define GLDS_C(voff, sbase, ldsoff, imm)                                                                        \
    asm volatile("s_mov_b32 m0, %2\n\ts_nop 0\n\tglobal_load_lds_dwordx4 %0, %1 offset:%3 sc1"                   \
                 :: "v"(voff), "s"(sbase), "s"(ldsoff), "i"(imm) : "memory")
#define LGKM0_BAR()    asm volatile("s_waitcnt lgkmcnt(0)\n\ts_barrier" ::: "memory")
#define SB() __builtin_amdgcn_sched_barrier(0)
#define CAT_(a, b) a##b
#define CAT(a, b) CAT_(a, b)
#define PIN(x) asm volatile("" : "+v"(x))
#define OADV(p_, d_) do { p_ += (d_); asm volatile("" : "+s"(p_)); } while (0)
#define VADD(d, a, b)    asm volatile("v_add_f32 %0, %1, %2" : "=v"(d) : "v"(a), "v"(b))
#define KEEP(x) asm volatile("" :: "v"(x))
#define MFA16(c, a, b) asm volatile("v_mfma_f32_16x16x32_bf16 %0, %1, %2, %0" : "+v"(c) : "v"(a), "v"(b))
#define MFA16Z(c, a, b) asm volatile("v_mfma_f32_16x16x32_bf16 %0, %1, %2, 0" : "=&v"(c) : "v"(a), "v"(b))
#define MFA16C(c, a, b, c0) asm volatile("v_mfma_f32_16x16x32_bf16 %0, %1, %2, %3" : "=&v"(c) : "v"(a), "v"(b), "v"(c0))
#define MF_DONE() do { SB(); asm volatile("s_nop 7\n\ts_nop 7"); SB(); } while (0)
#define ST16_SC1(p, v) asm volatile("global_store_dwordx4 %0, %1, off sc1\n\ts_nop 1" :: "v"(p), "v"(v) : "memory")
#define ST4_SC1(p, v)  asm volatile("global_store_dword %0, %1, off sc1" :: "v"(p), "v"(v) : "memory")
#define FLAG_PUT(p_, val_, on_) do { u64 sv_, tv_; unsigned tz_; const u64 v_ = (val_); const unsigned m_ = __builtin_amdgcn_readfirstlane((on_) ? 1u : 0u); \
        asm volatile("s_cmp_eq_u32 %5, 0\n\ts_cbranch_scc1 1f\n\tv_mov_b64 %1, %3\n\tv_mov_b32 %2, 0\n\ts_mov_b64 %0, exec\n\ts_mov_b64 exec, 1\n\t" \
                     "global_store_dwordx2 %2, %1, %4 sc1\n\ts_mov_b64 exec, %0\n1:"                                                      \
                     : "=&s"(sv_), "=&v"(tv_), "=&v"(tz_) : "s"(v_), "s"(p_), "s"(m_) : "memory", "scc"); } while (0)
#define FLAG_PUTZ(p_, val_, nz_) do { u64 sv_, tv_; unsigned tz_; const u64 v_ = (val_);                                                   \
        asm volatile("s_cmp_lg_u32 %5, 0\n\ts_cbranch_scc1 1f\n\tv_mov_b64 %1, %3\n\tv_mov_b32 %2, 0\n\ts_mov_b64 %0, exec\n\ts_mov_b64 exec, 1\n\t" \
                     "global_store_dwordx2 %2, %1, %4 sc1\n\ts_mov_b64 exec, %0\n1:"                                                      \
                     : "=&s"(sv_), "=&v"(tv_), "=&v"(tz_) : "s"(v_), "s"(p_), "s"(nz_) : "memory", "scc"); } while (0)
#define FLAG_GET(r_, p_) asm volatile("global_load_dwordx2 %0, %1, off sc1" : "=v"(r_) : "v"(p_) : "memory")
#define FLAG_WAIT(r_, p_, tag_, N) do { unsigned c_;                                                                                      \
        asm volatile("s_waitcnt vmcnt(%4)\n\ts_mov_b32 %1, 0x40000\n"                                                                     \
                     "1:\n\tv_cmp_ne_u64 vcc, %3, %0\n\ts_cmp_eq_u64 vcc, 0\n\ts_cbranch_scc1 2f\n\t"                                      \
                     "s_sleep 2\n\tglobal_load_dwordx2 %0, %2, off sc1\n\ts_waitcnt vmcnt(0)\n\ts_sub_u32 %1, %1, 1\n\ts_cbranch_scc0 1b\n"  \
                     "2:" : "+v"(r_), "=&s"(c_) : "v"(p_), "s"(tag_), "i"(N) : "memory", "vcc", "scc"); } while (0)
#define FLAG_WAITM(r_, p_, tag_, N, m_) do { unsigned c_;                                                                                 \
        asm volatile("s_waitcnt vmcnt(%4)\n\ts_mov_b32 %1, 0x40000\n"                                                                     \
                     "1:\n\tv_cmp_ne_u64 vcc, %3, %0\n\ts_and_b32 vcc_lo, vcc_lo, %5\n\ts_cbranch_scc0 2f\n\t"                             \
                     "s_sleep 2\n\tglobal_load_dwordx2 %0, %2, off sc1\n\ts_waitcnt vmcnt(0)\n\ts_sub_u32 %1, %1, 1\n\ts_cbranch_scc0 1b\n"  \
                     "2:" : "+v"(r_), "=&s"(c_) : "v"(p_), "s"(tag_), "i"(N), "s"(m_) : "memory", "vcc", "scc"); } while (0)
typedef __attribute__((address_space(3))) bf16x4 lds_bf16x4;
DI bf16x4 tr_rd(unsigned a) { return __builtin_amdgcn_ds_read_tr16_b64_v4i16((lds_bf16x4*)(uintptr_t)a); }
typedef __attribute__((address_space(3))) bf16x8 lds_bf16x8;
DI bf16x8 lds_rd(unsigned a) { return *(lds_bf16x8*)(uintptr_t)a; }
#define CAT8(lo_, hi_) bf16x8{lo_[0], lo_[1], lo_[2], lo_[3], hi_[0], hi_[1], hi_[2], hi_[3]}

#define POFF 32.f
#define LTHR 8192.f
DI float quad_max(float v) {
    auto r1 = __builtin_amdgcn_permlane16_swap(__float_as_uint(v), __float_as_uint(v), false, false);
    v = fmaxf(__uint_as_float(r1[0]), __uint_as_float(r1[1]));
    auto r2 = __builtin_amdgcn_permlane32_swap(__float_as_uint(v), __float_as_uint(v), false, false);
    return fmaxf(__uint_as_float(r2[0]), __uint_as_float(r2[1])); }
#define QWD 3
#define QS1 0x1dff
constexpr int q_xb(int s) { return s >= 1 && s <= 12; }
constexpr int q_xa(int s) { return s >= 0 && s <= 11; }
constexpr int q_s0(int s) { int n = 0; for (int i = 0; i < s; ++i) n += (QS1 >> i) & 1; return n; }
constexpr int q_kv(int kt, int f) { const int s = (kt - 13) * 16 + f - 12; return s >= 0 && s % 3 == 0 && s / 3 < 12 ? s / 3 : -1; }
constexpr int q_op(int s, int f) { return (f == 0 || f == 2 ? q_xb(s) : f == 8 || f == 10 ? q_xa(s) : f == 12 || f == 14 ? s + QWD < 16 : f == 3 ? s == 12
                                          : f == 4 ? (QS1 >> s) & 1 : 0) + (q_kv(s, f) >= 0); }
constexpr int q_after(int p, int kt) { int n = 0; for (int i = p + 1; i <= kt * 16 + 11; ++i) n += q_op(i >> 4, i & 15); return n; }
constexpr int q_min(int a, int b) { return a < b ? a : b; }
constexpr int q_wait(int kt) {
    int n = kt <= 2 ? 4 * (2 - kt) + q_after(-1, kt) : q_after((kt - 2) * 16 + 2, kt);
    if (kt + 1 >= QWD) n = q_min(n, q_after((kt + 1 - QWD) * 16 + 14, kt));
    if (kt == 13) n = q_min(n, q_after(12 * 16 + 3, kt));
    return n; }
constexpr int q_fl() { int s = 0; while (q_s0(s + 1) < 8) ++s; int kt = s; while (q_wait(kt) > q_after(s * 16 + 6, kt)) ++kt; return kt; }
constexpr int Q_NST = q_s0(16);
static_assert(Q_NST >= 8 && QS1 < 0x2000, "");
constexpr int o_xp(int st, int i) { return st <= 5 ? 64 + 4 * (4 * (st - 4) + i) : 32 * (st - 3) + (i == 0 ? 0 : i == 1 ? 4 : i == 2 ? 12 : 16); }
constexpr int o_wp(int st, int i) { return st <= 7 ? 16 * (st - 4) + 4 + 6 * i : 32 * (st - 5) + 24 + 4 * i; }
constexpr int o_x(int h) { for (int st = 4; st < 16; ++st) for (int i = 0; i < 4; ++i) if (o_xp(st, i) == h) return 4 * st + i; return -1; }
constexpr int o_w(int h) { for (int st = 4; st < 16; ++st) for (int i = 0; i < 2; ++i) if (o_wp(st, i) == h) return 2 * st + i; return -1; }
constexpr int o_after(int p, int e) { int n = 0; for (int h = p + 1; h < e; ++h) n += (o_x(h) >= 0) + (o_w(h) >= 0); return n; }
constexpr int o_wait(int kt) { return o_after(kt == 11 ? o_wp(15, 1) : o_xp(kt + 1, 3), 32 * kt + 24); }
constexpr int L_PFT = 149504, L_DUMP = 155648;
constexpr int CFS = 128;
constexpr unsigned F1S = 0xfff642ffu, F1F = 0xfff5310fu, F2S = 0xff6f204fu, F2F = 0xff5f13ffu;

__global__ __launch_bounds__(512, 2) void k_all(const u16* __restrict__ x, const u16* __restrict__ ckv, const u16* __restrict__ wuk, const u16* __restrict__ wuv,
                                                const u16* __restrict__ w_r, char* __restrict__ ws, const u16* __restrict__ wq, const u16* __restrict__ wo,
                                                const u16* __restrict__ w_gate, const u16* __restrict__ w_up,
                                                const u16* __restrict__ w_down, u16* __restrict__ out) {
    extern __shared__ __attribute__((aligned(16))) char lds[];
    const int b = blockIdx.x, xcd = b & 7, idx = b >> 3, head = xcd * 4 + (idx >> 3), jj = idx & 7;
    const int hp = head ^ 1;
    const unsigned ldsbase = (unsigned)(uintptr_t)(__attribute__((address_space(3))) char*)lds;
    const int wave = __builtin_amdgcn_readfirstlane(threadIdx.x >> 6);
    const int qbA = 15 - jj, qbB = jj;
#define KROW(g) (((2 * ((g) & 3) + ((g) >> 2)) * 8 + jj) * 32)
    const int qw = wave < 4 ? wave : 7 - wave, qb = wave < 4 ? qbA : qbB;
    bf16x8 qf[8];
    u64 tag;
    u16* outp = out;
    int ri1, ri2;
    PH_STAMP(0);
    SST(0);
    ZST(0);
    u32x4 pk[16];
    bf16x8 xr[4][2][2];
    u32x4 rv;
    const char* xg;
#define QXL1(j) asm volatile("global_load_dwordx4 %0, %1, %2 offset:%3 sc1" : "=v"(xr[(j) >> 2][(j) & 1][((j) >> 1) & 1]) : "v"(xvo), "s"(xg + (size_t)((j) >> 2) * 262144), \
                            "i"(((j) & 1) * 2048 + (((j) >> 1) & 1) * 1024) : "memory")
    unsigned xvo, qwo;
    const u16* wqb;
#define QWOFF() ({ unsigned l_ = threadIdx.x & 63; asm volatile("" : "+v"(l_)); (unsigned)(((l_ >> 2) & 7) * (NH * HD) + 32 * (l_ >> 5) + 8 * ((l_ & 3) ^ ((l_ >> 3) & 2))) * 2u; })
    {
        constexpr int LDW = NH * HD, K = DC;
        int tid = threadIdx.x; asm volatile("" : "+v"(tid));
        const int lane = tid & 63;
        const int wm = wave >> 2, wn = wave & 3;
        const int row = b * 8 + wave;
        u32x4 lx[2], lw[2]; u32x2 ep;
        asm volatile("global_load_dwordx4 %0, %5, %6\n\tglobal_load_dwordx4 %1, %5, %6 offset:1024\n\tglobal_load_dwordx4 %2, %5, %7\n\tglobal_load_dwordx4 %3, %5, %7 offset:1024\n\t"
                     "global_load_dwordx2 %4, %8, off sc1"
                     : "=&v"(lx[0]), "=&v"(lx[1]), "=&v"(lw[0]), "=&v"(lw[1]), "=&v"(ep) : "v"(lane * 16), "s"(x + (unsigned)(row * DM)), "s"(w_r), "v"(ws + WS_FLAG + F_EPOCH) : "memory");
#define XKEY(r) (((r) >> 1) & 7)
        unsigned xoff[4], woff[2];
        { const unsigned x0_ = (unsigned)(KROW(wave) + (lane >> 3)) * (K * 2) + (unsigned)((lane & 7) ^ (lane >> 4)) * 16u;
          xoff[0] = x0_; xoff[1] = (x0_ ^ 64u) + 7168u; xoff[2] = x0_ + 14336u; xoff[3] = xoff[1] + 14336u;
          woff[1] = (unsigned)((8 * wave + ((lane >> 2) & 7)) * LDW + hp * HD + 32 * (lane >> 5) + 8 * ((lane & 3) ^ (2 * (wave & 1)))) * 2u; woff[0] = woff[1] + 896u; }
        const char* xsp = (const char*)ckv;
        const char* wsp = (const char*)wuk - 896;
        const char* wsv = (const char*)wuv - 2944;
        const unsigned m0b = ldsbase + wave * 4096;
#define KM0(g, w_) asm volatile("s_add_u32 m0, %0, %1" :: "s"(m0b), "i"(((g) & 1) * 65536 + (w_) * 32768) : "memory", "scc")
#define KRX(i) asm volatile("global_load_lds_dwordx4 %0, %1 offset:%2" :: "v"(xoff[i]), "s"(xsp), "i"((i) * 1024) : "memory")
#define KRW(i) asm volatile("global_load_lds_dwordx4 %0, %1 offset:%2 nt" :: "v"(woff[(i) & 1]), "s"((i) < 2 ? wsp : wsv), "i"((i) * 1024) : "memory")
#define STAGE(g) do { KM0(g, 0); asm volatile("s_nop 0"); KRX(0); KRX(1); KRX(2); KRX(3); KM0(g, 1); asm volatile("s_nop 0"); KRW(0); KRW(1); KRW(2); KRW(3); \
                      OADV(xsp, 128); OADV(wsp, 64 * LDW * 2); OADV(wsv, 64 * LDW * 2); } while (0)
#define KST(i)
        const unsigned pfl = ldsbase + L_DUMP + wave * 1024;
#define PFX(g) do { unsigned l_ = threadIdx.x & 63; asm volatile("" : "+v"(l_)); l_ = ((l_ >> 3) * DC + (l_ & 7) * 8) * 2u; GLDS_S(l_, pfxp + (g) * 64, pfl, 0); } while (0)
#define PFQ(i) do { unsigned l_ = threadIdx.x & 63; asm volatile("" : "+v"(l_)); l_ = ((l_ >> 4) * LDW + (l_ & 15) * 8) * 2u; GLDS_S(l_, pfqp + (size_t)(4 * (i)) * LDW, pfl, 0); } while (0)
        STAGE(0);
        SB(); ZST(1);
        int w2 = wave; asm volatile("" : "+s"(w2));
        xg = ws + WS_XF + (unsigned)((w2 < 4 ? qbA * 4 + w2 : qbB * 4 + 7 - w2) * 4096);
        wqb = wq + (unsigned)(8 * w2 * LDW + head * HD);
        const u16* pfqp = wq + (unsigned)((128 * jj + 16 * w2) * LDW + head * HD);
        const u16* pfxp = ckv + (unsigned)((KROW(w2) + 8 * (head & 3)) * DC);
        unsigned xa[2][2], wa[2][2];
        const int l15 = lane & 15, q4 = lane >> 4;
#pragma unroll
        for (int s = 0; s < 2; ++s) {
            const int r0 = l15;
            xa[0][s] = ldsbase + (unsigned)((wm * 128 + r0) * 128 + ((((4 * s + q4) ^ XKEY(r0))) << 4));
            wa[0][s] = (ldsbase + 32768u + (unsigned)(q4 * 4096 + wn * 512 + (l15 >> 2) * 64 + (q4 & 1) * 32 + (lane & 3) * 8)) ^ (32u * s);
            xa[1][s] = xa[0][s] + 65536u; wa[1][s] = wa[0][s] + 65536u;
            PIN(xa[0][s]); PIN(xa[1][s]); PIN(wa[0][s]); PIN(wa[1][s]); }
        u64 pf0;
        { u64 pw_ = (u64)(((w2 & 2) ? wuv : wuk) + (unsigned)((128 + 192 * (w2 & 1) + jj) * LDW + hp * HD)), pc_ = (u64)ckv + (unsigned)(b * 8192 + (w2 & 3) * 2048);
          asm volatile("" : "+s"(pw_), "+s"(pc_));
          unsigned o0_ = (unsigned)(lane >> 1) * (8 * LDW * 2) + (lane & 1) * 128u, o1_ = (lane & 15) * 128u; PIN(o0_); PIN(o1_);
          pf0 = (lane < 48 ? pw_ : pc_) + (lane < 48 ? o0_ : o1_); PIN(pf0); }
        asm volatile("" : "+s"(pfqp), "+s"(pfxp), "+s"(xg), "+s"(wqb));
        SB();
        asm volatile("s_waitcnt vmcnt(8)" : "+v"(lx[0]), "+v"(lx[1]), "+v"(lw[0]), "+v"(lw[1]), "+v"(ep) :: "memory");
        KST(1); ZST(2);
#define HALF(v_, i) __uint_as_float(((i) & 1) ? (v_) & 0xffff0000u : (v_) << 16)
#define BOTH(v_) f32x2{HALF(v_, 0), HALF(v_, 1)}
        {
            f32x2 v[8];
#pragma unroll
            for (int i = 0; i < 8; ++i) v[i] = BOTH(lx[i >> 2][i & 3]);
            const float pv_ = __uint_as_float(__builtin_amdgcn_readfirstlane(__float_as_uint(v[0][0])));
            f32x2 s2 = f32x2{0.f, 0.f}, q2 = f32x2{0.f, 0.f};
#pragma unroll
            for (int i = 0; i < 8; ++i) { const f32x2 d = v[i] - pv_; s2 += d; q2 += d * d; }
            float s = s2[0] + s2[1], q = q2[0] + q2[1];
            DPP_ADD(s, 0x111, 0xf); DPP_ADD(q, 0x111, 0xf); DPP_ADD(s, 0x112, 0xf); DPP_ADD(q, 0x112, 0xf); DPP_ADD(s, 0x114, 0xf); DPP_ADD(q, 0x114, 0xf);
            DPP_ADD(s, 0x118, 0xf); DPP_ADD(q, 0x118, 0xf); DPP_ADD(s, 0x142, 0xa); DPP_ADD(q, 0x142, 0xa); DPP_ADD(s, 0x143, 0xc); DPP_ADD(q, 0x143, 0xc);
            const float dm = __uint_as_float(__builtin_amdgcn_readlane(__float_as_uint(s), 63)) * (1.f / DM);
            const float var_bf = rbf(fmaxf(fmaf(-dm, dm, __uint_as_float(__builtin_amdgcn_readlane(__float_as_uint(q), 63)) * (1.f / DM)), 0.f));
            const float mean_bf = rbf(pv_ + dm), den = rbf(__builtin_amdgcn_sqrtf(rbf(var_bf + 1e-5f))), rden = __builtin_amdgcn_rcpf(den);
            u32x4 xw[2]; float r2[2] = {0.f, 0.f};
#pragma unroll
            for (int i = 0; i < 8; ++i) {
                const f32x2 c_ = v[i] - mean_bf;
                const unsigned cp = cvtpk(c_[0], c_[1]);
                const f32x2 cb = BOTH(cp);
                f32x2 n_ = cb * rden;
                n_ += (cb - n_ * den) * rden;
                const unsigned np = cvtpk(n_[0], n_[1]);
                const unsigned wp = lw[i >> 2][i & 3];
                r2[i & 1] = __builtin_amdgcn_fdot2_f32_bf16(__builtin_bit_cast(bfx2, np), __builtin_bit_cast(bfx2, wp), r2[i & 1], false);
                xw[i >> 2][i & 3] = np;
            }
            char* lr = lds + 131072 + wave * 2064 + lane * 16;
            *(u32x4*)lr = xw[0]; *(u32x4*)(lr + 1024) = xw[1];
            s = r2[0] + r2[1];
            DPP_ADD(s, 0x111, 0xf); DPP_ADD(s, 0x112, 0xf); DPP_ADD(s, 0x114, 0xf); DPP_ADD(s, 0x118, 0xf); DPP_ADD(s, 0x142, 0xa); DPP_ADD(s, 0x143, 0xc);
            if (lane == 63) *(float*)(lds + 131072 + wave * 2064 + 2048) = rbf(s);
        }
        SB(); ZST(3);
        STAGE(1);
        asm volatile("s_bitcmp0_b32 %2, 2\n\ts_cbranch_scc1 1f\n\ts_mov_b32 m0, %1\n\ts_nop 0\n\tglobal_load_lds_dword %0, off\n1:" :: "v"(pf0), "s"(pfl), "s"(wave) : "memory", "scc");
        tag = (((u64)(unsigned)__builtin_amdgcn_readfirstlane(ep[1]) << 32) | (unsigned)__builtin_amdgcn_readfirstlane(ep[0])) + 0x9E3779B97F4A7C15ull;
        float rr;
        const unsigned l16 = (unsigned)lane * 16u;
        u32x2 xi_[4];
#define XI_RD() do { int t_ = threadIdx.x; asm volatile("" : "+v"(t_)); const int r8 = t_ & 7, pc = t_ >> 3;                               \
            const char* lr = lds + 131072 + r8 * 2064 + (pc >> 2) * 64 + ((pc >> 1) & 1) * 32 + (pc & 1) * 8;                              \
            xi_[0] = *(const u32x2*)lr; xi_[1] = *(const u32x2*)(lr + 16); xi_[2] = *(const u32x2*)(lr + 1024); xi_[3] = *(const u32x2*)(lr + 1040); \
            rr = *(const float*)(lds + 131072 + wave * 2064 + 2048); } while (0)
#define XI_ST() do { int t_ = threadIdx.x; asm volatile("" : "+v"(t_)); const int r8 = t_ & 7, pc = t_ >> 3;                               \
            char* dst = ws + WS_XF + (size_t)(pc >> 3) * 262144 + (b >> 1) * 2048 + (pc & 7) * 256 + ((b & 1) * 8 + r8) * 16;              \
            const u32x4 pa = u32x4{xi_[0][0], xi_[0][1], xi_[1][0], xi_[1][1]}, pb = u32x4{xi_[2][0], xi_[2][1], xi_[3][0], xi_[3][1]};    \
            ST16_SC1(dst, pa); ST16_SC1(dst + 2097152, pb); } while (0)
        SB(); ZST(4);
        asm volatile("s_waitcnt vmcnt(%1)\n\ts_bitcmp1_b32 %0, 2\n\ts_cbranch_scc1 1f\n\ts_waitcnt vmcnt(8)\n1:\n\ts_barrier" :: "s"(wave), "i"(9) : "memory", "scc");
        KST(2); ZST(6);
        f32x4 a16[8][4];
        bf16x8 xf[2][4];
        bf16x4 wl[4], wh[4];
#define KXR(kt, q, mb) xf[(q) & 1][mb] = lds_rd(xa[(kt) & 1][(q) >> 1] + (unsigned)((((q) & 1) * 4 + (mb)) * 2048))
#define KWL(kt, s32, nb) wl[nb] = tr_rd(wa[(kt) & 1][(nb) & 1] + (unsigned)((s32) * 16384 + ((nb) >> 1) * 2048))
#define KWH(kt, s32, nb) wh[nb] = tr_rd(wa[(kt) & 1][(nb) & 1] + (unsigned)((s32) * 16384 + ((nb) >> 1) * 2048 + 256))
#define KMF(kt, q, g) do { const bf16x8 w_ = CAT8(wl[(g) >> 2], wh[(g) >> 2]); constexpr int m_ = ((q) & 1) * 4 + ((g) & 3);               \
            if ((kt) == 0 && (q) < 2) { if ((g) >= 8) MFA16Z(a16[m_][(g) >> 2], xf[(q) & 1][(g) & 3], w_); else MFA16Z(a16[m_][(g) >> 2], w_, xf[(q) & 1][(g) & 3]); } \
            else                      { if ((g) >= 8) MFA16(a16[m_][(g) >> 2], xf[(q) & 1][(g) & 3], w_);  else MFA16(a16[m_][(g) >> 2], w_, xf[(q) & 1][(g) & 3]); } } while (0)
        u64 lnf;
#define LNFP (ws + WS_FLAG + F_LN + (wave * 32 + (lane & 31)) * 8)
#define KRQ(i) do { GLDS_S(qwo, wqb + (size_t)(((i) >> 1) * 64) * LDW, ldsbase + wave * 2048 + ((i) >> 1) * 16384 + ((i) & 1) * 896, ((i) & 1) * 128); } while (0)
        constexpr int KRS = 3, KPFD = 3;
#define KG(kt, q, g) do { constexpr int nk_ = (kt) + ((q) == 3), nq_ = ((q) + 1) & 3;                                                     \
            constexpr int xi = ((q) & 1) ? ((g) == 1 ? 0 : (g) == 2 ? 1 : (g) == 5 ? 2 : (g) == 6 ? 3 : -1) : (((g) & 1) && (g) < 8 ? (g) >> 1 : -1); \
            constexpr int ri = (q) == 3 ? ((g) == 9 ? 0 : (g) == 10 ? 1 : (g) == 13 ? 2 : (g) == 14 ? 3 : -1) : (((g) & 1) && (g) > 8 ? ((g) - 9) >> 1 : -1); \
            KMF(kt, q, g); SB();                                                                                                        \
            if (xi >= 0 && nk_ < 8) KXR(nk_, nq_, xi & 3);                                                                              \
            if (((q) & 1) && nk_ < 8) { if (((g) & 3) == 3) KWL(nk_, nq_ >> 1, (g) >> 2); if (((g) & 3) == 0 && (g) > 0) KWH(nk_, nq_ >> 1, (((g) >> 2) + 3) & 3); } \
            if (!((q) & 1) && (g) == 0 && (kt) + (q) > 0) KWH(kt, (q) >> 1, 3);                                                          \
            { constexpr int G = (q) == 3 ? (g) : (q) == 0 ? 16 + (g) : -9, T = (kt) + ((q) == 3 ? 2 : 1);          \
              if (T >= 2 && T < 8) { if (G == 0) KM0(T, 0); if (G == 4 * KRS) KM0(T, 1);                                                 \
                  if (G >= 1 && (G - 1) % KRS == 0 && (G - 1) / KRS < 8) { if ((G - 1) / KRS < 4) KRX(((G - 1) / KRS) & 3); else KRW(((G - 1) / KRS) & 3); } \
                  if (G == 29) OADV(xsp, 128); if (G == 30) OADV(wsp, 64 * LDW * 2); if (G == 31) OADV(wsv, 64 * LDW * 2); } }                                                 \
            if ((q) == 1 && (kt) + KPFD < 8) { if ((kt) >= 1 && (g) == 13) PFX((kt) + KPFD); }                \
            if ((q) == 2 && (kt) == 6 && (g) == 9) { qwo = QWOFF(); PIN(qwo); }                                                         \
            if ((q) == 1 && (kt) == 7) { if ((g) == 9) asm volatile("global_load_dwordx4 %0, %1, %2 sc1" : "=v"(rv) : "v"(xvo), "s"(ws + WS_R + wave * 1024) : "memory"); \
                                         if ((g) == 10) QXL1(0); if ((g) == 13) QXL1(1); if ((g) == 14) QXL1(2); }                      \
            if ((q) == 2 && (kt) == 7 && (g) == 2) QXL1(3);                                                                             \
            if ((q) == 3 && (kt) == 6 && ri >= 0) KRQ(ri & 3);                                                                          \
            if ((q) == 0 && (kt) == 7 && ri >= 0 && 4 + ri < 2 * QWD) KRQ(4 + (ri & 3));                                                                    \
            if ((q) == 1 && (kt) == 1) { if ((g) == 8) XI_RD();                                                                       \
                if ((g) == 13) ST4_SC1((float*)(ws + WS_R) + row, rr);                               \
                if ((g) == 15) XI_ST(); }                                                                                               \
            if ((q) == 1 && ((kt) == 3 || (kt) == 4)) { if ((g) == 9) PFQ((2 * (kt) - 6) & 3); if ((g) == 11) PFQ((2 * (kt) - 5) & 3); }                                                    \
            if ((q) == 2 && (kt) == 5 && (g) == 12) FLAG_GET(lnf, LNFP);                                                                \
            SB(); } while (0)
#define KQ(kt, q) do { KG(kt, q, 0); KG(kt, q, 1); KG(kt, q, 2); KG(kt, q, 3); KG(kt, q, 4); KG(kt, q, 5); KG(kt, q, 6); KG(kt, q, 7);    \
                       KG(kt, q, 8); KG(kt, q, 9); KG(kt, q, 10); KG(kt, q, 11); KG(kt, q, 12); KG(kt, q, 13); KG(kt, q, 14); KG(kt, q, 15); } while (0)
#define KEXT(s) (((s) == 1 ? 3 : (s) == 5 ? 1 : 0) + ((s) >= 1 && (s) + KPFD < 8) + ((s) == 3 || (s) == 4 ? 2 : 0))
#define KSTEP(kt) do {                                                                                                                  \
        KQ(kt, 0); KQ(kt, 1); KQ(kt, 2);                                                                                                \
        if ((kt) == 6) FLAG_WAIT(lnf, LNFP, tag, 0);                                   \
                       \
        if ((kt) == 0) asm volatile("s_waitcnt vmcnt(%1) lgkmcnt(0)\n\ts_bitcmp1_b32 %0, 2\n\ts_cbranch_scc1 1f\n\ts_waitcnt vmcnt(%2)\n1:\n\ts_barrier" :: "s"(wave), "i"(KEXT(0) + 1), "i"(KEXT(0)) : "memory", "scc"); \
        else asm volatile("s_waitcnt vmcnt(%0) lgkmcnt(0)\n\ts_barrier" :: "i"(KEXT(kt)) : "memory");                                        \
        KST((kt) + 3);                                                                                                                  \
        if ((kt) == 2) FLAG_PUT(ws + WS_FLAG + F_LN + b * 8, tag, wave == 0);             \
        if ((kt) == 6) FLAG_PUT(ws + WS_FLAG + F_EPOCH, tag - 0x9E3779B97F4A7C15ull + 1, b == 0 && wave == 0);     \
        SB();                                                                                                                           \
        KQ(kt, 3);                                                                                                                      \
    } while (0)
        KXR(0, 0, 0); KXR(0, 0, 1); KWL(0, 0, 0); KWH(0, 0, 0); KXR(0, 0, 2); KXR(0, 0, 3);
        KWL(0, 0, 1); KWH(0, 0, 1); KWL(0, 0, 2); KWH(0, 0, 2); KWL(0, 0, 3); KWH(0, 0, 3);
        SB();
        KSTEP(0); ZST(7); KSTEP(1); KSTEP(2); KSTEP(3); KSTEP(4); KSTEP(5); KSTEP(6);
        xvo = l16;
        KQ(7, 0); KQ(7, 1); KQ(7, 2);
        if (wave < 4) {
            int t2_ = threadIdx.x; asm volatile("" : "+v"(t2_));
            unsigned l7_ = t2_ & 7, c7_ = jj >= 3 ? 7 : 8, b0_ = (unsigned)(((jj + 7) & 7) * 128 + head * 4 + (wave & 3)), b7_ = 3584u + head * 4 + (wave & 3);
            u64 pg_ = (u64)((wave & 1) ? w_up : w_gate), pd_ = (u64)w_down - 1792ull * 8192, po_ = (u64)wo - 2688ull * 8192;
            asm volatile("" : "+s"(c7_), "+s"(b0_), "+s"(b7_), "+s"(pg_), "+s"(pd_), "+s"(po_));
            const unsigned lt_ = l7_ < (unsigned)jj ? l7_ : 0;
            unsigned n_ = b0_ + (13u - lt_) * lt_ * 64u; n_ = l7_ == c7_ ? b7_ : n_;
            u64 p_ = n_ < 2688u ? pd_ : po_; p_ = n_ < 1792u ? pg_ : p_; n_ = n_ < 1792u ? n_ >> 1 : n_;
            *(u64*)(lds + L_PFT + t2_ * 8) = p_ + (u64)n_ * 8192u;
        }
        SB();
        asm volatile("s_waitcnt vmcnt(%0)\n\ts_barrier" :: "i"(2 * QWD + 1) : "memory");
        SB();
#define PKCV(i) do { const f32x4 a_ = ((i) & 2) ? a16[2 * ((i) >> 2)][(i) & 3] : a16[2 * ((i) >> 2) + ((i) & 1)][0];                       \
                     const f32x4 b_ = ((i) & 2) ? a16[2 * ((i) >> 2) + 1][(i) & 3] : a16[2 * ((i) >> 2) + ((i) & 1)][1];                   \
                     pk[i] = u32x4{cvtpk(a_[0], a_[1]), cvtpk(a_[2], a_[3]), cvtpk(b_[0], b_[1]), cvtpk(b_[2], b_[3])}; PIN(pk[i]); } while (0)
#define KL(i) do { KMF(7, 3, 2 * (i)); SB(); if ((i) >= 2) QXL1(2 * (i)); SB(); KMF(7, 3, 2 * (i) + 1); SB(); PKCV(i); if ((i) >= 2) QXL1(2 * (i) + 1); SB(); } while (0)
        KL(0); KL(1); KL(2); KL(3); KL(4); KL(5); KL(6); KL(7);
        KST(10);
        MF_DONE();
#pragma unroll
        for (int i = 8; i < 16; ++i) PKCV(i);
        SB();
        KST(11); ZST_OUT();
#undef PKCV
#undef KSTEP
#undef PFQ
#undef XKEY
    }
    PH_STAMP(1);
    const char* kvg = ws + WS_KV + (size_t)head * HSTR;
    const unsigned svv = xvo + 32768u; const char* sb_[4];
    const int NA = qbA + 1;
    {
        constexpr int LDW = NH * HD, STG = 16384;
        int tid = threadIdx.x; asm volatile("" : "+v"(tid));
        const int lane = tid & 63, l15 = lane & 15, q4 = lane >> 4;
        const unsigned woff = qwo, xvo2 = xvo + 262144u;
        const unsigned wa = ldsbase + (unsigned)((q4 >> 1) * 4096 + (4 * (q4 & 1) + (l15 >> 2)) * 64 + (q4 & 1) * 32 + (lane & 3) * 8), wa2 = wa ^ 32u;
        f32x4 acc[2][8];
        bf16x4 wl[8], wh[8];
#pragma unroll
        for (int i = 0; i < 4; ++i) {
            sb_[i] = ws + WS_KV + (size_t)hp * HSTR + (size_t)(4 * i + (wave >> 2) * 2 + (jj >> 2)) * 65536 + (jj & 3) * 8192 + (wave & 3) * 2048;
            asm volatile("" : "+s"(sb_[i])); }
#define KVST(i) asm volatile("global_store_dwordx4 %0, %1, %2 offset:%3 sc1" :: "v"(((i) & 2) ? svv : xvo), "v"(pk[i]), "s"(sb_[(i) >> 2]), "i"(((i) & 1) * 1024) : "memory")
        u64 kvf = 0;
#define QKV(kt, f) do { constexpr int g_ = q_kv(kt, f), p_ = g_ >> 2; if (g_ >= 0) {                                                       \
            if ((g_ & 3) == 0) asm volatile("s_mov_b32 m0, %0\n\ts_nop 0" :: "s"(ldsbase + (p_ == 1 ? 0 : 65536 + p_ * 16384) + wave * 4096) : "memory"); \
            asm volatile("global_load_lds_dwordx4 %0, %1 offset:%2 sc1" :: "v"(xvo), "s"(kvg + p_ * 32768 + wave * 4096), "i"((g_ & 3) * 1024) : "memory"); } } while (0)
#define KVFP (ws + WS_FLAG + ((lane & 8) ? F_TOP : F_KV + 2048 + (head * 8 + (lane & 7)) * 8))
        const bool top2 = b == 56;
        unsigned* tw = (unsigned*)(lds + 131072 + 2048);
#define QFA(s, f) ((((f) & 1) ? wa2 : wa) + (unsigned)(((s) & 3) * STG + ((f) >> 3) * 8192 + (((f) >> 2) & 1) * 1024 + (((f) >> 1) & 1) * 512))
#define QRL(s, f) wl[(f) & 7] = tr_rd(QFA(s, f))
#define QRH(s, f) wh[(f) & 7] = tr_rd(QFA(s, f) + 2048u)
#define QMF(a, kt, f) do { if ((kt) == 0 && (f) < 8) MFA16Z(acc[a][(f) & 7], w_, xr[0][a][0]); else MFA16(acc[a][(f) & 7], w_, xr[(kt) & 3][a][(f) >> 3]); } while (0)
#define QPAIR(kt, f, A, B) do { const bf16x8 w_ = CAT8(wl[(f) & 7], wh[(f) & 7]); constexpr int s_ = (kt) + (((f) + 4) >> 4), m_ = ((f) + 4) & 15;  \
            QMF(0, kt, f); SB(); if (s_ < 16) QRL(s_, m_); A; QKV(kt, f); SB();                                      \
            QMF(1, kt, f); SB(); if (s_ < 16) QRH(s_, m_); B; SB(); if (((f) & 1) && s_ < 16) { __builtin_amdgcn_s_waitcnt(0xC47F); SB(); } } while (0)
        const char* xsp = xg + 3 * 262144;
#define QX(r, rb, ks, v_) asm volatile("global_load_dwordx4 %0, %1, %2 offset:%3 sc1" : "+v"(xr[r][rb][ks]) : "v"(v_), "s"(xsp), "i"((rb) * 2048 + (ks) * 1024) : "memory")
        const unsigned woff1 = woff + 1024u, woff2 = woff + 128u;
        const char* wsp = (const char*)(wqb + (size_t)QWD * 64 * LDW) - 1024;
        const unsigned m0b = ldsbase + wave * 2048;
#define QM0(r) asm volatile("s_add_u32 m0, %0, %1" :: "s"(m0b), "i"((r) * STG) : "memory", "scc")
#define QW1(v_, imm_) asm volatile("global_load_lds_dwordx4 %0, %1 offset:%2" :: "v"(v_), "s"(wsp), "i"(imm_) : "memory")
#define QST(i)
#define QSTEP(kt) do { constexpr int r_ = (kt) & 3;                                                                                     \
        QPAIR(kt, 0, if (q_xb(kt)) QX((r_ + 3) & 3, 0, 1, xvo), );                                                                      \
        QPAIR(kt, 1, , );                                                                                                               \
        QPAIR(kt, 2, if (q_xb(kt)) QX((r_ + 3) & 3, 1, 1, xvo), );                                                                      \
        QPAIR(kt, 3, if ((kt) == 12) FLAG_GET(kvf, KVFP), );                                                                            \
        if ((kt) == 0 && __builtin_expect(top2, 0)) {                                                                                                        \
            unsigned t1 = tkey(rv[0], tid * 4), t2 = 0;                                                                                 \
            _Pragma("unroll") for (int j = 1; j < 4; ++j) merge_top2(t1, t2, tkey(rv[j], tid * 4 + j), 0);                              \
            DPP_TOP2(t1, t2, 0x111, 0xf); DPP_TOP2(t1, t2, 0x112, 0xf); DPP_TOP2(t1, t2, 0x114, 0xf); DPP_TOP2(t1, t2, 0x118, 0xf);     \
            DPP_TOP2(t1, t2, 0x142, 0xa); DPP_TOP2(t1, t2, 0x143, 0xc);                                                                 \
            if (lane == 63) { tw[wave * 2] = t1; tw[wave * 2 + 1] = t2; } SB(); }                                                       \
        QPAIR(kt, 4, if ((QS1 >> (kt)) & 1) KVST(q_s0(kt) & 15), if ((QS1 >> (kt)) & 1) KEEP(pk[q_s0(kt) & 15]));                       \
        QPAIR(kt, 5, , );                                                                                                               \
        QPAIR(kt, 6, , );                                                                                                               \
        QPAIR(kt, 7, , );                                                                                                               \
        QPAIR(kt, 8, if (q_xa(kt)) QX(r_, 0, 0, xvo2), );                                                                               \
        QPAIR(kt, 9, , );                                                                                                               \
        QPAIR(kt, 10, if (q_xa(kt)) QX(r_, 1, 0, xvo2), if ((kt) <= 11) OADV(xsp, 262144));                                             \
        QPAIR(kt, 11, if ((kt) + QWD < 16) QM0((r_ + QWD) & 3), );                                                                      \
        if ((kt) < 15) {                              \
            if (QWD == 4 || (kt) == 0 || (kt) >= 13) asm volatile("s_waitcnt vmcnt(%0) lgkmcnt(0)\n\ts_barrier" :: "i"(q_wait(kt)) : "memory"); \
            else asm volatile("s_waitcnt vmcnt(%0)\n\ts_barrier" :: "i"(q_wait(kt)) : "memory");                                        \
            if ((kt) == 13) { PIN(kvf); const unsigned sh_ = (lane & 8) ? 22u : 0u; const u64 ex_ = (tag << sh_) >> sh_;     \
                if (__builtin_expect(__builtin_amdgcn_ballot_w64((kvf >> sh_) != ex_) != 0, 0)) { unsigned c_; u64 t_;                    \
                    asm volatile("s_mov_b32 %1, 0x40000\n1:\n\ts_sleep 2\n\tglobal_load_dwordx2 %0, %3, off sc1\n\ts_waitcnt vmcnt(0)\n\tv_lshrrev_b64 %2, %5, %0\n\t" \
                                 "v_cmp_ne_u64 vcc, %4, %2\n\ts_cmp_eq_u64 vcc, 0\n\ts_cbranch_scc1 2f\n\ts_sub_u32 %1, %1, 1\n\ts_cbranch_scc0 1b\n2:" \
                                 : "+v"(kvf), "=&s"(c_), "=&v"(t_) : "v"(KVFP), "v"(ex_), "v"(sh_) : "memory", "vcc", "scc"); }           \
                const unsigned r_ = __builtin_amdgcn_readlane((unsigned)kvf, 8); ri1 = r_ & 2047u; ri2 = (r_ >> 11) & 2047u; }           \
            QST((kt) + 1); SB(); }                                                                                                      \
        QPAIR(kt, 12, if ((kt) + QWD < 16) QW1(woff1, 0), );                                                                            \
        QPAIR(kt, 13, , );                                                                                                              \
        QPAIR(kt, 14, if ((kt) + QWD < 16) QW1(woff2, 1024), if ((kt) + QWD < 15) OADV(wsp, 64 * LDW * 2));                             \
        if ((kt) == q_fl()) FLAG_PUT(ws + WS_FLAG + F_KV + 2048 + (hp * 8 + jj) * 8, tag, wave == 0);     \
        QPAIR(kt, 15, , );                                                                                                              \
        if ((kt) == 0 && __builtin_expect(top2, 0)) {                                                                                                        \
            unsigned t1 = tw[0], t2 = tw[1];                                                                                            \
            _Pragma("unroll") for (int w = 1; w < 8; ++w) merge_top2(t1, t2, tw[w * 2], tw[w * 2 + 1]);                                 \
            FLAG_PUT(ws + WS_FLAG + F_TOP, (tag << 22) | (unsigned)__builtin_amdgcn_readfirstlane((~t1 & 2047u) | (~t2 & 2047u) << 11), wave == 0); \
            } \
        SB();                                                                                                                           \
    } while (0)
        QRL(0, 0); QRH(0, 0); QRL(0, 1); QRH(0, 1); QRL(0, 2); QRH(0, 2); QRL(0, 3); QRH(0, 3); SB();
        asm volatile("s_waitcnt vmcnt(12)" : "+v"(rv) :: "memory");
        SB();
        QST(0);
        KST(12);
        QSTEP(0); QSTEP(1); QSTEP(2); QSTEP(3); QSTEP(4); QSTEP(5); QSTEP(6); QSTEP(7);
        QSTEP(8); QSTEP(9); QSTEP(10); QSTEP(11); QSTEP(12); QSTEP(13); QSTEP(14); QSTEP(15);
        MF_DONE();
        QST(16);
        {
            constexpr float qs = 0.08838834764831845f * 1.4426950408889634f;
#pragma unroll
            for (int i = 0; i < 8; ++i) { const f32x4 a_ = acc[i & 1][i & 6] * qs, b_ = acc[i & 1][i | 1] * qs;
                qf[i] = __builtin_bit_cast(bf16x8, u32x4{cvtpk(a_[0], a_[1]), cvtpk(a_[2], a_[3]), cvtpk(b_[0], b_[1]), cvtpk(b_[2], b_[3])}); PIN(qf[i]); }
        }
#undef QSTEP
    }
    PH_STAMP(2);
    const int kz = xcd & 1, kp = xcd >> 1, nt = kz * 4 + (idx >> 3), tile = jj * 8 + nt;
#define K64(s) ((head * 2) ^ (s))
#define OWOFF() ({ unsigned l_ = threadIdx.x & 63; asm volatile("" : "+v"(l_)); (unsigned)(((l_ >> 2) & 7) * DM + 32 * (l_ >> 5) + 8 * ((l_ & 3) ^ ((l_ >> 4) & 1))) * 2u; })
    const int n0 = (nt ^ 2) * 128, dn1 = (((nt ^ 3) - (nt ^ 2)) * 128 + 64) * 2, ntc = nt ^ 2 ^ (kp >> 1);
    const u16* wob = wo + (size_t)(8 * wave) * DM + n0;
    u64 cxf = tag + 1;
#define CFB (ws + WS_FLAG + F_CTX)
#define CFO() ({ unsigned l_ = threadIdx.x & 15, s0_ = (unsigned)((qb * 4 + qw) * 32 + head), s1_ = (unsigned)((qbB * 4 + 3 - (wave & 3)) * 32 + (head ^ 8)); \
                 asm volatile("" : "+v"(l_), "+s"(s0_), "+s"(s1_)); (l_ < 8 ? l_ ^ s0_ : s1_) * CFS; })
    u32x4 pc[8];
#define EAR_(v_, b_, pol_) "s_add_u32 m0, m0, 0x400\n\ts_nop 0\n\tglobal_load_lds_dwordx4 " v_ ", " b_ pol_ "\n\t"
#define EALD(h_) do { unsigned l_ = threadIdx.x & 63; asm volatile("" : "+v"(l_)); const unsigned lx_ = ((l_ >> 3) * DM + 8 * ((l_ & 7) ^ (l_ >> 3))) * 2u; \
        const int rb_ = ((h_) ? qbB * 8 + qw * 2 : qbA * 8 + (wave & 3) * 2) + (kp & 1);                                                  \
        asm volatile("s_mov_b32 m0, %5\n\t" EAR_("%0", "%3", "") EAR_("%1", "%3", "") EAR_("%2", "%4", " sc1") "global_load_lds_dwordx4 %2, %4 offset:1024 sc1" \
                     :: "v"(lx_), "v"(lx_ + 8 * DM * 2), "v"(l_ * 16u), "s"(x + (size_t)rb_ * 16 * DM + ntc * 128 + (kp >> 1) * 64),              \
                        "s"(ws + WS_XF + (size_t)(2 * ntc + (kp >> 1)) * 262144 + rb_ * 2048),                                             \
                        "s"(ldsbase + 131072 - 1024 + (wave & 3) * 8192 + (h_) * 4096) : "memory", "scc"); } while (0)
    const unsigned i1 = (((kz ? F1S : F1F) >> (4 * jj)) & 15u) * 16u + (kp * 4 + (idx >> 3));
    const unsigned i2 = (((kz ? F2S : F2F) >> (4 * jj)) & 15u) * 16u + (kp * 4 + (idx >> 3));
    const unsigned nf1 = i1 / 112u, nf2 = i2 / 112u;
    const unsigned k1 = i1 / 14u;
    unsigned ypub = 1;
    {
        int tid = threadIdx.x; asm volatile("" : "+v"(tid));
        const int lane = tid & 63;
        const unsigned l16 = (unsigned)lane * 16u;
#define EX(x) __builtin_amdgcn_exp2f(x)
#define TBAR() do { SB(); __builtin_amdgcn_s_waitcnt(0x0070); asm volatile("s_barrier" ::: "memory"); } while (0)
#define BRQ(t, r) do { constexpr int g_ = (r) >> 2; const int tt_ = (t) + (g_ == 2 ? 1 : 2);                                               \
            const unsigned o_ = (unsigned)((wave & 3) * 4096 + (g_ == 1 || g_ == 2 ? 16384 : 0));                                          \
            if (((r) & 3) == 0) GLDS_C(l16, kvg + (size_t)tt_ * 65536 + (g_ >= 2 ? 32768 : 0) + o_, ldsbase + (g_ >= 2 ? 0 : 65536) + (tt_ & 1) * 32768 + o_, 0); \
            else asm volatile("global_load_lds_dwordx4 %0, %1 offset:%2 sc1" :: "v"(l16), "s"(kvg + (size_t)tt_ * 65536 + (g_ >= 2 ? 32768 : 0) + o_), "i"(((r) & 3) * 1024) : "memory"); } while (0)
#define BRQ4(t, g) do { BRQ(t, 4 * (g)); BRQ(t, 4 * (g) + 1); BRQ(t, 4 * (g) + 2); BRQ(t, 4 * (g) + 3); } while (0)
#define BRQ2C(t, r) do { constexpr int g_ = (r) >> 2; const int tt_ = (t) + (g_ == 2 ? 1 : 2);                                             \
            const unsigned o_ = (unsigned)((wave & 3) * 4096 + (g_ == 1 || g_ == 2 ? 16384 : 0));                                          \
            asm volatile("s_cmp_eq_u32 %5, 0\n\ts_cbranch_scc1 1f\n\ts_mov_b32 m0, %2\n\ts_nop 0\n\tglobal_load_lds_dwordx4 %0, %1 offset:%3 sc1\n\t"  \
                         "global_load_lds_dwordx4 %0, %1 offset:%4 sc1\n1:"                                                               \
                         :: "v"(l16), "s"(kvg + (size_t)tt_ * 65536 + (g_ >= 2 ? 32768 : 0) + o_), "s"(ldsbase + (g_ >= 2 ? 0 : 65536) + (tt_ & 1) * 32768 + o_), \
                            "i"(((r) & 3) * 1024), "i"(((r) & 3) * 1024 + 1024), "s"(isb) : "memory", "scc"); } while (0)
        const unsigned isb = wave >> 2;
        u64 kf2 = tag;
        const char* kfb = ws + WS_FLAG + F_KV + head * 64;
#define KCHK() do { PIN(kf2); if (__builtin_expect((__builtin_amdgcn_ballot_w64(kf2 != tag) | (u64)((33u >> t) & 1u)) != 0, 0)) { \
            unsigned c_, a_;                                                                                                              \
            FLAG_PUTZ(ws + WS_FLAG + F_KV + (hp * 8 + jj) * 8, tag, (unsigned)(wave ^ 4) | (unsigned)t);                                  \
            asm volatile("s_cmp_lt_u32 %6, 5\n\ts_cbranch_scc1 3f\n\tv_lshrrev_b32 %2, 1, %5\n\tv_and_b32 %2, 56, %2\n\ts_mov_b32 %1, 0x40000\n" \
                         "1:\n\tglobal_load_dwordx2 %0, %2, %3 sc1\n\ts_cmp_eq_u32 %6, 5\n\ts_cbranch_scc1 3f\n\ts_waitcnt vmcnt(0)\n\tv_cmp_ne_u64 vcc, %4, %0\n\t" \
                         "s_cmp_eq_u64 vcc, 0\n\ts_cbranch_scc1 3f\n\ts_sleep 2\n\ts_sub_u32 %1, %1, 1\n\ts_cbranch_scc0 1b\n"           \
                         "3:" : "+v"(kf2), "=&s"(c_), "=&v"(a_) : "s"(kfb), "s"(tag), "v"(l16), "s"(t) : "memory", "vcc", "scc"); } SB(); } while (0)
        u64 pft = *(const u64*)(lds + L_PFT + (tid & 255) * 8); PIN(pft);
#define PFW1(i_) do { unsigned a_; const u64 p_ = ((u64)(unsigned)__builtin_amdgcn_readlane((int)(pft >> 32), i_) << 32) | (unsigned)__builtin_amdgcn_readlane((int)pft, i_); \
            asm volatile("v_lshlrev_b32 %0, 3, %2\n\ts_mov_b32 m0, %3\n\ts_nop 2\n\tglobal_load_lds_dword %0, %1"                        \
                         : "=&v"(a_) : "s"(p_), "v"(l16), "s"(ldsbase + 131072 + (wave & 3) * 8192) : "memory"); } while (0)
#define ONCE2(n_) int CAT(n_once, __LINE__) = (int)(n_); asm volatile("" : "+s"(CAT(n_once, __LINE__))); _Pragma("unroll 1") for (int z_ = 0; z_ < CAT(n_once, __LINE__); ++z_)
#define AST(i)
#define XST(i)
#define TS0()
#define TS1()
        AST(0);
        asm volatile("s_bitcmp0_b32 %0, 2\n\ts_cbranch_scc1 1f\n\ts_setprio 1\n1:" :: "s"(wave) : "scc");
        {
            f32x4 o[8][2], ls[2], ng[2], S[2][4];
            bf16x8 pf[2][2], fr[16], ones;
            float e[16];
            constexpr bool HOT = false;
            const unsigned lb16 = ldsbase + l16;
            unsigned ak = 65536u + lb16, av = lb16;
            int t = 0;
#define KOFF(f) ((((f) & 3) * 2 + (((f) >> 2) & 1)) * 1024)
#define RD(kb, n) do { constexpr int k_ = ((kb) + ((n) >> 4)) & 3, n_ = (n) & 15;                                                          \
            fr[n_] = n_ < 8 ? lds_rd(av + (unsigned)(((k_ + 3) & 3) * 8192 + n_ * 1024)) : lds_rd(ak + (unsigned)(((k_ + 1) & 3) * 8192 + KOFF(n_ & 7))); } while (0)
#define MX(p, n, a) MFA16(o[n][a], fr[n], pf[(p) ^ 1][a])
#define MY(p, n, h) do { constexpr int a_ = ((n) >> 1) & 1, t_ = 2 * ((n) & 1) + (h);                                                      \
                         if (t_) MFA16(S[(p) ^ 1][(((n) >> 1) & 2) + a_], fr[((n) & 12) + t_], qf[2 * t_ + a_]);                           \
                         else MFA16C(S[(p) ^ 1][(((n) >> 1) & 2) + a_], fr[(n) & 12], qf[a_], ng[a_]); } while (0)
#define GX(p, n, F1, F2) do { MX(p, n, 0); SB(); F1; SB(); MX(p, n, 1); SB(); F2; SB(); } while (0)
#define GY(p, n, F1, F2) do { MY(p, n, 0); SB(); F1; SB(); MY(p, n, 1); SB(); F2; SB(); } while (0)
#define GL(p, F1, F2) do { MFA16(ls[0], ones, pf[(p) ^ 1][0]); SB(); F1; SB(); MFA16(ls[1], ones, pf[(p) ^ 1][1]); SB(); F2; SB(); } while (0)
#define SV(p, i) S[p][(((i) >> 1) & 2) + ((i) >> 3)][(i) & 3]
#define E1(p, i) do { e[i] = EX(SV(p, i)); if (HOT) KEEP(e[i]); else PIN(e[i]); } while (0)
#define C1(p, j) do { u32x4 w_ = __builtin_bit_cast(u32x4, pf[p][(j) >> 2]); w_[(j) & 3] = cvtpk(e[2 * (j)], e[2 * (j) + 1]);               \
                      pf[p][(j) >> 2] = __builtin_bit_cast(bf16x8, w_); if (HOT) KEEP(w_[(j) & 3]); else PIN(pf[p][(j) >> 2]); } while (0)
#define DQ(kb, g) do { if (ISB_ && (kb) == 3) { if ((g) == 0) KCHK(); BRQ(t, g); } if (ISB_ && (kb) == 0 && (g) == 1) PFW1(t); } while (0)
#define W4() do { SB(); __builtin_amdgcn_s_waitcnt(0xC37F); } while (0)
#define US(kb, ISB) do { constexpr bool HOT = true, ISB_ = (ISB) == 1; constexpr int p_ = (kb) & 1; constexpr bool B_ = (kb) == 2;                    \
            GX(p_, 0, RD(kb, 7); if ((kb) == 3) { ak ^= 32768u; PIN(ak); }, E1(p_, 0); DQ(kb, 0));                                        \
            GX(p_, 1, RD(kb, 8); if ((kb) == 0) { av ^= 32768u; PIN(av); }, E1(p_, 1); DQ(kb, 1));                                        \
            GL(p_, E1(p_, 2), E1(p_, 3));                                                                                                 \
            GX(p_, 2, RD(kb, 9); C1(p_, 0), E1(p_, 4); DQ(kb, 2));                                                                        \
            GX(p_, 3, RD(kb, 10); W4(), E1(p_, 5); DQ(kb, 3));                                                                            \
            GX(p_, 4, RD(kb, 11); C1(p_, 1), E1(p_, 6); DQ(kb, 4));                                                                       \
            GX(p_, 5, RD(kb, 12); C1(p_, 2), E1(p_, 7); DQ(kb, 5));                                                                       \
            GX(p_, 6, RD(kb, 13); C1(p_, 3), E1(p_, 8); DQ(kb, 6));                                                                       \
            GX(p_, 7, RD(kb, 14); W4(), E1(p_, 9); DQ(kb, 7));                                                                            \
            GY(p_, 8, RD(kb, 15), E1(p_, 10); DQ(kb, 8));                                           \
            GY(p_, 9, if (!B_) RD(kb, 16); C1(p_, 4), E1(p_, 11); DQ(kb, 9));                                                             \
            GY(p_, 10, if (B_) { TS0(); if (ISB_) { __builtin_amdgcn_s_waitcnt(0x0071); asm volatile("s_barrier" ::: "memory"); } else TBAR(); \
                                 TS1(); SB(); RD(kb, 16); }                                                                               \
                       RD(kb, 17); C1(p_, 5), E1(p_, 12); DQ(kb, 10));                                                                    \
            GY(p_, 11, RD(kb, 18); if (!B_) W4(), E1(p_, 13); DQ(kb, 11));                                                                \
            GY(p_, 12, RD(kb, 19), E1(p_, 14); DQ(kb, 12));                                                \
            GY(p_, 13, RD(kb, 20); C1(p_, 6), E1(p_, 15); DQ(kb, 13));                                                                    \
            GY(p_, 14, RD(kb, 21); C1(p_, 7), DQ(kb, 14));                                 \
            GY(p_, 15, RD(kb, 22), DQ(kb, 15); W4());                                                                                     \
        } while (0)
            asm volatile("" : "+s"(outp));
            asm volatile("s_waitcnt vmcnt(4) lgkmcnt(0)\n\ts_barrier" ::: "memory");
            AST(1);
#pragma unroll
            for (int s_ = 0; s_ < 8; ++s_) fr[s_] = lds_rd(ak + (unsigned)KOFF(s_));
            pf[1][0] = bf16x8{}; PIN(pf[1][0]);
            { const unsigned o1 = (lane & 3) ? 0u : 0x3f803f80u; ones = __builtin_bit_cast(bf16x8, u32x4{o1, o1, o1, o1}); PIN(ones); }
            BRQ2C(-1, 12); BRQ2C(-1, 14);
            SB();
#define M0_(n, a) do { if ((n) & 3) MFA16(S[0][(((n) >> 1) & 2) + (a)], fr[n], qf[2 * ((n) & 3) + (a)]); else MFA16Z(S[0][(((n) >> 1) & 2) + (a)], fr[n], qf[a]); } while (0)
#define G0_(n) do { M0_(n, 0); SB(); if (Q_NST + (n) < 16) KVST((Q_NST + (n)) & 15); SB(); M0_(n, 1); SB(); RD(0, 8 + (n));               \
                    if (Q_NST + (n) < 16) KEEP(pk[(Q_NST + (n)) & 15]); SB(); } while (0)
            G0_(0); G0_(1); G0_(2); G0_(3); G0_(4); G0_(5); G0_(6); G0_(7);
#pragma unroll
            for (int d = 0; d < 18; ++d) { if (d < 16) MFA16Z(o[d >> 1][d & 1], pf[1][0], pf[1][0]); else MFA16Z(ls[d & 1], pf[1][0], pf[1][0]); }
            SB();
            { ONCE2(isb * min(jj, 1)) { PFW1(0); } }
            {
                PIN(S[0][0]); PIN(S[0][1]); PIN(S[0][2]); PIN(S[0][3]);
                if (__builtin_expect((qb | qw) == 0, 0)) {
#pragma unroll
                    for (int r = 0; r < 4; ++r) { S[0][2][r] = -__builtin_inff(); if (4 * (lane >> 4) + r > (lane & 15)) S[0][0][r] = S[0][3][r] = -__builtin_inff(); } }
#pragma unroll
                for (int a = 0; a < 2; ++a) {
                    float mx_ = SV(0, 8 * a);
#pragma unroll
                    for (int r = 1; r < 8; ++r) mx_ = fmaxf(mx_, SV(0, 8 * a + r));
                    mx_ = -(fmaxf(quad_max(mx_), -1e30f) + POFF);
                    ng[a] = f32x4{mx_, mx_, mx_, mx_}; PIN(ng[a]);
                }
#pragma unroll
                for (int i = 0; i < 16; ++i) { float v_ = SV(0, i) + ng[i >> 3][0]; PIN(v_); SV(0, i) = v_; }
                PIN(S[0][0]); PIN(S[0][1]); PIN(S[0][2]); PIN(S[0][3]);
            }
            SB();
            GY(0, 8, E1(0, 0), RD(0, 16); E1(0, 1)); GY(0, 9, E1(0, 2); C1(0, 0), RD(0, 17); E1(0, 3));
            GY(0, 10, E1(0, 4); C1(0, 1), RD(0, 18); E1(0, 5)); GY(0, 11, E1(0, 6); C1(0, 2), RD(0, 19); E1(0, 7));
            GY(0, 12, E1(0, 8); C1(0, 3), RD(0, 20); E1(0, 9)); GY(0, 13, E1(0, 10); C1(0, 4), RD(0, 21); E1(0, 11));
            GY(0, 14, E1(0, 12); C1(0, 5), RD(0, 22); E1(0, 13)); GY(0, 15, E1(0, 14); C1(0, 6), E1(0, 15); C1(0, 7));
            pf[1][1] = pf[1][0];
#define SCOPY() do { MF_DONE(); _Pragma("unroll") for (int i = 0; i < 4; ++i) { S[1][i] = S[0][i]; PIN(S[1][i]); } } while (0)
            if (__builtin_expect((qb | qw) == 0, 0)) { SCOPY(); pf[0][0] = pf[0][1] = bf16x8{}; PIN(pf[0][0]); PIN(pf[0][1]); }
            W4(); asm volatile("s_nop 1"); SB();
            AST(2);
            {
                const int nl = qb - (qw == 0), nB = isb ? nl : 0;
#pragma unroll 1
                while (t < nB) { US(1, 1); US(2, 1); US(3, 1); ++t; US(0, 1); }
#pragma unroll 1
                while (t < nl) { US(1, 0); US(2, 0); US(3, 0); ++t; US(0, 0); }
            }
            AST(3);
            SST(1);
#define PCOPY() do { SCOPY(); pf[0][0] = pf[1][0]; pf[0][1] = pf[1][1]; PIN(pf[0][0]); PIN(pf[0][1]); SB(); asm volatile("s_nop 1"); } while (0)
            {
                const int ne = (qb | qw) == 0 ? 0 : (qw + 3) & 3;
                if (__builtin_expect(ne >= 1, 1)) {
                    US(1, 2);
                    if (__builtin_expect(ne >= 2, 1)) {
                        US(2, 2);
                        if (__builtin_expect(ne == 3, 0)) {
                            if (isb) { KCHK(); asm volatile("s_setprio 0"); BRQ4(t, 0); BRQ4(t, 1); BRQ4(t, 2); BRQ4(t, 3); asm volatile("s_setprio 1"); }
                            US(3, 2); }
                    }
                }
                int np = ne; asm volatile("" : "+s"(np));
                if (__builtin_expect(np & 1, 0)) PCOPY();
                KEEP(ng[0]); KEEP(ng[1]);
                t = qb;
            }
#define RDV(n) fr[n] = lds_rd(avf + (unsigned)((n) * 1024))
#define M1(i) do { e[i] = __builtin_amdgcn_fmed3f(e[i], 0.f, rmp[(i) & 3]); PIN(e[i]); } while (0)
#define E2(i) E1(1, i)
#define CST(i) asm volatile("global_store_dwordx4 %0, %1, %2 offset:%3 sc1\n\ts_nop 1" :: "v"(l16), "v"(pc[i]), "s"(cb + ((i) >> 2) * 262144), "i"((((i) >> 1) & 1) * 1024 + ((i) & 1) * 2048) : "memory")
#define NRM(ISB, i) do { float y_[8];                                              \
                _Pragma("unroll") for (int z_ = 0; z_ < 8; ++z_) { y_[z_] = o[((i) & 6) + (z_ >> 2)][(i) & 1][z_ & 3] * inv[(i) & 1]; PIN(y_[z_]); } \
                pc[i] = u32x4{cvtpk(y_[0], y_[1]), cvtpk(y_[2], y_[3]), cvtpk(y_[4], y_[5]), cvtpk(y_[6], y_[7])};                        \
                PIN(pc[i]); if (!(ISB)) CST(i); SB(); } while (0)
#define XLD(b_) do { _Pragma("unroll") for (int n = 0; n < 8; ++n) asm volatile("global_load_dwordx4 %0, %1, %2 offset:%3 sc1" : "=v"(fr[n]) : "v"(l16 + (n >> 2) * 4096u), "s"(b_), "i"((n & 3) * 1024) : "memory"); \
                asm volatile("s_waitcnt vmcnt(0)" : "+v"(fr[0]), "+v"(fr[1]), "+v"(fr[2]), "+v"(fr[3]), "+v"(fr[4]), "+v"(fr[5]), "+v"(fr[6]), "+v"(fr[7]) :: "memory"); } while (0)
#define MFB(a_, b_, c_) __builtin_amdgcn_mfma_f32_16x16x32_bf16(a_, b_, c_, 0, 0, 0)
#define EXACT() do { __builtin_amdgcn_s_waitcnt(0xC07F); MF_DONE();                                                                                                           \
                float mr_[2] = {-1e30f, -1e30f}; const int nu_ = 4 * qb + qw2;                                                            \
                ls[0] = ls[1] = f32x4{}; _Pragma("unroll") for (int d = 0; d < 16; ++d) o[d >> 1][d & 1] = f32x4{};                       \
                _Pragma("unroll 1") for (int u = 0; u <= nu_; ++u) {                                                                      \
                    const char* kb_ = kvg + (size_t)(u >> 2) * 65536 + (u & 3) * 8192;                                                    \
                    XLD(kb_);                                                                                                             \
                    _Pragma("unroll") for (int i = 0; i < 4; ++i) { f32x4 s_ = f32x4{};                                                   \
                        _Pragma("unroll") for (int d = 0; d < 4; ++d) s_ = MFB(fr[2 * d + (i >> 1)], qf[2 * d + (i & 1)], s_);            \
                        S[1][i] = s_; }                                                                                                   \
                    if (u == nu_) { _Pragma("unroll") for (int r = 0; r < 4; ++r) { S[1][2][r] = -__builtin_inff();                       \
                        if (4 * (ln2 >> 4) + r > (ln2 & 15)) S[1][0][r] = S[1][3][r] = -__builtin_inff(); } }                             \
                    XLD(kb_ + 32768);                                                                                                     \
                    _Pragma("unroll") for (int a = 0; a < 2; ++a) {                                                                       \
                        float mx_ = SV(1, 8 * a); _Pragma("unroll") for (int r = 1; r < 8; ++r) mx_ = fmaxf(mx_, SV(1, 8 * a + r));       \
                        mx_ = fmaxf(quad_max(mx_), mr_[a]); const float al_ = EX(mr_[a] - mx_); mr_[a] = mx_;                             \
                        u32x4 w_; _Pragma("unroll") for (int j = 0; j < 4; ++j) w_[j] = cvtpk(EX(SV(1, 8 * a + 2 * j) - mx_), EX(SV(1, 8 * a + 2 * j + 1) - mx_)); \
                        const bf16x8 p_ = __builtin_bit_cast(bf16x8, w_);                                                                 \
                        ls[a] = MFB(ones, p_, ls[a] * al_);                                                                               \
                        _Pragma("unroll") for (int d = 0; d < 8; ++d) o[d][a] = MFB(fr[d], p_, o[d][a] * al_); } }                        \
                pf[1][0] = pf[1][1] = bf16x8{}; _Pragma("unroll") for (int n = 4; n < 8; ++n) fr[n] = bf16x8{};     \
                PIN(pf[1][0]); PIN(pf[1][1]); MF_DONE(); } while (0)
#define TAIL(ISB) do { float rmp[4];                                                                                         \
                XST(0);                                                                                                                   \
                MX(1, 0, 0); SB();                                                                                                        \
                int ln2 = lane, qw2 = qw; asm volatile("" : "+v"(ln2), "+s"(qw2));   \
                fr[7] = lds_rd(av + (unsigned)((qw2 + 3) & 3) * 8192u + 7168u); SB();                                                     \
                MX(1, 0, 1); SB(); E2(0); SB();                                                                                           \
                GX(1, 1, E2(1), E2(2));                                                                                                   \
                GL(1, E2(3), E2(8));                                                                                                      \
                float fl_ = (float)((ln2 & 15) - 4 * (ln2 >> 4)); PIN(fl_);                                                               \
                unsigned avf = lb16 + (unsigned)(t & 1) * 32768u + (unsigned)qw2 * 8192u; PIN(avf); SB();                                 \
                GX(1, 2, E2(9), E2(10));                                                                             \
                GX(1, 3, E2(11), _Pragma("unroll") for (int r = 0; r < 4; ++r) { rmp[r] = (fl_ + (0.5f - r)) * 1.2089258196146292e24f; PIN(rmp[r]); }); \
                if (__builtin_expect(qw2 == 2, 0)) {                               \
                    if (ISB) TBAR(); else LGKM0_BAR(); }                                                                                  \
                SB();                                                                                                                     \
                RDV(0); RDV(1); RDV(2); RDV(3); SB();                                                                                     \
                GX(1, 4, E2(12), M1(0); M1(1));                                                                                           \
                GX(1, 5, RDV(4); E2(13), M1(2); M1(3));                                                                                   \
                GX(1, 6, RDV(5); E2(14), C1(1, 0); C1(1, 1));                                                                             \
                GX(1, 7, RDV(6); E2(15), C1(1, 4); C1(1, 5));                                                                             \
                RDV(7); M1(12); M1(13); M1(14); M1(15); e[4] = e[5] = e[6] = e[7] = 0.f; C1(1, 2); C1(1, 3); SB();                        \
                C1(1, 6); C1(1, 7); SB();                                                                                                 \
                XST(1);                                                                                                                   \
                if (__builtin_expect(qw2 < 2, 0)) { if (ISB) TBAR(); else LGKM0_BAR(); }                                                  \
                SB();                                                                                                                     \
                XST(2); XST(3);                                                                                                           \
                float inv[2];                                                                                                \
                int hd2 = head; asm volatile("" : "+s"(hd2)); const char* cb = ws + WS_CTX + (unsigned)(hd2 * 524288 + (qb * 8 + qw2 * 2) * 2048); SB(); \
                GL(0, , ); GX(0, 0, , ); GX(0, 1, , ); GX(0, 2, , );                                                                      \
                PIN(ls[0]); PIN(ls[1]);                                                                                                   \
                const bool c_ = __builtin_amdgcn_ballot_w64(!(ls[0][0] + ls[1][0] <= LTHR)) != 0; SB();                                   \
                GX(0, 3, , );                                                                                                             \
                if (__builtin_expect(c_, 0)) EXACT();                                                                                     \
                SB();                                                                                                                     \
                inv[0] = __builtin_amdgcn_rcpf(ls[0][0]); inv[1] = __builtin_amdgcn_rcpf(ls[1][0]); PIN(inv[0]); PIN(inv[1]); SB();       \
                GX(0, 4, , asm volatile("s_nop 0"));                                                                                      \
                NRM(ISB, 0); GX(0, 5, , ); NRM(ISB, 1); GX(0, 6, , ); NRM(ISB, 2); GX(0, 7, , ); NRM(ISB, 3);                             \
                XST(4);                                                                                                                   \
                NRM(ISB, 4); NRM(ISB, 5); NRM(ISB, 6); NRM(ISB, 7);                                                                       \
                if (ISB) { asm volatile("s_setprio 0"); asm volatile("" : "+s"(t)); KCHK(); if (t + 2 < NA) { BRQ4(t, 0); BRQ4(t, 1); BRQ4(t, 3); } BRQ4(t, 2); \
                           CST(0); CST(1); CST(2); CST(3); CST(4); CST(5); CST(6); CST(7); }                                              \
                XST(5);                                                                                                                   \
            } while (0)
#define SPIN() do { PIN(S[1][0]); PIN(S[1][1]); PIN(S[1][2]); PIN(S[1][3]); } while (0)
            { ONCE2(isb ^ 1) { SPIN(); TAIL(0); } }
            { ONCE2(isb) { SPIN(); TAIL(1); } }
            SST(2);
            AST(4);
        }
        if (wave >= 4) {
            int t = qbB + 1;
            const int wv = wave & 3;
            XST(6);
#define WOLD(h_) do { const unsigned ow_ = OWOFF(); const char* wp_ = (const char*)(wo + (size_t)(K64(2 * (h_)) * 64 + 16 * wv) * DM + n0);  \
                _Pragma("unroll") for (int i = 0; i < 8; ++i)                                                                             \
                    GLDS_S(ow_, wp_ + (size_t)((i >> 2) * 64 + 8 * ((i >> 1) & 1)) * (DM * 2) + (i & 1) * dn1,                             \
                           ldsbase + 65536 + (h_) * 32768 + (i >> 2) * 16384 + (2 * wv + ((i >> 1) & 1)) * 2048 + (i & 1) * 1024, 0); } while (0)
            {
                const u32x4 em = u32x4{~0u, ~0u, ~0u, ~0u};
                const char* eb = ws + WS_PART + (size_t)(tile * 8 + wv) * 24576 + kp * 6144;
                asm volatile("global_store_dwordx4 %0, %1, %2 sc1\n\tglobal_store_dwordx4 %0, %1, %2 offset:1024 sc1\n\tglobal_store_dwordx4 %0, %1, %3 sc1\n\t"
                             "global_store_dwordx4 %0, %1, %3 offset:1024 sc1\n\ts_waitcnt vmcnt(12) lgkmcnt(0)"
                             :: "v"(l16), "v"(em), "s"(eb), "s"(eb + 4 * 24576) : "memory"); }
            if (jj == 7) { WOLD(1); EALD(0); EALD(1); }
            asm volatile("s_barrier" ::: "memory");
#define BATCH() do { KCHK(); if (t + 2 < NA) { BRQ4(t, 0); BRQ4(t, 1); BRQ4(t, 3); } BRQ4(t, 2); } while (0)
#define BSTEP(N) do { asm volatile("s_waitcnt vmcnt(%0)\n\ts_barrier" :: "i"(N) : "memory"); ++t; } while (0)
            u32x4 fw[16], wd[16], xq[4], gp[8];
            f32x4 fa[8];
            int ln_ = lane; asm volatile("" : "+v"(ln_));
#define FMM(w_, i0, a_, z_) do { asm volatile("s_setprio 1");                                                              \
                    _Pragma("unroll") for (int i = 0; i < 8; ++i) PIN(w_[(i0) + i]);                                                      \
                    PIN(a_);                                                                                                              \
                    _Pragma("unroll") for (int j = 0; j < 8; ++j) { u32x4 f_;                                                             \
                        _Pragma("unroll") for (int d = 0; d < 4; ++d) f_[d] = __builtin_amdgcn_perm(w_[(i0) + 2 * d + 1][j >> 1], w_[(i0) + 2 * d][j >> 1], (j & 1) ? 0x07060302u : 0x05040100u); \
                        fa[j] = __builtin_amdgcn_mfma_f32_16x16x32_bf16(__builtin_bit_cast(bf16x8, a_), __builtin_bit_cast(bf16x8, f_), (z_) ? f32x4{} : fa[j], 0, 0, 0); } \
                    _Pragma("unroll") for (int j = 0; j < 8; ++j) PIN(fa[j]);                                                             \
                    asm volatile("s_setprio 0"); } while (0)
#define FOUT() f32x4{ln_ >= 32 ? fa[4][0] : fa[0][0], ln_ >= 32 ? fa[5][0] : fa[1][0], ln_ >= 32 ? fa[6][0] : fa[2][0], ln_ >= 32 ? fa[7][0] : fa[3][0]}
            const int c1 = 2 * (i1 - 14 * k1) + (wv & 1);
            const unsigned r4o = (unsigned)(ln_ & 15) * 16u;
            const char* cxp = ws + WS_FLAG + F_CTX + ((qbB * 4 + qw) * 32 + head) * CFS;
            if (nf1 == 0) {
                const unsigned evo = (unsigned)((16 * (ln_ >> 5) + 4 * ((ln_ >> 4) & 1)) * FF * 2) + r4o;
                const u16* fwp = (wv < 2 ? w_gate : w_up) + (size_t)(k1 * 128) * FF + c1 * 128;
#define FLD(h_, c_) do { _Pragma("unroll") for (int i = 0; i < 8; ++i)                                                                    \
                    asm volatile("global_load_dwordx4 %0, %1, %2" : c_(fw[((h_) & 1) * 8 + i]) : "v"(evo), "s"(fwp + (size_t)(32 * (h_) + 8 * (i >> 2) + (i & 3)) * FF) : "memory"); } while (0)
                BATCH();
                { const int xr_ = (ln_ & 4) ? ri2 : ri1;
#pragma unroll
                  for (int i = 0; i < 4; ++i)
                      asm volatile("global_load_dwordx4 %0, %1, %2 offset:%3 sc1" : "=v"(xq[i]) : "v"((unsigned)((xr_ >> 4) * 2048 + (xr_ & 15) * 16 + (ln_ >> 4) * 256)),
                                   "s"(ws + WS_XF + (size_t)(k1 * 2 + (i >> 1)) * 262144), "i"((i & 1) * 1024) : "memory"); }
                FLD(0, "=v"); FLD(1, "=v"); BSTEP(20);
                BATCH(); asm volatile("s_waitcnt vmcnt(16)" ::: "memory");
                FLAG_PUT(cxp, tag, 1);
                FMM(fw, 0, xq[0], 1); FLD(2, "=v"); FMM(fw, 8, xq[1], 0); FLD(3, "=v"); BSTEP(17);
                BATCH(); asm volatile("s_waitcnt vmcnt(16)" ::: "memory");
                FMM(fw, 0, xq[2], 0); FMM(fw, 8, xq[3], 0); const f32x4 sum = FOUT();
                ST16_SC1((float*)(ws + WS_GP) + ((size_t)k1 * 4 + (wv >> 1) * 2 + ((ln_ >> 4) & 1)) * FF + c1 * 128 + (ln_ & 15) * 8 + (ln_ >> 5) * 4, sum);
                PFW1(7); BSTEP(2);
            }
            const char* pnd = nf1 ? cxp : ws + WS_FLAG + F_GU + (c1 * 16 + (wv >> 1) * 8 + k1) * 8;
            const int tp = nf1 ? t + 1 : t;
#define PUB(N) do { if (t == tp) { asm volatile("s_waitcnt vmcnt(%0)" :: "i"(N) : "memory"); FLAG_PUT(pnd, tag, 1); } } while (0)
            const int f0 = (nf2 ? 0 : i2 >> 3) * 256 + wv * 64;
            const char* gfp = ws + WS_FLAG + F_GU + (f0 >> 7) * 128 + (ln_ & 15) * 8;
            u64 gf = tag + 1;
            char* vs = lds + (NA & 1) * 32768;
#define GFLD() asm volatile("global_load_dwordx2 %0, %1, off sc1" : "+v"(gf) : "v"(gfp) : "memory")
#define WDLD(i, n_) do { const unsigned dvo = (unsigned)((ln_ >> 4) * 8 * DM * 2) + r4o;                                                  \
                    const char* db_ = (const char*)(w_down + (size_t)((nf2 ? 0 : i2 >> 3) * 256 + wv * 64) * DM + (i2 & 7) * 128) + 4096; \
                    asm volatile("s_cmp_lg_u32 %11, 0\n\ts_cbranch_scc1 1f\n\t"                                                          \
                                 "global_load_dwordx4 %0, %8, %9 offset:-4096\n\tglobal_load_dwordx4 %1, %8, %9 offset:-2048\n\tglobal_load_dwordx4 %2, %8, %9\n\t" \
                                 "global_load_dwordx4 %3, %8, %9 offset:2048\n\tglobal_load_dwordx4 %4, %8, %10 offset:-4096\n\tglobal_load_dwordx4 %5, %8, %10 offset:-2048\n\t" \
                                 "global_load_dwordx4 %6, %8, %10\n\tglobal_load_dwordx4 %7, %8, %10 offset:2048\n1:"  \
                                 : "=&v"(wd[i]), "=&v"(wd[i + 1]), "=&v"(wd[i + 2]), "=&v"(wd[i + 3]), "=&v"(wd[i + 4]), "=&v"(wd[i + 5]), "=&v"(wd[i + 6]), "=&v"(wd[i + 7]) \
                                 : "v"(dvo), "s"(db_ + (i) * 8192), "s"(db_ + (i) * 8192 + 8192), "s"(nf2) : "memory", "scc");            \
                    EALD((i) >> 3);                                                                                                       \
                    asm volatile("s_waitcnt vmcnt(%1)\n\ts_cmp_eq_u32 %0, 0\n\ts_cbranch_scc1 1f\n\ts_waitcnt vmcnt(%2)\n1:"                \
                                 :: "s"(nf2), "i"(12 + (n_)), "i"(4 + (n_)) : "memory", "scc");                                           \
                    BSTEP(63); } while (0)
#pragma unroll 1
            while (t < NA - 4) { BATCH(); PUB(17); BSTEP(0); }
            if (jj < 7) {
                BATCH(); PUB(16); WDLD(0, 0);
                BATCH(); PUB(16); GFLD(); WDLD(8, 0);
            }
            if (jj < 6) {
                BATCH();
                if (nf2 == 0) {
                    FLAG_WAIT(gf, gfp, tag, 63);
#pragma unroll
                    for (int i = 0; i < 8; ++i)
                        asm volatile("global_load_dwordx4 %0, %1, %2 sc1" : "=v"(gp[i]) : "v"((unsigned)((ln_ >> 4) * FF * 4) + r4o), "s"(ws + WS_GP + ((size_t)i * 4 * FF + f0) * 4) : "memory");
                    *(unsigned*)vs = 0;
                    asm volatile("s_waitcnt lgkmcnt(0)" ::: "memory");
                }
                WOLD(NA & 1);
                BSTEP(8);
            } else if (jj == 6) WOLD(0);
            asm volatile("global_load_dwordx2 %0, %1, %2 sc1" : "+v"(cxf) : "v"(CFO()), "s"(CFB) : "memory");
            WOLD((NA & 1) ^ 1);
            asm volatile("s_waitcnt vmcnt(16)" ::: "memory");
            if (jj == 7) FLAG_PUT(cxp, tag, 1);
            if (nf2 == 0) {
                f32x4 g4;
#pragma unroll
                for (int i = 0; i < 8; ++i) { PIN(gp[i]);
#pragma unroll
                    for (int j = 0; j < 4; ++j) { if (i) VADD(g4[j], g4[j], __uint_as_float(gp[i][j])); else g4[j] = __uint_as_float(gp[0][j]); } }
                float hv[2];
#pragma unroll
                for (int k = 0; k < 2; ++k) { auto rr = __builtin_amdgcn_permlane32_swap(__float_as_uint(g4[k]), __float_as_uint(g4[2 + k]), false, false);
                    const float gb = rbf(__uint_as_float(rr[0])), ub = rbf(__uint_as_float(rr[1]));
                    hv[k] = rbf(gb / (1.f + __expf(-gb))) * ub; PIN(hv[k]); }
                char* hl = vs + 8192 + wv * 256;
                *(unsigned*)(hl + ((ln_ >> 4) & 1) * 128 + (ln_ & 15) * 8 + (ln_ >> 5) * 4) = cvtpk(hv[0], hv[1]);
                asm volatile("" ::: "memory");
                xq[0] = *(const u32x4*)(hl + ((ln_ >> 2) & 1) * 128 + (ln_ >> 4) * 16); xq[1] = *(const u32x4*)(hl + ((ln_ >> 2) & 1) * 128 + 64 + (ln_ >> 4) * 16);
                FMM(wd, 0, xq[0], 1); FMM(wd, 8, xq[1], 0);
                *(f32x4*)(vs + 4096 + wv * 1024 + ln_ * 16) = FOUT();
                unsigned n_;
                asm volatile("s_waitcnt lgkmcnt(0)\n\ts_mov_b64 exec, 1\n\tds_add_rtn_u32 %0, %1, %2\n\ts_mov_b64 exec, -1\n\ts_waitcnt lgkmcnt(0)"
                             : "=&v"(n_) : "v"(ldsbase + (NA & 1) * 32768u), "v"(1u) : "memory");
                ypub = __builtin_amdgcn_readfirstlane(n_) ^ 3u;
                if (ypub == 0) {
                    const f32x4* yr = (const f32x4*)(vs + 4096) + ln_;
                    const f32x4 a = (yr[0] + yr[64]) + (yr[128] + yr[192]);
                    ST16_SC1((float*)(ws + WS_YP) + ((size_t)(((ln_ >> 4) & 1) * 8 + (i2 & 7)) * 16 + (i2 >> 3)) * 128 + (ln_ & 15) * 8 + (ln_ >> 5) * 4, a);
                }
            }
            AST(5);
        }
    }
    PH_STAMP(3);
    {
        constexpr int LDW = 1024, STG = 16384;
        int tid = threadIdx.x; asm volatile("" : "+v"(tid));
        const int lane = tid & 63, l15 = lane & 15, q4 = lane >> 4;
        const unsigned xvo = (unsigned)lane * 16u;
        if (!(kp & 1)) {
#pragma unroll
            for (int i = 0; i < 8; i += 2) { const u32x4 t_ = pc[i]; pc[i] = pc[i + 1]; pc[i + 1] = t_; PIN(pc[i]); PIN(pc[i + 1]); } }
        bf16x8 yr_[4][2][2];
#pragma unroll
        for (int i = 0; i < 8; ++i) yr_[i >> 2][i & 1][(i >> 1) & 1] = __builtin_bit_cast(bf16x8, pc[i]);
        unsigned wa[2][2];
        wa[0][0] = ldsbase + (unsigned)(65536 + ((kp >> 1) ^ 1) * 1024 + (q4 >> 1) * 2048 + (4 * (q4 & 1) + (l15 >> 2)) * 64 + (q4 & 1) * 16 + (lane & 2) * 16 + (lane & 1) * 8);
        wa[0][1] = wa[0][0] ^ 16u; wa[1][0] = wa[0][0] ^ 1024u; wa[1][1] = wa[0][1] ^ 1024u;
        PIN(wa[0][0]); PIN(wa[0][1]); PIN(wa[1][0]); PIN(wa[1][1]);
        const char* cg = ws + WS_CTX + (qb * 8 + qw * 2) * 2048;
        unsigned xok;
#define XLD8 "global_load_dwordx4 %0, %10, %12 sc1\n\tglobal_load_dwordx4 %1, %10, %13 sc1\n\tglobal_load_dwordx4 %2, %10, %12 offset:1024 sc1\n\tglobal_load_dwordx4 %3, %10, %13 offset:1024 sc1\n\t" \
             "global_load_dwordx4 %4, %11, %12 sc1\n\tglobal_load_dwordx4 %5, %11, %13 sc1\n\tglobal_load_dwordx4 %6, %11, %12 offset:1024 sc1\n\tglobal_load_dwordx4 %7, %11, %13 offset:1024 sc1\n\t"
#define XOUT(c_) c_(yr_[2][0][0]), c_(yr_[2][1][0]), c_(yr_[2][0][1]), c_(yr_[2][1][1]), c_(yr_[3][0][0]), c_(yr_[3][1][0]), c_(yr_[3][0][1]), c_(yr_[3][1][1])
#define XIN "v"(xvo), "v"(xvo + 262144u), "s"(cg + (size_t)K64(2) * 262144 + ((kp ^ 1) & 1) * 2048), "s"(cg + (size_t)K64(2) * 262144 + (kp & 1) * 2048), "s"(tag)
        SST(3);
        SB();
        asm volatile("s_waitcnt vmcnt(0)" ::: "memory");
        FLAG_PUTZ(ws + WS_FLAG + ((wave & 4) ? F_Y + ((i2 & 7) * 16 + ((i2 >> 3) & 15)) * 8 : F_CTX + ((qbA * 4 + wave) * 32 + head) * CFS), tag, (wave >> 2) * ypub);
        asm volatile("v_cmp_ne_u64 vcc, %14, %8\n\ts_and_b32 %9, vcc_lo, 14\n\ts_cbranch_scc1 1f\n\t"
                     XLD8 "\n1:\n\ts_barrier" : XOUT("=&v"), "+v"(cxf), "=&s"(xok) : XIN : "memory", "vcc", "scc");
        SST(4);
        f32x4 acc[2][8];
        bf16x4 wl[8], wh[8];
#define OFA(s, k_, j) (wa[(j) >> 2][(j) & 1] + (unsigned)(((s) & 3) * STG + (k_) * 8192 + (((j) >> 1) & 1) * 512))
#define ORL(i, s, k_, j) wl[i] = tr_rd(OFA(s, k_, j))
#define ORH(i, s, k_, j) wh[i] = tr_rd(OFA(s, k_, j) + 4096u)
        ORL(0, 0, 0, 0); ORH(0, 0, 0, 0); ORL(1, 0, 0, 1); ORH(1, 0, 0, 1); ORL(2, 0, 0, 2); ORH(2, 0, 0, 2); ORL(3, 0, 0, 3); ORH(3, 0, 0, 3); SB();
        unsigned wvo = OWOFF() + (unsigned)K64(4) * (64 * LDW * 2), xso = xvo + (unsigned)K64(4) * 262144u;
        unsigned cfo = CFO();
        PIN(wvo); PIN(xso); PIN(cfo);
        const char* cfb = CFB;
        const int r0 = qb * 128 + qw * 32 + (kp & 1) * 16, ch = kp >> 1;
        const unsigned nrt = min((unsigned)(ri1 - r0) >> 4, (unsigned)(ri2 - r0) >> 4);
        u64 yf = 0;
#define YFP (ws + WS_FLAG + F_Y + (ntc * 16 + (l15 < 14 ? l15 : 0)) * 8)
#define OMF(a, s, k_, j) do { if ((s) == 0 && (k_) == 0) MFA16Z(acc[a][j], w_, yr_[0][a][0]); else MFA16(acc[a][j], w_, yr_[(s) & 3][a][k_]); } while (0)
#define ORQ(h) do { constexpr int x_ = o_x(h), w_ = o_w(h);                                                                               \
            if (x_ >= 0) { asm volatile("global_load_dwordx4 %0, %1, %2 offset:%3 sc1" : "=v"(yr_[(x_ >> 2) & 3][x_ & 1][(x_ >> 1) & 1])   \
                                        : "v"(xso), "s"(cg + ((x_ ^ kp ^ 1) & 1) * 2048), "i"(((x_ >> 1) & 1) * 1024) : "memory");         \
                           if ((x_ & 3) == 3) { xso ^= (unsigned)((x_ >> 2) ^ ((x_ >> 2) + 1)) * 262144u; PIN(xso); } }                    \
            if (o_w((h) + 1) >= 0 && !(o_w((h) + 1) & 1)) asm volatile("s_mov_b32 m0, %0" :: "s"(ldsbase + wave * 2048 + (((o_w((h) + 1) >> 1) & 7) ^ 4) * STG) : "memory"); \
            if (w_ >= 0) { asm volatile("global_load_lds_dwordx4 %0, %1 offset:%2" :: "v"(wvo), "s"((const char*)wob + (w_ & 1) * (dn1 - 1024)), "i"((w_ & 1) * 1024) : "memory"); \
                           if (w_ & 1) { wvo ^= (unsigned)((w_ >> 1) ^ ((w_ >> 1) + 1)) * (64 * LDW * 2); PIN(wvo); } } } while (0)
#define OPAIR(s, f, A) do { const bf16x8 w_ = CAT8(wl[(f) & 7], wh[(f) & 7]); constexpr int s_ = (s) + (((f) + 4) >> 4), m_ = ((f) + 4) & 15; \
            OMF(0, s, (f) >> 3, (f) & 7); SB(); ORL(m_ & 7, s_, m_ >> 3, m_ & 7); ORQ(32 * (s) + 2 * (f)); A; SB();                       \
            OMF(1, s, (f) >> 3, (f) & 7); SB(); ORH(m_ & 7, s_, m_ >> 3, m_ & 7); ORQ(32 * (s) + 2 * (f) + 1); SB(); if (((f) & 1)) { __builtin_amdgcn_s_waitcnt(0xC47F); SB(); } } while (0)
#define OTG(h_) do { wa[h_][0] ^= 65536u; wa[h_][1] ^= 65536u; PIN(wa[h_][0]); PIN(wa[h_][1]); } while (0)
#define OST(i)
#define OSTEP(s, POST) do {                                                                                                               \
        OPAIR(s, 0, ); OPAIR(s, 1, ); OPAIR(s, 2, ); OPAIR(s, 3, ); OPAIR(s, 4, ); OPAIR(s, 5, ); OPAIR(s, 6, ); OPAIR(s, 7, );           \
        OPAIR(s, 8, ); OPAIR(s, 9, if (((s) & 3) == 3) OTG(0)); OPAIR(s, 10, ); OPAIR(s, 11, );                                           \
        if ((s) >= 3) asm volatile("s_waitcnt vmcnt(%0)\n\ts_barrier" :: "i"(o_wait(s)) : "memory");                                      \
        SB(); POST; OST((s) + 1);                                                                                                         \
        OPAIR(s, 12, ); OPAIR(s, 13, if (((s) & 3) == 3) OTG(1)); OPAIR(s, 14, ); OPAIR(s, 15, );                                         \
    } while (0)
        PH_STAMP(4);
        SST(5);
        OST(0);
        SB();
        OSTEP(0, );
        asm volatile("s_cmp_eq_u32 %3, 0\n\ts_cbranch_scc1 1f\n\tglobal_load_dwordx2 %0, %1, %2 sc1\n1:" : "+v"(cxf) : "v"(cfo), "s"(cfb), "s"(xok) : "memory", "scc");
        OSTEP(1, );
        asm volatile("s_waitcnt vmcnt(8)\n\ts_cmp_eq_u32 %9, 0\n\ts_cbranch_scc1 3f\n\ts_mov_b32 %9, 0x40000\n\ts_waitcnt vmcnt(4)\n"
                     "1:\n\tv_cmp_ne_u64 vcc, %14, %8\n\ts_and_b32 vcc_lo, vcc_lo, 14\n\ts_cbranch_scc0 2f\n\t"
                     "global_load_dwordx2 %8, %15, %16 sc1\n\ts_sleep 4\n\ts_waitcnt vmcnt(0)\n\ts_sub_u32 %9, %9, 1\n\ts_cbranch_scc0 1b\n2:\n\t" XLD8 "s_waitcnt vmcnt(4)\n3:"
                     : XOUT("+v"), "+v"(cxf), "+s"(xok) : XIN, "v"(cfo), "s"(cfb) : "memory", "vcc", "scc");
        const unsigned inc = (unsigned)__builtin_amdgcn_ballot_w64(cxf != tag) & 0x1f0u;
        SB();
        OSTEP(2, if (__builtin_expect(inc != 0, 0)) asm volatile("global_load_dwordx2 %0, %1, %2 sc1" : "+v"(cxf) : "v"(cfo), "s"(cfb) : "memory"); SB());
        asm volatile("s_waitcnt vmcnt(8)" ::: "memory"); SB();
        OSTEP(3, );
        OSTEP(4, if (__builtin_expect(inc != 0, 0)) FLAG_WAITM(cxf, cfb + cfo, tag, 63, 0x1f0u); SB());
        OSTEP(5, ); OSTEP(6, );
        OSTEP(7, if (__builtin_expect(nrt == 0, 0)) asm volatile("global_load_dwordx2 %0, %1, off sc1" : "+v"(yf) : "v"(YFP) : "memory"); SB());
        OSTEP(8, ); OSTEP(9, ); OSTEP(10, );
        u32x4 yq[7], pv[6], zn[2]; u32x2 ea[4]; unsigned rq;
        if (__builtin_expect(nrt == 0, 0)) {
            FLAG_WAIT(yf, YFP, tag, 63);
            asm volatile("global_load_dwordx4 %0, %8, %10 sc1\n\tglobal_load_dwordx4 %1, %8, %10 offset:512 sc1\n\tglobal_load_dwordx4 %2, %8, %10 offset:1024 sc1\n\t"
                         "global_load_dwordx4 %3, %8, %10 offset:1536 sc1\n\tglobal_load_dwordx4 %4, %8, %10 offset:2048 sc1\n\tglobal_load_dwordx4 %5, %8, %10 offset:2560 sc1\n\t"
                         "global_load_dwordx4 %6, %8, %10 offset:3072 sc1\n\tglobal_load_dword %7, %9, %10 sc1"
                         : "=&v"(yq[0]), "=&v"(yq[1]), "=&v"(yq[2]), "=&v"(yq[3]), "=&v"(yq[4]), "=&v"(yq[5]), "=&v"(yq[6]), "=&v"(rq)
                         : "v"((unsigned)WS_YP + (unsigned)((((lane >> 5) * 8 + ntc) * 16 + (q4 & 1) * 7) * 128 + ch * 64 + 4 * l15) * 4u),
                           "v"((unsigned)WS_R + (unsigned)(lane >= 32 ? ri2 : ri1) * 4u), "s"(ws) : "memory");
        }
        SB();
        OSTEP(11, );
        char* pb = ws + WS_PART + (size_t)(tile * 8 + wave) * 24576;
        const char* tfb = ws + WS_FLAG + F_PARK + (tile * 8 + wave) * 256;
        const unsigned tfo = (unsigned)((lane & 3) ^ kp) * 64u;
        const unsigned ea0 = ldsbase + 131072 + (wave & 3) * 8192 + (wave >> 2) * 4096 + l15 * 128 + (((lane >> 5) * 2 ^ (lane & 7)) << 4) + (q4 & 1) * 8;
        typedef __attribute__((address_space(3))) u32x2 lds_u32x2;
#define OSND(a, j) do { constexpr int x_ = ((a) ^ 1) | (((j) >> 1) & 2 ^ 2);                                             \
            u32x4 p_ = u32x4{cvtpk(acc[a][j][0], acc[a][j][1]), cvtpk(acc[a][j][2], acc[a][j][3]), cvtpk(acc[a][(j) + 1][0], acc[a][(j) + 1][1]), cvtpk(acc[a][(j) + 1][2], acc[a][(j) + 1][3])}; \
            if (x_ == 1) { _Pragma("unroll") for (int k = 0; k < 4; ++k) p_[k] = min(p_[k], 0xfffffffeu); }     \
            asm volatile("global_store_dwordx4 %0, %1, %2 offset:%3 sc1\n\ts_nop 1" :: "v"(xvo), "v"(p_), "s"(pb + (kp ^ x_) * 6144 + (x_ - 1) * 2048), "i"(((j) & 2) * 512) : "memory"); } while (0)
#define OUF(i, u) do { ORL(i, ((u) >> 1) & 3, (u) & 1, 4 + (((u) >> 3) & 3)); ORH(i, ((u) >> 1) & 3, (u) & 1, 4 + (((u) >> 3) & 3)); } while (0)
#define OQ(q) do { constexpr int p_ = (q) + 4; const bf16x8 w_ = CAT8(wl[(q) & 7], wh[(q) & 7]);                                           \
            if ((q) == 16 || (q) == 24) { asm volatile("s_waitcnt vmcnt(%0)" :: "i"((q) == 16 ? 4 : 0) : "memory"); SB(); }               \
            OMF(0, 12 + (((q) >> 3) & 3), ((q) >> 2) & 1, (q) & 3); SB();                                                                 \
            if (p_ < 32) ORL(p_ & 7, (p_ >> 3) & 3, (p_ >> 2) & 1, p_ & 3); else OUF(p_ & 7, p_ - 32);                                    \
            ORQ(384 + 2 * (q)); SB();                                                                                                     \
            OMF(1, 12 + (((q) >> 3) & 3), ((q) >> 2) & 1, (q) & 3); SB();                                                                 \
            if (p_ < 32) ORH(p_ & 7, (p_ >> 3) & 3, (p_ >> 2) & 1, p_ & 3); else if ((q) >= 30) OUF(((q) - 26) & 7, (q) - 26);            \
            if ((q) >= 10 && (q) < 14) ea[((q) - 10) & 3] = *(lds_u32x2*)(uintptr_t)(ea0 ^ (unsigned)((((q) - 10) & 1) * 16 + (((q) - 10) & 2) * 32)); \
            if ((q) == 14 || (q) == 15) zn[(q) & 1] = __builtin_bit_cast(u32x4, lds_rd(ldsbase + 131072 + 2048 + (wave & 3) * 8192 + (wave >> 2) * 4096 + ((q) & 1) * 1024 + xvo)); \
            SB(); } while (0)
#define OU(u) do { const bf16x8 w_ = CAT8(wl[(u) & 7], wh[(u) & 7]);                                                                      \
            OMF((u) >> 5, 12 + (((u) >> 1) & 3), (u) & 1, 4 + (((u) >> 3) & 3)); SB();                                                    \
            if ((u) + 6 < 64) OUF(((u) + 6) & 7, (u) + 6);                                                                                \
            if ((u) == 1) OSND(0, 0); if ((u) == 3) OSND(1, 0); if ((u) == 5) OSND(0, 2); if ((u) == 7) OSND(1, 2);                       \
            if ((u) == 19) OSND(0, 4);                                                                                                    \
            if ((u) == 22) { asm volatile("s_waitcnt vmcnt(1)" ::: "memory"); FLAG_PUT(tfb + kp * 64, tag, 1); }     \
            if ((u) == 35) { OSND(0, 6); asm volatile("s_setprio 0"); }                                                                   \
            if ((u) == 52) asm volatile("global_load_dwordx2 %0, %1, %2 sc1" : "=v"(cxf) : "v"(tfo), "s"(tfb) : "memory");               \
            SB(); } while (0)
#define OQ8(q) do { OQ(q); OQ((q) + 1); OQ((q) + 2); OQ((q) + 3); OQ((q) + 4); OQ((q) + 5); OQ((q) + 6); OQ((q) + 7); } while (0)
#define OU8(q) do { OU(q); OU((q) + 1); OU((q) + 2); OU((q) + 3); OU((q) + 4); OU((q) + 5); OU((q) + 6); OU((q) + 7); } while (0)
        asm volatile("s_setprio 1");
        OQ8(0); OQ8(8); OQ8(16); OQ8(24); OST(13); OU8(0); OU8(8); OU8(16); OU8(24); OU8(32); OU8(40); OU8(48); OU8(56);
        MF_DONE();
        OST(14);
        PH_STAMP(5);
        SST(6);
        SB();
        if (__builtin_expect(nrt == 0, 0)) {
            f32x4 ys;
#pragma unroll
            for (int i = 0; i < 7; ++i) PIN(yq[i]);
            PIN(rq);
            const float sig = rbf(1.f / (1.f + __expf(-__uint_as_float(rq))));
#pragma unroll
            for (int j = 0; j < 4; ++j) {
                const float s_ = ((__uint_as_float(yq[0][j]) + __uint_as_float(yq[1][j])) + (__uint_as_float(yq[2][j]) + __uint_as_float(yq[3][j])))
                               + ((__uint_as_float(yq[4][j]) + __uint_as_float(yq[5][j])) + __uint_as_float(yq[6][j]));
                auto rr = __builtin_amdgcn_permlane16_swap(__float_as_uint(s_), __float_as_uint(s_), false, false);
                ys[j] = rbf(__uint_as_float(rr[0]) + __uint_as_float(rr[1])) * sig; }
            char* yl = lds + 65536 + wave * 256;
            *(u32x2*)(yl + (lane >> 5) * 128 + (l15 >> 2) * 32 + (l15 & 1) * 16 + (l15 & 2) * 4) = u32x2{cvtpk(ys[0], ys[1]), cvtpk(ys[2], ys[3])};
            asm volatile("" ::: "memory");
            const int m = r0 + l15;
#pragma unroll
            for (int i = 0; i < 2; ++i) { const u32x4 yn = *(const u32x4*)(yl + (m == ri1 ? 0 : 128) + i * 64 + q4 * 16);
#pragma unroll
                for (int k = 0; k < 4; ++k) { const unsigned z_ = cvtpk(HALF(zn[i][k], 0) + HALF(yn[k], 0), HALF(zn[i][k], 1) + HALF(yn[k], 1));
                    zn[i][k] = m == ri1 || m == ri2 ? z_ : zn[i][k]; } }
        }
        float gg[2][8], xx[2][8];
#pragma unroll
        for (int i = 0; i < 2; ++i)
#pragma unroll
            for (int j = 0; j < 8; ++j) { gg[i][j] = HALF(zn[i][j >> 1], j); xx[i][j] = HALF(ea[2 * i + (j >> 2)][(j >> 1) & 1], j); PIN(gg[i][j]); PIN(xx[i][j]); }
        unsigned ovo = (unsigned)((l15 & 7) * DM + 32 * (l15 >> 3) + 8 * q4) * 2u; PIN(ovo);
        const char* ob = (const char*)(outp + (size_t)r0 * DM + ntc * 128 + 64 * ch);
        asm volatile("" : "+s"(ob));
        PH_STAMP(6);
        { unsigned c_, m_;
          asm volatile("s_waitcnt vmcnt(0)\n\ts_mov_b32 %7, 0x40000\n"
                       "1:\n\tv_cmp_ne_u64 vcc, %11, %6\n\ts_and_b32 vcc_lo, vcc_lo, 12\n\ts_cbranch_scc0 2f\n\t"
                       "global_load_dwordx2 %6, %12, %13 sc1\n\ts_waitcnt vmcnt(0)\n\ts_sub_u32 %7, %7, 1\n\ts_cbranch_scc0 1b\n"
                       "2:\n\tglobal_load_dwordx4 %1, %9, %10 offset:-1024 sc1\n\tglobal_load_dwordx4 %2, %9, %10 offset:1024 sc1\n\t"
                       "global_load_dwordx4 %4, %9, %10 sc1\n\tglobal_load_dwordx4 %5, %9, %10 offset:2048 sc1\n\t"
                       "global_load_dwordx4 %0, %9, %10 offset:-3072 sc1\n\tglobal_load_dwordx4 %3, %9, %10 offset:-2048 sc1"
                       : "=&v"(pv[0]), "=&v"(pv[1]), "=&v"(pv[2]), "=&v"(pv[3]), "=&v"(pv[4]), "=&v"(pv[5]), "+v"(cxf), "=&s"(c_), "=&v"(m_)
                       : "v"(xvo), "s"(pb + kp * 6144 + 3072), "s"(tag), "v"(tfo), "s"(tfb) : "memory", "vcc", "scc"); }
#pragma unroll
        for (int i = 0; i < 16; i += 2) { float g_[2];
#pragma unroll
            for (int e_ = 0; e_ < 2; ++e_) { float a_ = gg[i >> 3][(i & 7) + e_] * -1.4426950408889634f; PIN(a_); a_ = 1.f + __builtin_amdgcn_exp2f(a_); PIN(a_);
                g_[e_] = gg[i >> 3][(i & 7) + e_] * __builtin_amdgcn_rcpf(a_); PIN(g_[e_]); }
            const unsigned p_ = cvtpk(g_[0], g_[1]); gg[i >> 3][i & 7] = HALF(p_, 0); gg[i >> 3][(i & 7) + 1] = HALF(p_, 1); PIN(gg[i >> 3][i & 7]); PIN(gg[i >> 3][(i & 7) + 1]); }
        asm volatile("s_waitcnt vmcnt(0)" : "+v"(pv[0]), "+v"(pv[1]), "+v"(pv[2]), "+v"(pv[3]), "+v"(pv[4]), "+v"(pv[5]) :: "memory");
#pragma unroll 1
        for (int n_ = 0; n_ < 0x40000; ++n_) {
            const unsigned mx_ = max(max(max(pv[0][0], pv[0][1]), max(pv[0][2], pv[0][3])), max(max(pv[3][0], pv[3][1]), max(pv[3][2], pv[3][3])));
            if (__builtin_amdgcn_ballot_w64(mx_ == ~0u) == 0) break;
            asm volatile("global_load_dwordx4 %0, %2, %3 sc1\n\tglobal_load_dwordx4 %1, %2, %3 offset:1024 sc1\n\ts_waitcnt vmcnt(0)"
                         : "=&v"(pv[0]), "=&v"(pv[3]) : "v"(xvo), "s"(pb + kp * 6144) : "memory");
        }
        PH_STAMP(7);
        u32x4 ow[2];
#pragma unroll
        for (int i = 0; i < 2; ++i) {
            u32x4& ov = ow[i];
#pragma unroll
            for (int k = 0; k < 4; ++k) { float sv[2];
#pragma unroll
                for (int e_ = 0; e_ < 2; ++e_) sv[e_] = fmaf((acc[1][4 + 2 * i + (k >> 1)][2 * (k & 1) + e_] + HALF(pv[3 * i][k], e_)) + (HALF(pv[3 * i + 1][k], e_) + HALF(pv[3 * i + 2][k], e_)),
                                                             gg[i][2 * k + e_], xx[i][2 * k + e_]);
                ov[k] = cvtpk(sv[0], sv[1]); }
#pragma unroll
            for (int k = 0; k < 2; ++k) { auto rr = __builtin_amdgcn_permlane16_swap(ov[k], ov[2 + k], false, false); ov[k] = rr[0]; ov[2 + k] = rr[1]; }
        }
#pragma unroll
        for (int i = 0; i < 2; ++i) {
            u32x4 ov;
#pragma unroll
            for (int k = 0; k < 4; ++k) ov[k] = i ? __builtin_amdgcn_update_dpp(ow[1][k], ow[0][k], 0x128, 0xf, 0x3, false) : __builtin_amdgcn_update_dpp(ow[0][k], ow[1][k], 0x128, 0xf, 0xc, false);
            asm volatile("global_store_dwordx4 %0, %1, %2 sc1\n\ts_nop 1" :: "v"(ovo), "v"(ov), "s"(ob + i * (8 * DM * 2)) : "memory");
        }
        SST(7); SST_OUT();
        PH_STAMP(8);
        PH_STAMP(9);
    }
}

extern "C" void kernel_launch(void* const* d_in, const int* in_sizes, int n_in, void* d_out, int out_size, void* d_ws, size_t ws_size,
                              hipStream_t stream) {
    const u16* x    = (const u16*)d_in[0];
    const u16* ckv  = (const u16*)d_in[1];
    const u16* wq   = (const u16*)d_in[2];
    const u16* wuk  = (const u16*)d_in[3];
    const u16* wuv  = (const u16*)d_in[4];
    const u16* wo   = (const u16*)d_in[5];
    const u16* wr   = (const u16*)d_in[7];
    const u16* wg   = (const u16*)d_in[8];
    const u16* wu   = (const u16*)d_in[9];
    const u16* wd   = (const u16*)d_in[10];
    (void)in_sizes; (void)n_in; (void)out_size; (void)ws_size;
    (void)hipFuncSetAttribute((const void*)k_all, hipFuncAttributeMaxDynamicSharedMemorySize, 163840);
    k_all<<<256, 512, 163840, stream>>>(x, ckv, wuk, wuv, wr, (char*)d_ws + ((size_t)WSOFF << 20), wq, wo, wg, wu, wd, (u16*)d_out);
}
```

```cpp
#include <hip/hip_runtime.h>
#include <hip/hip_bf16.h>
#include <stdint.h>

typedef unsigned short u16;
typedef unsigned long long u64;
typedef short    bf16x8 __attribute__((ext_vector_type(8)));
typedef short    bf16x4 __attribute__((ext_vector_type(4)));
typedef float    f32x4  __attribute__((ext_vector_type(4)));
typedef float    f32x2  __attribute__((ext_vector_type(2)));
typedef __bf16   bfx2   __attribute__((ext_vector_type(2)));
typedef unsigned u32x4  __attribute__((ext_vector_type(4)));
typedef unsigned u32x2  __attribute__((ext_vector_type(2)));

#define DI __device__ __forceinline__

constexpr int SQ = 2048, DM = 1024, NH = 32, HD = 128, DC = 512, FF = 3584;

#define WSOFF 0
constexpr size_t WS_R    = 4u << 20;
constexpr size_t WS_FLAG = (4u << 20) + 65536;
constexpr size_t WS_GP   = 6u << 20;
constexpr size_t WS_YP   = 7u << 20;
constexpr size_t WS_PART = 96u << 20;
constexpr size_t WS_XF   = 16u << 20;
#define HSTR 1310720
constexpr size_t WS_KV   = 24u << 20;
constexpr size_t WS_CTX  = 72u << 20;
constexpr int F_EPOCH = 0, F_TOP = 1024, F_LN = 4096, F_KV = 8192, F_GU = 12288  , F_Y = 20480  , F_PARK = 524288  ,
              F_CTX = 65536  ;

#define PH_STAMP(i)
#define SST(i)
#define S2T(i)
#define SST_OUT()
#define ZST(i)
#define ZST_OUT()
DI unsigned cvtpk(float lo, float hi) { f32x2 v = {lo, hi}; bfx2 b = __builtin_convertvector(v, bfx2); return __builtin_bit_cast(unsigned, b); }
DI float rbf(float x) { return __uint_as_float(cvtpk(x, 0.f) << 16); }
#define DPP_ADD(v, ctrl, rmask) v += __uint_as_float(__builtin_amdgcn_update_dpp(0u, __float_as_uint(v), ctrl, rmask, 0xf, false))
DI unsigned tkey(unsigned fbits, int i) { unsigned u = fbits >> 16; u ^= (u & 0x8000u) ? 0xffffu : 0x8000u; return (u << 16) | (unsigned)(2047 - i); }
DI void merge_top2(unsigned& a1, unsigned& a2, unsigned b1, unsigned b2) { const unsigned lo = min(a1, b1); a1 = max(a1, b1); a2 = max(lo, max(a2, b2)); }
#define DPP_TOP2(a1, a2, ctrl, rmask) merge_top2(a1, a2, __builtin_amdgcn_update_dpp(0u, a1, ctrl, rmask, 0xf, false), __builtin_amdgcn_update_dpp(0u, a2, ctrl, rmask, 0xf, false))

#define GLDS_S(voff, sbase, ldsoff, imm)                                                                        \
    asm volatile("s_mov_b32 m0, %2\n\ts_nop 0\n\tglobal_load_lds_dwordx4 %0, %1 offset:%3"                       \
                 :: "v"(voff), "s"(sbase), "s"(ldsoff), "i"(imm) : "memory")
#define GLDS_C(voff, sbase, ldsoff, imm)                                                                        \
    asm volatile("s_mov_b32 m0, %2\n\ts_nop 0\n\tglobal_load_lds_dwordx4 %0, %1 offset:%3 sc1"                   \
                 :: "v"(voff), "s"(sbase), "s"(ldsoff), "i"(imm) : "memory")
#define LGKM0_BAR()    asm volatile("s_waitcnt lgkmcnt(0)\n\ts_barrier" ::: "memory")
#define SB() __builtin_amdgcn_sched_barrier(0)
#define CAT_(a, b) a##b
#define CAT(a, b) CAT_(a, b)
#define PIN(x) asm volatile("" : "+v"(x))
#define OADV(p_, d_) do { p_ += (d_); asm volatile("" : "+s"(p_)); } while (0)
#define VADD(d, a, b)    asm volatile("v_add_f32 %0, %1, %2" : "=v"(d) : "v"(a), "v"(b))
#define KEEP(x) asm volatile("" :: "v"(x))
#define MFA16(c, a, b) asm volatile("v_mfma_f32_16x16x32_bf16 %0, %1, %2, %0" : "+v"(c) : "v"(a), "v"(b))
#define MFA16Z(c, a, b) asm volatile("v_mfma_f32_16x16x32_bf16 %0, %1, %2, 0" : "=&v"(c) : "v"(a), "v"(b))
#define MFA16C(c, a, b, c0) asm volatile("v_mfma_f32_16x16x32_bf16 %0, %1, %2, %3" : "=&v"(c) : "v"(a), "v"(b), "v"(c0))
#define MF_DONE() do { SB(); asm volatile("s_nop 7\n\ts_nop 7"); SB(); } while (0)
#define ST16_SC1(p, v) asm volatile("global_store_dwordx4 %0, %1, off sc1\n\ts_nop 1" :: "v"(p), "v"(v) : "memory")
#define ST4_SC1(p, v)  asm volatile("global_store_dword %0, %1, off sc1" :: "v"(p), "v"(v) : "memory")
#define FLAG_PUT(p_, val_, on_) do { u64 sv_, tv_; unsigned tz_; const u64 v_ = (val_); const unsigned m_ = __builtin_amdgcn_readfirstlane((on_) ? 1u : 0u); \
        asm volatile("s_cmp_eq_u32 %5, 0\n\ts_cbranch_scc1 1f\n\tv_mov_b64 %1, %3\n\tv_mov_b32 %2, 0\n\ts_mov_b64 %0, exec\n\ts_mov_b64 exec, 1\n\t" \
                     "global_store_dwordx2 %2, %1, %4 sc1\n\ts_mov_b64 exec, %0\n1:"                                                      \
                     : "=&s"(sv_), "=&v"(tv_), "=&v"(tz_) : "s"(v_), "s"(p_), "s"(m_) : "memory", "scc"); } while (0)
#define FLAG_PUTZ(p_, val_, nz_) do { u64 sv_, tv_; unsigned tz_; const u64 v_ = (val_);                                                   \
        asm volatile("s_cmp_lg_u32 %5, 0\n\ts_cbranch_scc1 1f\n\tv_mov_b64 %1, %3\n\tv_mov_b32 %2, 0\n\ts_mov_b64 %0, exec\n\ts_mov_b64 exec, 1\n\t" \
                     "global_store_dwordx2 %2, %1, %4 sc1\n\ts_mov_b64 exec, %0\n1:"                                                      \
                     : "=&s"(sv_), "=&v"(tv_), "=&v"(tz_) : "s"(v_), "s"(p_), "s"(nz_) : "memory", "scc"); } while (0)
#define FLAG_GET(r_, p_) asm volatile("global_load_dwordx2 %0, %1, off sc1" : "=v"(r_) : "v"(p_) : "memory")
#define FLAG_WAIT(r_, p_, tag_, N) do { unsigned c_;                                                                                      \
        asm volatile("s_waitcnt vmcnt(%4)\n\ts_mov_b32 %1, 0x40000\n"                                                                     \
                     "1:\n\tv_cmp_ne_u64 vcc, %3, %0\n\ts_cmp_eq_u64 vcc, 0\n\ts_cbranch_scc1 2f\n\t"                                      \
                     "s_sleep 2\n\tglobal_load_dwordx2 %0, %2, off sc1\n\ts_waitcnt vmcnt(0)\n\ts_sub_u32 %1, %1, 1\n\ts_cbranch_scc0 1b\n"  \
                     "2:" : "+v"(r_), "=&s"(c_) : "v"(p_), "s"(tag_), "i"(N) : "memory", "vcc", "scc"); } while (0)
#define FLAG_WAITM(r_, p_, tag_, N, m_) do { unsigned c_;                                                                                 \
        asm volatile("s_waitcnt vmcnt(%4)\n\ts_mov_b32 %1, 0x40000\n"                                                                     \
                     "1:\n\tv_cmp_ne_u64 vcc, %3, %0\n\ts_and_b32 vcc_lo, vcc_lo, %5\n\ts_cbranch_scc0 2f\n\t"                             \
                     "s_sleep 2\n\tglobal_load_dwordx2 %0, %2, off sc1\n\ts_waitcnt vmcnt(0)\n\ts_sub_u32 %1, %1, 1\n\ts_cbranch_scc0 1b\n"  \
                     "2:" : "+v"(r_), "=&s"(c_) : "v"(p_), "s"(tag_), "i"(N), "s"(m_) : "memory", "vcc", "scc"); } while (0)
typedef __attribute__((address_space(3))) bf16x4 lds_bf16x4;
DI bf16x4 tr_rd(unsigned a) { return __builtin_amdgcn_ds_read_tr16_b64_v4i16((lds_bf16x4*)(uintptr_t)a); }
typedef __attribute__((address_space(3))) bf16x8 lds_bf16x8;
DI bf16x8 lds_rd(unsigned a) { return *(lds_bf16x8*)(uintptr_t)a; }
#define CAT8(lo_, hi_) bf16x8{lo_[0], lo_[1], lo_[2], lo_[3], hi_[0], hi_[1], hi_[2], hi_[3]}

#define POFF 32.f
#define LTHR 8192.f
DI float quad_max(float v) {
    auto r1 = __builtin_amdgcn_permlane16_swap(__float_as_uint(v), __float_as_uint(v), false, false);
    v = fmaxf(__uint_as_float(r1[0]), __uint_as_float(r1[1]));
    auto r2 = __builtin_amdgcn_permlane32_swap(__float_as_uint(v), __float_as_uint(v), false, false);
    return fmaxf(__uint_as_float(r2[0]), __uint_as_float(r2[1])); }
#define QWD 3
#define QS1 0x1dff
constexpr int q_xb(int s) { return s >= 1 && s <= 12; }
constexpr int q_xa(int s) { return s >= 0 && s <= 11; }
constexpr int q_s0(int s) { int n = 0; for (int i = 0; i < s; ++i) n += (QS1 >> i) & 1; return n; }
constexpr int q_kv(int kt, int f) { const int s = (kt - 13) * 16 + f - 12; return s >= 0 && s % 3 == 0 && s / 3 < 12 ? s / 3 : -1; }
constexpr int q_op(int s, int f) { return (f == 0 || f == 2 ? q_xb(s) : f == 8 || f == 10 ? q_xa(s) : f == 12 || f == 14 ? s + QWD < 16 : f == 3 ? s == 12
                                          : f == 4 ? (QS1 >> s) & 1 : 0) + (q_kv(s, f) >= 0); }
constexpr int q_after(int p, int kt) { int n = 0; for (int i = p + 1; i <= kt * 16 + 11; ++i) n += q_op(i >> 4, i & 15); return n; }
constexpr int q_min(int a, int b) { return a < b ? a : b; }
constexpr int q_wait(int kt) {
    int n = kt <= 2 ? 4 * (2 - kt) + q_after(-1, kt) : q_after((kt - 2) * 16 + 2, kt);
    if (kt + 1 >= QWD) n = q_min(n, q_after((kt + 1 - QWD) * 16 + 14, kt));
    if (kt == 13) n = q_min(n, q_after(12 * 16 + 3, kt));
    return n; }
constexpr int q_fl() { int s = 0; while (q_s0(s + 1) < 8) ++s; int kt = s; while (q_wait(kt) > q_after(s * 16 + 6, kt)) ++kt; return kt; }
constexpr int Q_NST = q_s0(16);
static_assert(Q_NST >= 8 && QS1 < 0x2000, "");
constexpr int o_xp(int st, int i) { return st <= 5 ? 64 + 4 * (4 * (st - 4) + i) : 32 * (st - 3) + (i == 0 ? 0 : i == 1 ? 4 : i == 2 ? 12 : 16); }
constexpr int o_wp(int st, int i) { return st <= 7 ? 16 * (st - 4) + 4 + 6 * i : 32 * (st - 5) + 24 + 4 * i; }
constexpr int o_x(int h) { for (int st = 4; st < 16; ++st) for (int i = 0; i < 4; ++i) if (o_xp(st, i) == h) return 4 * st + i; return -1; }
constexpr int o_w(int h) { for (int st = 4; st < 16; ++st) for (int i = 0; i < 2; ++i) if (o_wp(st, i) == h) return 2 * st + i; return -1; }
constexpr int o_after(int p, int e) { int n = 0; for (int h = p + 1; h < e; ++h) n += (o_x(h) >= 0) + (o_w(h) >= 0); return n; }
constexpr int o_wait(int kt) { return o_after(kt == 11 ? o_wp(15, 1) : o_xp(kt + 1, 3), 32 * kt + 24); }
constexpr int L_PFT = 149504, L_DUMP = 155648;
constexpr int CFS = 128;
constexpr unsigned F1S = 0xfff642ffu, F1F = 0xfff5310fu, F2S = 0xff6f204fu, F2F = 0xff5f13ffu;

__global__ __launch_bounds__(512, 2) void k_all(const u16* __restrict__ x, const u16* __restrict__ ckv, const u16* __restrict__ wuk, const u16* __restrict__ wuv,
                                                const u16* __restrict__ w_r, char* __restrict__ ws, const u16* __restrict__ wq, const u16* __restrict__ wo,
                                                const u16* __restrict__ w_gate, const u16* __restrict__ w_up,
                                                const u16* __restrict__ w_down, u16* __restrict__ out) {
    extern __shared__ __attribute__((aligned(16))) char lds[];
    const int b = blockIdx.x, xcd = b & 7, idx = b >> 3, head = xcd * 4 + (idx >> 3), jj = idx & 7;
    const int hp = head ^ 1;
    const unsigned ldsbase = (unsigned)(uintptr_t)(__attribute__((address_space(3))) char*)lds;
    const int wave = __builtin_amdgcn_readfirstlane(threadIdx.x >> 6);
    const int qbA = 15 - jj, qbB = jj;
#define KROW(g) (((2 * ((g) & 3) + ((g) >> 2)) * 8 + jj) * 32)
    const int qw = wave < 4 ? wave : 7 - wave, qb = wave < 4 ? qbA : qbB;
    bf16x8 qf[8];
    u64 tag;
    u16* outp = out;
    int ri1, ri2;
    PH_STAMP(0);
    SST(0);
    ZST(0);
    u32x4 pk[16];
    bf16x8 xr[4][2][2];
    u32x4 rv;
    const char* xg;
#define QXL1(j) asm volatile("global_load_dwordx4 %0, %1, %2 offset:%3 sc1" : "=v"(xr[(j) >> 2][(j) & 1][((j) >> 1) & 1]) : "v"(xvo), "s"(xg + (size_t)((j) >> 2) * 262144), \
                            "i"(((j) & 1) * 2048 + (((j) >> 1) & 1) * 1024) : "memory")
    unsigned xvo, qwo;
    const u16* wqb;
#define QWOFF() ({ unsigned l_ = threadIdx.x & 63; asm volatile("" : "+v"(l_)); (unsigned)(((l_ >> 2) & 7) * (NH * HD) + 32 * (l_ >> 5) + 8 * ((l_ & 3) ^ ((l_ >> 3) & 2))) * 2u; })
    {
        constexpr int LDW = NH * HD, K = DC;
        int tid = threadIdx.x; asm volatile("" : "+v"(tid));
        const int lane = tid & 63;
        const int wm = wave >> 2, wn = wave & 3;
        const int row = b * 8 + wave;
        u32x4 lx[2], lw[2]; u32x2 ep;
        asm volatile("global_load_dwordx4 %0, %5, %6\n\tglobal_load_dwordx4 %1, %5, %6 offset:1024\n\tglobal_load_dwordx4 %2, %5, %7\n\tglobal_load_dwordx4 %3, %5, %7 offset:1024\n\t"
                     "global_load_dwordx2 %4, %8, off sc1"
                     : "=&v"(lx[0]), "=&v"(lx[1]), "=&v"(lw[0]), "=&v"(lw[1]), "=&v"(ep) : "v"(lane * 16), "s"(x + (unsigned)(row * DM)), "s"(w_r), "v"(ws + WS_FLAG + F_EPOCH) : "memory");
#define XKEY(r) (((r) >> 1) & 7)
        unsigned xoff[4], woff[2];
        { const unsigned x0_ = (unsigned)(KROW(wave) + (lane >> 3)) * (K * 2) + (unsigned)((lane & 7) ^ (lane >> 4)) * 16u;
          xoff[0] = x0_; xoff[1] = (x0_ ^ 64u) + 7168u; xoff[2] = x0_ + 14336u; xoff[3] = xoff[1] + 14336u;
          woff[1] = (unsigned)((8 * wave + ((lane >> 2) & 7)) * LDW + hp * HD + 32 * (lane >> 5) + 8 * ((lane & 3) ^ (2 * (wave & 1)))) * 2u; woff[0] = woff[1] + 896u; }
        const char* xsp = (const char*)ckv;
        const char* wsp = (const char*)wuk - 896;
        const char* wsv = (const char*)wuv - 2944;
        const unsigned m0b = ldsbase + wave * 4096;
#define KM0(g, w_) asm volatile("s_add_u32 m0, %0, %1" :: "s"(m0b), "i"(((g) & 1) * 65536 + (w_) * 32768) : "memory", "scc")
#define KRX(i) asm volatile("global_load_lds_dwordx4 %0, %1 offset:%2" :: "v"(xoff[i]), "s"(xsp), "i"((i) * 1024) : "memory")
#define KRW(i) asm volatile("global_load_lds_dwordx4 %0, %1 offset:%2 nt" :: "v"(woff[(i) & 1]), "s"((i) < 2 ? wsp : wsv), "i"((i) * 1024) : "memory")
#define STAGE(g) do { KM0(g, 0); asm volatile("s_nop 0"); KRX(0); KRX(1); KRX(2); KRX(3); KM0(g, 1); asm volatile("s_nop 0"); KRW(0); KRW(1); KRW(2); KRW(3); \
                      OADV(xsp, 128); OADV(wsp, 64 * LDW * 2); OADV(wsv, 64 * LDW * 2); } while (0)
#define KST(i)
        const unsigned pfl = ldsbase + L_DUMP + wave * 1024;
#define PFX(g) do { unsigned l_ = threadIdx.x & 63; asm volatile("" : "+v"(l_)); l_ = ((l_ >> 3) * DC + (l_ & 7) * 8) * 2u; GLDS_S(l_, pfxp + (g) * 64, pfl, 0); } while (0)
#define PFQ(i) do { unsigned l_ = threadIdx.x & 63; asm volatile("" : "+v"(l_)); l_ = ((l_ >> 4) * LDW + (l_ & 15) * 8) * 2u; GLDS_S(l_, pfqp + (size_t)(4 * (i)) * LDW, pfl, 0); } while (0)
        STAGE(0);
        SB(); ZST(1);
        int w2 = wave; asm volatile("" : "+s"(w2));
        xg = ws + WS_XF + (unsigned)((w2 < 4 ? qbA * 4 + w2 : qbB * 4 + 7 - w2) * 4096);
        wqb = wq + (unsigned)(8 * w2 * LDW + head * HD);
        const u16* pfqp = wq + (unsigned)((128 * jj + 16 * w2) * LDW + head * HD);
        const u16* pfxp = ckv + (unsigned)((KROW(w2) + 8 * (head & 3)) * DC);
        unsigned xa[2][2], wa[2][2];
        const int l15 = lane & 15, q4 = lane >> 4;
#pragma unroll
        for (int s = 0; s < 2; ++s) {
            const int r0 = l15;
            xa[0][s] = ldsbase + (unsigned)((wm * 128 + r0) * 128 + ((((4 * s + q4) ^ XKEY(r0))) << 4));
            wa[0][s] = (ldsbase + 32768u + (unsigned)(q4 * 4096 + wn * 512 + (l15 >> 2) * 64 + (q4 & 1) * 32 + (lane & 3) * 8)) ^ (32u * s);
            xa[1][s] = xa[0][s] + 65536u; wa[1][s] = wa[0][s] + 65536u;
            PIN(xa[0][s]); PIN(xa[1][s]); PIN(wa[0][s]); PIN(wa[1][s]); }
        u64 pf0;
        { u64 pw_ = (u64)(((w2 & 2) ? wuv : wuk) + (unsigned)((128 + 192 * (w2 & 1) + jj) * LDW + hp * HD)), pc_ = (u64)ckv + (unsigned)(b * 8192 + (w2 & 3) * 2048);
          asm volatile("" : "+s"(pw_), "+s"(pc_));
          unsigned o0_ = (unsigned)(lane >> 1) * (8 * LDW * 2) + (lane & 1) * 128u, o1_ = (lane & 15) * 128u; PIN(o0_); PIN(o1_);
          pf0 = (lane < 48 ? pw_ : pc_) + (lane < 48 ? o0_ : o1_); PIN(pf0); }
        asm volatile("" : "+s"(pfqp), "+s"(pfxp), "+s"(xg), "+s"(wqb));
        SB();
        asm volatile("s_waitcnt vmcnt(8)" : "+v"(lx[0]), "+v"(lx[1]), "+v"(lw[0]), "+v"(lw[1]), "+v"(ep) :: "memory");
        KST(1); ZST(2);
#define HALF(v_, i) __uint_as_float(((i) & 1) ? (v_) & 0xffff0000u : (v_) << 16)
#define BOTH(v_) f32x2{HALF(v_, 0), HALF(v_, 1)}
        {
            f32x2 v[8];
#pragma unroll
            for (int i = 0; i < 8; ++i) v[i] = BOTH(lx[i >> 2][i & 3]);
            const float pv_ = __uint_as_float(__builtin_amdgcn_readfirstlane(__float_as_uint(v[0][0])));
            f32x2 s2 = f32x2{0.f, 0.f}, q2 = f32x2{0.f, 0.f};
#pragma unroll
            for (int i = 0; i < 8; ++i) { const f32x2 d = v[i] - pv_; s2 += d; q2 += d * d; }
            float s = s2[0] + s2[1], q = q2[0] + q2[1];
            DPP_ADD(s, 0x111, 0xf); DPP_ADD(q, 0x111, 0xf); DPP_ADD(s, 0x112, 0xf); DPP_ADD(q, 0x112, 0xf); DPP_ADD(s, 0x114, 0xf); DPP_ADD(q, 0x114, 0xf);
            DPP_ADD(s, 0x118, 0xf); DPP_ADD(q, 0x118, 0xf); DPP_ADD(s, 0x142, 0xa); DPP_ADD(q, 0x142, 0xa); DPP_ADD(s, 0x143, 0xc); DPP_ADD(q, 0x143, 0xc);
            const float dm = __uint_as_float(__builtin_amdgcn_readlane(__float_as_uint(s), 63)) * (1.f / DM);
            const float var_bf = rbf(fmaxf(fmaf(-dm, dm, __uint_as_float(__builtin_amdgcn_readlane(__float_as_uint(q), 63)) * (1.f / DM)), 0.f));
            const float mean_bf = rbf(pv_ + dm), den = rbf(__builtin_amdgcn_sqrtf(rbf(var_bf + 1e-5f))), rden = __builtin_amdgcn_rcpf(den);
            u32x4 xw[2]; float r2[2] = {0.f, 0.f};
#pragma unroll
            for (int i = 0; i < 8; ++i) {
                const f32x2 c_ = v[i] - mean_bf;
                const unsigned cp = cvtpk(c_[0], c_[1]);
                const f32x2 cb = BOTH(cp);
                f32x2 n_ = cb * rden;
                n_ += (cb - n_ * den) * rden;
                const unsigned np = cvtpk(n_[0], n_[1]);
                const unsigned wp = lw[i >> 2][i & 3];
                r2[i & 1] = __builtin_amdgcn_fdot2_f32_bf16(__builtin_bit_cast(bfx2, np), __builtin_bit_cast(bfx2, wp), r2[i & 1], false);
                xw[i >> 2][i & 3] = np;
            }
            char* lr = lds + 131072 + wave * 2064 + lane * 16;
            *(u32x4*)lr = xw[0]; *(u32x4*)(lr + 1024) = xw[1];
            s = r2[0] + r2[1];
            DPP_ADD(s, 0x111, 0xf); DPP_ADD(s, 0x112, 0xf); DPP_ADD(s, 0x114, 0xf); DPP_ADD(s, 0x118, 0xf); DPP_ADD(s, 0x142, 0xa); DPP_ADD(s, 0x143, 0xc);
            if (lane == 63) *(float*)(lds + 131072 + wave * 2064 + 2048) = rbf(s);
        }
        SB(); ZST(3);
        STAGE(1);
        asm volatile("s_bitcmp0_b32 %2, 2\n\ts_cbranch_scc1 1f\n\ts_mov_b32 m0, %1\n\ts_nop 0\n\tglobal_load_lds_dword %0, off\n1:" :: "v"(pf0), "s"(pfl), "s"(wave) : "memory", "scc");
        tag = (((u64)(unsigned)__builtin_amdgcn_readfirstlane(ep[1]) << 32) | (unsigned)__builtin_amdgcn_readfirstlane(ep[0])) + 0x9E3779B97F4A7C15ull;
        float rr;
        const unsigned l16 = (unsigned)lane * 16u;
        u32x2 xi_[4];
#define XI_RD() do { int t_ = threadIdx.x; asm volatile("" : "+v"(t_)); const int r8 = t_ & 7, pc = t_ >> 3;                               \
            const char* lr = lds + 131072 + r8 * 2064 + (pc >> 2) * 64 + ((pc >> 1) & 1) * 32 + (pc & 1) * 8;                              \
            xi_[0] = *(const u32x2*)lr; xi_[1] = *(const u32x2*)(lr + 16); xi_[2] = *(const u32x2*)(lr + 1024); xi_[3] = *(const u32x2*)(lr + 1040); \
            rr = *(const float*)(lds + 131072 + wave * 2064 + 2048); } while (0)
#define XI_ST() do { int t_ = threadIdx.x; asm volatile("" : "+v"(t_)); const int r8 = t_ & 7, pc = t_ >> 3;                               \
            char* dst = ws + WS_XF + (size_t)(pc >> 3) * 262144 + (b >> 1) * 2048 + (pc & 7) * 256 + ((b & 1) * 8 + r8) * 16;              \
            const u32x4 pa = u32x4{xi_[0][0], xi_[0][1], xi_[1][0], xi_[1][1]}, pb = u32x4{xi_[2][0], xi_[2][1], xi_[3][0], xi_[3][1]};    \
            ST16_SC1(dst, pa); ST16_SC1(dst + 2097152, pb); } while (0)
        SB(); ZST(4);
        asm volatile("s_waitcnt vmcnt(%1)\n\ts_bitcmp1_b32 %0, 2\n\ts_cbranch_scc1 1f\n\ts_waitcnt vmcnt(8)\n1:\n\ts_barrier" :: "s"(wave), "i"(9) : "memory", "scc");
        KST(2); ZST(6);
        f32x4 a16[8][4];
        bf16x8 xf[2][4];
        bf16x4 wl[4], wh[4];
#define KXR(kt, q, mb) xf[(q) & 1][mb] = lds_rd(xa[(kt) & 1][(q) >> 1] + (unsigned)((((q) & 1) * 4 + (mb)) * 2048))
#define KWL(kt, s32, nb) wl[nb] = tr_rd(wa[(kt) & 1][(nb) & 1] + (unsigned)((s32) * 16384 + ((nb) >> 1) * 2048))
#define KWH(kt, s32, nb) wh[nb] = tr_rd(wa[(kt) & 1][(nb) & 1] + (unsigned)((s32) * 16384 + ((nb) >> 1) * 2048 + 256))
#define KMF(kt, q, g) do { const bf16x8 w_ = CAT8(wl[(g) >> 2], wh[(g) >> 2]); constexpr int m_ = ((q) & 1) * 4 + ((g) & 3);               \
            if ((kt) == 0 && (q) < 2) { if ((g) >= 8) MFA16Z(a16[m_][(g) >> 2], xf[(q) & 1][(g) & 3], w_); else MFA16Z(a16[m_][(g) >> 2], w_, xf[(q) & 1][(g) & 3]); } \
            else                      { if ((g) >= 8) MFA16(a16[m_][(g) >> 2], xf[(q) & 1][(g) & 3], w_);  else MFA16(a16[m_][(g) >> 2], w_, xf[(q) & 1][(g) & 3]); } } while (0)
        u64 lnf;
#define LNFP (ws + WS_FLAG + F_LN + (wave * 32 + (lane & 31)) * 8)
#define KRQ(i) do { GLDS_S(qwo, wqb + (size_t)(((i) >> 1) * 64) * LDW, ldsbase + wave * 2048 + ((i) >> 1) * 16384 + ((i) & 1) * 896, ((i) & 1) * 128); } while (0)
        constexpr int KRS = 3, KPFD = 3;
#define KG(kt, q, g) do { constexpr int nk_ = (kt) + ((q) == 3), nq_ = ((q) + 1) & 3;                                                     \
            constexpr int xi = ((q) & 1) ? ((g) == 1 ? 0 : (g) == 2 ? 1 : (g) == 5 ? 2 : (g) == 6 ? 3 : -1) : (((g) & 1) && (g) < 8 ? (g) >> 1 : -1); \
            constexpr int ri = (q) == 3 ? ((g) == 9 ? 0 : (g) == 10 ? 1 : (g) == 13 ? 2 : (g) == 14 ? 3 : -1) : (((g) & 1) && (g) > 8 ? ((g) - 9) >> 1 : -1); \
            KMF(kt, q, g); SB();                                                                                                        \
            if (xi >= 0 && nk_ < 8) KXR(nk_, nq_, xi & 3);                                                                              \
            if (((q) & 1) && nk_ < 8) { if (((g) & 3) == 3) KWL(nk_, nq_ >> 1, (g) >> 2); if (((g) & 3) == 0 && (g) > 0) KWH(nk_, nq_ >> 1, (((g) >> 2) + 3) & 3); } \
            if (!((q) & 1) && (g) == 0 && (kt) + (q) > 0) KWH(kt, (q) >> 1, 3);                                                          \
            { constexpr int G = (q) == 3 ? (g) : (q) == 0 ? 16 + (g) : -9, T = (kt) + ((q) == 3 ? 2 : 1);          \
              if (T >= 2 && T < 8) { if (G == 0) KM0(T, 0); if (G == 4 * KRS) KM0(T, 1);                                                 \
                  if (G >= 1 && (G - 1) % KRS == 0 && (G - 1) / KRS < 8) { if ((G - 1) / KRS < 4) KRX(((G - 1) / KRS) & 3); else KRW(((G - 1) / KRS) & 3); } \
                  if (G == 29) OADV(xsp, 128); if (G == 30) OADV(wsp, 64 * LDW * 2); if (G == 31) OADV(wsv, 64 * LDW * 2); } }                                                 \
            if ((q) == 1 && (kt) + KPFD < 8) { if ((kt) >= 1 && (g) == 13) PFX((kt) + KPFD); }                \
            if ((q) == 2 && (kt) == 6 && (g) == 9) { qwo = QWOFF(); PIN(qwo); }                                                         \
            if ((q) == 1 && (kt) == 7) { if ((g) == 9) asm volatile("global_load_dwordx4 %0, %1, %2 sc1" : "=v"(rv) : "v"(xvo), "s"(ws + WS_R + wave * 1024) : "memory"); \
                                         if ((g) == 10) QXL1(0); if ((g) == 13) QXL1(1); if ((g) == 14) QXL1(2); }                      \
            if ((q) == 2 && (kt) == 7 && (g) == 2) QXL1(3);                                                                             \
            if ((q) == 3 && (kt) == 6 && ri >= 0) KRQ(ri & 3);                                                                          \
            if ((q) == 0 && (kt) == 7 && ri >= 0 && 4 + ri < 2 * QWD) KRQ(4 + (ri & 3));                                                                    \
            if ((q) == 1 && (kt) == 1) { if ((g) == 8) XI_RD();                                                                       \
                if ((g) == 13) ST4_SC1((float*)(ws + WS_R) + row, rr);                               \
                if ((g) == 15) XI_ST(); }                                                                                               \
            if ((q) == 1 && ((kt) == 3 || (kt) == 4)) { if ((g) == 9) PFQ((2 * (kt) - 6) & 3); if ((g) == 11) PFQ((2 * (kt) - 5) & 3); }                                                    \
            if ((q) == 2 && (kt) == 5 && (g) == 12) FLAG_GET(lnf, LNFP);                                                                \
            SB(); } while (0)
#define KQ(kt, q) do { KG(kt, q, 0); KG(kt, q, 1); KG(kt, q, 2); KG(kt, q, 3); KG(kt, q, 4); KG(kt, q, 5); KG(kt, q, 6); KG(kt, q, 7);    \
                       KG(kt, q, 8); KG(kt, q, 9); KG(kt, q, 10); KG(kt, q, 11); KG(kt, q, 12); KG(kt, q, 13); KG(kt, q, 14); KG(kt, q, 15); } while (0)
#define KEXT(s) (((s) == 1 ? 3 : (s) == 5 ? 1 : 0) + ((s) >= 1 && (s) + KPFD < 8) + ((s) == 3 || (s) == 4 ? 2 : 0))
#define KSTEP(kt) do {                                                                                                                  \
        KQ(kt, 0); KQ(kt, 1); KQ(kt, 2);                                                                                                \
        if ((kt) == 6) FLAG_WAIT(lnf, LNFP, tag, 0);                                   \
                       \
        if ((kt) == 0) asm volatile("s_waitcnt vmcnt(%1) lgkmcnt(0)\n\ts_bitcmp1_b32 %0, 2\n\ts_cbranch_scc1 1f\n\ts_waitcnt vmcnt(%2)\n1:\n\ts_barrier" :: "s"(wave), "i"(KEXT(0) + 1), "i"(KEXT(0)) : "memory", "scc"); \
        else asm volatile("s_waitcnt vmcnt(%0) lgkmcnt(0)\n\ts_barrier" :: "i"(KEXT(kt)) : "memory");                                        \
        KST((kt) + 3);                                                                                                                  \
        if ((kt) == 2) FLAG_PUT(ws + WS_FLAG + F_LN + b * 8, tag, wave == 0);             \
        if ((kt) == 6) FLAG_PUT(ws + WS_FLAG + F_EPOCH, tag - 0x9E3779B97F4A7C15ull + 1, b == 0 && wave == 0);     \
        SB();                                                                                                                           \
        KQ(kt, 3);                                                                                                                      \
    } while (0)
        KXR(0, 0, 0); KXR(0, 0, 1); KWL(0, 0, 0); KWH(0, 0, 0); KXR(0, 0, 2); KXR(0, 0, 3);
        KWL(0, 0, 1); KWH(0, 0, 1); KWL(0, 0, 2); KWH(0, 0, 2); KWL(0, 0, 3); KWH(0, 0, 3);
        SB();
        KSTEP(0); ZST(7); KSTEP(1); KSTEP(2); KSTEP(3); KSTEP(4); KSTEP(5); KSTEP(6);
        xvo = l16;
        KQ(7, 0); KQ(7, 1); KQ(7, 2);
        if (wave < 4) {
            int t2_ = threadIdx.x; asm volatile("" : "+v"(t2_));
            unsigned l7_ = t2_ & 7, c7_ = jj >= 3 ? 7 : 8, b0_ = (unsigned)(((jj + 7) & 7) * 128 + head * 4 + (wave & 3)), b7_ = 3584u + head * 4 + (wave & 3);
            u64 pg_ = (u64)((wave & 1) ? w_up : w_gate), pd_ = (u64)w_down - 1792ull * 8192, po_ = (u64)wo - 2688ull * 8192;
            asm volatile("" : "+s"(c7_), "+s"(b0_), "+s"(b7_), "+s"(pg_), "+s"(pd_), "+s"(po_));
            const unsigned lt_ = l7_ < (unsigned)jj ? l7_ : 0;
            unsigned n_ = b0_ + (13u - lt_) * lt_ * 64u; n_ = l7_ == c7_ ? b7_ : n_;
            u64 p_ = n_ < 2688u ? pd_ : po_; p_ = n_ < 1792u ? pg_ : p_; n_ = n_ < 1792u ? n_ >> 1 : n_;
            *(u64*)(lds + L_PFT + t2_ * 8) = p_ + (u64)n_ * 8192u;
        }
        SB();
        asm volatile("s_waitcnt vmcnt(%0)\n\ts_barrier" :: "i"(2 * QWD + 1) : "memory");
        SB();
#define PKCV(i) do { const f32x4 a_ = ((i) & 2) ? a16[2 * ((i) >> 2)][(i) & 3] : a16[2 * ((i) >> 2) + ((i) & 1)][0];                       \
                     const f32x4 b_ = ((i) & 2) ? a16[2 * ((i) >> 2) + 1][(i) & 3] : a16[2 * ((i) >> 2) + ((i) & 1)][1];                   \
                     pk[i] = u32x4{cvtpk(a_[0], a_[1]), cvtpk(a_[2], a_[3]), cvtpk(b_[0], b_[1]), cvtpk(b_[2], b_[3])}; PIN(pk[i]); } while (0)
#define KL(i) do { KMF(7, 3, 2 * (i)); SB(); if ((i) >= 2) QXL1(2 * (i)); SB(); KMF(7, 3, 2 * (i) + 1); SB(); PKCV(i); if ((i) >= 2) QXL1(2 * (i) + 1); SB(); } while (0)
        KL(0); KL(1); KL(2); KL(3); KL(4); KL(5); KL(6); KL(7);
        KST(10);
        MF_DONE();
#pragma unroll
        for (int i = 8; i < 16; ++i) PKCV(i);
        SB();
        KST(11); ZST_OUT();
#undef PKCV
#undef KSTEP
#undef PFQ
#undef XKEY
    }
    PH_STAMP(1);
    const char* kvg = ws + WS_KV + (size_t)head * HSTR;
    const unsigned svv = xvo + 32768u; const char* sb_[4];
    u64 pft; bf16x8 ones; unsigned ak, av;
    const int NA = qbA + 1;
    {
        constexpr int LDW = NH * HD, STG = 16384;
        int tid = threadIdx.x; asm volatile("" : "+v"(tid));
        const int lane = tid & 63, l15 = lane & 15, q4 = lane >> 4;
        const unsigned woff = qwo, xvo2 = xvo + 262144u;
        const unsigned wa = ldsbase + (unsigned)((q4 >> 1) * 4096 + (4 * (q4 & 1) + (l15 >> 2)) * 64 + (q4 & 1) * 32 + (lane & 3) * 8), wa2 = wa ^ 32u;
        f32x4 acc[2][8];
        bf16x4 wl[8], wh[8];
#pragma unroll
        for (int i = 0; i < 4; ++i) {
            sb_[i] = ws + WS_KV + (size_t)hp * HSTR + (size_t)(4 * i + (wave >> 2) * 2 + (jj >> 2)) * 65536 + (jj & 3) * 8192 + (wave & 3) * 2048;
            asm volatile("" : "+s"(sb_[i])); }
#define KVST(i) asm volatile("global_store_dwordx4 %0, %1, %2 offset:%3 sc1" :: "v"(((i) & 2) ? svv : xvo), "v"(pk[i]), "s"(sb_[(i) >> 2]), "i"(((i) & 1) * 1024) : "memory")
        u64 kvf = 0;
#define QKV(kt, f) do { constexpr int g_ = q_kv(kt, f), p_ = g_ >> 2; if (g_ >= 0) {                                                       \
            if ((g_ & 3) == 0) asm volatile("s_mov_b32 m0, %0\n\ts_nop 0" :: "s"(ldsbase + (p_ == 1 ? 0 : 65536 + p_ * 16384) + wave * 4096) : "memory"); \
            asm volatile("global_load_lds_dwordx4 %0, %1 offset:%2 sc1" :: "v"(xvo), "s"(kvg + p_ * 32768 + wave * 4096), "i"((g_ & 3) * 1024) : "memory"); } } while (0)
#define KVFP (ws + WS_FLAG + ((lane & 8) ? F_TOP : F_KV + 2048 + (head * 8 + (lane & 7)) * 8))
        const bool top2 = b == 56;
        unsigned* tw = (unsigned*)(lds + 131072 + 2048);
#define QFA(s, f) ((((f) & 1) ? wa2 : wa) + (unsigned)(((s) & 3) * STG + ((f) >> 3) * 8192 + (((f) >> 2) & 1) * 1024 + (((f) >> 1) & 1) * 512))
#define QRL(s, f) wl[(f) & 7] = tr_rd(QFA(s, f))
#define QRH(s, f) wh[(f) & 7] = tr_rd(QFA(s, f) + 2048u)
#define QMF(a, kt, f) do { if ((kt) == 0 && (f) < 8) MFA16Z(acc[a][(f) & 7], w_, xr[0][a][0]); else MFA16(acc[a][(f) & 7], w_, xr[(kt) & 3][a][(f) >> 3]); } while (0)
#define ZP0 238
#define QPAIR(kt, f, A, B) do { const bf16x8 w_ = CAT8(wl[(f) & 7], wh[(f) & 7]); constexpr int s_ = (kt) + (((f) + 4) >> 4), m_ = ((f) + 4) & 15;  \
            QMF(0, kt, f); SB(); if (s_ < 16) QRL(s_, m_); A; QKV(kt, f); SB();                                      \
            QMF(1, kt, f); SB(); if (s_ < 16) QRH(s_, m_); B;                                                                             \
            { constexpr int z_ = (kt) * 16 + (f) - ZP0;                    \
                                                                    \
              if (z_ == 8) { pft = *(const u64*)(lds + L_PFT + (tid & 255) * 8); }                                                       \
              if (z_ == 15) { av = ldsbase + xvo; ak = av + 65536u; PIN(av); PIN(ak); }                                                   \
              if (z_ == 16) { const unsigned o1 = (lane & 3) ? 0u : 0x3f803f80u; ones = __builtin_bit_cast(bf16x8, u32x4{o1, o1, o1, o1}); PIN(ones); }     \
              if (z_ == 14) PIN(pft); } SB(); if (((f) & 1) && s_ < 16) { __builtin_amdgcn_s_waitcnt(0xC47F); SB(); } } while (0)
        const char* xsp = xg + 3 * 262144;
#define QX(r, rb, ks, v_) asm volatile("global_load_dwordx4 %0, %1, %2 offset:%3 sc1" : "+v"(xr[r][rb][ks]) : "v"(v_), "s"(xsp), "i"((rb) * 2048 + (ks) * 1024) : "memory")
        const unsigned woff1 = woff + 1024u, woff2 = woff + 128u;
        const char* wsp = (const char*)(wqb + (size_t)QWD * 64 * LDW) - 1024;
        const unsigned m0b = ldsbase + wave * 2048;
#define QM0(r) asm volatile("s_add_u32 m0, %0, %1" :: "s"(m0b), "i"((r) * STG) : "memory", "scc")
#define QW1(v_, imm_) asm volatile("global_load_lds_dwordx4 %0, %1 offset:%2" :: "v"(v_), "s"(wsp), "i"(imm_) : "memory")
#define QST(i)
#define QSTEP(kt) do { constexpr int r_ = (kt) & 3;                                                                                     \
        QPAIR(kt, 0, if (q_xb(kt)) QX((r_ + 3) & 3, 0, 1, xvo), );                                                                      \
        QPAIR(kt, 1, , );                                                                                                               \
        QPAIR(kt, 2, if (q_xb(kt)) QX((r_ + 3) & 3, 1, 1, xvo), );                                                                      \
        QPAIR(kt, 3, if ((kt) == 12) FLAG_GET(kvf, KVFP), );                                                                            \
        if ((kt) == 0 && __builtin_expect(top2, 0)) {                                                                                                        \
            unsigned t1 = tkey(rv[0], tid * 4), t2 = 0;                                                                                 \
            _Pragma("unroll") for (int j = 1; j < 4; ++j) merge_top2(t1, t2, tkey(rv[j], tid * 4 + j), 0);                              \
            DPP_TOP2(t1, t2, 0x111, 0xf); DPP_TOP2(t1, t2, 0x112, 0xf); DPP_TOP2(t1, t2, 0x114, 0xf); DPP_TOP2(t1, t2, 0x118, 0xf);     \
            DPP_TOP2(t1, t2, 0x142, 0xa); DPP_TOP2(t1, t2, 0x143, 0xc);                                                                 \
            if (lane == 63) { tw[wave * 2] = t1; tw[wave * 2 + 1] = t2; } SB(); }                                                       \
        QPAIR(kt, 4, if ((QS1 >> (kt)) & 1) KVST(q_s0(kt) & 15), if ((QS1 >> (kt)) & 1) KEEP(pk[q_s0(kt) & 15]));                       \
        QPAIR(kt, 5, , );                                                                                                               \
        QPAIR(kt, 6, , );                                                                                                               \
        QPAIR(kt, 7, , );                                                                                                               \
        QPAIR(kt, 8, if (q_xa(kt)) QX(r_, 0, 0, xvo2), );                                                                               \
        QPAIR(kt, 9, , );                                                                                                               \
        QPAIR(kt, 10, if (q_xa(kt)) QX(r_, 1, 0, xvo2), if ((kt) <= 11) OADV(xsp, 262144));                                             \
        QPAIR(kt, 11, if ((kt) + QWD < 16) QM0((r_ + QWD) & 3), );                                                                      \
        if ((kt) < 15) {                              \
            if (QWD == 4 || (kt) == 0 || (kt) >= 13) asm volatile("s_waitcnt vmcnt(%0) lgkmcnt(0)\n\ts_barrier" :: "i"(q_wait(kt)) : "memory"); \
            else asm volatile("s_waitcnt vmcnt(%0)\n\ts_barrier" :: "i"(q_wait(kt)) : "memory");                                        \
            if ((kt) == 13) { PIN(kvf); const unsigned sh_ = (lane & 8) ? 22u : 0u; const u64 ex_ = (tag << sh_) >> sh_;     \
                if (__builtin_expect(__builtin_amdgcn_ballot_w64((kvf >> sh_) != ex_) != 0, 0)) { unsigned c_; u64 t_;                    \
                    asm volatile("s_mov_b32 %1, 0x40000\n1:\n\ts_sleep 2\n\tglobal_load_dwordx2 %0, %3, off sc1\n\ts_waitcnt vmcnt(0)\n\tv_lshrrev_b64 %2, %5, %0\n\t" \
                                 "v_cmp_ne_u64 vcc, %4, %2\n\ts_cmp_eq_u64 vcc, 0\n\ts_cbranch_scc1 2f\n\ts_sub_u32 %1, %1, 1\n\ts_cbranch_scc0 1b\n2:" \
                                 : "+v"(kvf), "=&s"(c_), "=&v"(t_) : "v"(KVFP), "v"(ex_), "v"(sh_) : "memory", "vcc", "scc"); }           \
                const unsigned r_ = __builtin_amdgcn_readlane((unsigned)kvf, 8); ri1 = r_ & 2047u; ri2 = (r_ >> 11) & 2047u; }           \
            QST((kt) + 1); SB(); }                                                                                                      \
        QPAIR(kt, 12, if ((kt) + QWD < 16) QW1(woff1, 0), );                                                                            \
        QPAIR(kt, 13, , );                                                                                                              \
        QPAIR(kt, 14, if ((kt) + QWD < 16) QW1(woff2, 1024), if ((kt) + QWD < 15) OADV(wsp, 64 * LDW * 2));                             \
        if ((kt) == q_fl()) FLAG_PUT(ws + WS_FLAG + F_KV + 2048 + (hp * 8 + jj) * 8, tag, wave == 0);     \
        QPAIR(kt, 15, , );                                                                                                              \
        if ((kt) == 0 && __builtin_expect(top2, 0)) {                                                                                                        \
            unsigned t1 = tw[0], t2 = tw[1];                                                                                            \
            _Pragma("unroll") for (int w = 1; w < 8; ++w) merge_top2(t1, t2, tw[w * 2], tw[w * 2 + 1]);                                 \
            FLAG_PUT(ws + WS_FLAG + F_TOP, (tag << 22) | (unsigned)__builtin_amdgcn_readfirstlane((~t1 & 2047u) | (~t2 & 2047u) << 11), wave == 0); \
            } \
        SB();                                                                                                                           \
    } while (0)
        QRL(0, 0); QRH(0, 0); QRL(0, 1); QRH(0, 1); QRL(0, 2); QRH(0, 2); QRL(0, 3); QRH(0, 3); SB();
        asm volatile("s_waitcnt vmcnt(12)" : "+v"(rv) :: "memory");
        SB();
        QST(0);
        KST(12);
        QSTEP(0); QSTEP(1); QSTEP(2); QSTEP(3); QSTEP(4); QSTEP(5); QSTEP(6); QSTEP(7);
        QSTEP(8); QSTEP(9); QSTEP(10); QSTEP(11); QSTEP(12); QSTEP(13); QSTEP(14); QSTEP(15);
        MF_DONE();
        QST(16);
        {
            constexpr float qs = 0.08838834764831845f * 1.4426950408889634f;
#pragma unroll
            for (int i = 0; i < 8; ++i) { const f32x4 a_ = acc[i & 1][i & 6] * qs, b_ = acc[i & 1][i | 1] * qs;
                qf[i] = __builtin_bit_cast(bf16x8, u32x4{cvtpk(a_[0], a_[1]), cvtpk(a_[2], a_[3]), cvtpk(b_[0], b_[1]), cvtpk(b_[2], b_[3])}); PIN(qf[i]); }
        }
#undef QSTEP
    }
    PH_STAMP(2);
    const int kz = xcd & 1, kp = xcd >> 1, nt = kz * 4 + (idx >> 3), tile = jj * 8 + nt;
#define K64(s) ((head * 2) ^ (s))
#define OWOFF() ({ unsigned l_ = threadIdx.x & 63; asm volatile("" : "+v"(l_)); (unsigned)(((l_ >> 2) & 7) * DM + 32 * (l_ >> 5) + 8 * ((l_ & 3) ^ ((l_ >> 4) & 1))) * 2u; })
    const int n0 = (nt ^ 2) * 128, dn1 = (((nt ^ 3) - (nt ^ 2)) * 128 + 64) * 2, ntc = nt ^ 2 ^ (kp >> 1);
    const u16* wob = wo + (size_t)(8 * wave) * DM + n0;
    u64 cxf = tag + 1;
#define CFB (ws + WS_FLAG + F_CTX)
#define CFO() ({ unsigned l_ = threadIdx.x & 15, s0_ = (unsigned)((qb * 4 + qw) * 32 + head), s1_ = (unsigned)((qbB * 4 + 3 - (wave & 3)) * 32 + (head ^ 8)); \
                 asm volatile("" : "+v"(l_), "+s"(s0_), "+s"(s1_)); (l_ < 8 ? l_ ^ s0_ : s1_) * CFS; })
    u32x4 pc[8];
#define EAR_(v_, b_, pol_) "s_add_u32 m0, m0, 0x400\n\ts_nop 0\n\tglobal_load_lds_dwordx4 " v_ ", " b_ pol_ "\n\t"
#define EALD(h_) do { unsigned l_ = threadIdx.x & 63; asm volatile("" : "+v"(l_)); const unsigned lx_ = ((l_ >> 3) * DM + 8 * ((l_ & 7) ^ (l_ >> 3))) * 2u; \
        const int rb_ = ((h_) ? qbB * 8 + qw * 2 : qbA * 8 + (wave & 3) * 2) + (kp & 1);                                                  \
        asm volatile("s_mov_b32 m0, %5\n\t" EAR_("%0", "%3", "") EAR_("%1", "%3", "") EAR_("%2", "%4", " sc1") "global_load_lds_dwordx4 %2, %4 offset:1024 sc1" \
                     :: "v"(lx_), "v"(lx_ + 8 * DM * 2), "v"(l_ * 16u), "s"(x + (size_t)rb_ * 16 * DM + ntc * 128 + (kp >> 1) * 64),              \
                        "s"(ws + WS_XF + (size_t)(2 * ntc + (kp >> 1)) * 262144 + rb_ * 2048),                                             \
                        "s"(ldsbase + 131072 - 1024 + (wave & 3) * 8192 + (h_) * 4096) : "memory", "scc"); } while (0)
    const unsigned i1 = (((kz ? F1S : F1F) >> (4 * jj)) & 15u) * 16u + (kp * 4 + (idx >> 3));
    const unsigned i2 = (((kz ? F2S : F2F) >> (4 * jj)) & 15u) * 16u + (kp * 4 + (idx >> 3));
    const unsigned nf1 = i1 / 112u, nf2 = i2 / 112u;
    const unsigned k1 = i1 / 14u;
    unsigned ypub = 1;
    {
        int tid = threadIdx.x; asm volatile("" : "+v"(tid));
        const int lane = tid & 63;
        const unsigned l16 = (unsigned)lane * 16u;
#define EX(x) __builtin_amdgcn_exp2f(x)
#define TBAR() do { SB(); __builtin_amdgcn_s_waitcnt(0x0070); asm volatile("s_barrier" ::: "memory"); } while (0)
#define BRQ(t, r) do { constexpr int g_ = (r) >> 2; const int tt_ = (t) + (g_ == 2 ? 1 : 2);                                               \
            const unsigned o_ = (unsigned)((wave & 3) * 4096 + (g_ == 1 || g_ == 2 ? 16384 : 0));                                          \
            if (((r) & 3) == 0) GLDS_C(l16, kvg + (size_t)tt_ * 65536 + (g_ >= 2 ? 32768 : 0) + o_, ldsbase + (g_ >= 2 ? 0 : 65536) + (tt_ & 1) * 32768 + o_, 0); \
            else asm volatile("global_load_lds_dwordx4 %0, %1 offset:%2 sc1" :: "v"(l16), "s"(kvg + (size_t)tt_ * 65536 + (g_ >= 2 ? 32768 : 0) + o_), "i"(((r) & 3) * 1024) : "memory"); } while (0)
#define BRQ4(t, g) do { BRQ(t, 4 * (g)); BRQ(t, 4 * (g) + 1); BRQ(t, 4 * (g) + 2); BRQ(t, 4 * (g) + 3); } while (0)
#define BRQ2C(t, r) do { constexpr int g_ = (r) >> 2; const int tt_ = (t) + (g_ == 2 ? 1 : 2);                                             \
            const unsigned o_ = (unsigned)((wave & 3) * 4096 + (g_ == 1 || g_ == 2 ? 16384 : 0));                                          \
            asm volatile("s_cmp_eq_u32 %5, 0\n\ts_cbranch_scc1 1f\n\ts_mov_b32 m0, %2\n\ts_nop 0\n\tglobal_load_lds_dwordx4 %0, %1 offset:%3 sc1\n\t"  \
                         "global_load_lds_dwordx4 %0, %1 offset:%4 sc1\n1:"                                                               \
                         :: "v"(l16), "s"(kvg + (size_t)tt_ * 65536 + (g_ >= 2 ? 32768 : 0) + o_), "s"(ldsbase + (g_ >= 2 ? 0 : 65536) + (tt_ & 1) * 32768 + o_), \
                            "i"(((r) & 3) * 1024), "i"(((r) & 3) * 1024 + 1024), "s"(isb) : "memory", "scc"); } while (0)
        const unsigned isb = wave >> 2;
        u64 kf2 = tag;
        const char* kfb = ws + WS_FLAG + F_KV + head * 64;
#define KCHK() do { PIN(kf2); if (__builtin_expect((__builtin_amdgcn_ballot_w64(kf2 != tag) | (u64)((33u >> t) & 1u)) != 0, 0)) { \
            unsigned c_, a_;                                                                                                              \
            FLAG_PUTZ(ws + WS_FLAG + F_KV + (hp * 8 + jj) * 8, tag, (unsigned)(wave ^ 4) | (unsigned)t);                                  \
            asm volatile("s_cmp_lt_u32 %6, 5\n\ts_cbranch_scc1 3f\n\tv_lshrrev_b32 %2, 1, %5\n\tv_and_b32 %2, 56, %2\n\ts_mov_b32 %1, 0x40000\n" \
                         "1:\n\tglobal_load_dwordx2 %0, %2, %3 sc1\n\ts_cmp_eq_u32 %6, 5\n\ts_cbranch_scc1 3f\n\ts_waitcnt vmcnt(0)\n\tv_cmp_ne_u64 vcc, %4, %0\n\t" \
                         "s_cmp_eq_u64 vcc, 0\n\ts_cbranch_scc1 3f\n\ts_sleep 2\n\ts_sub_u32 %1, %1, 1\n\ts_cbranch_scc0 1b\n"           \
                         "3:" : "+v"(kf2), "=&s"(c_), "=&v"(a_) : "s"(kfb), "s"(tag), "v"(l16), "s"(t) : "memory", "vcc", "scc"); } SB(); } while (0)
#define PFW1(i_) do { unsigned a_; const u64 p_ = ((u64)(unsigned)__builtin_amdgcn_readlane((int)(pft >> 32), i_) << 32) | (unsigned)__builtin_amdgcn_readlane((int)pft, i_); \
            asm volatile("v_lshlrev_b32 %0, 3, %2\n\ts_mov_b32 m0, %3\n\ts_nop 2\n\tglobal_load_lds_dword %0, %1"                        \
                         : "=&v"(a_) : "s"(p_), "v"(l16), "s"(ldsbase + 131072 + (wave & 3) * 8192) : "memory"); } while (0)
#define ONCE2(n_) int CAT(n_once, __LINE__) = (int)(n_); asm volatile("" : "+s"(CAT(n_once, __LINE__))); _Pragma("unroll 1") for (int z_ = 0; z_ < CAT(n_once, __LINE__); ++z_)
#define AST(i)
#define XST(i)
#define TS0()
#define TS1()
        AST(0);
        asm volatile("s_bitcmp0_b32 %0, 2\n\ts_cbranch_scc1 1f\n\ts_setprio 1\n1:" :: "s"(wave) : "scc");
        {
            f32x4 o[8][2], ls[2], ng[2], S[2][4];
            bf16x8 pf[2][2], fr[16];
            float e[16];
            constexpr bool ZC = false;
            constexpr bool HOT = false;
            const unsigned lb16 = ldsbase + l16;
            int t = 0;
#define KOFF(f) ((((f) & 3) * 2 + (((f) >> 2) & 1)) * 1024)
#define RD(kb, n) do { constexpr int k_ = ((kb) + ((n) >> 4)) & 3, n_ = (n) & 15;                                                          \
            fr[n_] = n_ < 8 ? lds_rd(av + (unsigned)(((k_ + 3) & 3) * 8192 + n_ * 1024)) : lds_rd(ak + (unsigned)(((k_ + 1) & 3) * 8192 + KOFF(n_ & 7))); } while (0)
#define MX(p, n, a) do { if (ZC) MFA16Y(o[n][a], fr[n], pf[(p) ^ 1][a]); else MFA16(o[n][a], fr[n], pf[(p) ^ 1][a]); } while (0)
#define MY(p, n, h) do { constexpr int a_ = ((n) >> 1) & 1, t_ = 2 * ((n) & 1) + (h);                                                      \
                         if (t_) MFA16(S[(p) ^ 1][(((n) >> 1) & 2) + a_], fr[((n) & 12) + t_], qf[2 * t_ + a_]);                           \
                         else MFA16C(S[(p) ^ 1][(((n) >> 1) & 2) + a_], fr[(n) & 12], qf[a_], ng[a_]); } while (0)
#define GX(p, n, F1, F2) do { MX(p, n, 0); SB(); F1; SB(); MX(p, n, 1); SB(); F2; SB(); } while (0)
#define GY(p, n, F1, F2) do { MY(p, n, 0); SB(); F1; SB(); MY(p, n, 1); SB(); F2; SB(); } while (0)
#define ML(p, a) do { if (ZC) MFA16Y(ls[a], ones, pf[(p) ^ 1][a]); else MFA16(ls[a], ones, pf[(p) ^ 1][a]); } while (0)
#define GL(p, F1, F2) do { ML(p, 0); SB(); F1; SB(); ML(p, 1); SB(); F2; SB(); } while (0)
#define SV(p, i) S[p][(((i) >> 1) & 2) + ((i) >> 3)][(i) & 3]
#define E1(p, i) do { e[i] = EX(SV(p, i)); if (HOT) KEEP(e[i]); else PIN(e[i]); } while (0)
#define C1(p, j) do { u32x4 w_ = __builtin_bit_cast(u32x4, pf[p][(j) >> 2]); w_[(j) & 3] = cvtpk(e[2 * (j)], e[2 * (j) + 1]);               \
                      pf[p][(j) >> 2] = __builtin_bit_cast(bf16x8, w_); if (HOT) KEEP(w_[(j) & 3]); else PIN(pf[p][(j) >> 2]); } while (0)
#define DQ(kb, g) do { if (ISB_ && (kb) == 3) { if ((g) == 0) KCHK(); BRQ(t, g); } if (ISB_ && (kb) == 0 && (g) == 1) PFW1(t); } while (0)
#define W4() do { SB(); __builtin_amdgcn_s_waitcnt(0xC37F); } while (0)
#define US(kb, ISB) do { constexpr bool HOT = true, ZC = (ISB) == 3, ISB_ = (ISB) == 1; constexpr int p_ = (kb) & 1; constexpr bool B_ = (kb) == 2;                    \
            GX(p_, 0, RD(kb, 7); if ((kb) == 3) { ak ^= 32768u; PIN(ak); }, E1(p_, 0); DQ(kb, 0));                                        \
            GX(p_, 1, RD(kb, 8); if ((kb) == 0) { av ^= 32768u; PIN(av); }, E1(p_, 1); DQ(kb, 1));                                        \
            GL(p_, E1(p_, 2), E1(p_, 3));                                                                                                 \
            GX(p_, 2, RD(kb, 9); C1(p_, 0), E1(p_, 4); DQ(kb, 2));                                                                        \
            GX(p_, 3, RD(kb, 10); W4(), E1(p_, 5); DQ(kb, 3));                                                                            \
            GX(p_, 4, RD(kb, 11); C1(p_, 1), E1(p_, 6); DQ(kb, 4));                                                                       \
            GX(p_, 5, RD(kb, 12); C1(p_, 2), E1(p_, 7); DQ(kb, 5));                                                                       \
            GX(p_, 6, RD(kb, 13); C1(p_, 3), E1(p_, 8); DQ(kb, 6));                                                                       \
            GX(p_, 7, RD(kb, 14); W4(), E1(p_, 9); DQ(kb, 7));                                                                            \
            GY(p_, 8, RD(kb, 15), E1(p_, 10); DQ(kb, 8));                                           \
            GY(p_, 9, if (!B_) RD(kb, 16); C1(p_, 4), E1(p_, 11); DQ(kb, 9));                                                             \
            GY(p_, 10, if (B_) { TS0(); if (ISB_) { __builtin_amdgcn_s_waitcnt(0x0071); asm volatile("s_barrier" ::: "memory"); } else TBAR(); \
                                 TS1(); SB(); RD(kb, 16); }                                                                               \
                       RD(kb, 17); C1(p_, 5), E1(p_, 12); DQ(kb, 10));                                                                    \
            GY(p_, 11, RD(kb, 18); if (!B_) W4(), E1(p_, 13); DQ(kb, 11));                                                                \
            GY(p_, 12, RD(kb, 19), E1(p_, 14); DQ(kb, 12));                                                \
            GY(p_, 13, RD(kb, 20); C1(p_, 6), E1(p_, 15); DQ(kb, 13));                                                                    \
            GY(p_, 14, RD(kb, 21); C1(p_, 7), DQ(kb, 14));                                 \
            GY(p_, 15, RD(kb, 22), DQ(kb, 15); W4());                                                                                     \
        } while (0)
            asm volatile("" : "+s"(outp));
            asm volatile("s_waitcnt vmcnt(4) lgkmcnt(0)\n\ts_barrier" ::: "memory");
            AST(1);
#pragma unroll
            for (int s_ = 0; s_ < 8; ++s_) fr[s_] = lds_rd(ak + (unsigned)KOFF(s_));
            BRQ2C(-1, 12); BRQ2C(-1, 14);
            SB();
#define M0_(n, a) do { if ((n) & 3) MFA16(S[0][(((n) >> 1) & 2) + (a)], fr[n], qf[2 * ((n) & 3) + (a)]); else MFA16Z(S[0][(((n) >> 1) & 2) + (a)], fr[n], qf[a]); } while (0)
#define G0_(n) do { M0_(n, 0); SB(); if (Q_NST + (n) < 16) KVST((Q_NST + (n)) & 15); SB(); M0_(n, 1); SB(); RD(0, 8 + (n));               \
                    if (Q_NST + (n) < 16) KEEP(pk[(Q_NST + (n)) & 15]); SB(); } while (0)
            G0_(0); G0_(1); G0_(2); G0_(3); G0_(4); G0_(5); G0_(6); G0_(7);
            MF_DONE();
            { ONCE2(isb * min(jj, 1)) { PFW1(0); } }
            {
                PIN(S[0][0]); PIN(S[0][1]); PIN(S[0][2]); PIN(S[0][3]);
                if (__builtin_expect((qb | qw) == 0, 0)) {
#pragma unroll
                    for (int r = 0; r < 4; ++r) { S[0][2][r] = -__builtin_inff(); if (4 * (lane >> 4) + r > (lane & 15)) S[0][0][r] = S[0][3][r] = -__builtin_inff(); } }
#pragma unroll
                for (int a = 0; a < 2; ++a) {
                    float mx_;
#define MX3(d, a_, b_, c_) asm("v_max3_f32 %0, %1, %2, %3" : "=v"(d) : "v"(a_), "v"(b_), "v"(c_))
                    MX3(mx_, SV(0, 8 * a), SV(0, 8 * a + 1), SV(0, 8 * a + 2)); MX3(mx_, mx_, SV(0, 8 * a + 3), SV(0, 8 * a + 4)); MX3(mx_, mx_, SV(0, 8 * a + 5), SV(0, 8 * a + 6));
                    { float m7_ = SV(0, 8 * a + 7), n_ = -1e30f; MX3(mx_, mx_, m7_, n_); }
                    { auto r1 = __builtin_amdgcn_permlane16_swap(__float_as_uint(mx_), __float_as_uint(mx_), false, false); MX3(mx_, r1[0], r1[1], r1[1]);
                      auto r2 = __builtin_amdgcn_permlane32_swap(__float_as_uint(mx_), __float_as_uint(mx_), false, false); MX3(mx_, r2[0], r2[1], r2[1]); }
                    mx_ = -POFF - mx_;
                    ng[a] = f32x4{mx_, mx_, mx_, mx_}; PIN(ng[a]);
                }
#pragma unroll
                for (int i = 0; i < 16; ++i) { float v_ = SV(0, i) + ng[i >> 3][0]; PIN(v_); SV(0, i) = v_; }
                PIN(S[0][0]); PIN(S[0][1]); PIN(S[0][2]); PIN(S[0][3]);
            }
            SB();
            GY(0, 8, E1(0, 0), RD(0, 16); E1(0, 1)); GY(0, 9, E1(0, 2); C1(0, 0), RD(0, 17); E1(0, 3));
            GY(0, 10, E1(0, 4); C1(0, 1), RD(0, 18); E1(0, 5)); GY(0, 11, E1(0, 6); C1(0, 2), RD(0, 19); E1(0, 7));
            GY(0, 12, E1(0, 8); C1(0, 3), RD(0, 20); E1(0, 9)); GY(0, 13, E1(0, 10); C1(0, 4), RD(0, 21); E1(0, 11));
            GY(0, 14, E1(0, 12); C1(0, 5), RD(0, 22); E1(0, 13)); GY(0, 15, E1(0, 14); C1(0, 6), E1(0, 15); C1(0, 7));
#define SCOPY() do { MF_DONE(); _Pragma("unroll") for (int i = 0; i < 4; ++i) { S[1][i] = S[0][i]; PIN(S[1][i]); } } while (0)
            if (__builtin_expect((qb | qw) == 0, 0)) { SCOPY(); pf[0][0] = pf[0][1] = bf16x8{}; PIN(pf[0][0]); PIN(pf[0][1]); }
            W4(); asm volatile("s_nop 1"); SB();
            AST(2);
            const int nu = 4 * qb + qw - 2;
#define MFA16Y(c, a, b) asm volatile("v_mfma_f32_16x16x32_bf16 %0, %1, %2, 0" : "+v"(c) : "v"(a), "v"(b))
#pragma unroll
            for (int d = 0; d < 18; ++d) { if (d < 16) asm volatile("" : "=v"(o[d >> 1][d & 1])); else asm volatile("" : "=v"(ls[d & 1])); }
            if (__builtin_expect(nu < 0, 0)) { const bf16x8 z_ = bf16x8{}; asm volatile("s_nop 1" :: "v"(z_));
#pragma unroll
                for (int d = 0; d < 18; ++d) { if (d < 16) MFA16Y(o[d >> 1][d & 1], z_, z_); else MFA16Y(ls[d & 1], z_, z_); } }
            { int n1_ = nu; asm volatile("" : "+s"(n1_)); ONCE2(min(n1_ + 2, 2) >> 1) { US(1, 3); } }
            {
                const int nl = nu >> 2, nB = isb ? nl : 0;
#pragma unroll 1
                while (t < nB) { US(2, 1); US(3, 1); ++t; US(0, 1); US(1, 1); }
#pragma unroll 1
                while (t < nl) { US(2, 0); US(3, 0); ++t; US(0, 0); US(1, 0); }
            }
            AST(3);
            SST(1);
#define PCOPY() do { SCOPY(); pf[0][0] = pf[1][0]; pf[0][1] = pf[1][1]; PIN(pf[0][0]); PIN(pf[0][1]); SB(); asm volatile("s_nop 1"); } while (0)
            {
                const int nr = nu < 0 ? 0 : nu & 3, ne = nu < -1 ? 0 : nu + 1;
                if (__builtin_expect(nr >= 1, 1)) {
                    US(2, 2);
                    if (__builtin_expect(nr >= 2, 1)) {
                        if (isb) { KCHK(); asm volatile("s_setprio 0"); BRQ4(t, 0); BRQ4(t, 1); BRQ4(t, 2); BRQ4(t, 3); asm volatile("s_setprio 1"); }
                        US(3, 2);
                        if (__builtin_expect(nr == 3, 0)) US(0, 2);
                    }
                }
                int np = ne; asm volatile("" : "+s"(np));
                if (__builtin_expect(np & 1, 0)) PCOPY();
                KEEP(ng[0]); KEEP(ng[1]);
                t = qb;
            }
#define RDV(n) fr[n] = lds_rd(avf + (unsigned)((n) * 1024))
#define M1(i) do { e[i] = __builtin_amdgcn_fmed3f(e[i], 0.f, rmp[(i) & 3]); PIN(e[i]); } while (0)
#define E2(i) E1(1, i)
#define CST(i) asm volatile("global_store_dwordx4 %0, %1, %2 offset:%3 sc1\n\ts_nop 1" :: "v"(l16), "v"(pc[i]), "s"(cb + ((i) >> 2) * 262144), "i"((((i) >> 1) & 1) * 1024 + ((i) & 1) * 2048) : "memory")
#define NRM(ISB, i) do { float y_[8];                                              \
                _Pragma("unroll") for (int z_ = 0; z_ < 8; ++z_) { y_[z_] = o[((i) & 6) + (z_ >> 2)][(i) & 1][z_ & 3] * inv[(i) & 1]; PIN(y_[z_]); } \
                pc[i] = u32x4{cvtpk(y_[0], y_[1]), cvtpk(y_[2], y_[3]), cvtpk(y_[4], y_[5]), cvtpk(y_[6], y_[7])};                        \
                PIN(pc[i]); if (!(ISB)) CST(i); SB(); } while (0)
#define XLD(b_) do { _Pragma("unroll") for (int n = 0; n < 8; ++n) asm volatile("global_load_dwordx4 %0, %1, %2 offset:%3 sc1" : "=v"(fr[n]) : "v"(l16 + (n >> 2) * 4096u), "s"(b_), "i"((n & 3) * 1024) : "memory"); \
                asm volatile("s_waitcnt vmcnt(0)" : "+v"(fr[0]), "+v"(fr[1]), "+v"(fr[2]), "+v"(fr[3]), "+v"(fr[4]), "+v"(fr[5]), "+v"(fr[6]), "+v"(fr[7]) :: "memory"); } while (0)
#define MFB(a_, b_, c_) __builtin_amdgcn_mfma_f32_16x16x32_bf16(a_, b_, c_, 0, 0, 0)
#define EXACT() do { __builtin_amdgcn_s_waitcnt(0xC07F); MF_DONE();                                                                                                           \
                float mr_[2] = {-1e30f, -1e30f}; const int nu_ = 4 * qb + qw2;                                                            \
                ls[0] = ls[1] = f32x4{}; _Pragma("unroll") for (int d = 0; d < 16; ++d) o[d >> 1][d & 1] = f32x4{};                       \
                _Pragma("unroll 1") for (int u = 0; u <= nu_; ++u) {                                                                      \
                    const char* kb_ = kvg + (size_t)(u >> 2) * 65536 + (u & 3) * 8192;                                                    \
                    XLD(kb_);                                                                                                             \
                    _Pragma("unroll") for (int i = 0; i < 4; ++i) { f32x4 s_ = f32x4{};                                                   \
                        _Pragma("unroll") for (int d = 0; d < 4; ++d) s_ = MFB(fr[2 * d + (i >> 1)], qf[2 * d + (i & 1)], s_);            \
                        S[1][i] = s_; }                                                                                                   \
                    if (u == nu_) { _Pragma("unroll") for (int r = 0; r < 4; ++r) { S[1][2][r] = -__builtin_inff();                       \
                        if (4 * (ln2 >> 4) + r > (ln2 & 15)) S[1][0][r] = S[1][3][r] = -__builtin_inff(); } }                             \
                    XLD(kb_ + 32768);                                                                                                     \
                    _Pragma("unroll") for (int a = 0; a < 2; ++a) {                                                                       \
                        float mx_ = SV(1, 8 * a); _Pragma("unroll") for (int r = 1; r < 8; ++r) mx_ = fmaxf(mx_, SV(1, 8 * a + r));       \
                        mx_ = fmaxf(quad_max(mx_), mr_[a]); const float al_ = EX(mr_[a] - mx_); mr_[a] = mx_;                             \
                        u32x4 w_; _Pragma("unroll") for (int j = 0; j < 4; ++j) w_[j] = cvtpk(EX(SV(1, 8 * a + 2 * j) - mx_), EX(SV(1, 8 * a + 2 * j + 1) - mx_)); \
                        const bf16x8 p_ = __builtin_bit_cast(bf16x8, w_);                                                                 \
                        ls[a] = MFB(ones, p_, ls[a] * al_);                                                                               \
                        _Pragma("unroll") for (int d = 0; d < 8; ++d) o[d][a] = MFB(fr[d], p_, o[d][a] * al_); } }                        \
                pf[1][0] = pf[1][1] = bf16x8{}; _Pragma("unroll") for (int n = 4; n < 8; ++n) fr[n] = bf16x8{};     \
                PIN(pf[1][0]); PIN(pf[1][1]); MF_DONE(); } while (0)
#define TAIL(ISB) do { float rmp[4];                                                                                         \
                XST(0);                                                                                                                   \
                MX(1, 0, 0); SB();                                                                                                        \
                int ln2 = lane, qw2 = qw; asm volatile("" : "+v"(ln2), "+s"(qw2));   \
                fr[7] = lds_rd(av + (unsigned)((qw2 + 3) & 3) * 8192u + 7168u); SB();                                                     \
                MX(1, 0, 1); SB(); E2(0); SB();                                                                                           \
                GX(1, 1, E2(1), E2(2));                                                                                                   \
                GL(1, E2(3), E2(8));                                                                                                      \
                float fl_ = (float)((ln2 & 15) - 4 * (ln2 >> 4)); PIN(fl_);                                                               \
                unsigned avf = lb16 + (unsigned)(t & 1) * 32768u + (unsigned)qw2 * 8192u; PIN(avf); SB();                                 \
                GX(1, 2, E2(9), E2(10));                                                                             \
                GX(1, 3, E2(11), _Pragma("unroll") for (int r = 0; r < 4; ++r) { rmp[r] = (fl_ + (0.5f - r)) * 1.2089258196146292e24f; PIN(rmp[r]); }); \
                if (__builtin_expect(qw2 == 2, 0)) {                               \
                    if (ISB) TBAR(); else LGKM0_BAR(); }                                                                                  \
                SB();                                                                                                                     \
                RDV(0); RDV(1); RDV(2); RDV(3); SB();                                                                                     \
                GX(1, 4, E2(12), M1(0); M1(1));                                                                                           \
                GX(1, 5, RDV(4); E2(13), M1(2); M1(3));                                                                                   \
                GX(1, 6, RDV(5); E2(14), C1(1, 0); C1(1, 1));                                                                             \
                GX(1, 7, RDV(6); E2(15), C1(1, 4); C1(1, 5));                                                                             \
                RDV(7); M1(12); M1(13); M1(14); M1(15); e[4] = e[5] = e[6] = e[7] = 0.f; C1(1, 2); C1(1, 3); SB();                        \
                C1(1, 6); C1(1, 7); SB();                                                                                                 \
                XST(1);                                                                                                                   \
                if (__builtin_expect(qw2 < 2, 0)) { if (ISB) TBAR(); else LGKM0_BAR(); }                                                  \
                SB();                                                                                                                     \
                XST(2); XST(3);                                                                                                           \
                float inv[2];                                                                                                \
                int hd2 = head; asm volatile("" : "+s"(hd2)); const char* cb = ws + WS_CTX + (unsigned)(hd2 * 524288 + (qb * 8 + qw2 * 2) * 2048); SB(); \
                GL(0, , ); GX(0, 0, , ); GX(0, 1, , ); GX(0, 2, , );                                                                      \
                PIN(ls[0]); PIN(ls[1]);                                                                                                   \
                const bool c_ = __builtin_amdgcn_ballot_w64(!(ls[0][0] + ls[1][0] <= LTHR)) != 0; SB();                                   \
                GX(0, 3, , );                                                                                                             \
                if (__builtin_expect(c_, 0)) EXACT();                                                                                     \
                SB();                                                                                                                     \
                inv[0] = __builtin_amdgcn_rcpf(ls[0][0]); inv[1] = __builtin_amdgcn_rcpf(ls[1][0]); PIN(inv[0]); PIN(inv[1]); SB();       \
                GX(0, 4, , asm volatile("s_nop 0"));                                                                                      \
                NRM(ISB, 0); GX(0, 5, , ); NRM(ISB, 1); GX(0, 6, , ); NRM(ISB, 2); GX(0, 7, , ); NRM(ISB, 3);                             \
                XST(4);                                                                                                                   \
                NRM(ISB, 4); NRM(ISB, 5); NRM(ISB, 6); NRM(ISB, 7);                                                                       \
                if (ISB) { asm volatile("s_setprio 0"); asm volatile("" : "+s"(t)); KCHK(); if (t + 2 < NA) { BRQ4(t, 0); BRQ4(t, 1); BRQ4(t, 3); } BRQ4(t, 2); \
                           CST(0); CST(1); CST(2); CST(3); CST(4); CST(5); CST(6); CST(7); }                                              \
                XST(5);                                                                                                                   \
            } while (0)
#define SPIN() do { PIN(S[1][0]); PIN(S[1][1]); PIN(S[1][2]); PIN(S[1][3]); } while (0)
            { ONCE2(isb ^ 1) { SPIN(); TAIL(0); } }
            { ONCE2(isb) { SPIN(); TAIL(1); } }
            SST(2);
            AST(4);
        }
        if (wave >= 4) {
            int t = qbB + 1;
            const int wv = wave & 3;
            XST(6);
#define WOLD(h_) do { const unsigned ow_ = OWOFF(); const char* wp_ = (const char*)(wo + (size_t)(K64(2 * (h_)) * 64 + 16 * wv) * DM + n0);  \
                _Pragma("unroll") for (int i = 0; i < 8; ++i)                                                                             \
                    GLDS_S(ow_, wp_ + (size_t)((i >> 2) * 64 + 8 * ((i >> 1) & 1)) * (DM * 2) + (i & 1) * dn1,                             \
                           ldsbase + 65536 + (h_) * 32768 + (i >> 2) * 16384 + (2 * wv + ((i >> 1) & 1)) * 2048 + (i & 1) * 1024, 0); } while (0)
            {
                const u32x4 em = u32x4{~0u, ~0u, ~0u, ~0u};
                const char* eb = ws + WS_PART + (size_t)(tile * 8 + wv) * 24576 + kp * 6144;
                asm volatile("global_store_dwordx4 %0, %1, %2 sc1\n\tglobal_store_dwordx4 %0, %1, %2 offset:1024 sc1\n\tglobal_store_dwordx4 %0, %1, %3 sc1\n\t"
                             "global_store_dwordx4 %0, %1, %3 offset:1024 sc1\n\ts_waitcnt vmcnt(12) lgkmcnt(0)"
                             :: "v"(l16), "v"(em), "s"(eb), "s"(eb + 4 * 24576) : "memory"); }
            if (jj == 7) { WOLD(1); EALD(0); EALD(1); }
            asm volatile("s_barrier" ::: "memory");
#define BATCH() do { KCHK(); if (t + 2 < NA) { BRQ4(t, 0); BRQ4(t, 1); BRQ4(t, 3); } BRQ4(t, 2); } while (0)
#define BSTEP(N) do { asm volatile("s_waitcnt vmcnt(%0)\n\ts_barrier" :: "i"(N) : "memory"); ++t; } while (0)
            u32x4 fw[16], wd[16], xq[4], gp[8];
            f32x4 fa[8];
            int ln_ = lane; asm volatile("" : "+v"(ln_));
#define FMM(w_, i0, a_, z_) do { asm volatile("s_setprio 1");                                                              \
                    _Pragma("unroll") for (int i = 0; i < 8; ++i) PIN(w_[(i0) + i]);                                                      \
                    PIN(a_);                                                                                                              \
                    _Pragma("unroll") for (int j = 0; j < 8; ++j) { u32x4 f_;                                                             \
                        _Pragma("unroll") for (int d = 0; d < 4; ++d) f_[d] = __builtin_amdgcn_perm(w_[(i0) + 2 * d + 1][j >> 1], w_[(i0) + 2 * d][j >> 1], (j & 1) ? 0x07060302u : 0x05040100u); \
                        fa[j] = __builtin_amdgcn_mfma_f32_16x16x32_bf16(__builtin_bit_cast(bf16x8, a_), __builtin_bit_cast(bf16x8, f_), (z_) ? f32x4{} : fa[j], 0, 0, 0); } \
                    _Pragma("unroll") for (int j = 0; j < 8; ++j) PIN(fa[j]);                                                             \
                    asm volatile("s_setprio 0"); } while (0)
#define FOUT() f32x4{ln_ >= 32 ? fa[4][0] : fa[0][0], ln_ >= 32 ? fa[5][0] : fa[1][0], ln_ >= 32 ? fa[6][0] : fa[2][0], ln_ >= 32 ? fa[7][0] : fa[3][0]}
            const int c1 = 2 * (i1 - 14 * k1) + (wv & 1);
            const unsigned r4o = (unsigned)(ln_ & 15) * 16u;
            const char* cxp = ws + WS_FLAG + F_CTX + ((qbB * 4 + qw) * 32 + head) * CFS;
            if (nf1 == 0) {
                const unsigned evo = (unsigned)((16 * (ln_ >> 5) + 4 * ((ln_ >> 4) & 1)) * FF * 2) + r4o;
                const u16* fwp = (wv < 2 ? w_gate : w_up) + (size_t)(k1 * 128) * FF + c1 * 128;
#define FLD(h_, c_) do { _Pragma("unroll") for (int i = 0; i < 8; ++i)                                                                    \
                    asm volatile("global_load_dwordx4 %0, %1, %2" : c_(fw[((h_) & 1) * 8 + i]) : "v"(evo), "s"(fwp + (size_t)(32 * (h_) + 8 * (i >> 2) + (i & 3)) * FF) : "memory"); } while (0)
                BATCH();
                { const int xr_ = (ln_ & 4) ? ri2 : ri1;
#pragma unroll
                  for (int i = 0; i < 4; ++i)
                      asm volatile("global_load_dwordx4 %0, %1, %2 offset:%3 sc1" : "=v"(xq[i]) : "v"((unsigned)((xr_ >> 4) * 2048 + (xr_ & 15) * 16 + (ln_ >> 4) * 256)),
                                   "s"(ws + WS_XF + (size_t)(k1 * 2 + (i >> 1)) * 262144), "i"((i & 1) * 1024) : "memory"); }
                FLD(0, "=v"); FLD(1, "=v"); BSTEP(20);
                BATCH(); asm volatile("s_waitcnt vmcnt(16)" ::: "memory");
                FLAG_PUT(cxp, tag, 1);
                FMM(fw, 0, xq[0], 1); FLD(2, "=v"); FMM(fw, 8, xq[1], 0); FLD(3, "=v"); BSTEP(17);
                BATCH(); asm volatile("s_waitcnt vmcnt(16)" ::: "memory");
                FMM(fw, 0, xq[2], 0); FMM(fw, 8, xq[3], 0); const f32x4 sum = FOUT();
                ST16_SC1((float*)(ws + WS_GP) + ((size_t)k1 * 4 + (wv >> 1) * 2 + ((ln_ >> 4) & 1)) * FF + c1 * 128 + (ln_ & 15) * 8 + (ln_ >> 5) * 4, sum);
                PFW1(7); BSTEP(2);
            }
            const char* pnd = nf1 ? cxp : ws + WS_FLAG + F_GU + (c1 * 16 + (wv >> 1) * 8 + k1) * 8;
            const int tp = nf1 ? t + 1 : t;
#define PUB(N) do { if (t == tp) { asm volatile("s_waitcnt vmcnt(%0)" :: "i"(N) : "memory"); FLAG_PUT(pnd, tag, 1); } } while (0)
            const int f0 = (nf2 ? 0 : i2 >> 3) * 256 + wv * 64;
            const char* gfp = ws + WS_FLAG + F_GU + (f0 >> 7) * 128 + (ln_ & 15) * 8;
            u64 gf = tag + 1;
            char* vs = lds + (NA & 1) * 32768;
#define GFLD() asm volatile("global_load_dwordx2 %0, %1, off sc1" : "+v"(gf) : "v"(gfp) : "memory")
#define WDLD(i, n_) do { const unsigned dvo = (unsigned)((ln_ >> 4) * 8 * DM * 2) + r4o;                                                  \
                    const char* db_ = (const char*)(w_down + (size_t)((nf2 ? 0 : i2 >> 3) * 256 + wv * 64) * DM + (i2 & 7) * 128) + 4096; \
                    asm volatile("s_cmp_lg_u32 %11, 0\n\ts_cbranch_scc1 1f\n\t"                                                          \
                                 "global_load_dwordx4 %0, %8, %9 offset:-4096\n\tglobal_load_dwordx4 %1, %8, %9 offset:-2048\n\tglobal_load_dwordx4 %2, %8, %9\n\t" \
                                 "global_load_dwordx4 %3, %8, %9 offset:2048\n\tglobal_load_dwordx4 %4, %8, %10 offset:-4096\n\tglobal_load_dwordx4 %5, %8, %10 offset:-2048\n\t" \
                                 "global_load_dwordx4 %6, %8, %10\n\tglobal_load_dwordx4 %7, %8, %10 offset:2048\n1:"  \
                                 : "=&v"(wd[i]), "=&v"(wd[i + 1]), "=&v"(wd[i + 2]), "=&v"(wd[i + 3]), "=&v"(wd[i + 4]), "=&v"(wd[i + 5]), "=&v"(wd[i + 6]), "=&v"(wd[i + 7]) \
                                 : "v"(dvo), "s"(db_ + (i) * 8192), "s"(db_ + (i) * 8192 + 8192), "s"(nf2) : "memory", "scc");            \
                    EALD((i) >> 3);                                                                                                       \
                    asm volatile("s_waitcnt vmcnt(%1)\n\ts_cmp_eq_u32 %0, 0\n\ts_cbranch_scc1 1f\n\ts_waitcnt vmcnt(%2)\n1:"                \
                                 :: "s"(nf2), "i"(12 + (n_)), "i"(4 + (n_)) : "memory", "scc");                                           \
                    BSTEP(63); } while (0)
#pragma unroll 1
            while (t < NA - 4) { BATCH(); PUB(17); BSTEP(0); }
            if (jj < 7) {
                BATCH(); PUB(16); WDLD(0, 0);
                BATCH(); PUB(16); GFLD(); WDLD(8, 0);
            }
            if (jj < 6) {
                BATCH();
                if (nf2 == 0) {
                    FLAG_WAIT(gf, gfp, tag, 63);
#pragma unroll
                    for (int i = 0; i < 8; ++i)
                        asm volatile("global_load_dwordx4 %0, %1, %2 sc1" : "=v"(gp[i]) : "v"((unsigned)((ln_ >> 4) * FF * 4) + r4o), "s"(ws + WS_GP + ((size_t)i * 4 * FF + f0) * 4) : "memory");
                    *(unsigned*)vs = 0;
                    asm volatile("s_waitcnt lgkmcnt(0)" ::: "memory");
                }
                WOLD(NA & 1);
                BSTEP(8);
            } else if (jj == 6) WOLD(0);
            asm volatile("global_load_dwordx2 %0, %1, %2 sc1" : "+v"(cxf) : "v"(CFO()), "s"(CFB) : "memory");
            WOLD((NA & 1) ^ 1);
            asm volatile("s_waitcnt vmcnt(16)" ::: "memory");
            if (jj == 7) FLAG_PUT(cxp, tag, 1);
            if (nf2 == 0) {
                f32x4 g4;
#pragma unroll
                for (int i = 0; i < 8; ++i) { PIN(gp[i]);
#pragma unroll
                    for (int j = 0; j < 4; ++j) { if (i) VADD(g4[j], g4[j], __uint_as_float(gp[i][j])); else g4[j] = __uint_as_float(gp[0][j]); } }
                float hv[2];
#pragma unroll
                for (int k = 0; k < 2; ++k) { auto rr = __builtin_amdgcn_permlane32_swap(__float_as_uint(g4[k]), __float_as_uint(g4[2 + k]), false, false);
                    const float gb = rbf(__uint_as_float(rr[0])), ub = rbf(__uint_as_float(rr[1]));
                    hv[k] = rbf(gb / (1.f + __expf(-gb))) * ub; PIN(hv[k]); }
                char* hl = vs + 8192 + wv * 256;
                *(unsigned*)(hl + ((ln_ >> 4) & 1) * 128 + (ln_ & 15) * 8 + (ln_ >> 5) * 4) = cvtpk(hv[0], hv[1]);
                asm volatile("" ::: "memory");
                xq[0] = *(const u32x4*)(hl + ((ln_ >> 2) & 1) * 128 + (ln_ >> 4) * 16); xq[1] = *(const u32x4*)(hl + ((ln_ >> 2) & 1) * 128 + 64 + (ln_ >> 4) * 16);
                FMM(wd, 0, xq[0], 1); FMM(wd, 8, xq[1], 0);
                *(f32x4*)(vs + 4096 + wv * 1024 + ln_ * 16) = FOUT();
                unsigned n_;
                asm volatile("s_waitcnt lgkmcnt(0)\n\ts_mov_b64 exec, 1\n\tds_add_rtn_u32 %0, %1, %2\n\ts_mov_b64 exec, -1\n\ts_waitcnt lgkmcnt(0)"
                             : "=&v"(n_) : "v"(ldsbase + (NA & 1) * 32768u), "v"(1u) : "memory");
                ypub = __builtin_amdgcn_readfirstlane(n_) ^ 3u;
                if (ypub == 0) {
                    const f32x4* yr = (const f32x4*)(vs + 4096) + ln_;
                    const f32x4 a = (yr[0] + yr[64]) + (yr[128] + yr[192]);
                    ST16_SC1((float*)(ws + WS_YP) + ((size_t)(((ln_ >> 4) & 1) * 8 + (i2 & 7)) * 16 + (i2 >> 3)) * 128 + (ln_ & 15) * 8 + (ln_ >> 5) * 4, a);
                }
            }
            AST(5);
        }
    }
    PH_STAMP(3);
    {
        constexpr int LDW = 1024, STG = 16384;
        int tid = threadIdx.x; asm volatile("" : "+v"(tid));
        const int lane = tid & 63, l15 = lane & 15, q4 = lane >> 4;
        const unsigned xvo = (unsigned)lane * 16u;
        if (!(kp & 1)) {
#pragma unroll
            for (int i = 0; i < 8; i += 2) { const u32x4 t_ = pc[i]; pc[i] = pc[i + 1]; pc[i + 1] = t_; PIN(pc[i]); PIN(pc[i + 1]); } }
        bf16x8 yr_[4][2][2];
#pragma unroll
        for (int i = 0; i < 8; ++i) yr_[i >> 2][i & 1][(i >> 1) & 1] = __builtin_bit_cast(bf16x8, pc[i]);
        unsigned wa[2][2];
        wa[0][0] = ldsbase + (unsigned)(65536 + ((kp >> 1) ^ 1) * 1024 + (q4 >> 1) * 2048 + (4 * (q4 & 1) + (l15 >> 2)) * 64 + (q4 & 1) * 16 + (lane & 2) * 16 + (lane & 1) * 8);
        wa[0][1] = wa[0][0] ^ 16u; wa[1][0] = wa[0][0] ^ 1024u; wa[1][1] = wa[0][1] ^ 1024u;
        PIN(wa[0][0]); PIN(wa[0][1]); PIN(wa[1][0]); PIN(wa[1][1]);
        const char* cg = ws + WS_CTX + (qb * 8 + qw * 2) * 2048;
        unsigned xok;
#define XLD8 "global_load_dwordx4 %0, %10, %12 sc1\n\tglobal_load_dwordx4 %1, %10, %13 sc1\n\tglobal_load_dwordx4 %2, %10, %12 offset:1024 sc1\n\tglobal_load_dwordx4 %3, %10, %13 offset:1024 sc1\n\t" \
             "global_load_dwordx4 %4, %11, %12 sc1\n\tglobal_load_dwordx4 %5, %11, %13 sc1\n\tglobal_load_dwordx4 %6, %11, %12 offset:1024 sc1\n\tglobal_load_dwordx4 %7, %11, %13 offset:1024 sc1\n\t"
#define XOUT(c_) c_(yr_[2][0][0]), c_(yr_[2][1][0]), c_(yr_[2][0][1]), c_(yr_[2][1][1]), c_(yr_[3][0][0]), c_(yr_[3][1][0]), c_(yr_[3][0][1]), c_(yr_[3][1][1])
#define XIN "v"(xvo), "v"(xvo + 262144u), "s"(cg + (size_t)K64(2) * 262144 + ((kp ^ 1) & 1) * 2048), "s"(cg + (size_t)K64(2) * 262144 + (kp & 1) * 2048), "s"(tag)
        SST(3);
        SB();
        asm volatile("s_waitcnt vmcnt(0)" ::: "memory");
        FLAG_PUTZ(ws + WS_FLAG + ((wave & 4) ? F_Y + ((i2 & 7) * 16 + ((i2 >> 3) & 15)) * 8 : F_CTX + ((qbA * 4 + wave) * 32 + head) * CFS), tag, (wave >> 2) * ypub);
        asm volatile("v_cmp_ne_u64 vcc, %14, %8\n\ts_and_b32 %9, vcc_lo, 14\n\ts_cbranch_scc1 1f\n\t"
                     XLD8 "\n1:\n\ts_barrier" : XOUT("=&v"), "+v"(cxf), "=&s"(xok) : XIN : "memory", "vcc", "scc");
        SST(4);
        f32x4 acc[2][8];
        bf16x4 wl[8], wh[8];
#define OFA(s, k_, j) (wa[(j) >> 2][(j) & 1] + (unsigned)(((s) & 3) * STG + (k_) * 8192 + (((j) >> 1) & 1) * 512))
#define ORL(i, s, k_, j) wl[i] = tr_rd(OFA(s, k_, j))
#define ORH(i, s, k_, j) wh[i] = tr_rd(OFA(s, k_, j) + 4096u)
        ORL(0, 0, 0, 0); ORH(0, 0, 0, 0); ORL(1, 0, 0, 1); ORH(1, 0, 0, 1); ORL(2, 0, 0, 2); ORH(2, 0, 0, 2); ORL(3, 0, 0, 3); ORH(3, 0, 0, 3); SB();
        unsigned wvo = OWOFF() + (unsigned)K64(4) * (64 * LDW * 2), xso = xvo + (unsigned)K64(4) * 262144u;
        unsigned cfo = CFO();
        PIN(wvo); PIN(xso); PIN(cfo);
        const char* cfb = CFB;
        const int r0 = qb * 128 + qw * 32 + (kp & 1) * 16, ch = kp >> 1;
        const unsigned nrt = min((unsigned)(ri1 - r0) >> 4, (unsigned)(ri2 - r0) >> 4);
        u64 yf = 0;
#define YFP (ws + WS_FLAG + F_Y + (ntc * 16 + (l15 < 14 ? l15 : 0)) * 8)
#define OMF(a, s, k_, j) do { if ((s) == 0 && (k_) == 0) MFA16Z(acc[a][j], w_, yr_[0][a][0]); else MFA16(acc[a][j], w_, yr_[(s) & 3][a][k_]); } while (0)
#define ORQ(h) do { constexpr int x_ = o_x(h), w_ = o_w(h);                                                                               \
            if (x_ >= 0) { asm volatile("global_load_dwordx4 %0, %1, %2 offset:%3 sc1" : "=v"(yr_[(x_ >> 2) & 3][x_ & 1][(x_ >> 1) & 1])   \
                                        : "v"(xso), "s"(cg + ((x_ ^ kp ^ 1) & 1) * 2048), "i"(((x_ >> 1) & 1) * 1024) : "memory");         \
                           if ((x_ & 3) == 3) { xso ^= (unsigned)((x_ >> 2) ^ ((x_ >> 2) + 1)) * 262144u; PIN(xso); } }                    \
            if (o_w((h) + 1) >= 0 && !(o_w((h) + 1) & 1)) asm volatile("s_mov_b32 m0, %0" :: "s"(ldsbase + wave * 2048 + (((o_w((h) + 1) >> 1) & 7) ^ 4) * STG) : "memory"); \
            if (w_ >= 0) { asm volatile("global_load_lds_dwordx4 %0, %1 offset:%2" :: "v"(wvo), "s"((const char*)wob + (w_ & 1) * (dn1 - 1024)), "i"((w_ & 1) * 1024) : "memory"); \
                           if (w_ & 1) { wvo ^= (unsigned)((w_ >> 1) ^ ((w_ >> 1) + 1)) * (64 * LDW * 2); PIN(wvo); } } } while (0)
#define OPAIR(s, f, A) do { const bf16x8 w_ = CAT8(wl[(f) & 7], wh[(f) & 7]); constexpr int s_ = (s) + (((f) + 4) >> 4), m_ = ((f) + 4) & 15; \
            OMF(0, s, (f) >> 3, (f) & 7); SB(); ORL(m_ & 7, s_, m_ >> 3, m_ & 7); ORQ(32 * (s) + 2 * (f)); A; SB();                       \
            OMF(1, s, (f) >> 3, (f) & 7); SB(); ORH(m_ & 7, s_, m_ >> 3, m_ & 7); ORQ(32 * (s) + 2 * (f) + 1); SB(); if (((f) & 1)) { __builtin_amdgcn_s_waitcnt(0xC47F); SB(); } } while (0)
#define OTG(h_) do { wa[h_][0] ^= 65536u; wa[h_][1] ^= 65536u; PIN(wa[h_][0]); PIN(wa[h_][1]); } while (0)
#define OST(i)
#define OSTEP(s, POST) do {                                                                                                               \
        OPAIR(s, 0, ); OPAIR(s, 1, ); OPAIR(s, 2, ); OPAIR(s, 3, ); OPAIR(s, 4, ); OPAIR(s, 5, ); OPAIR(s, 6, ); OPAIR(s, 7, );           \
        OPAIR(s, 8, ); OPAIR(s, 9, if (((s) & 3) == 3) OTG(0)); OPAIR(s, 10, ); OPAIR(s, 11, );                                           \
        if ((s) >= 3) asm volatile("s_waitcnt vmcnt(%0)\n\ts_barrier" :: "i"(o_wait(s)) : "memory");                                      \
        SB(); POST; OST((s) + 1);                                                                                                         \
        OPAIR(s, 12, ); OPAIR(s, 13, if (((s) & 3) == 3) OTG(1)); OPAIR(s, 14, ); OPAIR(s, 15, );                                         \
    } while (0)
        PH_STAMP(4);
        SST(5);
        OST(0);
        SB();
        OSTEP(0, );
#define MSK "14"
#define MSKI
        asm volatile("s_cmp_eq_u32 %3, 0\n\ts_cbranch_scc1 1f\n\tglobal_load_dwordx2 %0, %1, %2 sc1\n1:" : "+v"(cxf) : "v"(cfo), "s"(cfb), "s"(xok) : "memory", "scc");
        OSTEP(1, );
        asm volatile("s_waitcnt vmcnt(8)\n\ts_cmp_eq_u32 %9, 0\n\ts_cbranch_scc1 3f\n\ts_mov_b32 %9, 0x40000\n\ts_waitcnt vmcnt(4)\n"
                     "1:\n\tv_cmp_ne_u64 vcc, %14, %8\n\ts_and_b32 vcc_lo, vcc_lo, " MSK "\n\ts_cbranch_scc0 2f\n\t"
                     "global_load_dwordx2 %8, %15, %16 sc1\n\ts_sleep 4\n\ts_waitcnt vmcnt(0)\n\ts_sub_u32 %9, %9, 1\n\ts_cbranch_scc0 1b\n2:\n\t" XLD8 "s_waitcnt vmcnt(4)\n3:"
                     : XOUT("+v"), "+v"(cxf), "+s"(xok) : XIN, "v"(cfo), "s"(cfb), "s"(wave) MSKI : "memory", "vcc", "scc");
        const unsigned inc = (unsigned)__builtin_amdgcn_ballot_w64(cxf != tag) & 0x1f0u;
        SB();
        OSTEP(2, if (__builtin_expect(inc != 0, 0)) asm volatile("global_load_dwordx2 %0, %1, %2 sc1" : "+v"(cxf) : "v"(cfo), "s"(cfb) : "memory"); SB());
        asm volatile("s_waitcnt vmcnt(8)" ::: "memory"); SB();
        OSTEP(3, );
        OSTEP(4, if (__builtin_expect(inc != 0, 0)) FLAG_WAITM(cxf, cfb + cfo, tag, 63, 0x1f0u); SB());
        OSTEP(5, ); OSTEP(6, );
        OSTEP(7, if (__builtin_expect(nrt == 0, 0)) asm volatile("global_load_dwordx2 %0, %1, off sc1" : "+v"(yf) : "v"(YFP) : "memory"); SB());
        OSTEP(8, ); OSTEP(9, ); OSTEP(10, );
        u32x4 yq[7], pv[6], zn[2]; u32x2 ea[4]; unsigned rq;
        if (__builtin_expect(nrt == 0, 0)) {
            FLAG_WAIT(yf, YFP, tag, 63);
            asm volatile("global_load_dwordx4 %0, %8, %10 sc1\n\tglobal_load_dwordx4 %1, %8, %10 offset:512 sc1\n\tglobal_load_dwordx4 %2, %8, %10 offset:1024 sc1\n\t"
                         "global_load_dwordx4 %3, %8, %10 offset:1536 sc1\n\tglobal_load_dwordx4 %4, %8, %10 offset:2048 sc1\n\tglobal_load_dwordx4 %5, %8, %10 offset:2560 sc1\n\t"
                         "global_load_dwordx4 %6, %8, %10 offset:3072 sc1\n\tglobal_load_dword %7, %9, %10 sc1"
                         : "=&v"(yq[0]), "=&v"(yq[1]), "=&v"(yq[2]), "=&v"(yq[3]), "=&v"(yq[4]), "=&v"(yq[5]), "=&v"(yq[6]), "=&v"(rq)
                         : "v"((unsigned)WS_YP + (unsigned)((((lane >> 5) * 8 + ntc) * 16 + (q4 & 1) * 7) * 128 + ch * 64 + 4 * l15) * 4u),
                           "v"((unsigned)WS_R + (unsigned)(lane >= 32 ? ri2 : ri1) * 4u), "s"(ws) : "memory");
        }
        SB();
        OSTEP(11, );
        char* pb = ws + WS_PART + (size_t)(tile * 8 + wave) * 24576;
        const char* tfb = ws + WS_FLAG + F_PARK + (tile * 8 + wave) * 256;
        const unsigned tfo = (unsigned)((lane & 3) ^ kp) * 64u;
        const unsigned ea0 = ldsbase + 131072 + (wave & 3) * 8192 + (wave >> 2) * 4096 + l15 * 128 + (((lane >> 5) * 2 ^ (lane & 7)) << 4) + (q4 & 1) * 8;
        typedef __attribute__((address_space(3))) u32x2 lds_u32x2;
#define OSND(a, j) do { constexpr int x_ = ((a) ^ 1) | (((j) >> 1) & 2 ^ 2);                                             \
            u32x4 p_ = u32x4{cvtpk(acc[a][j][0], acc[a][j][1]), cvtpk(acc[a][j][2], acc[a][j][3]), cvtpk(acc[a][(j) + 1][0], acc[a][(j) + 1][1]), cvtpk(acc[a][(j) + 1][2], acc[a][(j) + 1][3])}; \
            if (x_ == 1) { _Pragma("unroll") for (int k = 0; k < 4; ++k) p_[k] = min(p_[k], 0xfffffffeu); }     \
            asm volatile("global_store_dwordx4 %0, %1, %2 offset:%3 sc1\n\ts_nop 1" :: "v"(xvo), "v"(p_), "s"(pb + (kp ^ x_) * 6144 + (x_ - 1) * 2048), "i"(((j) & 2) * 512) : "memory"); } while (0)
#define OUF(i, u) do { ORL(i, ((u) >> 1) & 3, (u) & 1, 4 + (((u) >> 3) & 3)); ORH(i, ((u) >> 1) & 3, (u) & 1, 4 + (((u) >> 3) & 3)); } while (0)
#define OQ(q) do { constexpr int p_ = (q) + 4; const bf16x8 w_ = CAT8(wl[(q) & 7], wh[(q) & 7]);                                           \
            if ((q) == 16 || (q) == 24) { asm volatile("s_waitcnt vmcnt(%0)" :: "i"((q) == 16 ? 4 : 0) : "memory"); SB(); }               \
            OMF(0, 12 + (((q) >> 3) & 3), ((q) >> 2) & 1, (q) & 3); SB();                                                                 \
            if (p_ < 32) ORL(p_ & 7, (p_ >> 3) & 3, (p_ >> 2) & 1, p_ & 3); else OUF(p_ & 7, p_ - 32);                                    \
            ORQ(384 + 2 * (q)); SB();                                                                                                     \
            OMF(1, 12 + (((q) >> 3) & 3), ((q) >> 2) & 1, (q) & 3); SB();                                                                 \
            if (p_ < 32) ORH(p_ & 7, (p_ >> 3) & 3, (p_ >> 2) & 1, p_ & 3); else if ((q) >= 30) OUF(((q) - 26) & 7, (q) - 26);            \
            if ((q) >= 10 && (q) < 14) ea[((q) - 10) & 3] = *(lds_u32x2*)(uintptr_t)(ea0 ^ (unsigned)((((q) - 10) & 1) * 16 + (((q) - 10) & 2) * 32)); \
            if ((q) == 14 || (q) == 15) zn[(q) & 1] = __builtin_bit_cast(u32x4, lds_rd(ldsbase + 131072 + 2048 + (wave & 3) * 8192 + (wave >> 2) * 4096 + ((q) & 1) * 1024 + xvo)); \
            SB(); } while (0)
#define OU(u) do { const bf16x8 w_ = CAT8(wl[(u) & 7], wh[(u) & 7]);                                                                      \
            OMF((u) >> 5, 12 + (((u) >> 1) & 3), (u) & 1, 4 + (((u) >> 3) & 3)); SB();                                                    \
            if ((u) + 6 < 64) OUF(((u) + 6) & 7, (u) + 6);                                                                                \
            if ((u) == 1) OSND(0, 0); if ((u) == 3) OSND(1, 0); if ((u) == 5) OSND(0, 2); if ((u) == 7) OSND(1, 2);                       \
            if ((u) == 19) OSND(0, 4);                                                                                                    \
            if ((u) == 22) { asm volatile("s_waitcnt vmcnt(1)" ::: "memory"); FLAG_PUT(tfb + kp * 64, tag, 1); }     \
            if ((u) == 35) { OSND(0, 6); asm volatile("s_setprio 0"); S2T(1); }                                                                   \
            if ((u) == 52) asm volatile("global_load_dwordx2 %0, %1, %2 sc1" : "=v"(cxf) : "v"(tfo), "s"(tfb) : "memory");               \
            SB(); } while (0)
#define OQ8(q) do { OQ(q); OQ((q) + 1); OQ((q) + 2); OQ((q) + 3); OQ((q) + 4); OQ((q) + 5); OQ((q) + 6); OQ((q) + 7); } while (0)
#define OU8(q) do { OU(q); OU((q) + 1); OU((q) + 2); OU((q) + 3); OU((q) + 4); OU((q) + 5); OU((q) + 6); OU((q) + 7); } while (0)
        asm volatile("s_setprio 1");
        OQ8(0); OQ8(8); OQ8(16); OQ8(24); OST(13); OU8(0); OU8(8); OU8(16); OU8(24); OU8(32); OU8(40); OU8(48); OU8(56);
        MF_DONE();
        OST(14);
        PH_STAMP(5);
        SST(6); S2T(2);
        SB();
        if (__builtin_expect(nrt == 0, 0)) {
            f32x4 ys;
#pragma unroll
            for (int i = 0; i < 7; ++i) PIN(yq[i]);
            PIN(rq);
            const float sig = rbf(1.f / (1.f + __expf(-__uint_as_float(rq))));
#pragma unroll
            for (int j = 0; j < 4; ++j) {
                const float s_ = ((__uint_as_float(yq[0][j]) + __uint_as_float(yq[1][j])) + (__uint_as_float(yq[2][j]) + __uint_as_float(yq[3][j])))
                               + ((__uint_as_float(yq[4][j]) + __uint_as_float(yq[5][j])) + __uint_as_float(yq[6][j]));
                auto rr = __builtin_amdgcn_permlane16_swap(__float_as_uint(s_), __float_as_uint(s_), false, false);
                ys[j] = rbf(__uint_as_float(rr[0]) + __uint_as_float(rr[1])) * sig; }
            char* yl = lds + 65536 + wave * 256;
            *(u32x2*)(yl + (lane >> 5) * 128 + (l15 >> 2) * 32 + (l15 & 1) * 16 + (l15 & 2) * 4) = u32x2{cvtpk(ys[0], ys[1]), cvtpk(ys[2], ys[3])};
            asm volatile("" ::: "memory");
            const int m = r0 + l15;
#pragma unroll
            for (int i = 0; i < 2; ++i) { const u32x4 yn = *(const u32x4*)(yl + (m == ri1 ? 0 : 128) + i * 64 + q4 * 16);
#pragma unroll
                for (int k = 0; k < 4; ++k) { const unsigned z_ = cvtpk(HALF(zn[i][k], 0) + HALF(yn[k], 0), HALF(zn[i][k], 1) + HALF(yn[k], 1));
                    zn[i][k] = m == ri1 || m == ri2 ? z_ : zn[i][k]; } }
        }
        float gg[2][8], xx[2][8];
#pragma unroll
        for (int i = 0; i < 2; ++i)
#pragma unroll
            for (int j = 0; j < 8; ++j) { gg[i][j] = HALF(zn[i][j >> 1], j); xx[i][j] = HALF(ea[2 * i + (j >> 2)][(j >> 1) & 1], j); PIN(gg[i][j]); PIN(xx[i][j]); }
        unsigned ovo = (unsigned)((l15 & 7) * DM + 32 * (l15 >> 3) + 8 * q4) * 2u; PIN(ovo);
        const char* ob = (const char*)(outp + (size_t)r0 * DM + ntc * 128 + 64 * ch);
        asm volatile("" : "+s"(ob));
        PH_STAMP(6);
        { unsigned c_, m_;
          asm volatile("s_waitcnt vmcnt(0)\n\ts_mov_b32 %7, 0x40000\n"
                       "1:\n\tv_cmp_ne_u64 vcc, %11, %6\n\ts_and_b32 vcc_lo, vcc_lo, 12\n\ts_cbranch_scc0 2f\n\t"
                       "global_load_dwordx2 %6, %12, %13 sc1\n\ts_waitcnt vmcnt(0)\n\ts_sub_u32 %7, %7, 1\n\ts_cbranch_scc0 1b\n"
                       "2:\n\tglobal_load_dwordx4 %1, %9, %10 offset:-1024 sc1\n\tglobal_load_dwordx4 %2, %9, %10 offset:1024 sc1\n\t"
                       "global_load_dwordx4 %4, %9, %10 sc1\n\tglobal_load_dwordx4 %5, %9, %10 offset:2048 sc1\n\t"
                       "global_load_dwordx4 %0, %9, %10 offset:-3072 sc1\n\tglobal_load_dwordx4 %3, %9, %10 offset:-2048 sc1"
                       : "=&v"(pv[0]), "=&v"(pv[1]), "=&v"(pv[2]), "=&v"(pv[3]), "=&v"(pv[4]), "=&v"(pv[5]), "+v"(cxf), "=&s"(c_), "=&v"(m_)
                       : "v"(xvo), "s"(pb + kp * 6144 + 3072), "s"(tag), "v"(tfo), "s"(tfb) : "memory", "vcc", "scc"); }
        S2T(3);
#pragma unroll
        for (int i = 0; i < 16; i += 2) { float g_[2];
#pragma unroll
            for (int e_ = 0; e_ < 2; ++e_) { float a_ = gg[i >> 3][(i & 7) + e_] * -1.4426950408889634f; PIN(a_); a_ = 1.f + __builtin_amdgcn_exp2f(a_); PIN(a_);
                g_[e_] = gg[i >> 3][(i & 7) + e_] * __builtin_amdgcn_rcpf(a_); PIN(g_[e_]); }
            const unsigned p_ = cvtpk(g_[0], g_[1]); gg[i >> 3][i & 7] = HALF(p_, 0); gg[i >> 3][(i & 7) + 1] = HALF(p_, 1); PIN(gg[i >> 3][i & 7]); PIN(gg[i >> 3][(i & 7) + 1]); }
        S2T(4);
        asm volatile("s_waitcnt vmcnt(0)" : "+v"(pv[0]), "+v"(pv[1]), "+v"(pv[2]), "+v"(pv[3]), "+v"(pv[4]), "+v"(pv[5]) :: "memory");
        S2T(5);
#pragma unroll 1
        for (int n_ = 0; n_ < 0x40000; ++n_) {
            const unsigned mx_ = max(max(max(pv[0][0], pv[0][1]), max(pv[0][2], pv[0][3])), max(max(pv[3][0], pv[3][1]), max(pv[3][2], pv[3][3])));
            if (__builtin_amdgcn_ballot_w64(mx_ == ~0u) == 0) break;
            asm volatile("global_load_dwordx4 %0, %2, %3 sc1\n\tglobal_load_dwordx4 %1, %2, %3 offset:1024 sc1\n\ts_waitcnt vmcnt(0)"
                         : "=&v"(pv[0]), "=&v"(pv[3]) : "v"(xvo), "s"(pb + kp * 6144) : "memory");
        }
        PH_STAMP(7); S2T(6);
        u32x4 ow[2];
#pragma unroll
        for (int i = 0; i < 2; ++i) {
            u32x4& ov = ow[i];
#pragma unroll
            for (int k = 0; k < 4; ++k) { float sv[2];
#pragma unroll
                for (int e_ = 0; e_ < 2; ++e_) sv[e_] = fmaf((acc[1][4 + 2 * i + (k >> 1)][2 * (k & 1) + e_] + HALF(pv[3 * i][k], e_)) + (HALF(pv[3 * i + 1][k], e_) + HALF(pv[3 * i + 2][k], e_)),
                                                             gg[i][2 * k + e_], xx[i][2 * k + e_]);
                ov[k] = cvtpk(sv[0], sv[1]); }
#pragma unroll
            for (int k = 0; k < 2; ++k) { auto rr = __builtin_amdgcn_permlane16_swap(ov[k], ov[2 + k], false, false); ov[k] = rr[0]; ov[2 + k] = rr[1]; }
        }
#pragma unroll
        for (int i = 0; i < 2; ++i) {
            u32x4 ov;
#pragma unroll
            for (int k = 0; k < 4; ++k) ov[k] = i ? __builtin_amdgcn_update_dpp(ow[1][k], ow[0][k], 0x128, 0xf, 0x3, false) : __builtin_amdgcn_update_dpp(ow[0][k], ow[1][k], 0x128, 0xf, 0xc, false);
            asm volatile("global_store_dwordx4 %0, %1, %2 sc1\n\ts_nop 1" :: "v"(ovo), "v"(ov), "s"(ob + i * (8 * DM * 2)) : "memory");
        }
        SST(7); SST_OUT();
        PH_STAMP(8);
        PH_STAMP(9);
    }
}

extern "C" void kernel_launch(void* const* d_in, const int* in_sizes, int n_in, void* d_out, int out_size, void* d_ws, size_t ws_size,
                              hipStream_t stream) {
    const u16* x    = (const u16*)d_in[0];
    const u16* ckv  = (const u16*)d_in[1];
    const u16* wq   = (const u16*)d_in[2];
    const u16* wuk  = (const u16*)d_in[3];
    const u16* wuv  = (const u16*)d_in[4];
    const u16* wo   = (const u16*)d_in[5];
    const u16* wr   = (const u16*)d_in[7];
    const u16* wg   = (const u16*)d_in[8];
    const u16* wu   = (const u16*)d_in[9];
    const u16* wd   = (const u16*)d_in[10];
    (void)in_sizes; (void)n_in; (void)out_size; (void)ws_size;
    (void)hipFuncSetAttribute((const void*)k_all, hipFuncAttributeMaxDynamicSharedMemorySize, 163840);
    k_all<<<256, 512, 163840, stream>>>(x, ckv, wuk, wuv, wr, (char*)d_ws + ((size_t)WSOFF << 20), wq, wo, wg, wu, wd, (u16*)d_out);
}
```

```cpp
#include <hip/hip_runtime.h>
#include <hip/hip_bf16.h>
#include <stdint.h>

typedef unsigned short u16;
typedef unsigned long long u64;
typedef short    bf16x8 __attribute__((ext_vector_type(8)));
typedef short    bf16x4 __attribute__((ext_vector_type(4)));
typedef float    f32x4  __attribute__((ext_vector_type(4)));
typedef float    f32x2  __attribute__((ext_vector_type(2)));
typedef __bf16   bfx2   __attribute__((ext_vector_type(2)));
typedef unsigned u32x4  __attribute__((ext_vector_type(4)));
typedef unsigned u32x2  __attribute__((ext_vector_type(2)));

#define DI __device__ __forceinline__

constexpr int SQ = 2048, DM = 1024, NH = 32, HD = 128, DC = 512, FF = 3584;

#define WSOFF 0
constexpr size_t WS_R    = 4u << 20;
constexpr size_t WS_FLAG = (4u << 20) + 65536;
constexpr size_t WS_GP   = 6u << 20;
constexpr size_t WS_YP   = 7u << 20;
constexpr size_t WS_PART = 96u << 20;
constexpr size_t WS_XF   = 16u << 20;
#define HSTR 1310720
constexpr size_t WS_KV   = 24u << 20;
constexpr size_t WS_CTX  = 72u << 20;
constexpr int F_EPOCH = 0, F_TOP = 1024, F_LN = 4096, F_KV = 8192, F_GU = 12288  , F_Y = 20480  , F_PARK = 524288  ,
              F_CTX = 65536  ;

#define PH_STAMP(i)
#define SST(i)
#define S2T(i)
#define SST_OUT()
#define ZST(i)
#define ZST_OUT()
DI unsigned cvtpk(float lo, float hi) { f32x2 v = {lo, hi}; bfx2 b = __builtin_convertvector(v, bfx2); return __builtin_bit_cast(unsigned, b); }
DI float rbf(float x) { return __uint_as_float(cvtpk(x, 0.f) << 16); }
#define DPP_ADD(v, ctrl, rmask) v += __uint_as_float(__builtin_amdgcn_update_dpp(0u, __float_as_uint(v), ctrl, rmask, 0xf, false))
DI unsigned tkey(unsigned fbits, int i) { unsigned u = fbits >> 16; u ^= (u & 0x8000u) ? 0xffffu : 0x8000u; return (u << 16) | (unsigned)(2047 - i); }
DI void merge_top2(unsigned& a1, unsigned& a2, unsigned b1, unsigned b2) { const unsigned lo = min(a1, b1); a1 = max(a1, b1); a2 = max(lo, max(a2, b2)); }
#define DPP_TOP2(a1, a2, ctrl, rmask) merge_top2(a1, a2, __builtin_amdgcn_update_dpp(0u, a1, ctrl, rmask, 0xf, false), __builtin_amdgcn_update_dpp(0u, a2, ctrl, rmask, 0xf, false))

#define GLDS_S(voff, sbase, ldsoff, imm)                                                                        \
    asm volatile("s_mov_b32 m0, %2\n\ts_nop 0\n\tglobal_load_lds_dwordx4 %0, %1 offset:%3"                       \
                 :: "v"(voff), "s"(sbase), "s"(ldsoff), "i"(imm) : "memory")
#define GLDS_C(voff, sbase, ldsoff, imm)                                                                        \
    asm volatile("s_mov_b32 m0, %2\n\ts_nop 0\n\tglobal_load_lds_dwordx4 %0, %1 offset:%3 sc1"                   \
                 :: "v"(voff), "s"(sbase), "s"(ldsoff), "i"(imm) : "memory")
#define LGKM0_BAR()    asm volatile("s_waitcnt lgkmcnt(0)\n\ts_barrier" ::: "memory")
#define SB() __builtin_amdgcn_sched_barrier(0)
#define CAT_(a, b) a##b
#define CAT(a, b) CAT_(a, b)
#define PIN(x) asm volatile("" : "+v"(x))
#define OADV(p_, d_) do { p_ += (d_); asm volatile("" : "+s"(p_)); } while (0)
#define VADD(d, a, b)    asm volatile("v_add_f32 %0, %1, %2" : "=v"(d) : "v"(a), "v"(b))
#define KEEP(x) asm volatile("" :: "v"(x))
#define MFA16(c, a, b) asm volatile("v_mfma_f32_16x16x32_bf16 %0, %1, %2, %0" : "+v"(c) : "v"(a), "v"(b))
#define MFA16Z(c, a, b) asm volatile("v_mfma_f32_16x16x32_bf16 %0, %1, %2, 0" : "=&v"(c) : "v"(a), "v"(b))
#define MFA16C(c, a, b, c0) asm volatile("v_mfma_f32_16x16x32_bf16 %0, %1, %2, %3" : "=&v"(c) : "v"(a), "v"(b), "v"(c0))
#define MF_DONE() do { SB(); asm volatile("s_nop 7\n\ts_nop 7"); SB(); } while (0)
#define ST16_SC1(p, v) asm volatile("global_store_dwordx4 %0, %1, off sc1\n\ts_nop 1" :: "v"(p), "v"(v) : "memory")
#define ST4_SC1(p, v)  asm volatile("global_store_dword %0, %1, off sc1" :: "v"(p), "v"(v) : "memory")
#define FLAG_PUT(p_, val_, on_) do { u64 sv_, tv_; unsigned tz_; const u64 v_ = (val_); const unsigned m_ = __builtin_amdgcn_readfirstlane((on_) ? 1u : 0u); \
        asm volatile("s_cmp_eq_u32 %5, 0\n\ts_cbranch_scc1 1f\n\tv_mov_b64 %1, %3\n\tv_mov_b32 %2, 0\n\ts_mov_b64 %0, exec\n\ts_mov_b64 exec, 1\n\t" \
                     "global_store_dwordx2 %2, %1, %4 sc1\n\ts_mov_b64 exec, %0\n1:"                                                      \
                     : "=&s"(sv_), "=&v"(tv_), "=&v"(tz_) : "s"(v_), "s"(p_), "s"(m_) : "memory", "scc"); } while (0)
#define FLAG_PUTZ(p_, val_, nz_) do { u64 sv_, tv_; unsigned tz_; const u64 v_ = (val_);                                                   \
        asm volatile("s_cmp_lg_u32 %5, 0\n\ts_cbranch_scc1 1f\n\tv_mov_b64 %1, %3\n\tv_mov_b32 %2, 0\n\ts_mov_b64 %0, exec\n\ts_mov_b64 exec, 1\n\t" \
                     "global_store_dwordx2 %2, %1, %4 sc1\n\ts_mov_b64 exec, %0\n1:"                                                      \
                     : "=&s"(sv_), "=&v"(tv_), "=&v"(tz_) : "s"(v_), "s"(p_), "s"(nz_) : "memory", "scc"); } while (0)
#define FLAG_GET(r_, p_) asm volatile("global_load_dwordx2 %0, %1, off sc1" : "=v"(r_) : "v"(p_) : "memory")
#define FLAG_WAIT(r_, p_, tag_, N) do { unsigned c_;                                                                                      \
        asm volatile("s_waitcnt vmcnt(%4)\n\ts_mov_b32 %1, 0x40000\n"                                                                     \
                     "1:\n\tv_cmp_ne_u64 vcc, %3, %0\n\ts_cmp_eq_u64 vcc, 0\n\ts_cbranch_scc1 2f\n\t"                                      \
                     "s_sleep 2\n\tglobal_load_dwordx2 %0, %2, off sc1\n\ts_waitcnt vmcnt(0)\n\ts_sub_u32 %1, %1, 1\n\ts_cbranch_scc0 1b\n"  \
                     "2:" : "+v"(r_), "=&s"(c_) : "v"(p_), "s"(tag_), "i"(N) : "memory", "vcc", "scc"); } while (0)
#define FLAG_WAITM(r_, p_, tag_, N, m_) do { unsigned c_;                                                                                 \
        asm volatile("s_waitcnt vmcnt(%4)\n\ts_mov_b32 %1, 0x40000\n"                                                                     \
                     "1:\n\tv_cmp_ne_u64 vcc, %3, %0\n\ts_and_b32 vcc_lo, vcc_lo, %5\n\ts_cbranch_scc0 2f\n\t"                             \
                     "s_sleep 2\n\tglobal_load_dwordx2 %0, %2, off sc1\n\ts_waitcnt vmcnt(0)\n\ts_sub_u32 %1, %1, 1\n\ts_cbranch_scc0 1b\n"  \
                     "2:" : "+v"(r_), "=&s"(c_) : "v"(p_), "s"(tag_), "i"(N), "s"(m_) : "memory", "vcc", "scc"); } while (0)
typedef __attribute__((address_space(3))) bf16x4 lds_bf16x4;
DI bf16x4 tr_rd(unsigned a) { return __builtin_amdgcn_ds_read_tr16_b64_v4i16((lds_bf16x4*)(uintptr_t)a); }
typedef __attribute__((address_space(3))) bf16x8 lds_bf16x8;
DI bf16x8 lds_rd(unsigned a) { return *(lds_bf16x8*)(uintptr_t)a; }
#define CAT8(lo_, hi_) bf16x8{lo_[0], lo_[1], lo_[2], lo_[3], hi_[0], hi_[1], hi_[2], hi_[3]}

#define POFF 32.f
#define LTHR 8192.f
DI float quad_max(float v) {
    auto r1 = __builtin_amdgcn_permlane16_swap(__float_as_uint(v), __float_as_uint(v), false, false);
    v = fmaxf(__uint_as_float(r1[0]), __uint_as_float(r1[1]));
    auto r2 = __builtin_amdgcn_permlane32_swap(__float_as_uint(v), __float_as_uint(v), false, false);
    return fmaxf(__uint_as_float(r2[0]), __uint_as_float(r2[1])); }
#define QWD 3
#define QS1 0x1dff
constexpr int q_xb(int s) { return s >= 1 && s <= 12; }
constexpr int q_xa(int s) { return s >= 0 && s <= 11; }
constexpr int q_s0(int s) { int n = 0; for (int i = 0; i < s; ++i) n += (QS1 >> i) & 1; return n; }
constexpr int q_kv(int kt, int f) { const int s = (kt - 13) * 16 + f - 12; return s >= 0 && s % 3 == 0 && s / 3 < 12 ? s / 3 : -1; }
constexpr int q_op(int s, int f) { return (f == 0 || f == 2 ? q_xb(s) : f == 8 || f == 10 ? q_xa(s) : f == 12 || f == 14 ? s + QWD < 16 : f == 3 ? s == 12
                                          : f == 4 ? (QS1 >> s) & 1 : 0) + (q_kv(s, f) >= 0); }
constexpr int q_after(int p, int kt) { int n = 0; for (int i = p + 1; i <= kt * 16 + 11; ++i) n += q_op(i >> 4, i & 15); return n; }
constexpr int q_min(int a, int b) { return a < b ? a : b; }
constexpr int q_wait(int kt) {
    int n = kt <= 2 ? 4 * (2 - kt) + q_after(-1, kt) : q_after((kt - 2) * 16 + 2, kt);
    if (kt + 1 >= QWD) n = q_min(n, q_after((kt + 1 - QWD) * 16 + 14, kt));
    if (kt == 13) n = q_min(n, q_after(12 * 16 + 3, kt));
    return n; }
constexpr int q_fl() { int s = 0; while (q_s0(s + 1) < 8) ++s; int kt = s; while (q_wait(kt) > q_after(s * 16 + 6, kt)) ++kt; return kt; }
constexpr int Q_NST = q_s0(16);
static_assert(Q_NST >= 8 && QS1 < 0x2000, "");
constexpr int o_xp(int st, int i) { return st <= 5 ? 64 + 4 * (4 * (st - 4) + i) : 32 * (st - 3) + (i == 0 ? 0 : i == 1 ? 4 : i == 2 ? 12 : 16); }
constexpr int o_wp(int st, int i) { return st <= 7 ? 16 * (st - 4) + 4 + 6 * i : 32 * (st - 5) + 24 + 4 * i; }
constexpr int o_x(int h) { for (int st = 4; st < 16; ++st) for (int i = 0; i < 4; ++i) if (o_xp(st, i) == h) return 4 * st + i; return -1; }
constexpr int o_w(int h) { for (int st = 4; st < 16; ++st) for (int i = 0; i < 2; ++i) if (o_wp(st, i) == h) return 2 * st + i; return -1; }
constexpr int o_after(int p, int e) { int n = 0; for (int h = p + 1; h < e; ++h) n += (o_x(h) >= 0) + (o_w(h) >= 0); return n; }
constexpr int o_wait(int kt) { return o_after(kt == 11 ? o_wp(15, 1) : o_xp(kt + 1, 3), 32 * kt + 24); }
constexpr int L_PFT = 149504, L_DUMP = 155648;
constexpr int CFS = 128;
constexpr unsigned F1S = 0xfff642ffu, F1F = 0xfff5310fu, F2S = 0xff6f204fu, F2F = 0xff5f13ffu;

__global__ __launch_bounds__(512, 2) void k_all(const u16* __restrict__ x, const u16* __restrict__ ckv, const u16* __restrict__ wuk, const u16* __restrict__ wuv,
                                                const u16* __restrict__ w_r, char* __restrict__ ws, const u16* __restrict__ wq, const u16* __restrict__ wo,
                                                const u16* __restrict__ w_gate, const u16* __restrict__ w_up,
                                                const u16* __restrict__ w_down, u16* __restrict__ out) {
    extern __shared__ __attribute__((aligned(16))) char lds[];
    const int b = blockIdx.x, xcd = b & 7, idx = b >> 3, head = xcd * 4 + (idx >> 3), jj = idx & 7;
    const int hp = head ^ 1;
    const unsigned ldsbase = (unsigned)(uintptr_t)(__attribute__((address_space(3))) char*)lds;
    const int wave = __builtin_amdgcn_readfirstlane(threadIdx.x >> 6);
    const int qbA = 15 - jj, qbB = jj;
#define KROW(g) (((2 * ((g) & 3) + ((g) >> 2)) * 8 + jj) * 32)
    const int qw = wave < 4 ? wave : 7 - wave, qb = wave < 4 ? qbA : qbB;
    bf16x8 qf[8];
    u64 tag;
    u16* outp = out;
    int ri1, ri2;
    PH_STAMP(0);
    SST(0);
    ZST(0);
    u32x4 pk[16];
    bf16x8 xr[4][2][2];
    u32x4 rv;
    const char* xg;
#define QXL1(j) asm volatile("global_load_dwordx4 %0, %1, %2 offset:%3 sc1" : "=v"(xr[(j) >> 2][(j) & 1][((j) >> 1) & 1]) : "v"(xvo), "s"(xg + (size_t)((j) >> 2) * 262144), \
                            "i"(((j) & 1) * 2048 + (((j) >> 1) & 1) * 1024) : "memory")
    const char* sb_[4];
    unsigned qa, qa2; bf16x4 ql[8], qh[8];
#define QFA(s, f) ((((f) & 1) ? qa2 : qa) + (unsigned)(((s) & 3) * 16384 + ((f) >> 3) * 8192 + (((f) >> 2) & 1) * 1024 + (((f) >> 1) & 1) * 512))
#define QRL(s, f) ql[(f) & 7] = tr_rd(QFA(s, f))
#define QRH(s, f) qh[(f) & 7] = tr_rd(QFA(s, f) + 2048u)
    unsigned xvo, qwo;
    const u16* wqb;
#define QWOFF() ({ unsigned l_ = threadIdx.x & 63; asm volatile("" : "+v"(l_)); (unsigned)(((l_ >> 2) & 7) * (NH * HD) + 32 * (l_ >> 5) + 8 * ((l_ & 3) ^ ((l_ >> 3) & 2))) * 2u; })
    {
        constexpr int LDW = NH * HD, K = DC;
        int tid = threadIdx.x; asm volatile("" : "+v"(tid));
        const int lane = tid & 63;
        const int wm = wave >> 2, wn = wave & 3;
        const int row = b * 8 + wave;
        u32x4 lx[2], lw[2]; u32x2 ep;
        asm volatile("global_load_dwordx4 %0, %5, %6\n\tglobal_load_dwordx4 %1, %5, %6 offset:1024\n\tglobal_load_dwordx4 %2, %5, %7\n\tglobal_load_dwordx4 %3, %5, %7 offset:1024\n\t"
                     "global_load_dwordx2 %4, %8, off sc1"
                     : "=&v"(lx[0]), "=&v"(lx[1]), "=&v"(lw[0]), "=&v"(lw[1]), "=&v"(ep) : "v"(lane * 16), "s"(x + (unsigned)(row * DM)), "s"(w_r), "v"(ws + WS_FLAG + F_EPOCH) : "memory");
#define XKEY(r) (((r) >> 1) & 7)
        unsigned xoff[4], woff[2];
        { const unsigned x0_ = (unsigned)(KROW(wave) + (lane >> 3)) * (K * 2) + (unsigned)((lane & 7) ^ (lane >> 4)) * 16u;
          xoff[0] = x0_; xoff[1] = (x0_ ^ 64u) + 7168u; xoff[2] = x0_ + 14336u; xoff[3] = xoff[1] + 14336u;
          woff[1] = (unsigned)((8 * wave + ((lane >> 2) & 7)) * LDW + hp * HD + 32 * (lane >> 5) + 8 * ((lane & 3) ^ (2 * (wave & 1)))) * 2u; woff[0] = woff[1] + 896u; }
        const char* xsp = (const char*)ckv;
        const char* wsp = (const char*)wuk - 896;
        const char* wsv = (const char*)wuv - 2944;
        const unsigned m0b = ldsbase + wave * 4096;
#define KM0(g, w_) asm volatile("s_add_u32 m0, %0, %1" :: "s"(m0b), "i"(((g) & 1) * 65536 + (w_) * 32768) : "memory", "scc")
#define KRX(i) asm volatile("global_load_lds_dwordx4 %0, %1 offset:%2" :: "v"(xoff[i]), "s"(xsp), "i"((i) * 1024) : "memory")
#define KRW(i) asm volatile("global_load_lds_dwordx4 %0, %1 offset:%2 nt" :: "v"(woff[(i) & 1]), "s"((i) < 2 ? wsp : wsv), "i"((i) * 1024) : "memory")
#define STAGE(g) do { KM0(g, 0); asm volatile("s_nop 0"); KRX(0); KRX(1); KRX(2); KRX(3); KM0(g, 1); asm volatile("s_nop 0"); KRW(0); KRW(1); KRW(2); KRW(3); \
                      OADV(xsp, 128); OADV(wsp, 64 * LDW * 2); OADV(wsv, 64 * LDW * 2); } while (0)
#define KST(i)
        const unsigned pfl = ldsbase + L_DUMP + wave * 1024;
#define PFX(g) do { unsigned l_ = threadIdx.x & 63; asm volatile("" : "+v"(l_)); l_ = ((l_ >> 3) * DC + (l_ & 7) * 8) * 2u; GLDS_S(l_, pfxp + (g) * 64, pfl, 0); } while (0)
#define PFQ(i) do { unsigned l_ = threadIdx.x & 63; asm volatile("" : "+v"(l_)); l_ = ((l_ >> 4) * LDW + (l_ & 15) * 8) * 2u; GLDS_S(l_, pfqp + (size_t)(4 * (i)) * LDW, pfl, 0); } while (0)
        STAGE(0);
        SB(); ZST(1);
        int w2 = wave; asm volatile("" : "+s"(w2));
        xg = ws + WS_XF + (unsigned)((w2 < 4 ? qbA * 4 + w2 : qbB * 4 + 7 - w2) * 4096);
        wqb = wq + (unsigned)(8 * w2 * LDW + head * HD);
        const u16* pfqp = wq + (unsigned)((128 * jj + 16 * w2) * LDW + head * HD);
        const u16* pfxp = ckv + (unsigned)((KROW(w2) + 8 * (head & 3)) * DC);
        unsigned xa[2][2], wa[2][2];
        const int l15 = lane & 15, q4 = lane >> 4;
#pragma unroll
        for (int s = 0; s < 2; ++s) {
            const int r0 = l15;
            xa[0][s] = ldsbase + (unsigned)((wm * 128 + r0) * 128 + ((((4 * s + q4) ^ XKEY(r0))) << 4));
            wa[0][s] = (ldsbase + 32768u + (unsigned)(q4 * 4096 + wn * 512 + (l15 >> 2) * 64 + (q4 & 1) * 32 + (lane & 3) * 8)) ^ (32u * s);
            xa[1][s] = xa[0][s] + 65536u; wa[1][s] = wa[0][s] + 65536u;
            PIN(xa[0][s]); PIN(xa[1][s]); PIN(wa[0][s]); PIN(wa[1][s]); }
        u64 pf0;
        { u64 pw_ = (u64)(((w2 & 2) ? wuv : wuk) + (unsigned)((128 + 192 * (w2 & 1) + jj) * LDW + hp * HD)), pc_ = (u64)ckv + (unsigned)(b * 8192 + (w2 & 3) * 2048);
          asm volatile("" : "+s"(pw_), "+s"(pc_));
          unsigned o0_ = (unsigned)(lane >> 1) * (8 * LDW * 2) + (lane & 1) * 128u, o1_ = (lane & 15) * 128u; PIN(o0_); PIN(o1_);
          pf0 = (lane < 48 ? pw_ : pc_) + (lane < 48 ? o0_ : o1_); PIN(pf0); }
        asm volatile("" : "+s"(pfqp), "+s"(pfxp), "+s"(xg), "+s"(wqb), "+s"(outp));
        SB();
        asm volatile("s_waitcnt vmcnt(8)" : "+v"(lx[0]), "+v"(lx[1]), "+v"(lw[0]), "+v"(lw[1]), "+v"(ep) :: "memory");
        KST(1); ZST(2);
#define HALF(v_, i) __uint_as_float(((i) & 1) ? (v_) & 0xffff0000u : (v_) << 16)
#define BOTH(v_) f32x2{HALF(v_, 0), HALF(v_, 1)}
        {
            f32x2 v[8];
#pragma unroll
            for (int i = 0; i < 8; ++i) v[i] = BOTH(lx[i >> 2][i & 3]);
            const float pv_ = __uint_as_float(__builtin_amdgcn_readfirstlane(__float_as_uint(v[0][0])));
            f32x2 s2 = f32x2{0.f, 0.f}, q2 = f32x2{0.f, 0.f};
#pragma unroll
            for (int i = 0; i < 8; ++i) { const f32x2 d = v[i] - pv_; s2 += d; q2 += d * d; }
            float s = s2[0] + s2[1], q = q2[0] + q2[1];
            DPP_ADD(s, 0x111, 0xf); DPP_ADD(q, 0x111, 0xf); DPP_ADD(s, 0x112, 0xf); DPP_ADD(q, 0x112, 0xf); DPP_ADD(s, 0x114, 0xf); DPP_ADD(q, 0x114, 0xf);
            DPP_ADD(s, 0x118, 0xf); DPP_ADD(q, 0x118, 0xf); DPP_ADD(s, 0x142, 0xa); DPP_ADD(q, 0x142, 0xa); DPP_ADD(s, 0x143, 0xc); DPP_ADD(q, 0x143, 0xc);
            const float dm = __uint_as_float(__builtin_amdgcn_readlane(__float_as_uint(s), 63)) * (1.f / DM);
            const float var_bf = rbf(fmaxf(fmaf(-dm, dm, __uint_as_float(__builtin_amdgcn_readlane(__float_as_uint(q), 63)) * (1.f / DM)), 0.f));
            const float mean_bf = rbf(pv_ + dm), den = rbf(__builtin_amdgcn_sqrtf(rbf(var_bf + 1e-5f))), rden = __builtin_amdgcn_rcpf(den);
            u32x4 xw[2]; float r2[2] = {0.f, 0.f};
#pragma unroll
            for (int i = 0; i < 8; ++i) {
                const f32x2 c_ = v[i] - mean_bf;
                const unsigned cp = cvtpk(c_[0], c_[1]);
                const f32x2 cb = BOTH(cp);
                f32x2 n_ = cb * rden;
                n_ += (cb - n_ * den) * rden;
                const unsigned np = cvtpk(n_[0], n_[1]);
                const unsigned wp = lw[i >> 2][i & 3];
                r2[i & 1] = __builtin_amdgcn_fdot2_f32_bf16(__builtin_bit_cast(bfx2, np), __builtin_bit_cast(bfx2, wp), r2[i & 1], false);
                xw[i >> 2][i & 3] = np;
            }
            char* lr = lds + 131072 + wave * 2064 + lane * 16;
            *(u32x4*)lr = xw[0]; *(u32x4*)(lr + 1024) = xw[1];
            s = r2[0] + r2[1];
            DPP_ADD(s, 0x111, 0xf); DPP_ADD(s, 0x112, 0xf); DPP_ADD(s, 0x114, 0xf); DPP_ADD(s, 0x118, 0xf); DPP_ADD(s, 0x142, 0xa); DPP_ADD(s, 0x143, 0xc);
            if (lane == 63) *(float*)(lds + 131072 + wave * 2064 + 2048) = rbf(s);
        }
        SB(); ZST(3);
        STAGE(1);
        asm volatile("s_bitcmp0_b32 %2, 2\n\ts_cbranch_scc1 1f\n\ts_mov_b32 m0, %1\n\ts_nop 0\n\tglobal_load_lds_dword %0, off\n1:" :: "v"(pf0), "s"(pfl), "s"(wave) : "memory", "scc");
        tag = (((u64)(unsigned)__builtin_amdgcn_readfirstlane(ep[1]) << 32) | (unsigned)__builtin_amdgcn_readfirstlane(ep[0])) + 0x9E3779B97F4A7C15ull;
        float rr;
        const unsigned l16 = (unsigned)lane * 16u;
        u32x2 xi_[4];
#define XI_RD() do { int t_ = threadIdx.x; asm volatile("" : "+v"(t_)); const int r8 = t_ & 7, pc = t_ >> 3;                               \
            const char* lr = lds + 131072 + r8 * 2064 + (pc >> 2) * 64 + ((pc >> 1) & 1) * 32 + (pc & 1) * 8;                              \
            xi_[0] = *(const u32x2*)lr; xi_[1] = *(const u32x2*)(lr + 16); xi_[2] = *(const u32x2*)(lr + 1024); xi_[3] = *(const u32x2*)(lr + 1040); \
            rr = *(const float*)(lds + 131072 + wave * 2064 + 2048); } while (0)
#define XI_ST() do { int t_ = threadIdx.x; asm volatile("" : "+v"(t_)); const int r8 = t_ & 7, pc = t_ >> 3;                               \
            char* dst = ws + WS_XF + (size_t)(pc >> 3) * 262144 + (b >> 1) * 2048 + (pc & 7) * 256 + ((b & 1) * 8 + r8) * 16;              \
            const u32x4 pa = u32x4{xi_[0][0], xi_[0][1], xi_[1][0], xi_[1][1]}, pb = u32x4{xi_[2][0], xi_[2][1], xi_[3][0], xi_[3][1]};    \
            ST16_SC1(dst, pa); ST16_SC1(dst + 2097152, pb); } while (0)
        SB(); ZST(4);
        asm volatile("s_waitcnt vmcnt(%1)\n\ts_bitcmp1_b32 %0, 2\n\ts_cbranch_scc1 1f\n\ts_waitcnt vmcnt(8)\n1:\n\ts_barrier" :: "s"(wave), "i"(9) : "memory", "scc");
        KST(2); ZST(6);
        f32x4 a16[8][4];
        bf16x8 xf[2][4];
        bf16x4 wl[4], wh[4];
#define KXR(kt, q, mb) xf[(q) & 1][mb] = lds_rd(xa[(kt) & 1][(q) >> 1] + (unsigned)((((q) & 1) * 4 + (mb)) * 2048))
#define KWL(kt, s32, nb) wl[nb] = tr_rd(wa[(kt) & 1][(nb) & 1] + (unsigned)((s32) * 16384 + ((nb) >> 1) * 2048))
#define KWH(kt, s32, nb) wh[nb] = tr_rd(wa[(kt) & 1][(nb) & 1] + (unsigned)((s32) * 16384 + ((nb) >> 1) * 2048 + 256))
#define KMF(kt, q, g) do { const bf16x8 w_ = CAT8(wl[(g) >> 2], wh[(g) >> 2]); constexpr int m_ = ((q) & 1) * 4 + ((g) & 3);               \
            if ((kt) == 0 && (q) < 2) { if ((g) >= 8) MFA16Z(a16[m_][(g) >> 2], xf[(q) & 1][(g) & 3], w_); else MFA16Z(a16[m_][(g) >> 2], w_, xf[(q) & 1][(g) & 3]); } \
            else                      { if ((g) >= 8) MFA16(a16[m_][(g) >> 2], xf[(q) & 1][(g) & 3], w_);  else MFA16(a16[m_][(g) >> 2], w_, xf[(q) & 1][(g) & 3]); } } while (0)
        u64 lnf;
#define LNFP (ws + WS_FLAG + F_LN + (wave * 32 + (lane & 31)) * 8)
#define KRQ(i) do { GLDS_S(qwo, wqb + (size_t)(((i) >> 1) * 64) * LDW, ldsbase + wave * 2048 + ((i) >> 1) * 16384 + ((i) & 1) * 896, ((i) & 1) * 128); } while (0)
        constexpr int KRS = 3, KPFD = 3;
#define KG(kt, q, g) do { constexpr int nk_ = (kt) + ((q) == 3), nq_ = ((q) + 1) & 3;                                                     \
            constexpr int xi = ((q) & 1) ? ((g) == 1 ? 0 : (g) == 2 ? 1 : (g) == 5 ? 2 : (g) == 6 ? 3 : -1) : (((g) & 1) && (g) < 8 ? (g) >> 1 : -1); \
            constexpr int ri = (q) == 3 ? ((g) == 9 ? 0 : (g) == 10 ? 1 : (g) == 13 ? 2 : (g) == 14 ? 3 : -1) : (((g) & 1) && (g) > 8 ? ((g) - 9) >> 1 : -1); \
            KMF(kt, q, g); SB();                                                                                                        \
            if (xi >= 0 && nk_ < 8) KXR(nk_, nq_, xi & 3);                                                                              \
            if (((q) & 1) && nk_ < 8) { if (((g) & 3) == 3) KWL(nk_, nq_ >> 1, (g) >> 2); if (((g) & 3) == 0 && (g) > 0) KWH(nk_, nq_ >> 1, (((g) >> 2) + 3) & 3); } \
            if (!((q) & 1) && (g) == 0 && (kt) + (q) > 0) KWH(kt, (q) >> 1, 3);                                                          \
            { constexpr int G = (q) == 3 ? (g) : (q) == 0 ? 16 + (g) : -9, T = (kt) + ((q) == 3 ? 2 : 1);          \
              if (T >= 2 && T < 8) { if (G == 0) KM0(T, 0); if (G == 4 * KRS) KM0(T, 1);                                                 \
                  if (G >= 1 && (G - 1) % KRS == 0 && (G - 1) / KRS < 8) { if ((G - 1) / KRS < 4) KRX(((G - 1) / KRS) & 3); else KRW(((G - 1) / KRS) & 3); } \
                  if (G == 29) OADV(xsp, 128); if (G == 30) OADV(wsp, 64 * LDW * 2); if (G == 31) OADV(wsv, 64 * LDW * 2); } }                                                 \
            if ((q) == 1 && (kt) + KPFD < 8) { if ((kt) >= 1 && (g) == 13) PFX((kt) + KPFD); }                \
            if ((q) == 2 && (kt) == 6 && (g) == 9) { qwo = QWOFF(); PIN(qwo); }                                                         \
            if ((q) == 1 && (kt) == 7) { if ((g) == 9) asm volatile("global_load_dwordx4 %0, %1, %2 sc1" : "=v"(rv) : "v"(xvo), "s"(ws + WS_R + wave * 1024) : "memory"); \
                                         if ((g) == 10) QXL1(0); if ((g) == 13) QXL1(1); if ((g) == 14) QXL1(2); }                      \
            if ((q) == 2 && (kt) == 7 && (g) == 2) QXL1(3);                                                                             \
            if ((q) == 2 && (kt) == 7 && (g) == 9) { int w_ = wave; asm volatile("" : "+s"(w_));                                        \
                sb_[0] = ws + WS_KV + (unsigned)(hp * HSTR + ((w_ >> 2) * 2 + (jj >> 2)) * 65536 + (jj & 3) * 8192 + (w_ & 3) * 2048); asm volatile("" : "+s"(sb_[0])); } \
            if ((q) == 2 && (kt) == 7 && (g) >= 11 && (g) < 14) { sb_[((g) - 10) & 3] = sb_[0] + ((g) - 10) * 262144; asm volatile("" : "+s"(sb_[((g) - 10) & 3])); } \
            if ((q) == 2 && (kt) == 7 && (g) == 14) { qa = ldsbase + (unsigned)((q4 >> 1) * 4096 + (4 * (q4 & 1) + (l15 >> 2)) * 64 + (q4 & 1) * 32 + (lane & 3) * 8); PIN(qa); } \
            if ((q) == 2 && (kt) == 7 && (g) == 15) { qa2 = qa ^ 32u; PIN(qa2); }                                                                              \
            if ((q) == 3 && (kt) == 6 && ri >= 0) KRQ(ri & 3);                                                                          \
            if ((q) == 0 && (kt) == 7 && ri >= 0 && 4 + ri < 2 * QWD) KRQ(4 + (ri & 3));                                                                    \
            if ((q) == 1 && (kt) == 1) { if ((g) == 8) XI_RD();                                                                       \
                if ((g) == 13) ST4_SC1((float*)(ws + WS_R) + row, rr);                               \
                if ((g) == 15) XI_ST(); }                                                                                               \
            if ((q) == 1 && ((kt) == 3 || (kt) == 4)) { if ((g) == 9) PFQ((2 * (kt) - 6) & 3); if ((g) == 11) PFQ((2 * (kt) - 5) & 3); }                                                    \
            if ((q) == 2 && (kt) == 5 && (g) == 12) FLAG_GET(lnf, LNFP);                                                                \
            SB(); } while (0)
#define KQ(kt, q) do { KG(kt, q, 0); KG(kt, q, 1); KG(kt, q, 2); KG(kt, q, 3); KG(kt, q, 4); KG(kt, q, 5); KG(kt, q, 6); KG(kt, q, 7);    \
                       KG(kt, q, 8); KG(kt, q, 9); KG(kt, q, 10); KG(kt, q, 11); KG(kt, q, 12); KG(kt, q, 13); KG(kt, q, 14); KG(kt, q, 15); } while (0)
#define KEXT(s) (((s) == 1 ? 3 : (s) == 5 ? 1 : 0) + ((s) >= 1 && (s) + KPFD < 8) + ((s) == 3 || (s) == 4 ? 2 : 0))
#define KSTEP(kt) do {                                                                                                                  \
        KQ(kt, 0); KQ(kt, 1); KQ(kt, 2);                                                                                                \
        if ((kt) == 6) FLAG_WAIT(lnf, LNFP, tag, 0);                                   \
                       \
        if ((kt) == 0) asm volatile("s_waitcnt vmcnt(%1) lgkmcnt(0)\n\ts_bitcmp1_b32 %0, 2\n\ts_cbranch_scc1 1f\n\ts_waitcnt vmcnt(%2)\n1:\n\ts_barrier" :: "s"(wave), "i"(KEXT(0) + 1), "i"(KEXT(0)) : "memory", "scc"); \
        else asm volatile("s_waitcnt vmcnt(%0) lgkmcnt(0)\n\ts_barrier" :: "i"(KEXT(kt)) : "memory");                                        \
        KST((kt) + 3);                                                                                                                  \
        if ((kt) == 2) FLAG_PUT(ws + WS_FLAG + F_LN + b * 8, tag, wave == 0);             \
        if ((kt) == 6) FLAG_PUT(ws + WS_FLAG + F_EPOCH, tag - 0x9E3779B97F4A7C15ull + 1, b == 0 && wave == 0);     \
        SB();                                                                                                                           \
        KQ(kt, 3);                                                                                                                      \
    } while (0)
        KXR(0, 0, 0); KXR(0, 0, 1); KWL(0, 0, 0); KWH(0, 0, 0); KXR(0, 0, 2); KXR(0, 0, 3);
        KWL(0, 0, 1); KWH(0, 0, 1); KWL(0, 0, 2); KWH(0, 0, 2); KWL(0, 0, 3); KWH(0, 0, 3);
        SB();
        KSTEP(0); ZST(7); KSTEP(1); KSTEP(2); KSTEP(3); KSTEP(4); KSTEP(5); KSTEP(6);
        xvo = l16;
        KQ(7, 0); KQ(7, 1); KQ(7, 2);
        if (wave < 4) {
            int t2_ = threadIdx.x; asm volatile("" : "+v"(t2_));
            unsigned l7_ = t2_ & 7, c7_ = jj >= 3 ? 7 : 8, b0_ = (unsigned)(((jj + 7) & 7) * 128 + head * 4 + (wave & 3)), b7_ = 3584u + head * 4 + (wave & 3);
            u64 pg_ = (u64)((wave & 1) ? w_up : w_gate), pd_ = (u64)w_down - 1792ull * 8192, po_ = (u64)wo - 2688ull * 8192;
            asm volatile("" : "+s"(c7_), "+s"(b0_), "+s"(b7_), "+s"(pg_), "+s"(pd_), "+s"(po_));
            const unsigned lt_ = l7_ < (unsigned)jj ? l7_ : 0;
            unsigned n_ = b0_ + (13u - lt_) * lt_ * 64u; n_ = l7_ == c7_ ? b7_ : n_;
            u64 p_ = n_ < 2688u ? pd_ : po_; p_ = n_ < 1792u ? pg_ : p_; n_ = n_ < 1792u ? n_ >> 1 : n_;
            *(u64*)(lds + L_PFT + t2_ * 8) = p_ + (u64)n_ * 8192u;
        }
        SB();
        asm volatile("s_waitcnt vmcnt(%0)\n\ts_barrier" :: "i"(2 * QWD + 1) : "memory");
        SB();
#define PKCV(i) do { const f32x4 a_ = ((i) & 2) ? a16[2 * ((i) >> 2)][(i) & 3] : a16[2 * ((i) >> 2) + ((i) & 1)][0];                       \
                     const f32x4 b_ = ((i) & 2) ? a16[2 * ((i) >> 2) + 1][(i) & 3] : a16[2 * ((i) >> 2) + ((i) & 1)][1];                   \
                     pk[i] = u32x4{cvtpk(a_[0], a_[1]), cvtpk(a_[2], a_[3]), cvtpk(b_[0], b_[1]), cvtpk(b_[2], b_[3])}; PIN(pk[i]); } while (0)
#define KL(i) do { KMF(7, 3, 2 * (i)); SB(); if ((i) >= 2) QXL1(2 * (i)); SB(); KMF(7, 3, 2 * (i) + 1); SB(); PKCV(i); if ((i) >= 2) QXL1(2 * (i) + 1); SB(); } while (0)
        KL(0); KL(1); KL(2); KL(3); KL(4); KL(5); KL(6); KL(7);
        KST(10);
        QRL(0, 0); QRH(0, 0); QRL(0, 1); QRH(0, 1); QRL(0, 2); QRH(0, 2); QRL(0, 3); QRH(0, 3);
        SB(); asm volatile("s_nop 3"); SB();
#pragma unroll
        for (int i = 8; i < 16; ++i) PKCV(i);
        SB();
        KST(11); ZST_OUT();
#undef PKCV
#undef KSTEP
#undef PFQ
#undef XKEY
    }
    PH_STAMP(1);
    const char* kvg = ws + WS_KV + (size_t)head * HSTR;
    const unsigned svv = xvo + 32768u;
    u64 pft; bf16x8 ones; unsigned ak, av;
    const int NA = qbA + 1;
    {
        constexpr int LDW = NH * HD, STG = 16384;
        int tid = threadIdx.x; asm volatile("" : "+v"(tid));
        const int lane = tid & 63;
        const unsigned woff = qwo, xvo2 = xvo + 262144u;
        f32x4 acc[2][8];
#define KVST(i) asm volatile("global_store_dwordx4 %0, %1, %2 offset:%3 sc1" :: "v"(((i) & 2) ? svv : xvo), "v"(pk[i]), "s"(sb_[(i) >> 2]), "i"(((i) & 1) * 1024) : "memory")
        u64 kvf = 0;
#define QKV(kt, f) do { constexpr int g_ = q_kv(kt, f), p_ = g_ >> 2; if (g_ >= 0) {                                                       \
            if ((g_ & 3) == 0) asm volatile("s_mov_b32 m0, %0\n\ts_nop 0" :: "s"(ldsbase + (p_ == 1 ? 0 : 65536 + p_ * 16384) + wave * 4096) : "memory"); \
            asm volatile("global_load_lds_dwordx4 %0, %1 offset:%2 sc1" :: "v"(xvo), "s"(kvg + p_ * 32768 + wave * 4096), "i"((g_ & 3) * 1024) : "memory"); } } while (0)
#define KVFP (ws + WS_FLAG + ((lane & 8) ? F_TOP : F_KV + 2048 + (head * 8 + (lane & 7)) * 8))
        const bool top2 = b == 56;
        unsigned* tw = (unsigned*)(lds + 131072 + 2048);
#define QMF(a, kt, f) do { if ((kt) == 0 && (f) < 8) MFA16Z(acc[a][(f) & 7], w_, xr[0][a][0]); else MFA16(acc[a][(f) & 7], w_, xr[(kt) & 3][a][(f) >> 3]); } while (0)
#define ZP0 238
#define QPAIR(kt, f, A, B) do { const bf16x8 w_ = CAT8(ql[(f) & 7], qh[(f) & 7]); constexpr int s_ = (kt) + (((f) + 4) >> 4), m_ = ((f) + 4) & 15;  \
            QMF(0, kt, f); SB(); if (s_ < 16) QRL(s_, m_); A; QKV(kt, f); SB();                                      \
            QMF(1, kt, f); SB(); if (s_ < 16) QRH(s_, m_); B;                                                                             \
            { constexpr int z_ = (kt) * 16 + (f) - ZP0;                    \
                                                                    \
              if (z_ == 8) { pft = *(const u64*)(lds + L_PFT + (tid & 255) * 8); }                                                       \
              if (z_ == 15) { av = ldsbase + xvo; ak = av + 65536u; PIN(av); PIN(ak); }                                                   \
              if (z_ == 16) { const unsigned o1 = (lane & 3) ? 0u : 0x3f803f80u; ones = __builtin_bit_cast(bf16x8, u32x4{o1, o1, o1, o1}); PIN(ones); }     \
              if (z_ == 14) PIN(pft); } SB(); if (((f) & 1) && s_ < 16) { __builtin_amdgcn_s_waitcnt(0xC47F); SB(); } } while (0)
        const char* xsp = xg + 3 * 262144;
#define QX(r, rb, ks, v_) asm volatile("global_load_dwordx4 %0, %1, %2 offset:%3 sc1" : "+v"(xr[r][rb][ks]) : "v"(v_), "s"(xsp), "i"((rb) * 2048 + (ks) * 1024) : "memory")
        const unsigned woff1 = woff + 1024u, woff2 = woff + 128u;
        const char* wsp = (const char*)(wqb + (size_t)QWD * 64 * LDW) - 1024;
        const unsigned m0b = ldsbase + wave * 2048;
#define QM0(r) asm volatile("s_add_u32 m0, %0, %1" :: "s"(m0b), "i"((r) * STG) : "memory", "scc")
#define QW1(v_, imm_) asm volatile("global_load_lds_dwordx4 %0, %1 offset:%2" :: "v"(v_), "s"(wsp), "i"(imm_) : "memory")
#define QST(i)
#define QSTEP(kt) do { constexpr int r_ = (kt) & 3;                                                                                     \
        QPAIR(kt, 0, if (q_xb(kt)) QX((r_ + 3) & 3, 0, 1, xvo), );                                                                      \
        QPAIR(kt, 1, , );                                                                                                               \
        QPAIR(kt, 2, if (q_xb(kt)) QX((r_ + 3) & 3, 1, 1, xvo), );                                                                      \
        QPAIR(kt, 3, if ((kt) == 12) FLAG_GET(kvf, KVFP), );                                                                            \
        if ((kt) == 0 && __builtin_expect(top2, 0)) {                                                                                                        \
            unsigned t1 = tkey(rv[0], tid * 4), t2 = 0;                                                                                 \
            _Pragma("unroll") for (int j = 1; j < 4; ++j) merge_top2(t1, t2, tkey(rv[j], tid * 4 + j), 0);                              \
            DPP_TOP2(t1, t2, 0x111, 0xf); DPP_TOP2(t1, t2, 0x112, 0xf); DPP_TOP2(t1, t2, 0x114, 0xf); DPP_TOP2(t1, t2, 0x118, 0xf);     \
            DPP_TOP2(t1, t2, 0x142, 0xa); DPP_TOP2(t1, t2, 0x143, 0xc);                                                                 \
            if (lane == 63) { tw[wave * 2] = t1; tw[wave * 2 + 1] = t2; } SB(); }                                                       \
        QPAIR(kt, 4, if ((QS1 >> (kt)) & 1) KVST(q_s0(kt) & 15), if ((QS1 >> (kt)) & 1) KEEP(pk[q_s0(kt) & 15]));                       \
        QPAIR(kt, 5, , );                                                                                                               \
        QPAIR(kt, 6, , );                                                                                                               \
        QPAIR(kt, 7, , );                                                                                                               \
        QPAIR(kt, 8, if (q_xa(kt)) QX(r_, 0, 0, xvo2), );                                                                               \
        QPAIR(kt, 9, , );                                                                                                               \
        QPAIR(kt, 10, if (q_xa(kt)) QX(r_, 1, 0, xvo2), if ((kt) <= 11) OADV(xsp, 262144));                                             \
        QPAIR(kt, 11, if ((kt) + QWD < 16) QM0((r_ + QWD) & 3), );                                                                      \
        if ((kt) < 15) {                              \
            if (QWD == 4 || (kt) == 0 || (kt) >= 13) asm volatile("s_waitcnt vmcnt(%0) lgkmcnt(0)\n\ts_barrier" :: "i"(q_wait(kt)) : "memory"); \
            else asm volatile("s_waitcnt vmcnt(%0)\n\ts_barrier" :: "i"(q_wait(kt)) : "memory");                                        \
            if ((kt) == 13) { PIN(kvf); const unsigned sh_ = (lane & 8) ? 22u : 0u; const u64 ex_ = (tag << sh_) >> sh_;     \
                if (__builtin_expect(__builtin_amdgcn_ballot_w64((kvf >> sh_) != ex_) != 0, 0)) { unsigned c_; u64 t_;                    \
                    asm volatile("s_mov_b32 %1, 0x40000\n1:\n\ts_sleep 2\n\tglobal_load_dwordx2 %0, %3, off sc1\n\ts_waitcnt vmcnt(0)\n\tv_lshrrev_b64 %2, %5, %0\n\t" \
                                 "v_cmp_ne_u64 vcc, %4, %2\n\ts_cmp_eq_u64 vcc, 0\n\ts_cbranch_scc1 2f\n\ts_sub_u32 %1, %1, 1\n\ts_cbranch_scc0 1b\n2:" \
                                 : "+v"(kvf), "=&s"(c_), "=&v"(t_) : "v"(KVFP), "v"(ex_), "v"(sh_) : "memory", "vcc", "scc"); }           \
                const unsigned r_ = __builtin_amdgcn_readlane((unsigned)kvf, 8); ri1 = r_ & 2047u; ri2 = (r_ >> 11) & 2047u; }           \
            QST((kt) + 1); SB(); }                                                                                                      \
        QPAIR(kt, 12, if ((kt) + QWD < 16) QW1(woff1, 0), );                                                                            \
        QPAIR(kt, 13, , );                                                                                                              \
        QPAIR(kt, 14, if ((kt) + QWD < 16) QW1(woff2, 1024), if ((kt) + QWD < 15) OADV(wsp, 64 * LDW * 2));                             \
        if ((kt) == q_fl()) FLAG_PUT(ws + WS_FLAG + F_KV + 2048 + (hp * 8 + jj) * 8, tag, wave == 0);     \
        QPAIR(kt, 15, , );                                                                                                              \
        if ((kt) == 0 && __builtin_expect(top2, 0)) {                                                                                                        \
            unsigned t1 = tw[0], t2 = tw[1];                                                                                            \
            _Pragma("unroll") for (int w = 1; w < 8; ++w) merge_top2(t1, t2, tw[w * 2], tw[w * 2 + 1]);                                 \
            FLAG_PUT(ws + WS_FLAG + F_TOP, (tag << 22) | (unsigned)__builtin_amdgcn_readfirstlane((~t1 & 2047u) | (~t2 & 2047u) << 11), wave == 0); \
            } \
        SB();                                                                                                                           \
    } while (0)
        asm volatile("s_waitcnt vmcnt(12)" : "+v"(rv) :: "memory");
        SB();
        QST(0);
        KST(12);
        QSTEP(0); QSTEP(1); QSTEP(2); QSTEP(3); QSTEP(4); QSTEP(5); QSTEP(6); QSTEP(7);
        QSTEP(8); QSTEP(9); QSTEP(10); QSTEP(11); QSTEP(12); QSTEP(13); QSTEP(14); QSTEP(15);
        MF_DONE();
        QST(16);
        {
            constexpr float qs = 0.08838834764831845f * 1.4426950408889634f;
#pragma unroll
            for (int i = 0; i < 8; ++i) { const f32x4 a_ = acc[i & 1][i & 6] * qs, b_ = acc[i & 1][i | 1] * qs;
                qf[i] = __builtin_bit_cast(bf16x8, u32x4{cvtpk(a_[0], a_[1]), cvtpk(a_[2], a_[3]), cvtpk(b_[0], b_[1]), cvtpk(b_[2], b_[3])}); PIN(qf[i]); }
        }
#undef QSTEP
    }
    PH_STAMP(2);
    const int kz = xcd & 1, kp = xcd >> 1, nt = kz * 4 + (idx >> 3), tile = jj * 8 + nt;
#define K64(s) ((head * 2) ^ (s))
#define OWOFF() ({ unsigned l_ = threadIdx.x & 63; asm volatile("" : "+v"(l_)); (unsigned)(((l_ >> 2) & 7) * DM + 32 * (l_ >> 5) + 8 * ((l_ & 3) ^ ((l_ >> 4) & 1))) * 2u; })
    const int n0 = (nt ^ 2) * 128, dn1 = (((nt ^ 3) - (nt ^ 2)) * 128 + 64) * 2, ntc = nt ^ 2 ^ (kp >> 1);
    const u16* wob = wo + (size_t)(8 * wave) * DM + n0;
    u64 cxf = tag + 1;
#define CFB (ws + WS_FLAG + F_CTX)
#define CFO() ({ unsigned l_ = threadIdx.x & 15, s0_ = (unsigned)((qb * 4 + qw) * 32 + head), s1_ = (unsigned)((qbB * 4 + 3 - (wave & 3)) * 32 + (head ^ 8)); \
                 asm volatile("" : "+v"(l_), "+s"(s0_), "+s"(s1_)); (l_ < 8 ? l_ ^ s0_ : s1_) * CFS; })
    u32x4 pc[8];
#define EAR_(v_, b_, pol_) "s_add_u32 m0, m0, 0x400\n\ts_nop 0\n\tglobal_load_lds_dwordx4 " v_ ", " b_ pol_ "\n\t"
#define EALD(h_) do { unsigned l_ = threadIdx.x & 63; asm volatile("" : "+v"(l_)); const unsigned lx_ = ((l_ >> 3) * DM + 8 * ((l_ & 7) ^ (l_ >> 3))) * 2u; \
        const int rb_ = ((h_) ? qbB * 8 + qw * 2 : qbA * 8 + (wave & 3) * 2) + (kp & 1);                                                  \
        asm volatile("s_mov_b32 m0, %5\n\t" EAR_("%0", "%3", "") EAR_("%1", "%3", "") EAR_("%2", "%4", " sc1") "global_load_lds_dwordx4 %2, %4 offset:1024 sc1" \
                     :: "v"(lx_), "v"(lx_ + 8 * DM * 2), "v"(l_ * 16u), "s"(x + (size_t)rb_ * 16 * DM + ntc * 128 + (kp >> 1) * 64),              \
                        "s"(ws + WS_XF + (size_t)(2 * ntc + (kp >> 1)) * 262144 + rb_ * 2048),                                             \
                        "s"(ldsbase + 131072 - 1024 + (wave & 3) * 8192 + (h_) * 4096) : "memory", "scc"); } while (0)
    const unsigned i1 = (((kz ? F1S : F1F) >> (4 * jj)) & 15u) * 16u + (kp * 4 + (idx >> 3));
    const unsigned i2 = (((kz ? F2S : F2F) >> (4 * jj)) & 15u) * 16u + (kp * 4 + (idx >> 3));
    const unsigned nf1 = i1 / 112u, nf2 = i2 / 112u;
    const unsigned k1 = i1 / 14u;
    unsigned ypub = 1;
    {
        int tid = threadIdx.x; asm volatile("" : "+v"(tid));
        const int lane = tid & 63;
        const unsigned l16 = (unsigned)lane * 16u;
#define EX(x) __builtin_amdgcn_exp2f(x)
#define TBAR() do { SB(); __builtin_amdgcn_s_waitcnt(0x0070); asm volatile("s_barrier" ::: "memory"); } while (0)
#define BRQ(t, r) do { constexpr int g_ = (r) >> 2; const int tt_ = (t) + (g_ == 2 ? 1 : 2);                                               \
            const unsigned o_ = (unsigned)((wave & 3) * 4096 + (g_ == 1 || g_ == 2 ? 16384 : 0));                                          \
            if (((r) & 3) == 0) GLDS_C(l16, kvg + (size_t)tt_ * 65536 + (g_ >= 2 ? 32768 : 0) + o_, ldsbase + (g_ >= 2 ? 0 : 65536) + (tt_ & 1) * 32768 + o_, 0); \
            else asm volatile("global_load_lds_dwordx4 %0, %1 offset:%2 sc1" :: "v"(l16), "s"(kvg + (size_t)tt_ * 65536 + (g_ >= 2 ? 32768 : 0) + o_), "i"(((r) & 3) * 1024) : "memory"); } while (0)
#define BRQ4(t, g) do { BRQ(t, 4 * (g)); BRQ(t, 4 * (g) + 1); BRQ(t, 4 * (g) + 2); BRQ(t, 4 * (g) + 3); } while (0)
#define BRQ2C(t, r) do { constexpr int g_ = (r) >> 2; const int tt_ = (t) + (g_ == 2 ? 1 : 2);                                             \
            const unsigned o_ = (unsigned)((wave & 3) * 4096 + (g_ == 1 || g_ == 2 ? 16384 : 0));                                          \
            asm volatile("s_cmp_eq_u32 %5, 0\n\ts_cbranch_scc1 1f\n\ts_mov_b32 m0, %2\n\ts_nop 0\n\tglobal_load_lds_dwordx4 %0, %1 offset:%3 sc1\n\t"  \
                         "global_load_lds_dwordx4 %0, %1 offset:%4 sc1\n1:"                                                               \
                         :: "v"(l16), "s"(kvg + (size_t)tt_ * 65536 + (g_ >= 2 ? 32768 : 0) + o_), "s"(ldsbase + (g_ >= 2 ? 0 : 65536) + (tt_ & 1) * 32768 + o_), \
                            "i"(((r) & 3) * 1024), "i"(((r) & 3) * 1024 + 1024), "s"(isb) : "memory", "scc"); } while (0)
        const unsigned isb = wave >> 2;
        u64 kf2 = tag;
        const char* kfb = ws + WS_FLAG + F_KV + head * 64;
#define KCHK() do { PIN(kf2); if (__builtin_expect((__builtin_amdgcn_ballot_w64(kf2 != tag) | (u64)((33u >> t) & 1u)) != 0, 0)) { \
            unsigned c_, a_;                                                                                                              \
            FLAG_PUTZ(ws + WS_FLAG + F_KV + (hp * 8 + jj) * 8, tag, (unsigned)(wave ^ 4) | (unsigned)t);                                  \
            asm volatile("s_cmp_lt_u32 %6, 5\n\ts_cbranch_scc1 3f\n\tv_lshrrev_b32 %2, 1, %5\n\tv_and_b32 %2, 56, %2\n\ts_mov_b32 %1, 0x40000\n" \
                         "1:\n\tglobal_load_dwordx2 %0, %2, %3 sc1\n\ts_cmp_eq_u32 %6, 5\n\ts_cbranch_scc1 3f\n\ts_waitcnt vmcnt(0)\n\tv_cmp_ne_u64 vcc, %4, %0\n\t" \
                         "s_cmp_eq_u64 vcc, 0\n\ts_cbranch_scc1 3f\n\ts_sleep 2\n\ts_sub_u32 %1, %1, 1\n\ts_cbranch_scc0 1b\n"           \
                         "3:" : "+v"(kf2), "=&s"(c_), "=&v"(a_) : "s"(kfb), "s"(tag), "v"(l16), "s"(t) : "memory", "vcc", "scc"); } SB(); } while (0)
#define PFW1(i_) do { unsigned a_; const u64 p_ = ((u64)(unsigned)__builtin_amdgcn_readlane((int)(pft >> 32), i_) << 32) | (unsigned)__builtin_amdgcn_readlane((int)pft, i_); \
            asm volatile("v_lshlrev_b32 %0, 3, %2\n\ts_mov_b32 m0, %3\n\ts_nop 2\n\tglobal_load_lds_dword %0, %1"                        \
                         : "=&v"(a_) : "s"(p_), "v"(l16), "s"(ldsbase + 131072 + (wave & 3) * 8192) : "memory"); } while (0)
#define ONCE2(n_) int CAT(n_once, __LINE__) = (int)(n_); asm volatile("" : "+s"(CAT(n_once, __LINE__))); _Pragma("unroll 1") for (int z_ = 0; z_ < CAT(n_once, __LINE__); ++z_)
#define AST(i)
#define XST(i)
#define TS0()
#define TS1()
        AST(0);
        asm volatile("s_bitcmp0_b32 %0, 2\n\ts_cbranch_scc1 1f\n\ts_setprio 1\n1:" :: "s"(wave) : "scc");
        {
            f32x4 o[8][2], ls[2], ng[2], S[2][4];
            bf16x8 pf[2][2], fr[16];
            float e[16];
            constexpr bool ZC = false;
            constexpr bool HOT = false;
            const unsigned lb16 = ldsbase + l16;
            int t = 0;
#define KOFF(f) ((((f) & 3) * 2 + (((f) >> 2) & 1)) * 1024)
#define RD(kb, n) do { constexpr int k_ = ((kb) + ((n) >> 4)) & 3, n_ = (n) & 15;                                                          \
            fr[n_] = n_ < 8 ? lds_rd(av + (unsigned)(((k_ + 3) & 3) * 8192 + n_ * 1024)) : lds_rd(ak + (unsigned)(((k_ + 1) & 3) * 8192 + KOFF(n_ & 7))); } while (0)
#define MX(p, n, a) do { if (ZC) MFA16Y(o[n][a], fr[n], pf[(p) ^ 1][a]); else MFA16(o[n][a], fr[n], pf[(p) ^ 1][a]); } while (0)
#define MY(p, n, h) do { constexpr int a_ = ((n) >> 1) & 1, t_ = 2 * ((n) & 1) + (h);                                                      \
                         if (t_) MFA16(S[(p) ^ 1][(((n) >> 1) & 2) + a_], fr[((n) & 12) + t_], qf[2 * t_ + a_]);                           \
                         else MFA16C(S[(p) ^ 1][(((n) >> 1) & 2) + a_], fr[(n) & 12], qf[a_], ng[a_]); } while (0)
#define GX(p, n, F1, F2) do { MX(p, n, 0); SB(); F1; SB(); MX(p, n, 1); SB(); F2; SB(); } while (0)
#define GY(p, n, F1, F2) do { MY(p, n, 0); SB(); F1; SB(); MY(p, n, 1); SB(); F2; SB(); } while (0)
#define ML(p, a) do { if (ZC) MFA16Y(ls[a], ones, pf[(p) ^ 1][a]); else MFA16(ls[a], ones, pf[(p) ^ 1][a]); } while (0)
#define GL(p, F1, F2) do { ML(p, 0); SB(); F1; SB(); ML(p, 1); SB(); F2; SB(); } while (0)
#define SV(p, i) S[p][(((i) >> 1) & 2) + ((i) >> 3)][(i) & 3]
#define E1(p, i) do { e[i] = EX(SV(p, i)); if (HOT) KEEP(e[i]); else PIN(e[i]); } while (0)
#define C1(p, j) do { u32x4 w_ = __builtin_bit_cast(u32x4, pf[p][(j) >> 2]); w_[(j) & 3] = cvtpk(e[2 * (j)], e[2 * (j) + 1]);               \
                      pf[p][(j) >> 2] = __builtin_bit_cast(bf16x8, w_); if (HOT) KEEP(w_[(j) & 3]); else PIN(pf[p][(j) >> 2]); } while (0)
#define DQ(kb, g) do { if (ISB_ && (kb) == 3) { if ((g) == 0) KCHK(); BRQ(t, g); } if (ISB_ && (kb) == 0 && (g) == 1) PFW1(t); } while (0)
#define W4() do { SB(); __builtin_amdgcn_s_waitcnt(0xC37F); } while (0)
#define US(kb, ISB) do { constexpr bool HOT = true, ZC = (ISB) == 3, ISB_ = (ISB) == 1; constexpr int p_ = (kb) & 1; constexpr bool B_ = (kb) == 2;                    \
            GX(p_, 0, RD(kb, 7); if ((kb) == 3) { ak ^= 32768u; PIN(ak); }, E1(p_, 0); DQ(kb, 0));                                        \
            GX(p_, 1, RD(kb, 8); if ((kb) == 0) { av ^= 32768u; PIN(av); }, E1(p_, 1); DQ(kb, 1));                                        \
            GL(p_, E1(p_, 2), E1(p_, 3));                                                                                                 \
            GX(p_, 2, RD(kb, 9); C1(p_, 0), E1(p_, 4); DQ(kb, 2));                                                                        \
            GX(p_, 3, RD(kb, 10); W4(), E1(p_, 5); DQ(kb, 3));                                                                            \
            GX(p_, 4, RD(kb, 11); C1(p_, 1), E1(p_, 6); DQ(kb, 4));                                                                       \
            GX(p_, 5, RD(kb, 12); C1(p_, 2), E1(p_, 7); DQ(kb, 5));                                                                       \
            GX(p_, 6, RD(kb, 13); C1(p_, 3), E1(p_, 8); DQ(kb, 6));                                                                       \
            GX(p_, 7, RD(kb, 14); W4(), E1(p_, 9); DQ(kb, 7));                                                                            \
            GY(p_, 8, RD(kb, 15), E1(p_, 10); DQ(kb, 8));                                           \
            GY(p_, 9, if (!B_) RD(kb, 16); C1(p_, 4), E1(p_, 11); DQ(kb, 9));                                                             \
            GY(p_, 10, if (B_) { TS0(); if (ISB_) { __builtin_amdgcn_s_waitcnt(0x0071); asm volatile("s_barrier" ::: "memory"); } else TBAR(); \
                                 TS1(); SB(); RD(kb, 16); }                                                                               \
                       RD(kb, 17); C1(p_, 5), E1(p_, 12); DQ(kb, 10));                                                                    \
            GY(p_, 11, RD(kb, 18); if (!B_) W4(), E1(p_, 13); DQ(kb, 11));                                                                \
            GY(p_, 12, RD(kb, 19), E1(p_, 14); DQ(kb, 12));                                                \
            GY(p_, 13, RD(kb, 20); C1(p_, 6), E1(p_, 15); DQ(kb, 13));                                                                    \
            GY(p_, 14, RD(kb, 21); C1(p_, 7), DQ(kb, 14));                                 \
            GY(p_, 15, RD(kb, 22), DQ(kb, 15); W4());                                                                                     \
        } while (0)
            asm volatile("s_waitcnt vmcnt(4) lgkmcnt(0)\n\ts_barrier" ::: "memory");
            AST(1);
#pragma unroll
            for (int s_ = 0; s_ < 8; ++s_) fr[s_] = lds_rd(ak + (unsigned)KOFF(s_));
            BRQ2C(-1, 12); BRQ2C(-1, 14);
            SB();
#define M0_(n, a) do { if ((n) & 3) MFA16(S[0][(((n) >> 1) & 2) + (a)], fr[n], qf[2 * ((n) & 3) + (a)]); else MFA16Z(S[0][(((n) >> 1) & 2) + (a)], fr[n], qf[a]); } while (0)
#define G0_(n) do { M0_(n, 0); SB(); if (Q_NST + (n) < 16) KVST((Q_NST + (n)) & 15); SB(); M0_(n, 1); SB(); RD(0, 8 + (n));               \
                    if (Q_NST + (n) < 16) KEEP(pk[(Q_NST + (n)) & 15]); SB(); } while (0)
            G0_(0); G0_(1); G0_(2); G0_(3); G0_(4); G0_(5); G0_(6); G0_(7);
            MF_DONE();
            { ONCE2(isb * min(jj, 1)) { PFW1(0); } }
            {
                PIN(S[0][0]); PIN(S[0][1]); PIN(S[0][2]); PIN(S[0][3]);
                if (__builtin_expect((qb | qw) == 0, 0)) {
#pragma unroll
                    for (int r = 0; r < 4; ++r) { S[0][2][r] = -__builtin_inff(); if (4 * (lane >> 4) + r > (lane & 15)) S[0][0][r] = S[0][3][r] = -__builtin_inff(); } }
#pragma unroll
                for (int a = 0; a < 2; ++a) {
                    float mx_;
#define MX3(d, a_, b_, c_) asm("v_max3_f32 %0, %1, %2, %3" : "=v"(d) : "v"(a_), "v"(b_), "v"(c_))
                    MX3(mx_, SV(0, 8 * a), SV(0, 8 * a + 1), SV(0, 8 * a + 2)); MX3(mx_, mx_, SV(0, 8 * a + 3), SV(0, 8 * a + 4)); MX3(mx_, mx_, SV(0, 8 * a + 5), SV(0, 8 * a + 6));
                    { float m7_ = SV(0, 8 * a + 7), n_ = -1e30f; MX3(mx_, mx_, m7_, n_); }
                    { auto r1 = __builtin_amdgcn_permlane16_swap(__float_as_uint(mx_), __float_as_uint(mx_), false, false); MX3(mx_, r1[0], r1[1], r1[1]);
                      auto r2 = __builtin_amdgcn_permlane32_swap(__float_as_uint(mx_), __float_as_uint(mx_), false, false); MX3(mx_, r2[0], r2[1], r2[1]); }
                    mx_ = -POFF - mx_;
                    ng[a] = f32x4{mx_, mx_, mx_, mx_}; PIN(ng[a]);
                }
#pragma unroll
                for (int i = 0; i < 16; ++i) { float v_ = SV(0, i) + ng[i >> 3][0]; PIN(v_); SV(0, i) = v_; }
                PIN(S[0][0]); PIN(S[0][1]); PIN(S[0][2]); PIN(S[0][3]);
            }
            SB();
            GY(0, 8, E1(0, 0), RD(0, 16); E1(0, 1)); GY(0, 9, E1(0, 2); C1(0, 0), RD(0, 17); E1(0, 3));
            GY(0, 10, E1(0, 4); C1(0, 1), RD(0, 18); E1(0, 5)); GY(0, 11, E1(0, 6); C1(0, 2), RD(0, 19); E1(0, 7));
            GY(0, 12, E1(0, 8); C1(0, 3), RD(0, 20); E1(0, 9)); GY(0, 13, E1(0, 10); C1(0, 4), RD(0, 21); E1(0, 11));
            GY(0, 14, E1(0, 12); C1(0, 5), RD(0, 22); E1(0, 13)); GY(0, 15, E1(0, 14); C1(0, 6), E1(0, 15); C1(0, 7));
#define SCOPY() do { MF_DONE(); _Pragma("unroll") for (int i = 0; i < 4; ++i) { S[1][i] = S[0][i]; PIN(S[1][i]); } } while (0)
            if (__builtin_expect((qb | qw) == 0, 0)) { SCOPY(); pf[0][0] = pf[0][1] = bf16x8{}; PIN(pf[0][0]); PIN(pf[0][1]); }
            W4(); asm volatile("s_nop 1"); SB();
            AST(2);
            const int nu = 4 * qb + qw - 2;
#define MFA16Y(c, a, b) asm volatile("v_mfma_f32_16x16x32_bf16 %0, %1, %2, 0" : "+v"(c) : "v"(a), "v"(b))
#pragma unroll
            for (int d = 0; d < 18; ++d) { if (d < 16) asm volatile("" : "=v"(o[d >> 1][d & 1])); else asm volatile("" : "=v"(ls[d & 1])); }
            if (__builtin_expect(nu < 0, 0)) { const bf16x8 z_ = bf16x8{}; asm volatile("s_nop 1" :: "v"(z_));
#pragma unroll
                for (int d = 0; d < 18; ++d) { if (d < 16) MFA16Y(o[d >> 1][d & 1], z_, z_); else MFA16Y(ls[d & 1], z_, z_); } }
            { int n1_ = nu; asm volatile("" : "+s"(n1_)); ONCE2(min(n1_ + 2, 2) >> 1) { US(1, 3); } }
            {
                const int nl = nu >> 2, nB = isb ? nl : 0;
#pragma unroll 1
                while (t < nB) { US(2, 1); US(3, 1); ++t; US(0, 1); US(1, 1); }
#pragma unroll 1
                while (t < nl) { US(2, 0); US(3, 0); ++t; US(0, 0); US(1, 0); }
            }
            AST(3);
            SST(1);
#define PCOPY() do { SCOPY(); pf[0][0] = pf[1][0]; pf[0][1] = pf[1][1]; PIN(pf[0][0]); PIN(pf[0][1]); SB(); asm volatile("s_nop 1"); } while (0)
            {
                const int nr = nu < 0 ? 0 : nu & 3, ne = nu < -1 ? 0 : nu + 1;
                if (__builtin_expect(nr >= 1, 1)) {
                    US(2, 2);
                    if (__builtin_expect(nr >= 2, 1)) {
                        if (isb) { KCHK(); asm volatile("s_setprio 0"); BRQ4(t, 0); BRQ4(t, 1); BRQ4(t, 2); BRQ4(t, 3); asm volatile("s_setprio 1"); }
                        US(3, 2);
                        if (__builtin_expect(nr == 3, 0)) US(0, 2);
                    }
                }
                int np = ne; asm volatile("" : "+s"(np));
                if (__builtin_expect(np & 1, 0)) PCOPY();
                KEEP(ng[0]); KEEP(ng[1]);
                t = qb;
            }
#define RDV(n) fr[n] = lds_rd(avf + (unsigned)((n) * 1024))
#define M1(i) do { e[i] = __builtin_amdgcn_fmed3f(e[i], 0.f, rmp[(i) & 3]); PIN(e[i]); } while (0)
#define E2(i) E1(1, i)
#define CST(i) asm volatile("global_store_dwordx4 %0, %1, %2 offset:%3 sc1\n\ts_nop 1" :: "v"(l16), "v"(pc[i]), "s"(cb + ((i) >> 2) * 262144), "i"((((i) >> 1) & 1) * 1024 + ((i) & 1) * 2048) : "memory")
#define NRM(ISB, i) do { float y_[8];                                              \
                _Pragma("unroll") for (int z_ = 0; z_ < 8; ++z_) { y_[z_] = o[((i) & 6) + (z_ >> 2)][(i) & 1][z_ & 3] * inv[(i) & 1]; PIN(y_[z_]); } \
                pc[i] = u32x4{cvtpk(y_[0], y_[1]), cvtpk(y_[2], y_[3]), cvtpk(y_[4], y_[5]), cvtpk(y_[6], y_[7])};                        \
                PIN(pc[i]); if (!(ISB)) CST(i); SB(); } while (0)
#define XLD(b_) do { _Pragma("unroll") for (int n = 0; n < 8; ++n) asm volatile("global_load_dwordx4 %0, %1, %2 offset:%3 sc1" : "=v"(fr[n]) : "v"(l16 + (n >> 2) * 4096u), "s"(b_), "i"((n & 3) * 1024) : "memory"); \
                asm volatile("s_waitcnt vmcnt(0)" : "+v"(fr[0]), "+v"(fr[1]), "+v"(fr[2]), "+v"(fr[3]), "+v"(fr[4]), "+v"(fr[5]), "+v"(fr[6]), "+v"(fr[7]) :: "memory"); } while (0)
#define MFB(a_, b_, c_) __builtin_amdgcn_mfma_f32_16x16x32_bf16(a_, b_, c_, 0, 0, 0)
#define EXACT() do { __builtin_amdgcn_s_waitcnt(0xC07F); MF_DONE();                                                                                                           \
                float mr_[2] = {-1e30f, -1e30f}; const int nu_ = 4 * qb + qw2;                                                            \
                ls[0] = ls[1] = f32x4{}; _Pragma("unroll") for (int d = 0; d < 16; ++d) o[d >> 1][d & 1] = f32x4{};                       \
                _Pragma("unroll 1") for (int u = 0; u <= nu_; ++u) {                                                                      \
                    const char* kb_ = kvg + (size_t)(u >> 2) * 65536 + (u & 3) * 8192;                                                    \
                    XLD(kb_);                                                                                                             \
                    _Pragma("unroll") for (int i = 0; i < 4; ++i) { f32x4 s_ = f32x4{};                                                   \
                        _Pragma("unroll") for (int d = 0; d < 4; ++d) s_ = MFB(fr[2 * d + (i >> 1)], qf[2 * d + (i & 1)], s_);            \
                        S[1][i] = s_; }                                                                                                   \
                    if (u == nu_) { _Pragma("unroll") for (int r = 0; r < 4; ++r) { S[1][2][r] = -__builtin_inff();                       \
                        if (4 * (ln2 >> 4) + r > (ln2 & 15)) S[1][0][r] = S[1][3][r] = -__builtin_inff(); } }                             \
                    XLD(kb_ + 32768);                                                                                                     \
                    _Pragma("unroll") for (int a = 0; a < 2; ++a) {                                                                       \
                        float mx_ = SV(1, 8 * a); _Pragma("unroll") for (int r = 1; r < 8; ++r) mx_ = fmaxf(mx_, SV(1, 8 * a + r));       \
                        mx_ = fmaxf(quad_max(mx_), mr_[a]); const float al_ = EX(mr_[a] - mx_); mr_[a] = mx_;                             \
                        u32x4 w_; _Pragma("unroll") for (int j = 0; j < 4; ++j) w_[j] = cvtpk(EX(SV(1, 8 * a + 2 * j) - mx_), EX(SV(1, 8 * a + 2 * j + 1) - mx_)); \
                        const bf16x8 p_ = __builtin_bit_cast(bf16x8, w_);                                                                 \
                        ls[a] = MFB(ones, p_, ls[a] * al_);                                                                               \
                        _Pragma("unroll") for (int d = 0; d < 8; ++d) o[d][a] = MFB(fr[d], p_, o[d][a] * al_); } }                        \
                pf[1][0] = pf[1][1] = bf16x8{}; _Pragma("unroll") for (int n = 4; n < 8; ++n) fr[n] = bf16x8{};     \
                PIN(pf[1][0]); PIN(pf[1][1]); MF_DONE(); } while (0)
#define TAIL(ISB) do { float rmp[4];                                                                                         \
                XST(0);                                                                                                                   \
                MX(1, 0, 0); SB();                                                                                                        \
                int ln2 = lane, qw2 = qw; asm volatile("" : "+v"(ln2), "+s"(qw2));   \
                fr[7] = lds_rd(av + (unsigned)((qw2 + 3) & 3) * 8192u + 7168u); SB();                                                     \
                MX(1, 0, 1); SB(); E2(0); SB();                                                                                           \
                GX(1, 1, E2(1), E2(2));                                                                                                   \
                GL(1, E2(3), E2(8));                                                                                                      \
                float fl_ = (float)((ln2 & 15) - 4 * (ln2 >> 4)); PIN(fl_);                                                               \
                unsigned avf = lb16 + (unsigned)(t & 1) * 32768u + (unsigned)qw2 * 8192u; PIN(avf); SB();                                 \
                GX(1, 2, E2(9), E2(10));                                                                             \
                GX(1, 3, E2(11), _Pragma("unroll") for (int r = 0; r < 4; ++r) { rmp[r] = (fl_ + (0.5f - r)) * 1.2089258196146292e24f; PIN(rmp[r]); }); \
                if (__builtin_expect(qw2 == 2, 0)) {                               \
                    if (ISB) TBAR(); else LGKM0_BAR(); }                                                                                  \
                SB();                                                                                                                     \
                RDV(0); RDV(1); RDV(2); RDV(3); SB();                                                                                     \
                GX(1, 4, E2(12), M1(0); M1(1));                                                                                           \
                GX(1, 5, RDV(4); E2(13), M1(2); M1(3));                                                                                   \
                GX(1, 6, RDV(5); E2(14), C1(1, 0); C1(1, 1));                                                                             \
                GX(1, 7, RDV(6); E2(15), C1(1, 4); C1(1, 5));                                                                             \
                RDV(7); M1(12); M1(13); M1(14); M1(15); e[4] = e[5] = e[6] = e[7] = 0.f; C1(1, 2); C1(1, 3); SB();                        \
                C1(1, 6); C1(1, 7); SB();                                                                                                 \
                XST(1);                                                                                                                   \
                if (__builtin_expect(qw2 < 2, 0)) { if (ISB) TBAR(); else LGKM0_BAR(); }                                                  \
                SB();                                                                                                                     \
                XST(2); XST(3);                                                                                                           \
                float inv[2];                                                                                                \
                int hd2 = head; asm volatile("" : "+s"(hd2)); const char* cb = ws + WS_CTX + (unsigned)(hd2 * 524288 + (qb * 8 + qw2 * 2) * 2048); SB(); \
                GL(0, , ); GX(0, 0, , ); GX(0, 1, , ); GX(0, 2, , );                                                                      \
                PIN(ls[0]); PIN(ls[1]);                                                                                                   \
                const bool c_ = __builtin_amdgcn_ballot_w64(!(ls[0][0] + ls[1][0] <= LTHR)) != 0; SB();                                   \
                GX(0, 3, , );                                                                                                             \
                if (__builtin_expect(c_, 0)) EXACT();                                                                                     \
                SB();                                                                                                                     \
                inv[0] = __builtin_amdgcn_rcpf(ls[0][0]); inv[1] = __builtin_amdgcn_rcpf(ls[1][0]); PIN(inv[0]); PIN(inv[1]); SB();       \
                GX(0, 4, , asm volatile("s_nop 0"));                                                                                      \
                NRM(ISB, 0); GX(0, 5, , ); NRM(ISB, 1); GX(0, 6, , ); NRM(ISB, 2); GX(0, 7, , ); NRM(ISB, 3);                             \
                XST(4);                                                                                                                   \
                NRM(ISB, 4); NRM(ISB, 5); NRM(ISB, 6); NRM(ISB, 7);                                                                       \
                if (ISB) { asm volatile("s_setprio 0"); asm volatile("" : "+s"(t)); KCHK(); if (t + 2 < NA) { BRQ4(t, 0); BRQ4(t, 1); BRQ4(t, 3); } BRQ4(t, 2); \
                           CST(0); CST(1); CST(2); CST(3); CST(4); CST(5); CST(6); CST(7); }                                              \
                XST(5);                                                                                                                   \
            } while (0)
#define SPIN() do { PIN(S[1][0]); PIN(S[1][1]); PIN(S[1][2]); PIN(S[1][3]); } while (0)
            { ONCE2(isb ^ 1) { SPIN(); TAIL(0); } }
            { ONCE2(isb) { SPIN(); TAIL(1); } }
            SST(2);
            AST(4);
        }
        if (wave >= 4) {
            int t = qbB + 1;
            const int wv = wave & 3;
            XST(6);
#define WOLD(h_) do { const unsigned ow_ = OWOFF(); const char* wp_ = (const char*)(wo + (size_t)(K64(2 * (h_)) * 64 + 16 * wv) * DM + n0);  \
                _Pragma("unroll") for (int i = 0; i < 8; ++i)                                                                             \
                    GLDS_S(ow_, wp_ + (size_t)((i >> 2) * 64 + 8 * ((i >> 1) & 1)) * (DM * 2) + (i & 1) * dn1,                             \
                           ldsbase + 65536 + (h_) * 32768 + (i >> 2) * 16384 + (2 * wv + ((i >> 1) & 1)) * 2048 + (i & 1) * 1024, 0); } while (0)
            {
                const u32x4 em = u32x4{~0u, ~0u, ~0u, ~0u};
                const char* eb = ws + WS_PART + (size_t)(tile * 8 + wv) * 24576 + kp * 6144;
                asm volatile("global_store_dwordx4 %0, %1, %2 sc1\n\tglobal_store_dwordx4 %0, %1, %2 offset:1024 sc1\n\tglobal_store_dwordx4 %0, %1, %3 sc1\n\t"
                             "global_store_dwordx4 %0, %1, %3 offset:1024 sc1\n\ts_waitcnt vmcnt(12) lgkmcnt(0)"
                             :: "v"(l16), "v"(em), "s"(eb), "s"(eb + 4 * 24576) : "memory"); }
            if (jj == 7) { WOLD(1); EALD(0); EALD(1); }
            asm volatile("s_barrier" ::: "memory");
#define BATCH() do { KCHK(); if (t + 2 < NA) { BRQ4(t, 0); BRQ4(t, 1); BRQ4(t, 3); } BRQ4(t, 2); } while (0)
#define BSTEP(N) do { asm volatile("s_waitcnt vmcnt(%0)\n\ts_barrier" :: "i"(N) : "memory"); ++t; } while (0)
            u32x4 fw[16], wd[16], xq[4], gp[8];
            f32x4 fa[8];
            int ln_ = lane; asm volatile("" : "+v"(ln_));
#define FMM(w_, i0, a_, z_) do { asm volatile("s_setprio 1");                                                              \
                    _Pragma("unroll") for (int i = 0; i < 8; ++i) PIN(w_[(i0) + i]);                                                      \
                    PIN(a_);                                                                                                              \
                    _Pragma("unroll") for (int j = 0; j < 8; ++j) { u32x4 f_;                                                             \
                        _Pragma("unroll") for (int d = 0; d < 4; ++d) f_[d] = __builtin_amdgcn_perm(w_[(i0) + 2 * d + 1][j >> 1], w_[(i0) + 2 * d][j >> 1], (j & 1) ? 0x07060302u : 0x05040100u); \
                        fa[j] = __builtin_amdgcn_mfma_f32_16x16x32_bf16(__builtin_bit_cast(bf16x8, a_), __builtin_bit_cast(bf16x8, f_), (z_) ? f32x4{} : fa[j], 0, 0, 0); } \
                    _Pragma("unroll") for (int j = 0; j < 8; ++j) PIN(fa[j]);                                                             \
                    asm volatile("s_setprio 0"); } while (0)
#define FOUT() f32x4{ln_ >= 32 ? fa[4][0] : fa[0][0], ln_ >= 32 ? fa[5][0] : fa[1][0], ln_ >= 32 ? fa[6][0] : fa[2][0], ln_ >= 32 ? fa[7][0] : fa[3][0]}
            const int c1 = 2 * (i1 - 14 * k1) + (wv & 1);
            const unsigned r4o = (unsigned)(ln_ & 15) * 16u;
            const char* cxp = ws + WS_FLAG + F_CTX + ((qbB * 4 + qw) * 32 + head) * CFS;
            if (nf1 == 0) {
                const unsigned evo = (unsigned)((16 * (ln_ >> 5) + 4 * ((ln_ >> 4) & 1)) * FF * 2) + r4o;
                const u16* fwp = (wv < 2 ? w_gate : w_up) + (size_t)(k1 * 128) * FF + c1 * 128;
#define FLD(h_, c_) do { _Pragma("unroll") for (int i = 0; i < 8; ++i)                                                                    \
                    asm volatile("global_load_dwordx4 %0, %1, %2" : c_(fw[((h_) & 1) * 8 + i]) : "v"(evo), "s"(fwp + (size_t)(32 * (h_) + 8 * (i >> 2) + (i & 3)) * FF) : "memory"); } while (0)
                BATCH();
                { const int xr_ = (ln_ & 4) ? ri2 : ri1;
#pragma unroll
                  for (int i = 0; i < 4; ++i)
                      asm volatile("global_load_dwordx4 %0, %1, %2 offset:%3 sc1" : "=v"(xq[i]) : "v"((unsigned)((xr_ >> 4) * 2048 + (xr_ & 15) * 16 + (ln_ >> 4) * 256)),
                                   "s"(ws + WS_XF + (size_t)(k1 * 2 + (i >> 1)) * 262144), "i"((i & 1) * 1024) : "memory"); }
                FLD(0, "=v"); FLD(1, "=v"); BSTEP(20);
                BATCH(); asm volatile("s_waitcnt vmcnt(16)" ::: "memory");
                FLAG_PUT(cxp, tag, 1);
                FMM(fw, 0, xq[0], 1); FLD(2, "=v"); FMM(fw, 8, xq[1], 0); FLD(3, "=v"); BSTEP(17);
                BATCH(); asm volatile("s_waitcnt vmcnt(16)" ::: "memory");
                FMM(fw, 0, xq[2], 0); FMM(fw, 8, xq[3], 0); const f32x4 sum = FOUT();
                ST16_SC1((float*)(ws + WS_GP) + ((size_t)k1 * 4 + (wv >> 1) * 2 + ((ln_ >> 4) & 1)) * FF + c1 * 128 + (ln_ & 15) * 8 + (ln_ >> 5) * 4, sum);
                PFW1(7); BSTEP(2);
            }
            const char* pnd = nf1 ? cxp : ws + WS_FLAG + F_GU + (c1 * 16 + (wv >> 1) * 8 + k1) * 8;
            const int tp = nf1 ? t + 1 : t;
#define PUB(N) do { if (t == tp) { asm volatile("s_waitcnt vmcnt(%0)" :: "i"(N) : "memory"); FLAG_PUT(pnd, tag, 1); } } while (0)
            const int f0 = (nf2 ? 0 : i2 >> 3) * 256 + wv * 64;
            const char* gfp = ws + WS_FLAG + F_GU + (f0 >> 7) * 128 + (ln_ & 15) * 8;
            u64 gf = tag + 1;
            char* vs = lds + (NA & 1) * 32768;
#define GFLD() asm volatile("global_load_dwordx2 %0, %1, off sc1" : "+v"(gf) : "v"(gfp) : "memory")
#define WDLD(i, n_) do { const unsigned dvo = (unsigned)((ln_ >> 4) * 8 * DM * 2) + r4o;                                                  \
                    const char* db_ = (const char*)(w_down + (size_t)((nf2 ? 0 : i2 >> 3) * 256 + wv * 64) * DM + (i2 & 7) * 128) + 4096; \
                    asm volatile("s_cmp_lg_u32 %11, 0\n\ts_cbranch_scc1 1f\n\t"                                                          \
                                 "global_load_dwordx4 %0, %8, %9 offset:-4096\n\tglobal_load_dwordx4 %1, %8, %9 offset:-2048\n\tglobal_load_dwordx4 %2, %8, %9\n\t" \
                                 "global_load_dwordx4 %3, %8, %9 offset:2048\n\tglobal_load_dwordx4 %4, %8, %10 offset:-4096\n\tglobal_load_dwordx4 %5, %8, %10 offset:-2048\n\t" \
                                 "global_load_dwordx4 %6, %8, %10\n\tglobal_load_dwordx4 %7, %8, %10 offset:2048\n1:"  \
                                 : "=&v"(wd[i]), "=&v"(wd[i + 1]), "=&v"(wd[i + 2]), "=&v"(wd[i + 3]), "=&v"(wd[i + 4]), "=&v"(wd[i + 5]), "=&v"(wd[i + 6]), "=&v"(wd[i + 7]) \
                                 : "v"(dvo), "s"(db_ + (i) * 8192), "s"(db_ + (i) * 8192 + 8192), "s"(nf2) : "memory", "scc");            \
                    EALD((i) >> 3);                                                                                                       \
                    asm volatile("s_waitcnt vmcnt(%1)\n\ts_cmp_eq_u32 %0, 0\n\ts_cbranch_scc1 1f\n\ts_waitcnt vmcnt(%2)\n1:"                \
                                 :: "s"(nf2), "i"(12 + (n_)), "i"(4 + (n_)) : "memory", "scc");                                           \
                    BSTEP(63); } while (0)
#pragma unroll 1
            while (t < NA - 4) { BATCH(); PUB(17); BSTEP(0); }
            if (jj < 7) {
                BATCH(); PUB(16); WDLD(0, 0);
                BATCH(); PUB(16); GFLD(); WDLD(8, 0);
            }
            if (jj < 6) {
                BATCH();
                if (nf2 == 0) {
                    FLAG_WAIT(gf, gfp, tag, 63);
#pragma unroll
                    for (int i = 0; i < 8; ++i)
                        asm volatile("global_load_dwordx4 %0, %1, %2 sc1" : "=v"(gp[i]) : "v"((unsigned)((ln_ >> 4) * FF * 4) + r4o), "s"(ws + WS_GP + ((size_t)i * 4 * FF + f0) * 4) : "memory");
                    *(unsigned*)vs = 0;
                    asm volatile("s_waitcnt lgkmcnt(0)" ::: "memory");
                }
                WOLD(NA & 1);
                BSTEP(8);
            } else if (jj == 6) WOLD(0);
            asm volatile("global_load_dwordx2 %0, %1, %2 sc1" : "+v"(cxf) : "v"(CFO()), "s"(CFB) : "memory");
            WOLD((NA & 1) ^ 1);
            asm volatile("s_waitcnt vmcnt(16)" ::: "memory");
            if (jj == 7) FLAG_PUT(cxp, tag, 1);
            if (nf2 == 0) {
                f32x4 g4;
#pragma unroll
                for (int i = 0; i < 8; ++i) { PIN(gp[i]);
#pragma unroll
                    for (int j = 0; j < 4; ++j) { if (i) VADD(g4[j], g4[j], __uint_as_float(gp[i][j])); else g4[j] = __uint_as_float(gp[0][j]); } }
                float hv[2];
#pragma unroll
                for (int k = 0; k < 2; ++k) { auto rr = __builtin_amdgcn_permlane32_swap(__float_as_uint(g4[k]), __float_as_uint(g4[2 + k]), false, false);
                    const float gb = rbf(__uint_as_float(rr[0])), ub = rbf(__uint_as_float(rr[1]));
                    hv[k] = rbf(gb / (1.f + __expf(-gb))) * ub; PIN(hv[k]); }
                char* hl = vs + 8192 + wv * 256;
                *(unsigned*)(hl + ((ln_ >> 4) & 1) * 128 + (ln_ & 15) * 8 + (ln_ >> 5) * 4) = cvtpk(hv[0], hv[1]);
                asm volatile("" ::: "memory");
                xq[0] = *(const u32x4*)(hl + ((ln_ >> 2) & 1) * 128 + (ln_ >> 4) * 16); xq[1] = *(const u32x4*)(hl + ((ln_ >> 2) & 1) * 128 + 64 + (ln_ >> 4) * 16);
                FMM(wd, 0, xq[0], 1); FMM(wd, 8, xq[1], 0);
                *(f32x4*)(vs + 4096 + wv * 1024 + ln_ * 16) = FOUT();
                unsigned n_;
                asm volatile("s_waitcnt lgkmcnt(0)\n\ts_mov_b64 exec, 1\n\tds_add_rtn_u32 %0, %1, %2\n\ts_mov_b64 exec, -1\n\ts_waitcnt lgkmcnt(0)"
                             : "=&v"(n_) : "v"(ldsbase + (NA & 1) * 32768u), "v"(1u) : "memory");
                ypub = __builtin_amdgcn_readfirstlane(n_) ^ 3u;
                if (ypub == 0) {
                    const f32x4* yr = (const f32x4*)(vs + 4096) + ln_;
                    const f32x4 a = (yr[0] + yr[64]) + (yr[128] + yr[192]);
                    ST16_SC1((float*)(ws + WS_YP) + ((size_t)(((ln_ >> 4) & 1) * 8 + (i2 & 7)) * 16 + (i2 >> 3)) * 128 + (ln_ & 15) * 8 + (ln_ >> 5) * 4, a);
                }
            }
            AST(5);
        }
    }
    PH_STAMP(3);
    {
        constexpr int LDW = 1024, STG = 16384;
        int tid = threadIdx.x; asm volatile("" : "+v"(tid));
        const int lane = tid & 63, l15 = lane & 15, q4 = lane >> 4;
        unsigned xvo = (unsigned)lane * 16u; PIN(xvo);
        if (!(kp & 1)) {
#pragma unroll
            for (int i = 0; i < 8; i += 2) { const u32x4 t_ = pc[i]; pc[i] = pc[i + 1]; pc[i + 1] = t_; PIN(pc[i]); PIN(pc[i + 1]); } }
        bf16x8 yr_[4][2][2];
#pragma unroll
        for (int i = 0; i < 8; ++i) yr_[i >> 2][i & 1][(i >> 1) & 1] = __builtin_bit_cast(bf16x8, pc[i]);
        unsigned wa[2][2];
        wa[0][0] = ldsbase + (unsigned)(65536 + ((kp >> 1) ^ 1) * 1024 + (q4 >> 1) * 2048 + (4 * (q4 & 1) + (l15 >> 2)) * 64 + (q4 & 1) * 16 + (lane & 2) * 16 + (lane & 1) * 8);
        wa[0][1] = wa[0][0] ^ 16u; wa[1][0] = wa[0][0] ^ 1024u; wa[1][1] = wa[0][1] ^ 1024u;
        PIN(wa[0][0]); PIN(wa[0][1]); PIN(wa[1][0]); PIN(wa[1][1]);
        const char* cg = ws + WS_CTX + (qb * 8 + qw * 2) * 2048;
        unsigned xok;
#define XLD8 "global_load_dwordx4 %0, %10, %12 sc1\n\tglobal_load_dwordx4 %1, %10, %13 sc1\n\tglobal_load_dwordx4 %2, %10, %12 offset:1024 sc1\n\tglobal_load_dwordx4 %3, %10, %13 offset:1024 sc1\n\t" \
             "global_load_dwordx4 %4, %11, %12 sc1\n\tglobal_load_dwordx4 %5, %11, %13 sc1\n\tglobal_load_dwordx4 %6, %11, %12 offset:1024 sc1\n\tglobal_load_dwordx4 %7, %11, %13 offset:1024 sc1\n\t"
#define XOUT(c_) c_(yr_[2][0][0]), c_(yr_[2][1][0]), c_(yr_[2][0][1]), c_(yr_[2][1][1]), c_(yr_[3][0][0]), c_(yr_[3][1][0]), c_(yr_[3][0][1]), c_(yr_[3][1][1])
#define XIN "v"(xvo), "v"(xvo2), "s"(xp0), "s"(xp1), "s"(tag)
        unsigned xvo2 = xvo + 262144u, wvo = OWOFF() + (unsigned)K64(4) * (64 * LDW * 2), xso = xvo + (unsigned)K64(4) * 262144u, cfo = CFO();
        PIN(xvo2); PIN(wvo); PIN(xso); PIN(cfo);
        const char* cg0 = cg + ((kp ^ 1) & 1) * 2048, * cg1 = cg + (kp & 1) * 2048, * xp0 = cg0 + (unsigned)K64(2) * 262144u, * xp1 = cg1 + (unsigned)K64(2) * 262144u;
        const char* cfb = CFB, * wb0 = (const char*)wob, * wb1 = wb0 + (dn1 - 1024);
        const char* fpp = ws + WS_FLAG + ((wave & 4) ? F_Y + ((i2 & 7) * 16 + ((i2 >> 3) & 15)) * 8 : F_CTX + ((qbA * 4 + wave) * 32 + head) * CFS);
        unsigned fpz = (wave >> 2) * ypub, m0w = ldsbase + wave * 2048;
        asm volatile("" : "+s"(cg0), "+s"(cg1), "+s"(xp0), "+s"(xp1), "+s"(cfb), "+s"(wb0), "+s"(wb1), "+s"(fpp), "+s"(fpz), "+s"(m0w));
        SST(3);
        SB();
        asm volatile("s_waitcnt vmcnt(0)" ::: "memory");
        FLAG_PUTZ(fpp, tag, fpz);
        asm volatile("v_cmp_ne_u64 vcc, %14, %8\n\ts_and_b32 %9, vcc_lo, 14\n\ts_cbranch_scc1 1f\n\t"
                     XLD8 "\n1:\n\ts_barrier" : XOUT("=&v"), "+v"(cxf), "=&s"(xok) : XIN : "memory", "vcc", "scc");
        SST(4);
        f32x4 acc[2][8];
        bf16x4 wl[8], wh[8];
#define OFA(s, k_, j) (wa[(j) >> 2][(j) & 1] + (unsigned)(((s) & 3) * STG + (k_) * 8192 + (((j) >> 1) & 1) * 512))
#define ORL(i, s, k_, j) wl[i] = tr_rd(OFA(s, k_, j))
#define ORH(i, s, k_, j) wh[i] = tr_rd(OFA(s, k_, j) + 4096u)
        ORL(0, 0, 0, 0); ORH(0, 0, 0, 0); ORL(1, 0, 0, 1); ORH(1, 0, 0, 1); ORL(2, 0, 0, 2); ORH(2, 0, 0, 2); ORL(3, 0, 0, 3); ORH(3, 0, 0, 3); SB();
        const int r0 = qb * 128 + qw * 32 + (kp & 1) * 16, ch = kp >> 1;
        const unsigned nrt = min((unsigned)(ri1 - r0) >> 4, (unsigned)(ri2 - r0) >> 4);
        u64 yf = 0;
#define YFP (ws + WS_FLAG + F_Y + (ntc * 16 + (l15 < 14 ? l15 : 0)) * 8)
#define OMF(a, s, k_, j) do { if ((s) == 0 && (k_) == 0) MFA16Z(acc[a][j], w_, yr_[0][a][0]); else MFA16(acc[a][j], w_, yr_[(s) & 3][a][k_]); } while (0)
#define ORQ(h) do { constexpr int x_ = o_x(h), w_ = o_w(h);                                                                               \
            if (x_ >= 0) { asm volatile("global_load_dwordx4 %0, %1, %2 offset:%3 sc1" : "=v"(yr_[(x_ >> 2) & 3][x_ & 1][(x_ >> 1) & 1])   \
                                        : "v"(xso), "s"((x_ & 1) ? cg1 : cg0), "i"(((x_ >> 1) & 1) * 1024) : "memory");         \
                           if ((x_ & 3) == 3) { xso ^= (unsigned)((x_ >> 2) ^ ((x_ >> 2) + 1)) * 262144u; PIN(xso); } }                    \
            if (o_w((h) + 1) >= 0 && !(o_w((h) + 1) & 1)) asm volatile("s_mov_b32 m0, %0" :: "s"(m0w + (((o_w((h) + 1) >> 1) & 7) ^ 4) * STG) : "memory"); \
            if (w_ >= 0) { asm volatile("global_load_lds_dwordx4 %0, %1 offset:%2" :: "v"(wvo), "s"((w_ & 1) ? wb1 : wb0), "i"((w_ & 1) * 1024) : "memory"); \
                           if (w_ & 1) { wvo ^= (unsigned)((w_ >> 1) ^ ((w_ >> 1) + 1)) * (64 * LDW * 2); PIN(wvo); } } } while (0)
#define OPAIR(s, f, A) do { const bf16x8 w_ = CAT8(wl[(f) & 7], wh[(f) & 7]); constexpr int s_ = (s) + (((f) + 4) >> 4), m_ = ((f) + 4) & 15; \
            OMF(0, s, (f) >> 3, (f) & 7); SB(); ORL(m_ & 7, s_, m_ >> 3, m_ & 7); ORQ(32 * (s) + 2 * (f)); A; SB();                       \
            OMF(1, s, (f) >> 3, (f) & 7); SB(); ORH(m_ & 7, s_, m_ >> 3, m_ & 7); ORQ(32 * (s) + 2 * (f) + 1); SB(); if (((f) & 1)) { __builtin_amdgcn_s_waitcnt(0xC47F); SB(); } } while (0)
#define OTG(h_) do { wa[h_][0] ^= 65536u; wa[h_][1] ^= 65536u; PIN(wa[h_][0]); PIN(wa[h_][1]); } while (0)
#define OST(i)
#define OSTEP(s, POST) do {                                                                                                               \
        OPAIR(s, 0, ); OPAIR(s, 1, ); OPAIR(s, 2, ); OPAIR(s, 3, ); OPAIR(s, 4, ); OPAIR(s, 5, ); OPAIR(s, 6, ); OPAIR(s, 7, );           \
        OPAIR(s, 8, ); OPAIR(s, 9, if (((s) & 3) == 3) OTG(0)); OPAIR(s, 10, ); OPAIR(s, 11, );                                           \
        if ((s) >= 3) asm volatile("s_waitcnt vmcnt(%0)\n\ts_barrier" :: "i"(o_wait(s)) : "memory");                                      \
        SB(); POST; OST((s) + 1);                                                                                                         \
        OPAIR(s, 12, ); OPAIR(s, 13, if (((s) & 3) == 3) OTG(1)); OPAIR(s, 14, ); OPAIR(s, 15, );                                         \
    } while (0)
        PH_STAMP(4);
        SST(5);
        OST(0);
        SB();
        OSTEP(0, );
#define MSK "14"
#define MSKI
        asm volatile("s_cmp_eq_u32 %3, 0\n\ts_cbranch_scc1 1f\n\tglobal_load_dwordx2 %0, %1, %2 sc1\n1:" : "+v"(cxf) : "v"(cfo), "s"(cfb), "s"(xok) : "memory", "scc");
        OSTEP(1, );
        asm volatile("s_waitcnt vmcnt(8)\n\ts_cmp_eq_u32 %9, 0\n\ts_cbranch_scc1 3f\n\ts_mov_b32 %9, 0x40000\n\ts_waitcnt vmcnt(4)\n"
                     "1:\n\tv_cmp_ne_u64 vcc, %14, %8\n\ts_and_b32 vcc_lo, vcc_lo, " MSK "\n\ts_cbranch_scc0 2f\n\t"
                     "global_load_dwordx2 %8, %15, %16 sc1\n\ts_sleep 4\n\ts_waitcnt vmcnt(0)\n\ts_sub_u32 %9, %9, 1\n\ts_cbranch_scc0 1b\n2:\n\t" XLD8 "s_waitcnt vmcnt(4)\n3:"
                     : XOUT("+v"), "+v"(cxf), "+s"(xok) : XIN, "v"(cfo), "s"(cfb), "s"(wave) MSKI : "memory", "vcc", "scc");
        const unsigned inc = (unsigned)__builtin_amdgcn_ballot_w64(cxf != tag) & 0x1f0u;
        SB();
        OSTEP(2, if (__builtin_expect(inc != 0, 0)) asm volatile("global_load_dwordx2 %0, %1, %2 sc1" : "+v"(cxf) : "v"(cfo), "s"(cfb) : "memory"); SB());
        asm volatile("s_waitcnt vmcnt(8)" ::: "memory"); SB();
        OSTEP(3, );
        OSTEP(4, if (__builtin_expect(inc != 0, 0)) FLAG_WAITM(cxf, cfb + cfo, tag, 63, 0x1f0u); SB());
        OSTEP(5, ); OSTEP(6, );
        OSTEP(7, if (__builtin_expect(nrt == 0, 0)) asm volatile("global_load_dwordx2 %0, %1, off sc1" : "+v"(yf) : "v"(YFP) : "memory"); SB());
        OSTEP(8, ); OSTEP(9, ); OSTEP(10, );
        u32x4 yq[7], pv[6], zn[2]; u32x2 ea[4]; unsigned rq;
        if (__builtin_expect(nrt == 0, 0)) {
            FLAG_WAIT(yf, YFP, tag, 63);
            asm volatile("global_load_dwordx4 %0, %8, %10 sc1\n\tglobal_load_dwordx4 %1, %8, %10 offset:512 sc1\n\tglobal_load_dwordx4 %2, %8, %10 offset:1024 sc1\n\t"
                         "global_load_dwordx4 %3, %8, %10 offset:1536 sc1\n\tglobal_load_dwordx4 %4, %8, %10 offset:2048 sc1\n\tglobal_load_dwordx4 %5, %8, %10 offset:2560 sc1\n\t"
                         "global_load_dwordx4 %6, %8, %10 offset:3072 sc1\n\tglobal_load_dword %7, %9, %10 sc1"
                         : "=&v"(yq[0]), "=&v"(yq[1]), "=&v"(yq[2]), "=&v"(yq[3]), "=&v"(yq[4]), "=&v"(yq[5]), "=&v"(yq[6]), "=&v"(rq)
                         : "v"((unsigned)WS_YP + (unsigned)((((lane >> 5) * 8 + ntc) * 16 + (q4 & 1) * 7) * 128 + ch * 64 + 4 * l15) * 4u),
                           "v"((unsigned)WS_R + (unsigned)(lane >= 32 ? ri2 : ri1) * 4u), "s"(ws) : "memory");
        }
        SB();
        OSTEP(11, );
        char* pb = ws + WS_PART + (size_t)(tile * 8 + wave) * 24576;
        const char* tfb = ws + WS_FLAG + F_PARK + (tile * 8 + wave) * 256;
        const unsigned tfo = (unsigned)((lane & 3) ^ kp) * 64u;
        const unsigned ea0 = ldsbase + 131072 + (wave & 3) * 8192 + (wave >> 2) * 4096 + l15 * 128 + (((lane >> 5) * 2 ^ (lane & 7)) << 4) + (q4 & 1) * 8;
        typedef __attribute__((address_space(3))) u32x2 lds_u32x2;
#define OSND(a, j) do { constexpr int x_ = ((a) ^ 1) | (((j) >> 1) & 2 ^ 2);                                             \
            u32x4 p_ = u32x4{cvtpk(acc[a][j][0], acc[a][j][1]), cvtpk(acc[a][j][2], acc[a][j][3]), cvtpk(acc[a][(j) + 1][0], acc[a][(j) + 1][1]), cvtpk(acc[a][(j) + 1][2], acc[a][(j) + 1][3])}; \
            if (x_ == 1) { _Pragma("unroll") for (int k = 0; k < 4; ++k) p_[k] = min(p_[k], 0xfffffffeu); }     \
            asm volatile("global_store_dwordx4 %0, %1, %2 offset:%3 sc1\n\ts_nop 1" :: "v"(xvo), "v"(p_), "s"(pb + (kp ^ x_) * 6144 + (x_ - 1) * 2048), "i"(((j) & 2) * 512) : "memory"); } while (0)
#define OUF(i, u) do { ORL(i, ((u) >> 1) & 3, (u) & 1, 4 + (((u) >> 3) & 3)); ORH(i, ((u) >> 1) & 3, (u) & 1, 4 + (((u) >> 3) & 3)); } while (0)
#define OQ(q) do { constexpr int p_ = (q) + 4; const bf16x8 w_ = CAT8(wl[(q) & 7], wh[(q) & 7]);                                           \
            if ((q) == 16 || (q) == 24) { asm volatile("s_waitcnt vmcnt(%0)" :: "i"((q) == 16 ? 4 : 0) : "memory"); SB(); }               \
            OMF(0, 12 + (((q) >> 3) & 3), ((q) >> 2) & 1, (q) & 3); SB();                                                                 \
            if (p_ < 32) ORL(p_ & 7, (p_ >> 3) & 3, (p_ >> 2) & 1, p_ & 3); else OUF(p_ & 7, p_ - 32);                                    \
            ORQ(384 + 2 * (q)); SB();                                                                                                     \
            OMF(1, 12 + (((q) >> 3) & 3), ((q) >> 2) & 1, (q) & 3); SB();                                                                 \
            if (p_ < 32) ORH(p_ & 7, (p_ >> 3) & 3, (p_ >> 2) & 1, p_ & 3); else if ((q) >= 30) OUF(((q) - 26) & 7, (q) - 26);            \
            if ((q) >= 10 && (q) < 14) ea[((q) - 10) & 3] = *(lds_u32x2*)(uintptr_t)(ea0 ^ (unsigned)((((q) - 10) & 1) * 16 + (((q) - 10) & 2) * 32)); \
            if ((q) == 14 || (q) == 15) zn[(q) & 1] = __builtin_bit_cast(u32x4, lds_rd(ldsbase + 131072 + 2048 + (wave & 3) * 8192 + (wave >> 2) * 4096 + ((q) & 1) * 1024 + xvo)); \
            SB(); } while (0)
#define OU(u) do { const bf16x8 w_ = CAT8(wl[(u) & 7], wh[(u) & 7]);                                                                      \
            OMF((u) >> 5, 12 + (((u) >> 1) & 3), (u) & 1, 4 + (((u) >> 3) & 3)); SB();                                                    \
            if ((u) + 6 < 64) OUF(((u) + 6) & 7, (u) + 6);                                                                                \
            if ((u) == 1) OSND(0, 0); if ((u) == 3) OSND(1, 0); if ((u) == 5) OSND(0, 2); if ((u) == 7) OSND(1, 2);                       \
            if ((u) == 19) OSND(0, 4);                                                                                                    \
            if ((u) == 22) { asm volatile("s_waitcnt vmcnt(1)" ::: "memory"); FLAG_PUT(tfb + kp * 64, tag, 1); }     \
            if ((u) == 35) { OSND(0, 6); asm volatile("s_setprio 0"); S2T(1); }                                                                   \
            if ((u) == 52) asm volatile("global_load_dwordx2 %0, %1, %2 sc1" : "=v"(cxf) : "v"(tfo), "s"(tfb) : "memory");               \
            SB(); } while (0)
#define OQ8(q) do { OQ(q); OQ((q) + 1); OQ((q) + 2); OQ((q) + 3); OQ((q) + 4); OQ((q) + 5); OQ((q) + 6); OQ((q) + 7); } while (0)
#define OU8(q) do { OU(q); OU((q) + 1); OU((q) + 2); OU((q) + 3); OU((q) + 4); OU((q) + 5); OU((q) + 6); OU((q) + 7); } while (0)
        asm volatile("s_setprio 1");
        OQ8(0); OQ8(8); OQ8(16); OQ8(24); OST(13); OU8(0); OU8(8); OU8(16); OU8(24); OU8(32); OU8(40); OU8(48); OU8(56);
        MF_DONE();
        OST(14);
        PH_STAMP(5);
        SST(6); S2T(2);
        SB();
        if (__builtin_expect(nrt == 0, 0)) {
            f32x4 ys;
#pragma unroll
            for (int i = 0; i < 7; ++i) PIN(yq[i]);
            PIN(rq);
            const float sig = rbf(1.f / (1.f + __expf(-__uint_as_float(rq))));
#pragma unroll
            for (int j = 0; j < 4; ++j) {
                const float s_ = ((__uint_as_float(yq[0][j]) + __uint_as_float(yq[1][j])) + (__uint_as_float(yq[2][j]) + __uint_as_float(yq[3][j])))
                               + ((__uint_as_float(yq[4][j]) + __uint_as_float(yq[5][j])) + __uint_as_float(yq[6][j]));
                auto rr = __builtin_amdgcn_permlane16_swap(__float_as_uint(s_), __float_as_uint(s_), false, false);
                ys[j] = rbf(__uint_as_float(rr[0]) + __uint_as_float(rr[1])) * sig; }
            char* yl = lds + 65536 + wave * 256;
            *(u32x2*)(yl + (lane >> 5) * 128 + (l15 >> 2) * 32 + (l15 & 1) * 16 + (l15 & 2) * 4) = u32x2{cvtpk(ys[0], ys[1]), cvtpk(ys[2], ys[3])};
            asm volatile("" ::: "memory");
            const int m = r0 + l15;
#pragma unroll
            for (int i = 0; i < 2; ++i) { const u32x4 yn = *(const u32x4*)(yl + (m == ri1 ? 0 : 128) + i * 64 + q4 * 16);
#pragma unroll
                for (int k = 0; k < 4; ++k) { const unsigned z_ = cvtpk(HALF(zn[i][k], 0) + HALF(yn[k], 0), HALF(zn[i][k], 1) + HALF(yn[k], 1));
                    zn[i][k] = m == ri1 || m == ri2 ? z_ : zn[i][k]; } }
        }
        float gg[2][8], xx[2][8];
#pragma unroll
        for (int i = 0; i < 2; ++i)
#pragma unroll
            for (int j = 0; j < 8; ++j) { gg[i][j] = HALF(zn[i][j >> 1], j); xx[i][j] = HALF(ea[2 * i + (j >> 2)][(j >> 1) & 1], j); PIN(gg[i][j]); PIN(xx[i][j]); }
        unsigned ovo = (unsigned)((l15 & 7) * DM + 32 * (l15 >> 3) + 8 * q4) * 2u; PIN(ovo);
        const char* ob = (const char*)(outp + (size_t)r0 * DM + ntc * 128 + 64 * ch);
        asm volatile("" : "+s"(ob));
        PH_STAMP(6);
        { unsigned c_, m_;
          asm volatile("s_waitcnt vmcnt(0)\n\ts_mov_b32 %7, 0x40000\n"
                       "1:\n\tv_cmp_ne_u64 vcc, %11, %6\n\ts_and_b32 vcc_lo, vcc_lo, 12\n\ts_cbranch_scc0 2f\n\t"
                       "global_load_dwordx2 %6, %12, %13 sc1\n\ts_waitcnt vmcnt(0)\n\ts_sub_u32 %7, %7, 1\n\ts_cbranch_scc0 1b\n"
                       "2:\n\tglobal_load_dwordx4 %1, %9, %10 offset:-1024 sc1\n\tglobal_load_dwordx4 %2, %9, %10 offset:1024 sc1\n\t"
                       "global_load_dwordx4 %4, %9, %10 sc1\n\tglobal_load_dwordx4 %5, %9, %10 offset:2048 sc1\n\t"
                       "global_load_dwordx4 %0, %9, %10 offset:-3072 sc1\n\tglobal_load_dwordx4 %3, %9, %10 offset:-2048 sc1"
                       : "=&v"(pv[0]), "=&v"(pv[1]), "=&v"(pv[2]), "=&v"(pv[3]), "=&v"(pv[4]), "=&v"(pv[5]), "+v"(cxf), "=&s"(c_), "=&v"(m_)
                       : "v"(xvo), "s"(pb + kp * 6144 + 3072), "s"(tag), "v"(tfo), "s"(tfb) : "memory", "vcc", "scc"); }
        S2T(3);
#pragma unroll
        for (int i = 0; i < 16; i += 2) { float g_[2];
#pragma unroll
            for (int e_ = 0; e_ < 2; ++e_) { float a_ = gg[i >> 3][(i & 7) + e_] * -1.4426950408889634f; PIN(a_); a_ = 1.f + __builtin_amdgcn_exp2f(a_); PIN(a_);
                g_[e_] = gg[i >> 3][(i & 7) + e_] * __builtin_amdgcn_rcpf(a_); PIN(g_[e_]); }
            const unsigned p_ = cvtpk(g_[0], g_[1]); gg[i >> 3][i & 7] = HALF(p_, 0); gg[i >> 3][(i & 7) + 1] = HALF(p_, 1); PIN(gg[i >> 3][i & 7]); PIN(gg[i >> 3][(i & 7) + 1]); }
        S2T(4);
        asm volatile("s_waitcnt vmcnt(0)" : "+v"(pv[0]), "+v"(pv[1]), "+v"(pv[2]), "+v"(pv[3]), "+v"(pv[4]), "+v"(pv[5]) :: "memory");
        S2T(5);
#pragma unroll 1
        for (int n_ = 0; n_ < 0x40000; ++n_) {
            const unsigned mx_ = max(max(max(pv[0][0], pv[0][1]), max(pv[0][2], pv[0][3])), max(max(pv[3][0], pv[3][1]), max(pv[3][2], pv[3][3])));
            if (__builtin_amdgcn_ballot_w64(mx_ == ~0u) == 0) break;
            asm volatile("global_load_dwordx4 %0, %2, %3 sc1\n\tglobal_load_dwordx4 %1, %2, %3 offset:1024 sc1\n\ts_waitcnt vmcnt(0)"
                         : "=&v"(pv[0]), "=&v"(pv[3]) : "v"(xvo), "s"(pb + kp * 6144) : "memory");
        }
        PH_STAMP(7); S2T(6);
        u32x4 ow[2];
#pragma unroll
        for (int i = 0; i < 2; ++i) {
            u32x4& ov = ow[i];
#pragma unroll
            for (int k = 0; k < 4; ++k) { float sv[2];
#pragma unroll
                for (int e_ = 0; e_ < 2; ++e_) sv[e_] = fmaf((acc[1][4 + 2 * i + (k >> 1)][2 * (k & 1) + e_] + HALF(pv[3 * i][k], e_)) + (HALF(pv[3 * i + 1][k], e_) + HALF(pv[3 * i + 2][k], e_)),
                                                             gg[i][2 * k + e_], xx[i][2 * k + e_]);
                ov[k] = cvtpk(sv[0], sv[1]); }
#pragma unroll
            for (int k = 0; k < 2; ++k) { auto rr = __builtin_amdgcn_permlane16_swap(ov[k], ov[2 + k], false, false); ov[k] = rr[0]; ov[2 + k] = rr[1]; }
        }
#pragma unroll
        for (int i = 0; i < 2; ++i) {
            u32x4 ov;
#pragma unroll
            for (int k = 0; k < 4; ++k) ov[k] = i ? __builtin_amdgcn_update_dpp(ow[1][k], ow[0][k], 0x128, 0xf, 0x3, false) : __builtin_amdgcn_update_dpp(ow[0][k], ow[1][k], 0x128, 0xf, 0xc, false);
            asm volatile("global_store_dwordx4 %0, %1, %2 sc1\n\ts_nop 1" :: "v"(ovo), "v"(ov), "s"(ob + i * (8 * DM * 2)) : "memory");
        }
        SST(7); SST_OUT();
        PH_STAMP(8);
        PH_STAMP(9);
    }
}

extern "C" void kernel_launch(void* const* d_in, const int* in_sizes, int n_in, void* d_out, int out_size, void* d_ws, size_t ws_size,
                              hipStream_t stream) {
    const u16* x    = (const u16*)d_in[0];
    const u16* ckv  = (const u16*)d_in[1];
    const u16* wq   = (const u16*)d_in[2];
    const u16* wuk  = (const u16*)d_in[3];
    const u16* wuv  = (const u16*)d_in[4];
    const u16* wo   = (const u16*)d_in[5];
    const u16* wr   = (const u16*)d_in[7];
    const u16* wg   = (const u16*)d_in[8];
    const u16* wu   = (const u16*)d_in[9];
    const u16* wd   = (const u16*)d_in[10];
    (void)in_sizes; (void)n_in; (void)out_size; (void)ws_size;
    (void)hipFuncSetAttribute((const void*)k_all, hipFuncAttributeMaxDynamicSharedMemorySize, 163840);
    k_all<<<256, 512, 163840, stream>>>(x, ckv, wuk, wuv, wr, (char*)d_ws + ((size_t)WSOFF << 20), wq, wo, wg, wu, wd, (u16*)d_out);
}
```

```cpp
#include <hip/hip_runtime.h>
#include <hip/hip_bf16.h>
#include <stdint.h>

typedef unsigned short u16;
typedef unsigned long long u64;
typedef short    bf16x8 __attribute__((ext_vector_type(8)));
typedef short    bf16x4 __attribute__((ext_vector_type(4)));
typedef float    f32x4  __attribute__((ext_vector_type(4)));
typedef float    f32x2  __attribute__((ext_vector_type(2)));
typedef __bf16   bfx2   __attribute__((ext_vector_type(2)));
typedef unsigned u32x4  __attribute__((ext_vector_type(4)));
typedef unsigned u32x2  __attribute__((ext_vector_type(2)));

#define DI __device__ __forceinline__

constexpr int SQ = 2048, DM = 1024, NH = 32, HD = 128, DC = 512, FF = 3584;

#define WSOFF 0
constexpr size_t WS_R    = 4u << 20;
constexpr size_t WS_FLAG = (4u << 20) + 65536;
constexpr size_t WS_GP   = 6u << 20;
constexpr size_t WS_YP   = 7u << 20;
constexpr size_t WS_PART = 96u << 20;
constexpr size_t WS_XF   = 16u << 20;
#define HSTR 1310720
constexpr size_t WS_KV   = 24u << 20;
constexpr size_t WS_CTX  = 72u << 20;
constexpr int F_EPOCH = 0, F_TOP = 1024, F_LN = 4096, F_KV = 8192, F_GU = 12288  , F_Y = 20480  , F_PARK = 524288  ,
              F_CTX = 65536  ;

#define PH_STAMP(i)
#define SST(i)
#define S2T(i)
#define SST_OUT()
#define ZST(i)
#define ZST_OUT()
DI unsigned cvtpk(float lo, float hi) { f32x2 v = {lo, hi}; bfx2 b = __builtin_convertvector(v, bfx2); return __builtin_bit_cast(unsigned, b); }
DI float rbf(float x) { return __uint_as_float(cvtpk(x, 0.f) << 16); }
#define DPP_ADD(v, ctrl, rmask) v += __uint_as_float(__builtin_amdgcn_update_dpp(0u, __float_as_uint(v), ctrl, rmask, 0xf, false))
DI unsigned tkey(unsigned fbits, int i) { unsigned u = fbits >> 16; u ^= (u & 0x8000u) ? 0xffffu : 0x8000u; return (u << 16) | (unsigned)(2047 - i); }
DI void merge_top2(unsigned& a1, unsigned& a2, unsigned b1, unsigned b2) { const unsigned lo = min(a1, b1); a1 = max(a1, b1); a2 = max(lo, max(a2, b2)); }
#define DPP_TOP2(a1, a2, ctrl, rmask) merge_top2(a1, a2, __builtin_amdgcn_update_dpp(0u, a1, ctrl, rmask, 0xf, false), __builtin_amdgcn_update_dpp(0u, a2, ctrl, rmask, 0xf, false))

#define GLDS_S(voff, sbase, ldsoff, imm)                                                                        \
    asm volatile("s_mov_b32 m0, %2\n\ts_nop 0\n\tglobal_load_lds_dwordx4 %0, %1 offset:%3"                       \
                 :: "v"(voff), "s"(sbase), "s"(ldsoff), "i"(imm) : "memory")
#define GLDS_C(voff, sbase, ldsoff, imm)                                                                        \
    asm volatile("s_mov_b32 m0, %2\n\ts_nop 0\n\tglobal_load_lds_dwordx4 %0, %1 offset:%3 sc1"                   \
                 :: "v"(voff), "s"(sbase), "s"(ldsoff), "i"(imm) : "memory")
#define LGKM0_BAR()    asm volatile("s_waitcnt lgkmcnt(0)\n\ts_barrier" ::: "memory")
#define SB() __builtin_amdgcn_sched_barrier(0)
#define CAT_(a, b) a##b
#define CAT(a, b) CAT_(a, b)
#define PIN(x) asm volatile("" : "+v"(x))
#define OADV(p_, d_) do { p_ += (d_); asm volatile("" : "+s"(p_)); } while (0)
#define VADD(d, a, b)    asm volatile("v_add_f32 %0, %1, %2" : "=v"(d) : "v"(a), "v"(b))
#define KEEP(x) asm volatile("" :: "v"(x))
#define MFA16(c, a, b) asm volatile("v_mfma_f32_16x16x32_bf16 %0, %1, %2, %0" : "+v"(c) : "v"(a), "v"(b))
#define MFA16Z(c, a, b) asm volatile("v_mfma_f32_16x16x32_bf16 %0, %1, %2, 0" : "=&v"(c) : "v"(a), "v"(b))
#define MFA16C(c, a, b, c0) asm volatile("v_mfma_f32_16x16x32_bf16 %0, %1, %2, %3" : "=&v"(c) : "v"(a), "v"(b), "v"(c0))
#define MF_DONE() do { SB(); asm volatile("s_nop 7\n\ts_nop 7"); SB(); } while (0)
#define ST16_SC1(p, v) asm volatile("global_store_dwordx4 %0, %1, off sc1\n\ts_nop 1" :: "v"(p), "v"(v) : "memory")
#define ST4_SC1(p, v)  asm volatile("global_store_dword %0, %1, off sc1" :: "v"(p), "v"(v) : "memory")
#define FLAG_PUT(p_, val_, on_) do { u64 sv_, tv_; unsigned tz_; const u64 v_ = (val_); const unsigned m_ = __builtin_amdgcn_readfirstlane((on_) ? 1u : 0u); \
        asm volatile("s_cmp_eq_u32 %5, 0\n\ts_cbranch_scc1 1f\n\tv_mov_b64 %1, %3\n\tv_mov_b32 %2, 0\n\ts_mov_b64 %0, exec\n\ts_mov_b64 exec, 1\n\t" \
                     "global_store_dwordx2 %2, %1, %4 sc1\n\ts_mov_b64 exec, %0\n1:"                                                      \
                     : "=&s"(sv_), "=&v"(tv_), "=&v"(tz_) : "s"(v_), "s"(p_), "s"(m_) : "memory", "scc"); } while (0)
#define FLAG_PUTZ(p_, val_, nz_) do { u64 sv_, tv_; unsigned tz_; const u64 v_ = (val_);                                                   \
        asm volatile("s_cmp_lg_u32 %5, 0\n\ts_cbranch_scc1 1f\n\tv_mov_b64 %1, %3\n\tv_mov_b32 %2, 0\n\ts_mov_b64 %0, exec\n\ts_mov_b64 exec, 1\n\t" \
                     "global_store_dwordx2 %2, %1, %4 sc1\n\ts_mov_b64 exec, %0\n1:"                                                      \
                     : "=&s"(sv_), "=&v"(tv_), "=&v"(tz_) : "s"(v_), "s"(p_), "s"(nz_) : "memory", "scc"); } while (0)
#define FLAG_GET(r_, p_) asm volatile("global_load_dwordx2 %0, %1, off sc1" : "=v"(r_) : "v"(p_) : "memory")
#define FLAG_WAIT(r_, p_, tag_, N) do { unsigned c_;                                                                                      \
        asm volatile("s_waitcnt vmcnt(%4)\n\ts_mov_b32 %1, 0x40000\n"                                                                     \
                     "1:\n\tv_cmp_ne_u64 vcc, %3, %0\n\ts_cmp_eq_u64 vcc, 0\n\ts_cbranch_scc1 2f\n\t"                                      \
                     "s_sleep 2\n\tglobal_load_dwordx2 %0, %2, off sc1\n\ts_waitcnt vmcnt(0)\n\ts_sub_u32 %1, %1, 1\n\ts_cbranch_scc0 1b\n"  \
                     "2:" : "+v"(r_), "=&s"(c_) : "v"(p_), "s"(tag_), "i"(N) : "memory", "vcc", "scc"); } while (0)
#define FLAG_WAITM(r_, p_, tag_, N, m_) do { unsigned c_;                                                                                 \
        asm volatile("s_waitcnt vmcnt(%4)\n\ts_mov_b32 %1, 0x40000\n"                                                                     \
                     "1:\n\tv_cmp_ne_u64 vcc, %3, %0\n\ts_and_b32 vcc_lo, vcc_lo, %5\n\ts_cbranch_scc0 2f\n\t"                             \
                     "s_sleep 2\n\tglobal_load_dwordx2 %0, %2, off sc1\n\ts_waitcnt vmcnt(0)\n\ts_sub_u32 %1, %1, 1\n\ts_cbranch_scc0 1b\n"  \
                     "2:" : "+v"(r_), "=&s"(c_) : "v"(p_), "s"(tag_), "i"(N), "s"(m_) : "memory", "vcc", "scc"); } while (0)
typedef __attribute__((address_space(3))) bf16x4 lds_bf16x4;
DI bf16x4 tr_rd(unsigned a) { return __builtin_amdgcn_ds_read_tr16_b64_v4i16((lds_bf16x4*)(uintptr_t)a); }
typedef __attribute__((address_space(3))) bf16x8 lds_bf16x8;
DI bf16x8 lds_rd(unsigned a) { return *(lds_bf16x8*)(uintptr_t)a; }
#define CAT8(lo_, hi_) bf16x8{lo_[0], lo_[1], lo_[2], lo_[3], hi_[0], hi_[1], hi_[2], hi_[3]}

#define POFF 32.f
#define LTHR 8192.f
DI float quad_max(float v) {
    auto r1 = __builtin_amdgcn_permlane16_swap(__float_as_uint(v), __float_as_uint(v), false, false);
    v = fmaxf(__uint_as_float(r1[0]), __uint_as_float(r1[1]));
    auto r2 = __builtin_amdgcn_permlane32_swap(__float_as_uint(v), __float_as_uint(v), false, false);
    return fmaxf(__uint_as_float(r2[0]), __uint_as_float(r2[1])); }
#define QWD 3
#define QS1 0xbfff
constexpr int q_xb(int s) { return s >= 1 && s <= 12; }
constexpr int q_xa(int s) { return s >= 0 && s <= 11; }
constexpr int q_s0(int s) { int n = 0; for (int i = 0; i < s; ++i) n += (QS1 >> i) & 1; return n; }
constexpr int q_kv(int kt, int f) { const int s = (kt - 13) * 16 + f - 12; return s >= 0 && s % 3 == 0 && s / 3 < 12 ? s / 3 : -1; }
constexpr int q_op(int s, int f) { return (f == 0 || f == 2 ? q_xb(s) : f == 8 || f == 10 ? q_xa(s) : f == 12 || f == 14 ? s + QWD < 16 : f == 3 ? s == 12
                                          : f == 4 ? (QS1 >> s) & 1 : 0) + (q_kv(s, f) >= 0); }
constexpr int q_after(int p, int kt) { int n = 0; for (int i = p + 1; i <= kt * 16 + 11; ++i) n += q_op(i >> 4, i & 15); return n; }
constexpr int q_min(int a, int b) { return a < b ? a : b; }
constexpr int q_wait(int kt) {
    int n = kt <= 2 ? 4 * (2 - kt) + q_after(-1, kt) : q_after((kt - 2) * 16 + 2, kt);
    if (kt + 1 >= QWD) n = q_min(n, q_after((kt + 1 - QWD) * 16 + 14, kt));
    if (kt == 13) n = q_min(n, q_after(12 * 16 + 3, kt));
    return n; }
constexpr int q_fl() { int s = 0; while (q_s0(s + 1) < 8) ++s; int kt = s; while (q_wait(kt) > q_after(s * 16 + 6, kt)) ++kt; return kt; }
constexpr int Q_NST = q_s0(16);
static_assert(Q_NST >= 8, "");
constexpr int o_xp(int st, int i) { return st <= 5 ? 64 + 4 * (4 * (st - 4) + i) : 32 * (st - 3) + (i == 0 ? 0 : i == 1 ? 4 : i == 2 ? 12 : 16); }
constexpr int o_wp(int st, int i) { return st <= 7 ? 16 * (st - 4) + 4 + 6 * i : 32 * (st - 5) + 24 + 4 * i; }
constexpr int o_x(int h) { for (int st = 4; st < 16; ++st) for (int i = 0; i < 4; ++i) if (o_xp(st, i) == h) return 4 * st + i; return -1; }
constexpr int o_w(int h) { for (int st = 4; st < 16; ++st) for (int i = 0; i < 2; ++i) if (o_wp(st, i) == h) return 2 * st + i; return -1; }
constexpr int o_after(int p, int e) { int n = 0; for (int h = p + 1; h < e; ++h) n += (o_x(h) >= 0) + (o_w(h) >= 0); return n; }
constexpr int o_wait(int kt) { return o_after(kt == 11 ? o_wp(15, 1) : o_xp(kt + 1, 3), 32 * kt + 24); }
constexpr int L_PFT = 149504, L_DUMP = 155648;
constexpr int CFS = 128;
constexpr unsigned F1S = 0xffff642fu, F1F = 0xffff5310u, F2S = 0xff64fff0u, F2F = 0xff53f21fu;
constexpr int F1T = 3;

__global__ __launch_bounds__(512, 2) void k_all(const u16* __restrict__ x, const u16* __restrict__ ckv, const u16* __restrict__ wuk, const u16* __restrict__ wuv,
                                                const u16* __restrict__ w_r, char* __restrict__ ws, const u16* __restrict__ wq, const u16* __restrict__ wo,
                                                const u16* __restrict__ w_gate, const u16* __restrict__ w_up,
                                                const u16* __restrict__ w_down, u16* __restrict__ out) {
    extern __shared__ __attribute__((aligned(16))) char lds[];
    const int b = blockIdx.x, xcd = b & 7, idx = b >> 3, head = xcd * 4 + (idx >> 3), jj = idx & 7;
    const int hp = head ^ 1;
    const unsigned ldsbase = (unsigned)(uintptr_t)(__attribute__((address_space(3))) char*)lds;
    const int wave = __builtin_amdgcn_readfirstlane(threadIdx.x >> 6);
    const int qbA = 15 - jj, qbB = jj;
#define KROW(g) (((2 * ((g) & 3) + ((g) >> 2)) * 8 + jj) * 32)
    const int qw = wave < 4 ? wave : 7 - wave, qb = wave < 4 ? qbA : qbB;
    bf16x8 qf[8];
    u64 tag;
    u16* outp = out;
    int ri1, ri2;
    PH_STAMP(0);
    SST(0);
    ZST(0);
    u32x4 pk[16];
    bf16x8 xr[4][2][2];
    u32x4 rv;
    const char* xg;
#define QXL1(j) asm volatile("global_load_dwordx4 %0, %1, %2 offset:%3 sc1" : "=v"(xr[(j) >> 2][(j) & 1][((j) >> 1) & 1]) : "v"(xvo), "s"(xg + (size_t)((j) >> 2) * 262144), \
                            "i"(((j) & 1) * 2048 + (((j) >> 1) & 1) * 1024) : "memory")
    const char* sb_[4];
    unsigned qa, qa2; bf16x4 ql[8], qh[8];
#define QFA(s, f) ((((f) & 1) ? qa2 : qa) + (unsigned)(((s) & 3) * 16384 + ((f) >> 3) * 8192 + (((f) >> 2) & 1) * 1024 + (((f) >> 1) & 1) * 512))
#define QRL(s, f) ql[(f) & 7] = tr_rd(QFA(s, f))
#define QRH(s, f) qh[(f) & 7] = tr_rd(QFA(s, f) + 2048u)
    unsigned xvo, qwo;
    const u16* wqb;
#define QWOFF() ({ unsigned l_ = threadIdx.x & 63; asm volatile("" : "+v"(l_)); (unsigned)(((l_ >> 2) & 7) * (NH * HD) + 32 * (l_ >> 5) + 8 * ((l_ & 3) ^ ((l_ >> 3) & 2))) * 2u; })
    {
        constexpr int LDW = NH * HD, K = DC;
        int tid = threadIdx.x; asm volatile("" : "+v"(tid));
        const int lane = tid & 63;
        const int wm = wave >> 2, wn = wave & 3;
        const int row = b * 8 + wave;
        u32x4 lx[2], lw[2]; u32x2 ep;
        asm volatile("global_load_dwordx4 %0, %5, %6\n\tglobal_load_dwordx4 %1, %5, %6 offset:1024\n\tglobal_load_dwordx4 %2, %5, %7\n\tglobal_load_dwordx4 %3, %5, %7 offset:1024\n\t"
                     "global_load_dwordx2 %4, %8, off sc1"
                     : "=&v"(lx[0]), "=&v"(lx[1]), "=&v"(lw[0]), "=&v"(lw[1]), "=&v"(ep) : "v"(lane * 16), "s"(x + (unsigned)(row * DM)), "s"(w_r), "v"(ws + WS_FLAG + F_EPOCH) : "memory");
#define XKEY(r) (((r) >> 1) & 7)
        unsigned xoff[4], woff[2];
        { const unsigned x0_ = (unsigned)(KROW(wave) + (lane >> 3)) * (K * 2) + (unsigned)((lane & 7) ^ (lane >> 4)) * 16u;
          xoff[0] = x0_; xoff[1] = (x0_ ^ 64u) + 7168u; xoff[2] = x0_ + 14336u; xoff[3] = xoff[1] + 14336u;
          woff[1] = (unsigned)((8 * wave + ((lane >> 2) & 7)) * LDW + hp * HD + 32 * (lane >> 5) + 8 * ((lane & 3) ^ (2 * (wave & 1)))) * 2u; woff[0] = woff[1] + 896u; }
        const char* xsp = (const char*)ckv;
        const char* wsp = (const char*)wuk - 896;
        const char* wsv = (const char*)wuv - 2944;
        const unsigned m0b = ldsbase + wave * 4096;
#define KM0(g, w_) asm volatile("s_add_u32 m0, %0, %1" :: "s"(m0b), "i"(((g) & 1) * 65536 + (w_) * 32768) : "memory", "scc")
#define KRX(i) asm volatile("global_load_lds_dwordx4 %0, %1 offset:%2" :: "v"(xoff[i]), "s"(xsp), "i"((i) * 1024) : "memory")
#define KRW(i) asm volatile("global_load_lds_dwordx4 %0, %1 offset:%2 nt" :: "v"(woff[(i) & 1]), "s"((i) < 2 ? wsp : wsv), "i"((i) * 1024) : "memory")
#define STAGE(g) do { KM0(g, 0); asm volatile("s_nop 0"); KRX(0); KRX(1); KRX(2); KRX(3); KM0(g, 1); asm volatile("s_nop 0"); KRW(0); KRW(1); KRW(2); KRW(3); \
                      OADV(xsp, 128); OADV(wsp, 64 * LDW * 2); OADV(wsv, 64 * LDW * 2); } while (0)
#define KST(i)
        const unsigned pfl = ldsbase + L_DUMP + wave * 1024;
#define PFX(g) do { unsigned l_ = threadIdx.x & 63; asm volatile("" : "+v"(l_)); l_ = ((l_ >> 3) * DC + (l_ & 7) * 8) * 2u; GLDS_S(l_, pfxp + (g) * 64, pfl, 0); } while (0)
#define PFQ(i) do { unsigned l_ = threadIdx.x & 63; asm volatile("" : "+v"(l_)); l_ = ((l_ >> 4) * LDW + (l_ & 15) * 8) * 2u; GLDS_S(l_, pfqp + (size_t)(4 * (i)) * LDW, pfl, 0); } while (0)
        STAGE(0);
        SB(); ZST(1);
        int w2 = wave; asm volatile("" : "+s"(w2));
        xg = ws + WS_XF + (unsigned)((w2 < 4 ? qbA * 4 + w2 : qbB * 4 + 7 - w2) * 4096);
        wqb = wq + (unsigned)(8 * w2 * LDW + head * HD);
        const u16* pfqp = wq + (unsigned)((128 * jj + 16 * w2) * LDW + head * HD);
        const u16* pfxp = ckv + (unsigned)((KROW(w2) + 8 * (head & 3)) * DC);
        unsigned xa[2][2], wa[2][2];
        const int l15 = lane & 15, q4 = lane >> 4;
#pragma unroll
        for (int s = 0; s < 2; ++s) {
            const int r0 = l15;
            xa[0][s] = ldsbase + (unsigned)((wm * 128 + r0) * 128 + ((((4 * s + q4) ^ XKEY(r0))) << 4));
            wa[0][s] = (ldsbase + 32768u + (unsigned)(q4 * 4096 + wn * 512 + (l15 >> 2) * 64 + (q4 & 1) * 32 + (lane & 3) * 8)) ^ (32u * s);
            xa[1][s] = xa[0][s] + 65536u; wa[1][s] = wa[0][s] + 65536u;
            PIN(xa[0][s]); PIN(xa[1][s]); PIN(wa[0][s]); PIN(wa[1][s]); }
        u64 pf0;
        { u64 pw_ = (u64)(((w2 & 2) ? wuv : wuk) + (unsigned)((128 + 192 * (w2 & 1) + jj) * LDW + hp * HD)), pc_ = (u64)ckv + (unsigned)(b * 8192 + (w2 & 3) * 2048);
          asm volatile("" : "+s"(pw_), "+s"(pc_));
          unsigned o0_ = (unsigned)(lane >> 1) * (8 * LDW * 2) + (lane & 1) * 128u, o1_ = (lane & 15) * 128u; PIN(o0_); PIN(o1_);
          pf0 = (lane < 48 ? pw_ : pc_) + (lane < 48 ? o0_ : o1_); PIN(pf0); }
        asm volatile("" : "+s"(pfqp), "+s"(pfxp), "+s"(xg), "+s"(wqb), "+s"(outp));
        SB();
        asm volatile("s_waitcnt vmcnt(8)" : "+v"(lx[0]), "+v"(lx[1]), "+v"(lw[0]), "+v"(lw[1]), "+v"(ep) :: "memory");
        KST(1); ZST(2);
#define HALF(v_, i) __uint_as_float(((i) & 1) ? (v_) & 0xffff0000u : (v_) << 16)
#define BOTH(v_) f32x2{HALF(v_, 0), HALF(v_, 1)}
        {
            f32x2 v[8];
#pragma unroll
            for (int i = 0; i < 8; ++i) v[i] = BOTH(lx[i >> 2][i & 3]);
            const float pv_ = __uint_as_float(__builtin_amdgcn_readfirstlane(__float_as_uint(v[0][0])));
            f32x2 s2 = f32x2{0.f, 0.f}, q2 = f32x2{0.f, 0.f};
#pragma unroll
            for (int i = 0; i < 8; ++i) { const f32x2 d = v[i] - pv_; s2 += d; q2 += d * d; }
            float s = s2[0] + s2[1], q = q2[0] + q2[1];
            DPP_ADD(s, 0x111, 0xf); DPP_ADD(q, 0x111, 0xf); DPP_ADD(s, 0x112, 0xf); DPP_ADD(q, 0x112, 0xf); DPP_ADD(s, 0x114, 0xf); DPP_ADD(q, 0x114, 0xf);
            DPP_ADD(s, 0x118, 0xf); DPP_ADD(q, 0x118, 0xf); DPP_ADD(s, 0x142, 0xa); DPP_ADD(q, 0x142, 0xa); DPP_ADD(s, 0x143, 0xc); DPP_ADD(q, 0x143, 0xc);
            const float dm = __uint_as_float(__builtin_amdgcn_readlane(__float_as_uint(s), 63)) * (1.f / DM);
            const float var_bf = rbf(fmaxf(fmaf(-dm, dm, __uint_as_float(__builtin_amdgcn_readlane(__float_as_uint(q), 63)) * (1.f / DM)), 0.f));
            const float mean_bf = rbf(pv_ + dm), den = rbf(__builtin_amdgcn_sqrtf(rbf(var_bf + 1e-5f))), rden = __builtin_amdgcn_rcpf(den);
            u32x4 xw[2]; float r2[2] = {0.f, 0.f};
#pragma unroll
            for (int i = 0; i < 8; ++i) {
                const f32x2 c_ = v[i] - mean_bf;
                const unsigned cp = cvtpk(c_[0], c_[1]);
                const f32x2 cb = BOTH(cp);
                f32x2 n_ = cb * rden;
                n_ += (cb - n_ * den) * rden;
                const unsigned np = cvtpk(n_[0], n_[1]);
                const unsigned wp = lw[i >> 2][i & 3];
                r2[i & 1] = __builtin_amdgcn_fdot2_f32_bf16(__builtin_bit_cast(bfx2, np), __builtin_bit_cast(bfx2, wp), r2[i & 1], false);
                xw[i >> 2][i & 3] = np;
            }
            char* lr = lds + 131072 + wave * 2064 + lane * 16;
            *(u32x4*)lr = xw[0]; *(u32x4*)(lr + 1024) = xw[1];
            s = r2[0] + r2[1];
            DPP_ADD(s, 0x111, 0xf); DPP_ADD(s, 0x112, 0xf); DPP_ADD(s, 0x114, 0xf); DPP_ADD(s, 0x118, 0xf); DPP_ADD(s, 0x142, 0xa); DPP_ADD(s, 0x143, 0xc);
            if (lane == 63) *(float*)(lds + 131072 + wave * 2064 + 2048) = rbf(s);
        }
        SB(); ZST(3);
        STAGE(1);
        asm volatile("s_bitcmp0_b32 %2, 2\n\ts_cbranch_scc1 1f\n\ts_mov_b32 m0, %1\n\ts_nop 0\n\tglobal_load_lds_dword %0, off\n1:" :: "v"(pf0), "s"(pfl), "s"(wave) : "memory", "scc");
        tag = (((u64)(unsigned)__builtin_amdgcn_readfirstlane(ep[1]) << 32) | (unsigned)__builtin_amdgcn_readfirstlane(ep[0])) + 0x9E3779B97F4A7C15ull;
        float rr;
        const unsigned l16 = (unsigned)lane * 16u;
        u32x2 xi_[4];
#define XI_RD() do { int t_ = threadIdx.x; asm volatile("" : "+v"(t_)); const int r8 = t_ & 7, pc = t_ >> 3;                               \
            const char* lr = lds + 131072 + r8 * 2064 + (pc >> 2) * 64 + ((pc >> 1) & 1) * 32 + (pc & 1) * 8;                              \
            xi_[0] = *(const u32x2*)lr; xi_[1] = *(const u32x2*)(lr + 16); xi_[2] = *(const u32x2*)(lr + 1024); xi_[3] = *(const u32x2*)(lr + 1040); \
            rr = *(const float*)(lds + 131072 + wave * 2064 + 2048); } while (0)
#define XI_ST() do { int t_ = threadIdx.x; asm volatile("" : "+v"(t_)); const int r8 = t_ & 7, pc = t_ >> 3;                               \
            char* dst = ws + WS_XF + (size_t)(pc >> 3) * 262144 + (b >> 1) * 2048 + (pc & 7) * 256 + ((b & 1) * 8 + r8) * 16;              \
            const u32x4 pa = u32x4{xi_[0][0], xi_[0][1], xi_[1][0], xi_[1][1]}, pb = u32x4{xi_[2][0], xi_[2][1], xi_[3][0], xi_[3][1]};    \
            ST16_SC1(dst, pa); ST16_SC1(dst + 2097152, pb); } while (0)
        SB(); ZST(4);
        asm volatile("s_waitcnt vmcnt(%1)\n\ts_bitcmp1_b32 %0, 2\n\ts_cbranch_scc1 1f\n\ts_waitcnt vmcnt(8)\n1:\n\ts_barrier" :: "s"(wave), "i"(9) : "memory", "scc");
        KST(2); ZST(6);
        f32x4 a16[8][4];
        bf16x8 xf[2][4];
        bf16x4 wl[4], wh[4];
#define KXR(kt, q, mb) xf[(q) & 1][mb] = lds_rd(xa[(kt) & 1][(q) >> 1] + (unsigned)((((q) & 1) * 4 + (mb)) * 2048))
#define KWL(kt, s32, nb) wl[nb] = tr_rd(wa[(kt) & 1][(nb) & 1] + (unsigned)((s32) * 16384 + ((nb) >> 1) * 2048))
#define KWH(kt, s32, nb) wh[nb] = tr_rd(wa[(kt) & 1][(nb) & 1] + (unsigned)((s32) * 16384 + ((nb) >> 1) * 2048 + 256))
#define KMF(kt, q, g) do { const bf16x8 w_ = CAT8(wl[(g) >> 2], wh[(g) >> 2]); constexpr int m_ = ((q) & 1) * 4 + ((g) & 3);               \
            if ((kt) == 0 && (q) < 2) { if ((g) >= 8) MFA16Z(a16[m_][(g) >> 2], xf[(q) & 1][(g) & 3], w_); else MFA16Z(a16[m_][(g) >> 2], w_, xf[(q) & 1][(g) & 3]); } \
            else                      { if ((g) >= 8) MFA16(a16[m_][(g) >> 2], xf[(q) & 1][(g) & 3], w_);  else MFA16(a16[m_][(g) >> 2], w_, xf[(q) & 1][(g) & 3]); } } while (0)
        u64 lnf;
#define LNFP (ws + WS_FLAG + F_LN + (wave * 32 + (lane & 31)) * 8)
#define KRQ(i) do { GLDS_S(qwo, wqb + (size_t)(((i) >> 1) * 64) * LDW, ldsbase + wave * 2048 + ((i) >> 1) * 16384 + ((i) & 1) * 896, ((i) & 1) * 128); } while (0)
        constexpr int KRS = 3, KPFD = 3;
#define KG(kt, q, g) do { constexpr int nk_ = (kt) + ((q) == 3), nq_ = ((q) + 1) & 3;                                                     \
            constexpr int xi = ((q) & 1) ? ((g) == 1 ? 0 : (g) == 2 ? 1 : (g) == 5 ? 2 : (g) == 6 ? 3 : -1) : (((g) & 1) && (g) < 8 ? (g) >> 1 : -1); \
            constexpr int ri = (q) == 3 ? ((g) == 9 ? 0 : (g) == 10 ? 1 : (g) == 13 ? 2 : (g) == 14 ? 3 : -1) : (((g) & 1) && (g) > 8 ? ((g) - 9) >> 1 : -1); \
            KMF(kt, q, g); SB();                                                                                                        \
            if (xi >= 0 && nk_ < 8) KXR(nk_, nq_, xi & 3);                                                                              \
            if (((q) & 1) && nk_ < 8) { if (((g) & 3) == 3) KWL(nk_, nq_ >> 1, (g) >> 2); if (((g) & 3) == 0 && (g) > 0) KWH(nk_, nq_ >> 1, (((g) >> 2) + 3) & 3); } \
            if (!((q) & 1) && (g) == 0 && (kt) + (q) > 0) KWH(kt, (q) >> 1, 3);                                                          \
            { constexpr int G = (q) == 3 ? (g) : (q) == 0 ? 16 + (g) : -9, T = (kt) + ((q) == 3 ? 2 : 1);          \
              if (T >= 2 && T < 8) { if (G == 0) KM0(T, 0); if (G == 4 * KRS) KM0(T, 1);                                                 \
                  if (G >= 1 && (G - 1) % KRS == 0 && (G - 1) / KRS < 8) { if ((G - 1) / KRS < 4) KRX(((G - 1) / KRS) & 3); else KRW(((G - 1) / KRS) & 3); } \
                  if (G == 29) OADV(xsp, 128); if (G == 30) OADV(wsp, 64 * LDW * 2); if (G == 31) OADV(wsv, 64 * LDW * 2); } }                                                 \
            if ((q) == 1 && (kt) + KPFD < 8) { if ((kt) >= 1 && (g) == 13) PFX((kt) + KPFD); }                \
            if ((q) == 2 && (kt) == 6 && (g) == 9) { qwo = QWOFF(); PIN(qwo); }                                                         \
            if ((q) == 1 && (kt) == 7) { if ((g) == 9) asm volatile("global_load_dwordx4 %0, %1, %2 sc1" : "=v"(rv) : "v"(xvo), "s"(ws + WS_R + wave * 1024) : "memory"); \
                                         if ((g) == 10) QXL1(0); if ((g) == 13) QXL1(1); if ((g) == 14) QXL1(2); }                      \
            if ((q) == 2 && (kt) == 7 && (g) == 2) QXL1(3);                                                                             \
            if ((q) == 2 && (kt) == 7 && (g) == 9) { int w_ = wave; asm volatile("" : "+s"(w_));                                        \
                sb_[0] = ws + WS_KV + (unsigned)(hp * HSTR + ((w_ >> 2) * 2 + (jj >> 2)) * 65536 + (jj & 3) * 8192 + (w_ & 3) * 2048); asm volatile("" : "+s"(sb_[0])); } \
            if ((q) == 2 && (kt) == 7 && (g) >= 11 && (g) < 14) { sb_[((g) - 10) & 3] = sb_[0] + ((g) - 10) * 262144; asm volatile("" : "+s"(sb_[((g) - 10) & 3])); } \
            if ((q) == 2 && (kt) == 7 && (g) == 14) { qa = ldsbase + (unsigned)((q4 >> 1) * 4096 + (4 * (q4 & 1) + (l15 >> 2)) * 64 + (q4 & 1) * 32 + (lane & 3) * 8); PIN(qa); } \
            if ((q) == 2 && (kt) == 7 && (g) == 15) { qa2 = qa ^ 32u; PIN(qa2); }                                                                              \
            if ((q) == 3 && (kt) == 6 && ri >= 0) KRQ(ri & 3);                                                                          \
            if ((q) == 0 && (kt) == 7 && ri >= 0 && 4 + ri < 2 * QWD) KRQ(4 + (ri & 3));                                                                    \
            if ((q) == 1 && (kt) == 1) { if ((g) == 8) XI_RD();                                                                       \
                if ((g) == 13) ST4_SC1((float*)(ws + WS_R) + row, rr);                               \
                if ((g) == 15) XI_ST(); }                                                                                               \
            if ((q) == 1 && ((kt) == 3 || (kt) == 4)) { if ((g) == 9) PFQ((2 * (kt) - 6) & 3); if ((g) == 11) PFQ((2 * (kt) - 5) & 3); }                                                    \
            if ((q) == 2 && (kt) == 5 && (g) == 12) FLAG_GET(lnf, LNFP);                                                                \
            SB(); } while (0)
#define KQ(kt, q) do { KG(kt, q, 0); KG(kt, q, 1); KG(kt, q, 2); KG(kt, q, 3); KG(kt, q, 4); KG(kt, q, 5); KG(kt, q, 6); KG(kt, q, 7);    \
                       KG(kt, q, 8); KG(kt, q, 9); KG(kt, q, 10); KG(kt, q, 11); KG(kt, q, 12); KG(kt, q, 13); KG(kt, q, 14); KG(kt, q, 15); } while (0)
#define KEXT(s) (((s) == 1 ? 3 : (s) == 5 ? 1 : 0) + ((s) >= 1 && (s) + KPFD < 8) + ((s) == 3 || (s) == 4 ? 2 : 0))
#define KSTEP(kt) do {                                                                                                                  \
        KQ(kt, 0); KQ(kt, 1); KQ(kt, 2);                                                                                                \
        if ((kt) == 6) FLAG_WAIT(lnf, LNFP, tag, 0);                                   \
                       \
        if ((kt) == 0) asm volatile("s_waitcnt vmcnt(%1) lgkmcnt(0)\n\ts_bitcmp1_b32 %0, 2\n\ts_cbranch_scc1 1f\n\ts_waitcnt vmcnt(%2)\n1:\n\ts_barrier" :: "s"(wave), "i"(KEXT(0) + 1), "i"(KEXT(0)) : "memory", "scc"); \
        else asm volatile("s_waitcnt vmcnt(%0) lgkmcnt(0)\n\ts_barrier" :: "i"(KEXT(kt)) : "memory");                                        \
        KST((kt) + 3);                                                                                                                  \
        if ((kt) == 2) FLAG_PUT(ws + WS_FLAG + F_LN + b * 8, tag, wave == 0);             \
        if ((kt) == 6) FLAG_PUT(ws + WS_FLAG + F_EPOCH, tag - 0x9E3779B97F4A7C15ull + 1, b == 0 && wave == 0);     \
        SB();                                                                                                                           \
        KQ(kt, 3);                                                                                                                      \
    } while (0)
        KXR(0, 0, 0); KXR(0, 0, 1); KWL(0, 0, 0); KWH(0, 0, 0); KXR(0, 0, 2); KXR(0, 0, 3);
        KWL(0, 0, 1); KWH(0, 0, 1); KWL(0, 0, 2); KWH(0, 0, 2); KWL(0, 0, 3); KWH(0, 0, 3);
        SB();
        KSTEP(0); ZST(7); KSTEP(1); KSTEP(2); KSTEP(3); KSTEP(4); KSTEP(5); KSTEP(6);
        xvo = l16;
        KQ(7, 0); KQ(7, 1); KQ(7, 2);
        if (wave < 4) {
            int t2_ = threadIdx.x; asm volatile("" : "+v"(t2_));
            unsigned l7_ = t2_ & 7, c7_ = jj >= 3 ? 7 : 8, b0_ = (unsigned)(((jj + 7) & 7) * 128 + head * 4 + (wave & 3)), b7_ = 3584u + head * 4 + (wave & 3);
            u64 pg_ = (u64)((wave & 1) ? w_up : w_gate), pd_ = (u64)w_down - 1792ull * 8192, po_ = (u64)wo - 2688ull * 8192;
            asm volatile("" : "+s"(c7_), "+s"(b0_), "+s"(b7_), "+s"(pg_), "+s"(pd_), "+s"(po_));
            const unsigned lt_ = l7_ < (unsigned)jj ? l7_ : 0;
            unsigned n_ = b0_ + (13u - lt_) * lt_ * 64u; n_ = l7_ == c7_ ? b7_ : n_;
            u64 p_ = n_ < 2688u ? pd_ : po_; p_ = n_ < 1792u ? pg_ : p_; n_ = n_ < 1792u ? n_ >> 1 : n_;
            *(u64*)(lds + L_PFT + t2_ * 8) = p_ + (u64)n_ * 8192u;
        }
        SB();
        asm volatile("s_waitcnt vmcnt(%0)\n\ts_barrier" :: "i"(2 * QWD + 1) : "memory");
        SB();
#define PKCV(i) do { const f32x4 a_ = ((i) & 2) ? a16[2 * ((i) >> 2)][(i) & 3] : a16[2 * ((i) >> 2) + ((i) & 1)][0];                       \
                     const f32x4 b_ = ((i) & 2) ? a16[2 * ((i) >> 2) + 1][(i) & 3] : a16[2 * ((i) >> 2) + ((i) & 1)][1];                   \
                     pk[i] = u32x4{cvtpk(a_[0], a_[1]), cvtpk(a_[2], a_[3]), cvtpk(b_[0], b_[1]), cvtpk(b_[2], b_[3])}; PIN(pk[i]); } while (0)
#define KL(i) do { KMF(7, 3, 2 * (i)); SB(); if ((i) >= 2) QXL1(2 * (i)); SB(); KMF(7, 3, 2 * (i) + 1); SB(); PKCV(i); if ((i) >= 2) QXL1(2 * (i) + 1); SB(); } while (0)
        KL(0); KL(1); KL(2); KL(3); KL(4); KL(5); KL(6); KL(7);
        KST(10);
        QRL(0, 0); QRH(0, 0); QRL(0, 1); QRH(0, 1); QRL(0, 2); QRH(0, 2); QRL(0, 3); QRH(0, 3);
        SB(); asm volatile("s_nop 3"); SB();
#pragma unroll
        for (int i = 8; i < 16; ++i) PKCV(i);
        SB();
        KST(11); ZST_OUT();
#undef PKCV
#undef KSTEP
#undef PFQ
#undef XKEY
    }
    PH_STAMP(1);
    const char* kvg = ws + WS_KV + (size_t)head * HSTR;
    const unsigned svv = xvo + 32768u;
    u64 pft; bf16x8 ones; unsigned ak, av;
    const int NA = qbA + 1;
    {
        constexpr int LDW = NH * HD, STG = 16384;
        int tid = threadIdx.x; asm volatile("" : "+v"(tid));
        const int lane = tid & 63;
        const unsigned woff = qwo, xvo2 = xvo + 262144u;
        f32x4 acc[2][8];
#define KVST(i) asm volatile("global_store_dwordx4 %0, %1, %2 offset:%3 sc1" :: "v"(((i) & 2) ? svv : xvo), "v"(pk[i]), "s"(sb_[(i) >> 2]), "i"(((i) & 1) * 1024) : "memory")
        u64 kvf = 0;
#define QKV(kt, f) do { constexpr int g_ = q_kv(kt, f), p_ = g_ >> 2; if (g_ >= 0) {                                                       \
            if ((g_ & 3) == 0) asm volatile("s_mov_b32 m0, %0\n\ts_nop 0" :: "s"(ldsbase + (p_ == 1 ? 0 : 65536 + p_ * 16384) + wave * 4096) : "memory"); \
            asm volatile("global_load_lds_dwordx4 %0, %1 offset:%2 sc1" :: "v"(xvo), "s"(kvg + p_ * 32768 + wave * 4096), "i"((g_ & 3) * 1024) : "memory"); } } while (0)
#define KVFP (ws + WS_FLAG + ((lane & 8) ? F_TOP : F_KV + 2048 + (head * 8 + (lane & 7)) * 8))
        const bool top2 = b == 56;
        unsigned* tw = (unsigned*)(lds + 131072 + 2048);
#define QMF(a, kt, f) do { if ((kt) == 0 && (f) < 8) MFA16Z(acc[a][(f) & 7], w_, xr[0][a][0]); else MFA16(acc[a][(f) & 7], w_, xr[(kt) & 3][a][(f) >> 3]); } while (0)
#define ZP0 238
#define QPAIR(kt, f, A, B) do { const bf16x8 w_ = CAT8(ql[(f) & 7], qh[(f) & 7]); constexpr int s_ = (kt) + (((f) + 4) >> 4), m_ = ((f) + 4) & 15;  \
            QMF(0, kt, f); SB(); if (s_ < 16) QRL(s_, m_); A; QKV(kt, f); SB();                                      \
            QMF(1, kt, f); SB(); if (s_ < 16) QRH(s_, m_); B;                                                                             \
            { constexpr int z_ = (kt) * 16 + (f) - ZP0;                    \
                                                                    \
              if (z_ == 8) { pft = *(const u64*)(lds + L_PFT + (tid & 255) * 8); }                                                       \
              if (z_ == 15) { av = ldsbase + xvo; ak = av + 65536u; PIN(av); PIN(ak); }                                                   \
              if (z_ == 16) { const unsigned o1 = (lane & 3) ? 0u : 0x3f803f80u; ones = __builtin_bit_cast(bf16x8, u32x4{o1, o1, o1, o1}); PIN(ones); }     \
              if (z_ == 14) PIN(pft); } SB(); if (((f) & 1) && s_ < 16) { __builtin_amdgcn_s_waitcnt(0xC47F); SB(); } } while (0)
        const char* xsp = xg + 3 * 262144;
#define QX(r, rb, ks, v_) asm volatile("global_load_dwordx4 %0, %1, %2 offset:%3 sc1" : "+v"(xr[r][rb][ks]) : "v"(v_), "s"(xsp), "i"((rb) * 2048 + (ks) * 1024) : "memory")
        const unsigned woff1 = woff + 1024u, woff2 = woff + 128u;
        const char* wsp = (const char*)(wqb + (size_t)QWD * 64 * LDW) - 1024;
        const unsigned m0b = ldsbase + wave * 2048;
#define QM0(r) asm volatile("s_add_u32 m0, %0, %1" :: "s"(m0b), "i"((r) * STG) : "memory", "scc")
#define QW1(v_, imm_) asm volatile("global_load_lds_dwordx4 %0, %1 offset:%2" :: "v"(v_), "s"(wsp), "i"(imm_) : "memory")
#define QST(i)
#define QSTEP(kt) do { constexpr int r_ = (kt) & 3;                                                                                     \
        QPAIR(kt, 0, if (q_xb(kt)) QX((r_ + 3) & 3, 0, 1, xvo), );                                                                      \
        QPAIR(kt, 1, , );                                                                                                               \
        QPAIR(kt, 2, if (q_xb(kt)) QX((r_ + 3) & 3, 1, 1, xvo), );                                                                      \
        QPAIR(kt, 3, if ((kt) == 12) FLAG_GET(kvf, KVFP), );                                                                            \
        if ((kt) == 0 && __builtin_expect(top2, 0)) {                                                                                                        \
            unsigned t1 = tkey(rv[0], tid * 4), t2 = 0;                                                                                 \
            _Pragma("unroll") for (int j = 1; j < 4; ++j) merge_top2(t1, t2, tkey(rv[j], tid * 4 + j), 0);                              \
            DPP_TOP2(t1, t2, 0x111, 0xf); DPP_TOP2(t1, t2, 0x112, 0xf); DPP_TOP2(t1, t2, 0x114, 0xf); DPP_TOP2(t1, t2, 0x118, 0xf);     \
            DPP_TOP2(t1, t2, 0x142, 0xa); DPP_TOP2(t1, t2, 0x143, 0xc);                                                                 \
            if (lane == 63) { tw[wave * 2] = t1; tw[wave * 2 + 1] = t2; } SB(); }                                                       \
        QPAIR(kt, 4, if ((QS1 >> (kt)) & 1) KVST(q_s0(kt) & 15), if ((QS1 >> (kt)) & 1) KEEP(pk[q_s0(kt) & 15]));                       \
        QPAIR(kt, 5, , );                                                                                                               \
        QPAIR(kt, 6, , );                                                                                                               \
        QPAIR(kt, 7, , );                                                                                                               \
        QPAIR(kt, 8, if (q_xa(kt)) QX(r_, 0, 0, xvo2), );                                                                               \
        QPAIR(kt, 9, , );                                                                                                               \
        QPAIR(kt, 10, if (q_xa(kt)) QX(r_, 1, 0, xvo2), if ((kt) <= 11) OADV(xsp, 262144));                                             \
        QPAIR(kt, 11, if ((kt) + QWD < 16) QM0((r_ + QWD) & 3), );                                                                      \
        if ((kt) < 15) {                              \
            if (QWD == 4 || (kt) == 0 || (kt) >= 13) asm volatile("s_waitcnt vmcnt(%0) lgkmcnt(0)\n\ts_barrier" :: "i"(q_wait(kt)) : "memory"); \
            else asm volatile("s_waitcnt vmcnt(%0)\n\ts_barrier" :: "i"(q_wait(kt)) : "memory");                                        \
            if ((kt) == 13) { PIN(kvf); const unsigned sh_ = (lane & 8) ? 22u : 0u; const u64 ex_ = (tag << sh_) >> sh_;     \
                if (__builtin_expect(__builtin_amdgcn_ballot_w64((kvf >> sh_) != ex_) != 0, 0)) { unsigned c_; u64 t_;                    \
                    asm volatile("s_mov_b32 %1, 0x40000\n1:\n\ts_sleep 2\n\tglobal_load_dwordx2 %0, %3, off sc1\n\ts_waitcnt vmcnt(0)\n\tv_lshrrev_b64 %2, %5, %0\n\t" \
                                 "v_cmp_ne_u64 vcc, %4, %2\n\ts_cmp_eq_u64 vcc, 0\n\ts_cbranch_scc1 2f\n\ts_sub_u32 %1, %1, 1\n\ts_cbranch_scc0 1b\n2:" \
                                 : "+v"(kvf), "=&s"(c_), "=&v"(t_) : "v"(KVFP), "v"(ex_), "v"(sh_) : "memory", "vcc", "scc"); }           \
                const unsigned r_ = __builtin_amdgcn_readlane((unsigned)kvf, 8); ri1 = r_ & 2047u; ri2 = (r_ >> 11) & 2047u; }           \
            QST((kt) + 1); SB(); }                                                                                                      \
        QPAIR(kt, 12, if ((kt) + QWD < 16) QW1(woff1, 0), );                                                                            \
        QPAIR(kt, 13, , );                                                                                                              \
        QPAIR(kt, 14, if ((kt) + QWD < 16) QW1(woff2, 1024), if ((kt) + QWD < 15) OADV(wsp, 64 * LDW * 2));                             \
        if ((kt) == q_fl()) FLAG_PUT(ws + WS_FLAG + F_KV + 2048 + (hp * 8 + jj) * 8, tag, wave == 0);     \
        QPAIR(kt, 15, , );                                                                                                              \
        if ((kt) == 0 && __builtin_expect(top2, 0)) {                                                                                                        \
            unsigned t1 = tw[0], t2 = tw[1];                                                                                            \
            _Pragma("unroll") for (int w = 1; w < 8; ++w) merge_top2(t1, t2, tw[w * 2], tw[w * 2 + 1]);                                 \
            FLAG_PUT(ws + WS_FLAG + F_TOP, (tag << 22) | (unsigned)__builtin_amdgcn_readfirstlane((~t1 & 2047u) | (~t2 & 2047u) << 11), wave == 0); \
            } \
        SB();                                                                                                                           \
    } while (0)
        asm volatile("s_waitcnt vmcnt(12)" : "+v"(rv) :: "memory");
        SB();
        QST(0);
        KST(12);
        QSTEP(0); QSTEP(1); QSTEP(2); QSTEP(3); QSTEP(4); QSTEP(5); QSTEP(6); QSTEP(7);
        QSTEP(8); QSTEP(9); QSTEP(10); QSTEP(11); QSTEP(12); QSTEP(13); QSTEP(14); QSTEP(15);
        MF_DONE();
        QST(16);
        {
            constexpr float qs = 0.08838834764831845f * 1.4426950408889634f;
#pragma unroll
            for (int i = 0; i < 8; ++i) { const f32x4 a_ = acc[i & 1][i & 6] * qs, b_ = acc[i & 1][i | 1] * qs;
                qf[i] = __builtin_bit_cast(bf16x8, u32x4{cvtpk(a_[0], a_[1]), cvtpk(a_[2], a_[3]), cvtpk(b_[0], b_[1]), cvtpk(b_[2], b_[3])}); PIN(qf[i]); }
        }
#undef QSTEP
    }
    PH_STAMP(2);
    const int kz = xcd & 1, kp = xcd >> 1, nt = kz * 4 + (idx >> 3), tile = jj * 8 + nt;
#define K64(s) ((head * 2) ^ (s))
#define OWOFF() ({ unsigned l_ = threadIdx.x & 63; asm volatile("" : "+v"(l_)); (unsigned)(((l_ >> 2) & 7) * DM + 32 * (l_ >> 5) + 8 * ((l_ & 3) ^ ((l_ >> 4) & 1))) * 2u; })
    const int n0 = (nt ^ 2) * 128, dn1 = (((nt ^ 3) - (nt ^ 2)) * 128 + 64) * 2, ntc = nt ^ 2 ^ (kp >> 1);
    const u16* wob = wo + (size_t)(8 * wave) * DM + n0;
    u64 cxf = tag + 1;
#define CFB (ws + WS_FLAG + F_CTX)
#define CFO() ({ unsigned l_ = threadIdx.x & 15, s0_ = (unsigned)((qb * 4 + qw) * 32 + head), s1_ = (unsigned)((qbB * 4 + 3 - (wave & 3)) * 32 + (head ^ 8)); \
                 asm volatile("" : "+v"(l_), "+s"(s0_), "+s"(s1_)); (l_ < 8 ? l_ ^ s0_ : s1_) * CFS; })
    u32x4 pc[8];
#define EAR_(v_, b_, pol_) "s_add_u32 m0, m0, 0x400\n\ts_nop 0\n\tglobal_load_lds_dwordx4 " v_ ", " b_ pol_ "\n\t"
#define EALD(h_) do { unsigned l_ = threadIdx.x & 63; asm volatile("" : "+v"(l_)); const unsigned lx_ = ((l_ >> 3) * DM + 8 * ((l_ & 7) ^ (l_ >> 3))) * 2u; \
        const int rb_ = ((h_) ? qbB * 8 + qw * 2 : qbA * 8 + (wave & 3) * 2) + (kp & 1);                                                  \
        asm volatile("s_mov_b32 m0, %5\n\t" EAR_("%0", "%3", "") EAR_("%1", "%3", "") EAR_("%2", "%4", " sc1") "global_load_lds_dwordx4 %2, %4 offset:1024 sc1" \
                     :: "v"(lx_), "v"(lx_ + 8 * DM * 2), "v"(l_ * 16u), "s"(x + (size_t)rb_ * 16 * DM + ntc * 128 + (kp >> 1) * 64),              \
                        "s"(ws + WS_XF + (size_t)(2 * ntc + (kp >> 1)) * 262144 + rb_ * 2048),                                             \
                        "s"(ldsbase + 131072 - 1024 + (wave & 3) * 8192 + (h_) * 4096) : "memory", "scc"); } while (0)
    const unsigned i1 = (((kz ? F1S : F1F) >> (4 * jj)) & 15u) * 16u + (kp * 4 + (idx >> 3));
    const unsigned i2 = (((kz ? F2S : F2F) >> (4 * jj)) & 15u) * 16u + (kp * 4 + (idx >> 3));
    const unsigned nf1 = i1 / 112u, nf2 = i2 / 112u;
    const unsigned k1 = i1 / 14u;
    unsigned ypub = 1;
    {
        int tid = threadIdx.x; asm volatile("" : "+v"(tid));
        const int lane = tid & 63;
        const unsigned l16 = (unsigned)lane * 16u;
#define EX(x) __builtin_amdgcn_exp2f(x)
#define TBAR() do { SB(); __builtin_amdgcn_s_waitcnt(0x0070); asm volatile("s_barrier" ::: "memory"); } while (0)
#define BRQ(t, r) do { constexpr int g_ = (r) >> 2; const int tt_ = (t) + (g_ == 2 ? 1 : 2);                                               \
            const unsigned o_ = (unsigned)((wave & 3) * 4096 + (g_ == 1 || g_ == 2 ? 16384 : 0));                                          \
            if (((r) & 3) == 0) GLDS_C(l16, kvg + (size_t)tt_ * 65536 + (g_ >= 2 ? 32768 : 0) + o_, ldsbase + (g_ >= 2 ? 0 : 65536) + (tt_ & 1) * 32768 + o_, 0); \
            else asm volatile("global_load_lds_dwordx4 %0, %1 offset:%2 sc1" :: "v"(l16), "s"(kvg + (size_t)tt_ * 65536 + (g_ >= 2 ? 32768 : 0) + o_), "i"(((r) & 3) * 1024) : "memory"); } while (0)
#define BRQ4(t, g) do { BRQ(t, 4 * (g)); BRQ(t, 4 * (g) + 1); BRQ(t, 4 * (g) + 2); BRQ(t, 4 * (g) + 3); } while (0)
#define BRQ2C(t, r) do { constexpr int g_ = (r) >> 2; const int tt_ = (t) + (g_ == 2 ? 1 : 2);                                             \
            const unsigned o_ = (unsigned)((wave & 3) * 4096 + (g_ == 1 || g_ == 2 ? 16384 : 0));                                          \
            asm volatile("s_cmp_eq_u32 %5, 0\n\ts_cbranch_scc1 1f\n\ts_mov_b32 m0, %2\n\ts_nop 0\n\tglobal_load_lds_dwordx4 %0, %1 offset:%3 sc1\n\t"  \
                         "global_load_lds_dwordx4 %0, %1 offset:%4 sc1\n1:"                                                               \
                         :: "v"(l16), "s"(kvg + (size_t)tt_ * 65536 + (g_ >= 2 ? 32768 : 0) + o_), "s"(ldsbase + (g_ >= 2 ? 0 : 65536) + (tt_ & 1) * 32768 + o_), \
                            "i"(((r) & 3) * 1024), "i"(((r) & 3) * 1024 + 1024), "s"(isb) : "memory", "scc"); } while (0)
        const unsigned isb = wave >> 2;
        u64 kf2 = tag;
        const char* kfb = ws + WS_FLAG + F_KV + head * 64;
#define KCHK() do { PIN(kf2); if (__builtin_expect((__builtin_amdgcn_ballot_w64(kf2 != tag) | (u64)((33u >> t) & 1u)) != 0, 0)) { \
            unsigned c_, a_;                                                                                                              \
            FLAG_PUTZ(ws + WS_FLAG + F_KV + (hp * 8 + jj) * 8, tag, (unsigned)(wave ^ 4) | (unsigned)t);                                  \
            asm volatile("s_cmp_lt_u32 %6, 5\n\ts_cbranch_scc1 3f\n\tv_lshrrev_b32 %2, 1, %5\n\tv_and_b32 %2, 56, %2\n\ts_mov_b32 %1, 0x40000\n" \
                         "1:\n\tglobal_load_dwordx2 %0, %2, %3 sc1\n\ts_cmp_eq_u32 %6, 5\n\ts_cbranch_scc1 3f\n\ts_waitcnt vmcnt(0)\n\tv_cmp_ne_u64 vcc, %4, %0\n\t" \
                         "s_cmp_eq_u64 vcc, 0\n\ts_cbranch_scc1 3f\n\ts_sleep 2\n\ts_sub_u32 %1, %1, 1\n\ts_cbranch_scc0 1b\n"           \
                         "3:" : "+v"(kf2), "=&s"(c_), "=&v"(a_) : "s"(kfb), "s"(tag), "v"(l16), "s"(t) : "memory", "vcc", "scc"); } SB(); } while (0)
#define PFW1(i_) do { unsigned a_; const u64 p_ = ((u64)(unsigned)__builtin_amdgcn_readlane((int)(pft >> 32), i_) << 32) | (unsigned)__builtin_amdgcn_readlane((int)pft, i_); \
            asm volatile("v_lshlrev_b32 %0, 3, %2\n\ts_mov_b32 m0, %3\n\ts_nop 2\n\tglobal_load_lds_dword %0, %1"                        \
                         : "=&v"(a_) : "s"(p_), "v"(l16), "s"(ldsbase + 131072 + (wave & 3) * 8192) : "memory"); } while (0)
#define ONCE2(n_) int CAT(n_once, __LINE__) = (int)(n_); asm volatile("" : "+s"(CAT(n_once, __LINE__))); _Pragma("unroll 1") for (int z_ = 0; z_ < CAT(n_once, __LINE__); ++z_)
#define AST(i)
#define XST(i)
#define TS0()
#define TS1()
        AST(0);
        asm volatile("s_bitcmp0_b32 %0, 2\n\ts_cbranch_scc1 1f\n\ts_setprio 1\n1:" :: "s"(wave) : "scc");
        {
            f32x4 o[8][2], ls[2], ng[2], S[2][4];
            bf16x8 pf[2][2], fr[16];
            float e[16];
            constexpr bool ZC = false;
            constexpr bool HOT = false;
            const unsigned lb16 = ldsbase + l16;
            int t = 0;
#define KOFF(f) ((((f) & 3) * 2 + (((f) >> 2) & 1)) * 1024)
#define RD(kb, n) do { constexpr int k_ = ((kb) + ((n) >> 4)) & 3, n_ = (n) & 15;                                                          \
            fr[n_] = n_ < 8 ? lds_rd(av + (unsigned)(((k_ + 3) & 3) * 8192 + n_ * 1024)) : lds_rd(ak + (unsigned)(((k_ + 1) & 3) * 8192 + KOFF(n_ & 7))); } while (0)
#define MX(p, n, a) do { if (ZC) MFA16Y(o[n][a], fr[n], pf[(p) ^ 1][a]); else MFA16(o[n][a], fr[n], pf[(p) ^ 1][a]); } while (0)
#define MY(p, n, h) do { constexpr int a_ = ((n) >> 1) & 1, t_ = 2 * ((n) & 1) + (h);                                                      \
                         if (t_) MFA16(S[(p) ^ 1][(((n) >> 1) & 2) + a_], fr[((n) & 12) + t_], qf[2 * t_ + a_]);                           \
                         else MFA16C(S[(p) ^ 1][(((n) >> 1) & 2) + a_], fr[(n) & 12], qf[a_], ng[a_]); } while (0)
#define GX(p, n, F1, F2) do { MX(p, n, 0); SB(); F1; SB(); MX(p, n, 1); SB(); F2; SB(); } while (0)
#define GY(p, n, F1, F2) do { MY(p, n, 0); SB(); F1; SB(); MY(p, n, 1); SB(); F2; SB(); } while (0)
#define ML(p, a) do { if (ZC) MFA16Y(ls[a], ones, pf[(p) ^ 1][a]); else MFA16(ls[a], ones, pf[(p) ^ 1][a]); } while (0)
#define GL(p, F1, F2) do { ML(p, 0); SB(); F1; SB(); ML(p, 1); SB(); F2; SB(); } while (0)
#define SV(p, i) S[p][(((i) >> 1) & 2) + ((i) >> 3)][(i) & 3]
#define E1(p, i) do { e[i] = EX(SV(p, i)); if (HOT) KEEP(e[i]); else PIN(e[i]); } while (0)
#define C1(p, j) do { u32x4 w_ = __builtin_bit_cast(u32x4, pf[p][(j) >> 2]); w_[(j) & 3] = cvtpk(e[2 * (j)], e[2 * (j) + 1]);               \
                      pf[p][(j) >> 2] = __builtin_bit_cast(bf16x8, w_); if (HOT) KEEP(w_[(j) & 3]); else PIN(pf[p][(j) >> 2]); } while (0)
#define DQ(kb, g) do { if (ISB_ && (kb) == 3) { if ((g) == 0) KCHK(); BRQ(t, g); } if (ISB_ && (kb) == 0 && (g) == 1) PFW1(t); } while (0)
#define W4() do { SB(); __builtin_amdgcn_s_waitcnt(0xC37F); } while (0)
#define US(kb, ISB) do { constexpr bool HOT = true, ZC = (ISB) == 3, ISB_ = (ISB) == 1; constexpr int p_ = (kb) & 1; constexpr bool B_ = (kb) == 2;                    \
            GX(p_, 0, RD(kb, 7); if ((kb) == 3) { ak ^= 32768u; PIN(ak); }, E1(p_, 0); DQ(kb, 0));                                        \
            GX(p_, 1, RD(kb, 8); if ((kb) == 0) { av ^= 32768u; PIN(av); }, E1(p_, 1); DQ(kb, 1));                                        \
            GL(p_, E1(p_, 2), E1(p_, 3));                                                                                                 \
            GX(p_, 2, RD(kb, 9); C1(p_, 0), E1(p_, 4); DQ(kb, 2));                                                                        \
            GX(p_, 3, RD(kb, 10); W4(), E1(p_, 5); DQ(kb, 3));                                                                            \
            GX(p_, 4, RD(kb, 11); C1(p_, 1), E1(p_, 6); DQ(kb, 4));                                                                       \
            GX(p_, 5, RD(kb, 12); C1(p_, 2), E1(p_, 7); DQ(kb, 5));                                                                       \
            GX(p_, 6, RD(kb, 13); C1(p_, 3), E1(p_, 8); DQ(kb, 6));                                                                       \
            GX(p_, 7, RD(kb, 14); W4(), E1(p_, 9); DQ(kb, 7));                                                                            \
            GY(p_, 8, RD(kb, 15), E1(p_, 10); DQ(kb, 8));                                           \
            GY(p_, 9, if (!B_) RD(kb, 16); C1(p_, 4), E1(p_, 11); DQ(kb, 9));                                                             \
            GY(p_, 10, if (B_) { TS0(); if (ISB_) { __builtin_amdgcn_s_waitcnt(0x0071); asm volatile("s_barrier" ::: "memory"); } else TBAR(); \
                                 TS1(); SB(); RD(kb, 16); }                                                                               \
                       RD(kb, 17); C1(p_, 5), E1(p_, 12); DQ(kb, 10));                                                                    \
            GY(p_, 11, RD(kb, 18); if (!B_) W4(), E1(p_, 13); DQ(kb, 11));                                                                \
            GY(p_, 12, RD(kb, 19), E1(p_, 14); DQ(kb, 12));                                                \
            GY(p_, 13, RD(kb, 20); C1(p_, 6), E1(p_, 15); DQ(kb, 13));                                                                    \
            GY(p_, 14, RD(kb, 21); C1(p_, 7), DQ(kb, 14));                                 \
            GY(p_, 15, RD(kb, 22), DQ(kb, 15); W4());                                                                                     \
        } while (0)
            asm volatile("s_waitcnt vmcnt(%0) lgkmcnt(0)\n\ts_barrier" :: "i"(4 + (QS1 >> 15)) : "memory");
            AST(1);
#pragma unroll
            for (int s_ = 0; s_ < 8; ++s_) fr[s_] = lds_rd(ak + (unsigned)KOFF(s_));
            BRQ2C(-1, 12); BRQ2C(-1, 14);
            SB();
#define M0_(n, a) do { if ((n) & 3) MFA16(S[0][(((n) >> 1) & 2) + (a)], fr[n], qf[2 * ((n) & 3) + (a)]); else MFA16Z(S[0][(((n) >> 1) & 2) + (a)], fr[n], qf[a]); } while (0)
#define G0_(n) do { M0_(n, 0); SB(); if (Q_NST + (n) < 16) KVST((Q_NST + (n)) & 15); SB(); M0_(n, 1); SB(); RD(0, 8 + (n));               \
                    if (Q_NST + (n) < 16) KEEP(pk[(Q_NST + (n)) & 15]); SB(); } while (0)
            G0_(0); G0_(1); G0_(2); G0_(3); G0_(4); G0_(5); G0_(6); G0_(7);
            MF_DONE();
            { ONCE2(isb * min(jj, 1)) { PFW1(0); } }
            {
                PIN(S[0][0]); PIN(S[0][1]); PIN(S[0][2]); PIN(S[0][3]);
                if (__builtin_expect((qb | qw) == 0, 0)) {
#pragma unroll
                    for (int r = 0; r < 4; ++r) { S[0][2][r] = -__builtin_inff(); if (4 * (lane >> 4) + r > (lane & 15)) S[0][0][r] = S[0][3][r] = -__builtin_inff(); } }
#pragma unroll
                for (int a = 0; a < 2; ++a) {
                    float mx_;
#define MX3(d, a_, b_, c_) asm("v_max3_f32 %0, %1, %2, %3" : "=v"(d) : "v"(a_), "v"(b_), "v"(c_))
                    MX3(mx_, SV(0, 8 * a), SV(0, 8 * a + 1), SV(0, 8 * a + 2)); MX3(mx_, mx_, SV(0, 8 * a + 3), SV(0, 8 * a + 4)); MX3(mx_, mx_, SV(0, 8 * a + 5), SV(0, 8 * a + 6));
                    { float m7_ = SV(0, 8 * a + 7), n_ = -1e30f; MX3(mx_, mx_, m7_, n_); }
                    { auto r1 = __builtin_amdgcn_permlane16_swap(__float_as_uint(mx_), __float_as_uint(mx_), false, false); MX3(mx_, r1[0], r1[1], r1[1]);
                      auto r2 = __builtin_amdgcn_permlane32_swap(__float_as_uint(mx_), __float_as_uint(mx_), false, false); MX3(mx_, r2[0], r2[1], r2[1]); }
                    mx_ = -POFF - mx_;
                    ng[a] = f32x4{mx_, mx_, mx_, mx_}; PIN(ng[a]);
                }
#pragma unroll
                for (int i = 0; i < 16; ++i) { float v_ = SV(0, i) + ng[i >> 3][0]; PIN(v_); SV(0, i) = v_; }
                PIN(S[0][0]); PIN(S[0][1]); PIN(S[0][2]); PIN(S[0][3]);
            }
            SB();
            GY(0, 8, E1(0, 0), RD(0, 16); E1(0, 1)); GY(0, 9, E1(0, 2); C1(0, 0), RD(0, 17); E1(0, 3));
            GY(0, 10, E1(0, 4); C1(0, 1), RD(0, 18); E1(0, 5)); GY(0, 11, E1(0, 6); C1(0, 2), RD(0, 19); E1(0, 7));
            GY(0, 12, E1(0, 8); C1(0, 3), RD(0, 20); E1(0, 9)); GY(0, 13, E1(0, 10); C1(0, 4), RD(0, 21); E1(0, 11));
            GY(0, 14, E1(0, 12); C1(0, 5), RD(0, 22); E1(0, 13)); GY(0, 15, E1(0, 14); C1(0, 6), E1(0, 15); C1(0, 7));
#define SCOPY() do { MF_DONE(); _Pragma("unroll") for (int i = 0; i < 4; ++i) { S[1][i] = S[0][i]; PIN(S[1][i]); } } while (0)
            if (__builtin_expect((qb | qw) == 0, 0)) { SCOPY(); pf[0][0] = pf[0][1] = bf16x8{}; PIN(pf[0][0]); PIN(pf[0][1]); }
            W4(); asm volatile("s_nop 1"); SB();
            AST(2);
            const int nu = 4 * qb + qw - 2;
#define MFA16Y(c, a, b) asm volatile("v_mfma_f32_16x16x32_bf16 %0, %1, %2, 0" : "+v"(c) : "v"(a), "v"(b))
#pragma unroll
            for (int d = 0; d < 18; ++d) { if (d < 16) asm volatile("" : "=v"(o[d >> 1][d & 1])); else asm volatile("" : "=v"(ls[d & 1])); }
            if (__builtin_expect(nu < 0, 0)) { const bf16x8 z_ = bf16x8{}; asm volatile("s_nop 1" :: "v"(z_));
#pragma unroll
                for (int d = 0; d < 18; ++d) { if (d < 16) MFA16Y(o[d >> 1][d & 1], z_, z_); else MFA16Y(ls[d & 1], z_, z_); } }
            { int n1_ = nu; asm volatile("" : "+s"(n1_)); ONCE2(min(n1_ + 2, 2) >> 1) { US(1, 3); } }
            {
                const int nl = nu >> 2, nB = isb ? nl : 0;
#pragma unroll 1
                while (t < nB) { US(2, 1); US(3, 1); ++t; US(0, 1); US(1, 1); }
#pragma unroll 1
                while (t < nl) { US(2, 0); US(3, 0); ++t; US(0, 0); US(1, 0); }
            }
            AST(3);
            SST(1);
#define PCOPY() do { SCOPY(); pf[0][0] = pf[1][0]; pf[0][1] = pf[1][1]; PIN(pf[0][0]); PIN(pf[0][1]); SB(); asm volatile("s_nop 1"); } while (0)
            {
                const int nr = nu < 0 ? 0 : nu & 3, ne = nu < -1 ? 0 : nu + 1;
                if (__builtin_expect(nr >= 1, 1)) {
                    US(2, 2);
                    if (__builtin_expect(nr >= 2, 1)) {
                        if (isb) { KCHK(); asm volatile("s_setprio 0"); BRQ4(t, 0); BRQ4(t, 1); BRQ4(t, 2); BRQ4(t, 3); asm volatile("s_setprio 1"); }
                        US(3, 2);
                        if (__builtin_expect(nr == 3, 0)) US(0, 2);
                    }
                }
                int np = ne; asm volatile("" : "+s"(np));
                if (__builtin_expect(np & 1, 0)) PCOPY();
                KEEP(ng[0]); KEEP(ng[1]);
                t = qb;
            }
#define RDV(n) fr[n] = lds_rd(avf + (unsigned)((n) * 1024))
#define M1(i) do { e[i] = __builtin_amdgcn_fmed3f(e[i], 0.f, rmp[(i) & 3]); PIN(e[i]); } while (0)
#define E2(i) E1(1, i)
#define CST(i) asm volatile("global_store_dwordx4 %0, %1, %2 offset:%3 sc1\n\ts_nop 1" :: "v"(l16), "v"(pc[i]), "s"(cb + ((i) >> 2) * 262144), "i"((((i) >> 1) & 1) * 1024 + ((i) & 1) * 2048) : "memory")
#define NRM(ISB, i) do { float y_[8];                                              \
                _Pragma("unroll") for (int z_ = 0; z_ < 8; ++z_) { y_[z_] = o[((i) & 6) + (z_ >> 2)][(i) & 1][z_ & 3] * inv[(i) & 1]; PIN(y_[z_]); } \
                pc[i] = u32x4{cvtpk(y_[0], y_[1]), cvtpk(y_[2], y_[3]), cvtpk(y_[4], y_[5]), cvtpk(y_[6], y_[7])};                        \
                PIN(pc[i]); if (!(ISB)) CST(i); SB(); } while (0)
#define XLD(b_) do { _Pragma("unroll") for (int n = 0; n < 8; ++n) asm volatile("global_load_dwordx4 %0, %1, %2 offset:%3 sc1" : "=v"(fr[n]) : "v"(l16 + (n >> 2) * 4096u), "s"(b_), "i"((n & 3) * 1024) : "memory"); \
                asm volatile("s_waitcnt vmcnt(0)" : "+v"(fr[0]), "+v"(fr[1]), "+v"(fr[2]), "+v"(fr[3]), "+v"(fr[4]), "+v"(fr[5]), "+v"(fr[6]), "+v"(fr[7]) :: "memory"); } while (0)
#define MFB(a_, b_, c_) __builtin_amdgcn_mfma_f32_16x16x32_bf16(a_, b_, c_, 0, 0, 0)
#define EXACT() do { __builtin_amdgcn_s_waitcnt(0xC07F); MF_DONE();                                                                                                           \
                float mr_[2] = {-1e30f, -1e30f}; const int nu_ = 4 * qb + qw2;                                                            \
                ls[0] = ls[1] = f32x4{}; _Pragma("unroll") for (int d = 0; d < 16; ++d) o[d >> 1][d & 1] = f32x4{};                       \
                _Pragma("unroll 1") for (int u = 0; u <= nu_; ++u) {                                                                      \
                    const char* kb_ = kvg + (size_t)(u >> 2) * 65536 + (u & 3) * 8192;                                                    \
                    XLD(kb_);                                                                                                             \
                    _Pragma("unroll") for (int i = 0; i < 4; ++i) { f32x4 s_ = f32x4{};                                                   \
                        _Pragma("unroll") for (int d = 0; d < 4; ++d) s_ = MFB(fr[2 * d + (i >> 1)], qf[2 * d + (i & 1)], s_);            \
                        S[1][i] = s_; }                                                                                                   \
                    if (u == nu_) { _Pragma("unroll") for (int r = 0; r < 4; ++r) { S[1][2][r] = -__builtin_inff();                       \
                        if (4 * (ln2 >> 4) + r > (ln2 & 15)) S[1][0][r] = S[1][3][r] = -__builtin_inff(); } }                             \
                    XLD(kb_ + 32768);                                                                                                     \
                    _Pragma("unroll") for (int a = 0; a < 2; ++a) {                                                                       \
                        float mx_ = SV(1, 8 * a); _Pragma("unroll") for (int r = 1; r < 8; ++r) mx_ = fmaxf(mx_, SV(1, 8 * a + r));       \
                        mx_ = fmaxf(quad_max(mx_), mr_[a]); const float al_ = EX(mr_[a] - mx_); mr_[a] = mx_;                             \
                        u32x4 w_; _Pragma("unroll") for (int j = 0; j < 4; ++j) w_[j] = cvtpk(EX(SV(1, 8 * a + 2 * j) - mx_), EX(SV(1, 8 * a + 2 * j + 1) - mx_)); \
                        const bf16x8 p_ = __builtin_bit_cast(bf16x8, w_);                                                                 \
                        ls[a] = MFB(ones, p_, ls[a] * al_);                                                                               \
                        _Pragma("unroll") for (int d = 0; d < 8; ++d) o[d][a] = MFB(fr[d], p_, o[d][a] * al_); } }                        \
                pf[1][0] = pf[1][1] = bf16x8{}; _Pragma("unroll") for (int n = 4; n < 8; ++n) fr[n] = bf16x8{};     \
                PIN(pf[1][0]); PIN(pf[1][1]); MF_DONE(); } while (0)
#define TAIL(ISB) do { float rmp[4];                                                                                         \
                XST(0);                                                                                                                   \
                MX(1, 0, 0); SB();                                                                                                        \
                int ln2 = lane, qw2 = qw; asm volatile("" : "+v"(ln2), "+s"(qw2));   \
                fr[7] = lds_rd(av + (unsigned)((qw2 + 3) & 3) * 8192u + 7168u); SB();                                                     \
                MX(1, 0, 1); SB(); E2(0); SB();                                                                                           \
                GX(1, 1, E2(1), E2(2));                                                                                                   \
                GL(1, E2(3), E2(8));                                                                                                      \
                float fl_ = (float)((ln2 & 15) - 4 * (ln2 >> 4)); PIN(fl_);                                                               \
                unsigned avf = lb16 + (unsigned)(t & 1) * 32768u + (unsigned)qw2 * 8192u; PIN(avf); SB();                                 \
                GX(1, 2, E2(9), E2(10));                                                                             \
                GX(1, 3, E2(11), _Pragma("unroll") for (int r = 0; r < 4; ++r) { rmp[r] = (fl_ + (0.5f - r)) * 1.2089258196146292e24f; PIN(rmp[r]); }); \
                if (__builtin_expect(qw2 == 2, 0)) {                               \
                    if (ISB) TBAR(); else LGKM0_BAR(); }                                                                                  \
                SB();                                                                                                                     \
                RDV(0); RDV(1); RDV(2); RDV(3); SB();                                                                                     \
                GX(1, 4, E2(12), M1(0); M1(1));                                                                                           \
                GX(1, 5, RDV(4); E2(13), M1(2); M1(3));                                                                                   \
                GX(1, 6, RDV(5); E2(14), C1(1, 0); C1(1, 1));                                                                             \
                GX(1, 7, RDV(6); E2(15), C1(1, 4); C1(1, 5));                                                                             \
                RDV(7); M1(12); M1(13); M1(14); M1(15); e[4] = e[5] = e[6] = e[7] = 0.f; C1(1, 2); C1(1, 3); SB();                        \
                C1(1, 6); C1(1, 7); SB();                                                                                                 \
                XST(1);                                                                                                                   \
                if (__builtin_expect(qw2 < 2, 0)) { if (ISB) TBAR(); else LGKM0_BAR(); }                                                  \
                SB();                                                                                                                     \
                XST(2); XST(3);                                                                                                           \
                float inv[2];                                                                                                \
                int hd2 = head; asm volatile("" : "+s"(hd2)); const char* cb = ws + WS_CTX + (unsigned)(hd2 * 524288 + (qb * 8 + qw2 * 2) * 2048); SB(); \
                GL(0, , ); GX(0, 0, , ); GX(0, 1, , ); GX(0, 2, , );                                                                      \
                PIN(ls[0]); PIN(ls[1]);                                                                                                   \
                const bool c_ = __builtin_amdgcn_ballot_w64(!(ls[0][0] + ls[1][0] <= LTHR)) != 0; SB();                                   \
                GX(0, 3, , );                                                                                                             \
                if (__builtin_expect(c_, 0)) EXACT();                                                                                     \
                SB();                                                                                                                     \
                inv[0] = __builtin_amdgcn_rcpf(ls[0][0]); inv[1] = __builtin_amdgcn_rcpf(ls[1][0]); PIN(inv[0]); PIN(inv[1]); SB();       \
                GX(0, 4, , asm volatile("s_nop 0"));                                                                                      \
                NRM(ISB, 0); GX(0, 5, , ); NRM(ISB, 1); GX(0, 6, , ); NRM(ISB, 2); GX(0, 7, , ); NRM(ISB, 3);                             \
                XST(4);                                                                                                                   \
                NRM(ISB, 4); NRM(ISB, 5); NRM(ISB, 6); NRM(ISB, 7);                                                                       \
                if (ISB) { asm volatile("s_setprio 0"); asm volatile("" : "+s"(t)); KCHK(); if (t + 2 < NA) { BRQ4(t, 0); BRQ4(t, 1); BRQ4(t, 3); } BRQ4(t, 2); \
                           CST(0); CST(1); CST(2); CST(3); CST(4); CST(5); CST(6); CST(7); }                                              \
                XST(5);                                                                                                                   \
            } while (0)
#define SPIN() do { PIN(S[1][0]); PIN(S[1][1]); PIN(S[1][2]); PIN(S[1][3]); } while (0)
            { ONCE2(isb ^ 1) { SPIN(); TAIL(0); } }
            { ONCE2(isb) { SPIN(); TAIL(1); } }
            SST(2);
            AST(4);
        }
        if (wave >= 4) {
            int t = qbB + 1;
            const int wv = wave & 3;
            XST(6);
#define WOLD(h_) do { const unsigned ow_ = OWOFF(); const char* wp_ = (const char*)(wo + (size_t)(K64(2 * (h_)) * 64 + 16 * wv) * DM + n0);  \
                _Pragma("unroll") for (int i = 0; i < 8; ++i)                                                                             \
                    GLDS_S(ow_, wp_ + (size_t)((i >> 2) * 64 + 8 * ((i >> 1) & 1)) * (DM * 2) + (i & 1) * dn1,                             \
                           ldsbase + 65536 + (h_) * 32768 + (i >> 2) * 16384 + (2 * wv + ((i >> 1) & 1)) * 2048 + (i & 1) * 1024, 0); } while (0)
            {
                const u32x4 em = u32x4{~0u, ~0u, ~0u, ~0u};
                const char* eb = ws + WS_PART + (size_t)(tile * 8 + wv) * 24576 + kp * 6144;
                asm volatile("global_store_dwordx4 %0, %1, %2 sc1\n\tglobal_store_dwordx4 %0, %1, %2 offset:1024 sc1\n\tglobal_store_dwordx4 %0, %1, %3 sc1\n\t"
                             "global_store_dwordx4 %0, %1, %3 offset:1024 sc1\n\ts_waitcnt vmcnt(12) lgkmcnt(0)"
                             :: "v"(l16), "v"(em), "s"(eb), "s"(eb + 4 * 24576) : "memory"); }
            if (jj == 7) { WOLD(1); EALD(0); EALD(1); }
            asm volatile("s_barrier" ::: "memory");
#define BATCH() do { KCHK(); if (t + 2 < NA) { BRQ4(t, 0); BRQ4(t, 1); BRQ4(t, 3); } BRQ4(t, 2); } while (0)
#define BSTEP(N) do { asm volatile("s_waitcnt vmcnt(%0)\n\ts_barrier" :: "i"(N) : "memory"); ++t; } while (0)
            u32x4 fw[16], wd[16], xq[4], gp[8];
            f32x4 fa[8];
            int ln_ = lane; asm volatile("" : "+v"(ln_));
#define FMM(w_, i0, a_, z_) do { asm volatile("s_setprio 1");                                                              \
                    _Pragma("unroll") for (int i = 0; i < 8; ++i) PIN(w_[(i0) + i]);                                                      \
                    PIN(a_);                                                                                                              \
                    _Pragma("unroll") for (int j = 0; j < 8; ++j) { u32x4 f_;                                                             \
                        _Pragma("unroll") for (int d = 0; d < 4; ++d) f_[d] = __builtin_amdgcn_perm(w_[(i0) + 2 * d + 1][j >> 1], w_[(i0) + 2 * d][j >> 1], (j & 1) ? 0x07060302u : 0x05040100u); \
                        fa[j] = __builtin_amdgcn_mfma_f32_16x16x32_bf16(__builtin_bit_cast(bf16x8, a_), __builtin_bit_cast(bf16x8, f_), (z_) ? f32x4{} : fa[j], 0, 0, 0); } \
                    _Pragma("unroll") for (int j = 0; j < 8; ++j) PIN(fa[j]);                                                             \
                    asm volatile("s_setprio 0"); } while (0)
#define FOUT() f32x4{ln_ >= 32 ? fa[4][0] : fa[0][0], ln_ >= 32 ? fa[5][0] : fa[1][0], ln_ >= 32 ? fa[6][0] : fa[2][0], ln_ >= 32 ? fa[7][0] : fa[3][0]}
            const int c1 = 2 * (i1 - 14 * k1) + (wv & 1);
            const unsigned r4o = (unsigned)(ln_ & 15) * 16u;
            const char* cxp = ws + WS_FLAG + F_CTX + ((qbB * 4 + qw) * 32 + head) * CFS;
            if (nf1 == 0) {
#pragma unroll 1
                while (t < F1T) { BATCH(); BSTEP(0); }
                const unsigned evo = (unsigned)((16 * (ln_ >> 5) + 4 * ((ln_ >> 4) & 1)) * FF * 2) + r4o;
                const u16* fwp = (wv < 2 ? w_gate : w_up) + (size_t)(k1 * 128) * FF + c1 * 128;
#define FLD(h_, c_) do { _Pragma("unroll") for (int i = 0; i < 8; ++i)                                                                    \
                    asm volatile("global_load_dwordx4 %0, %1, %2" : c_(fw[((h_) & 1) * 8 + i]) : "v"(evo), "s"(fwp + (size_t)(32 * (h_) + 8 * (i >> 2) + (i & 3)) * FF) : "memory"); } while (0)
                BATCH();
                { const int xr_ = (ln_ & 4) ? ri2 : ri1;
#pragma unroll
                  for (int i = 0; i < 4; ++i)
                      asm volatile("global_load_dwordx4 %0, %1, %2 offset:%3 sc1" : "=v"(xq[i]) : "v"((unsigned)((xr_ >> 4) * 2048 + (xr_ & 15) * 16 + (ln_ >> 4) * 256)),
                                   "s"(ws + WS_XF + (size_t)(k1 * 2 + (i >> 1)) * 262144), "i"((i & 1) * 1024) : "memory"); }
                FLD(0, "=v"); FLD(1, "=v"); BSTEP(20);
                BATCH(); asm volatile("s_waitcnt vmcnt(16)" ::: "memory");
                FLAG_PUT(cxp, tag, 1);
                FMM(fw, 0, xq[0], 1); FLD(2, "=v"); FMM(fw, 8, xq[1], 0); FLD(3, "=v"); BSTEP(17);
                BATCH(); asm volatile("s_waitcnt vmcnt(16)" ::: "memory");
                FMM(fw, 0, xq[2], 0); FMM(fw, 8, xq[3], 0); const f32x4 sum = FOUT();
                ST16_SC1((float*)(ws + WS_GP) + ((size_t)k1 * 4 + (wv >> 1) * 2 + ((ln_ >> 4) & 1)) * FF + c1 * 128 + (ln_ & 15) * 8 + (ln_ >> 5) * 4, sum);
                PFW1(7); BSTEP(2);
            }
            const char* pnd = nf1 ? cxp : ws + WS_FLAG + F_GU + (c1 * 16 + (wv >> 1) * 8 + k1) * 8;
            const int tp = nf1 ? t + 1 : t;
#define PUB(N) do { if (t == tp) { asm volatile("s_waitcnt vmcnt(%0)" :: "i"(N) : "memory"); FLAG_PUT(pnd, tag, 1); } } while (0)
            const int f0 = (nf2 ? 0 : i2 >> 3) * 256 + wv * 64;
            const char* gfp = ws + WS_FLAG + F_GU + (f0 >> 7) * 128 + (ln_ & 15) * 8;
            u64 gf = tag + 1;
            char* vs = lds + (NA & 1) * 32768;
#define GFLD() asm volatile("global_load_dwordx2 %0, %1, off sc1" : "+v"(gf) : "v"(gfp) : "memory")
#define WDLD(i, n_) do { const unsigned dvo = (unsigned)((ln_ >> 4) * 8 * DM * 2) + r4o;                                                  \
                    const char* db_ = (const char*)(w_down + (size_t)((nf2 ? 0 : i2 >> 3) * 256 + wv * 64) * DM + (i2 & 7) * 128) + 4096; \
                    asm volatile("s_cmp_lg_u32 %11, 0\n\ts_cbranch_scc1 1f\n\t"                                                          \
                                 "global_load_dwordx4 %0, %8, %9 offset:-4096\n\tglobal_load_dwordx4 %1, %8, %9 offset:-2048\n\tglobal_load_dwordx4 %2, %8, %9\n\t" \
                                 "global_load_dwordx4 %3, %8, %9 offset:2048\n\tglobal_load_dwordx4 %4, %8, %10 offset:-4096\n\tglobal_load_dwordx4 %5, %8, %10 offset:-2048\n\t" \
                                 "global_load_dwordx4 %6, %8, %10\n\tglobal_load_dwordx4 %7, %8, %10 offset:2048\n1:"  \
                                 : "=&v"(wd[i]), "=&v"(wd[i + 1]), "=&v"(wd[i + 2]), "=&v"(wd[i + 3]), "=&v"(wd[i + 4]), "=&v"(wd[i + 5]), "=&v"(wd[i + 6]), "=&v"(wd[i + 7]) \
                                 : "v"(dvo), "s"(db_ + (i) * 8192), "s"(db_ + (i) * 8192 + 8192), "s"(nf2) : "memory", "scc");            \
                    EALD((i) >> 3);                                                                                                       \
                    asm volatile("s_waitcnt vmcnt(%1)\n\ts_cmp_eq_u32 %0, 0\n\ts_cbranch_scc1 1f\n\ts_waitcnt vmcnt(%2)\n1:"                \
                                 :: "s"(nf2), "i"(12 + (n_)), "i"(4 + (n_)) : "memory", "scc");                                           \
                    BSTEP(63); } while (0)
#pragma unroll 1
            while (t < NA - 4) { BATCH(); PUB(17); BSTEP(0); }
            if (jj < 7) {
                BATCH(); PUB(16); WDLD(0, 0);
                BATCH(); PUB(16); GFLD(); WDLD(8, 0);
            }
            if (jj < 6) {
                BATCH();
                if (nf2 == 0) {
                    FLAG_WAIT(gf, gfp, tag, 63);
#pragma unroll
                    for (int i = 0; i < 8; ++i)
                        asm volatile("global_load_dwordx4 %0, %1, %2 sc1" : "=v"(gp[i]) : "v"((unsigned)((ln_ >> 4) * FF * 4) + r4o), "s"(ws + WS_GP + ((size_t)i * 4 * FF + f0) * 4) : "memory");
                    *(unsigned*)vs = 0;
                    asm volatile("s_waitcnt lgkmcnt(0)" ::: "memory");
                }
                WOLD(NA & 1);
                BSTEP(8);
            } else if (jj == 6) WOLD(0);
            asm volatile("global_load_dwordx2 %0, %1, %2 sc1" : "+v"(cxf) : "v"(CFO()), "s"(CFB) : "memory");
            WOLD((NA & 1) ^ 1);
            asm volatile("s_waitcnt vmcnt(16)" ::: "memory");
            if (jj == 7) FLAG_PUT(cxp, tag, 1);
            if (nf2 == 0) {
                f32x4 g4;
#pragma unroll
                for (int i = 0; i < 8; ++i) { PIN(gp[i]);
#pragma unroll
                    for (int j = 0; j < 4; ++j) { if (i) VADD(g4[j], g4[j], __uint_as_float(gp[i][j])); else g4[j] = __uint_as_float(gp[0][j]); } }
                float hv[2];
#pragma unroll
                for (int k = 0; k < 2; ++k) { auto rr = __builtin_amdgcn_permlane32_swap(__float_as_uint(g4[k]), __float_as_uint(g4[2 + k]), false, false);
                    const float gb = rbf(__uint_as_float(rr[0])), ub = rbf(__uint_as_float(rr[1]));
                    hv[k] = rbf(gb / (1.f + __expf(-gb))) * ub; PIN(hv[k]); }
                char* hl = vs + 8192 + wv * 256;
                *(unsigned*)(hl + ((ln_ >> 4) & 1) * 128 + (ln_ & 15) * 8 + (ln_ >> 5) * 4) = cvtpk(hv[0], hv[1]);
                asm volatile("" ::: "memory");
                xq[0] = *(const u32x4*)(hl + ((ln_ >> 2) & 1) * 128 + (ln_ >> 4) * 16); xq[1] = *(const u32x4*)(hl + ((ln_ >> 2) & 1) * 128 + 64 + (ln_ >> 4) * 16);
                FMM(wd, 0, xq[0], 1); FMM(wd, 8, xq[1], 0);
                *(f32x4*)(vs + 4096 + wv * 1024 + ln_ * 16) = FOUT();
                unsigned n_;
                asm volatile("s_waitcnt lgkmcnt(0)\n\ts_mov_b64 exec, 1\n\tds_add_rtn_u32 %0, %1, %2\n\ts_mov_b64 exec, -1\n\ts_waitcnt lgkmcnt(0)"
                             : "=&v"(n_) : "v"(ldsbase + (NA & 1) * 32768u), "v"(1u) : "memory");
                ypub = __builtin_amdgcn_readfirstlane(n_) ^ 3u;
                if (ypub == 0) {
                    const f32x4* yr = (const f32x4*)(vs + 4096) + ln_;
                    const f32x4 a = (yr[0] + yr[64]) + (yr[128] + yr[192]);
                    ST16_SC1((float*)(ws + WS_YP) + ((size_t)(((ln_ >> 4) & 1) * 8 + (i2 & 7)) * 16 + (i2 >> 3)) * 128 + (ln_ & 15) * 8 + (ln_ >> 5) * 4, a);
                }
            }
            AST(5);
        }
    }
    PH_STAMP(3);
    {
        constexpr int LDW = 1024, STG = 16384;
        int tid = threadIdx.x; asm volatile("" : "+v"(tid));
        const int lane = tid & 63, l15 = lane & 15, q4 = lane >> 4;
        unsigned xvo = (unsigned)lane * 16u; PIN(xvo);
        if (!(kp & 1)) {
#pragma unroll
            for (int i = 0; i < 8; i += 2) { const u32x4 t_ = pc[i]; pc[i] = pc[i + 1]; pc[i + 1] = t_; PIN(pc[i]); PIN(pc[i + 1]); } }
        bf16x8 yr_[4][2][2];
#pragma unroll
        for (int i = 0; i < 8; ++i) yr_[i >> 2][i & 1][(i >> 1) & 1] = __builtin_bit_cast(bf16x8, pc[i]);
        unsigned wa[2][2];
        wa[0][0] = ldsbase + (unsigned)(65536 + ((kp >> 1) ^ 1) * 1024 + (q4 >> 1) * 2048 + (4 * (q4 & 1) + (l15 >> 2)) * 64 + (q4 & 1) * 16 + (lane & 2) * 16 + (lane & 1) * 8);
        wa[0][1] = wa[0][0] ^ 16u; wa[1][0] = wa[0][0] ^ 1024u; wa[1][1] = wa[0][1] ^ 1024u;
        PIN(wa[0][0]); PIN(wa[0][1]); PIN(wa[1][0]); PIN(wa[1][1]);
        const char* cg = ws + WS_CTX + (qb * 8 + qw * 2) * 2048;
        unsigned xok;
#define XLD8 "global_load_dwordx4 %0, %10, %12 sc1\n\tglobal_load_dwordx4 %1, %10, %13 sc1\n\tglobal_load_dwordx4 %2, %10, %12 offset:1024 sc1\n\tglobal_load_dwordx4 %3, %10, %13 offset:1024 sc1\n\t" \
             "global_load_dwordx4 %4, %11, %12 sc1\n\tglobal_load_dwordx4 %5, %11, %13 sc1\n\tglobal_load_dwordx4 %6, %11, %12 offset:1024 sc1\n\tglobal_load_dwordx4 %7, %11, %13 offset:1024 sc1\n\t"
#define XOUT(c_) c_(yr_[2][0][0]), c_(yr_[2][1][0]), c_(yr_[2][0][1]), c_(yr_[2][1][1]), c_(yr_[3][0][0]), c_(yr_[3][1][0]), c_(yr_[3][0][1]), c_(yr_[3][1][1])
#define XIN "v"(xvo), "v"(xvo2), "s"(xp0), "s"(xp1), "s"(tag)
        unsigned xvo2 = xvo + 262144u, wvo = OWOFF() + (unsigned)K64(4) * (64 * LDW * 2), xso = xvo + (unsigned)K64(4) * 262144u, cfo = CFO();
        PIN(xvo2); PIN(wvo); PIN(xso); PIN(cfo);
        const char* cg0 = cg + ((kp ^ 1) & 1) * 2048, * cg1 = cg + (kp & 1) * 2048, * xp0 = cg0 + (unsigned)K64(2) * 262144u, * xp1 = cg1 + (unsigned)K64(2) * 262144u;
        const char* cfb = CFB, * wb0 = (const char*)wob, * wb1 = wb0 + (dn1 - 1024);
        const char* fpp = ws + WS_FLAG + ((wave & 4) ? F_Y + ((i2 & 7) * 16 + ((i2 >> 3) & 15)) * 8 : F_CTX + ((qbA * 4 + wave) * 32 + head) * CFS);
        unsigned fpz = (wave >> 2) * ypub, m0w = ldsbase + wave * 2048;
        asm volatile("" : "+s"(cg0), "+s"(cg1), "+s"(xp0), "+s"(xp1), "+s"(cfb), "+s"(wb0), "+s"(wb1), "+s"(fpp), "+s"(fpz), "+s"(m0w));
        SST(3);
        SB();
        asm volatile("s_waitcnt vmcnt(0)" ::: "memory");
        FLAG_PUTZ(fpp, tag, fpz);
        asm volatile("v_cmp_ne_u64 vcc, %14, %8\n\ts_and_b32 %9, vcc_lo, 14\n\ts_cbranch_scc1 1f\n\t"
                     XLD8 "\n1:\n\ts_barrier" : XOUT("=&v"), "+v"(cxf), "=&s"(xok) : XIN : "memory", "vcc", "scc");
        SST(4);
        f32x4 acc[2][8];
        bf16x4 wl[8], wh[8];
#define OFA(s, k_, j) (wa[(j) >> 2][(j) & 1] + (unsigned)(((s) & 3) * STG + (k_) * 8192 + (((j) >> 1) & 1) * 512))
#define ORL(i, s, k_, j) wl[i] = tr_rd(OFA(s, k_, j))
#define ORH(i, s, k_, j) wh[i] = tr_rd(OFA(s, k_, j) + 4096u)
        ORL(0, 0, 0, 0); ORH(0, 0, 0, 0); ORL(1, 0, 0, 1); ORH(1, 0, 0, 1); ORL(2, 0, 0, 2); ORH(2, 0, 0, 2); ORL(3, 0, 0, 3); ORH(3, 0, 0, 3); SB();
        const int r0 = qb * 128 + qw * 32 + (kp & 1) * 16, ch = kp >> 1;
        const unsigned nrt = min((unsigned)(ri1 - r0) >> 4, (unsigned)(ri2 - r0) >> 4);
        u64 yf = 0;
#define YFP (ws + WS_FLAG + F_Y + (ntc * 16 + (l15 < 14 ? l15 : 0)) * 8)
#define OMF(a, s, k_, j) do { if ((s) == 0 && (k_) == 0) MFA16Z(acc[a][j], w_, yr_[0][a][0]); else MFA16(acc[a][j], w_, yr_[(s) & 3][a][k_]); } while (0)
#define ORQ(h) do { constexpr int x_ = o_x(h), w_ = o_w(h);                                                                               \
            if (x_ >= 0) { asm volatile("global_load_dwordx4 %0, %1, %2 offset:%3 sc1" : "=v"(yr_[(x_ >> 2) & 3][x_ & 1][(x_ >> 1) & 1])   \
                                        : "v"(xso), "s"((x_ & 1) ? cg1 : cg0), "i"(((x_ >> 1) & 1) * 1024) : "memory");         \
                           if ((x_ & 3) == 3) { xso ^= (unsigned)((x_ >> 2) ^ ((x_ >> 2) + 1)) * 262144u; PIN(xso); } }                    \
            if (o_w((h) + 1) >= 0 && !(o_w((h) + 1) & 1)) asm volatile("s_mov_b32 m0, %0" :: "s"(m0w + (((o_w((h) + 1) >> 1) & 7) ^ 4) * STG) : "memory"); \
            if (w_ >= 0) { asm volatile("global_load_lds_dwordx4 %0, %1 offset:%2" :: "v"(wvo), "s"((w_ & 1) ? wb1 : wb0), "i"((w_ & 1) * 1024) : "memory"); \
                           if (w_ & 1) { wvo ^= (unsigned)((w_ >> 1) ^ ((w_ >> 1) + 1)) * (64 * LDW * 2); PIN(wvo); } } } while (0)
#define OPAIR(s, f, A) do { const bf16x8 w_ = CAT8(wl[(f) & 7], wh[(f) & 7]); constexpr int s_ = (s) + (((f) + 4) >> 4), m_ = ((f) + 4) & 15; \
            OMF(0, s, (f) >> 3, (f) & 7); SB(); ORL(m_ & 7, s_, m_ >> 3, m_ & 7); ORQ(32 * (s) + 2 * (f)); A; SB();                       \
            OMF(1, s, (f) >> 3, (f) & 7); SB(); ORH(m_ & 7, s_, m_ >> 3, m_ & 7); ORQ(32 * (s) + 2 * (f) + 1); SB(); if (((f) & 1)) { __builtin_amdgcn_s_waitcnt(0xC47F); SB(); } } while (0)
#define OTG(h_) do { wa[h_][0] ^= 65536u; wa[h_][1] ^= 65536u; PIN(wa[h_][0]); PIN(wa[h_][1]); } while (0)
#define OST(i)
#define OSTEP(s, POST) do {                                                                                                               \
        OPAIR(s, 0, ); OPAIR(s, 1, ); OPAIR(s, 2, ); OPAIR(s, 3, ); OPAIR(s, 4, ); OPAIR(s, 5, ); OPAIR(s, 6, ); OPAIR(s, 7, );           \
        OPAIR(s, 8, ); OPAIR(s, 9, if (((s) & 3) == 3) OTG(0)); OPAIR(s, 10, ); OPAIR(s, 11, );                                           \
        if ((s) >= 3) asm volatile("s_waitcnt vmcnt(%0)\n\ts_barrier" :: "i"(o_wait(s)) : "memory");                                      \
        SB(); POST; OST((s) + 1);                                                                                                         \
        OPAIR(s, 12, ); OPAIR(s, 13, if (((s) & 3) == 3) OTG(1)); OPAIR(s, 14, ); OPAIR(s, 15, );                                         \
    } while (0)
        PH_STAMP(4);
        SST(5);
        OST(0);
        SB();
        OSTEP(0, );
#define MSK "14"
#define MSKI
        asm volatile("s_cmp_eq_u32 %3, 0\n\ts_cbranch_scc1 1f\n\tglobal_load_dwordx2 %0, %1, %2 sc1\n1:" : "+v"(cxf) : "v"(cfo), "s"(cfb), "s"(xok) : "memory", "scc");
        OSTEP(1, );
        asm volatile("s_waitcnt vmcnt(8)\n\ts_cmp_eq_u32 %9, 0\n\ts_cbranch_scc1 3f\n\ts_mov_b32 %9, 0x40000\n\ts_waitcnt vmcnt(4)\n"
                     "1:\n\tv_cmp_ne_u64 vcc, %14, %8\n\ts_and_b32 vcc_lo, vcc_lo, " MSK "\n\ts_cbranch_scc0 2f\n\t"
                     "global_load_dwordx2 %8, %15, %16 sc1\n\ts_sleep 4\n\ts_waitcnt vmcnt(0)\n\ts_sub_u32 %9, %9, 1\n\ts_cbranch_scc0 1b\n2:\n\t" XLD8 "s_waitcnt vmcnt(4)\n3:"
                     : XOUT("+v"), "+v"(cxf), "+s"(xok) : XIN, "v"(cfo), "s"(cfb), "s"(wave) MSKI : "memory", "vcc", "scc");
        const unsigned inc = (unsigned)__builtin_amdgcn_ballot_w64(cxf != tag) & 0x1f0u;
        SB();
        OSTEP(2, if (__builtin_expect(inc != 0, 0)) asm volatile("global_load_dwordx2 %0, %1, %2 sc1" : "+v"(cxf) : "v"(cfo), "s"(cfb) : "memory"); SB());
        asm volatile("s_waitcnt vmcnt(8)" ::: "memory"); SB();
        OSTEP(3, );
        OSTEP(4, if (__builtin_expect(inc != 0, 0)) FLAG_WAITM(cxf, cfb + cfo, tag, 63, 0x1f0u); SB());
        OSTEP(5, ); OSTEP(6, );
        OSTEP(7, if (__builtin_expect(nrt == 0, 0)) asm volatile("global_load_dwordx2 %0, %1, off sc1" : "+v"(yf) : "v"(YFP) : "memory"); SB());
        OSTEP(8, ); OSTEP(9, ); OSTEP(10, );
        u32x4 yq[7], pv[6], zn[2]; u32x2 ea[4]; unsigned rq;
        if (__builtin_expect(nrt == 0, 0)) {
            FLAG_WAIT(yf, YFP, tag, 63);
            asm volatile("global_load_dwordx4 %0, %8, %10 sc1\n\tglobal_load_dwordx4 %1, %8, %10 offset:512 sc1\n\tglobal_load_dwordx4 %2, %8, %10 offset:1024 sc1\n\t"
                         "global_load_dwordx4 %3, %8, %10 offset:1536 sc1\n\tglobal_load_dwordx4 %4, %8, %10 offset:2048 sc1\n\tglobal_load_dwordx4 %5, %8, %10 offset:2560 sc1\n\t"
                         "global_load_dwordx4 %6, %8, %10 offset:3072 sc1\n\tglobal_load_dword %7, %9, %10 sc1"
                         : "=&v"(yq[0]), "=&v"(yq[1]), "=&v"(yq[2]), "=&v"(yq[3]), "=&v"(yq[4]), "=&v"(yq[5]), "=&v"(yq[6]), "=&v"(rq)
                         : "v"((unsigned)WS_YP + (unsigned)((((lane >> 5) * 8 + ntc) * 16 + (q4 & 1) * 7) * 128 + ch * 64 + 4 * l15) * 4u),
                           "v"((unsigned)WS_R + (unsigned)(lane >= 32 ? ri2 : ri1) * 4u), "s"(ws) : "memory");
        }
        SB();
        OSTEP(11, );
        char* pb = ws + WS_PART + (size_t)(tile * 8 + wave) * 24576;
        const char* tfb = ws + WS_FLAG + F_PARK + (tile * 8 + wave) * 256;
        const unsigned tfo = (unsigned)((lane & 3) ^ kp) * 64u;
        const unsigned ea0 = ldsbase + 131072 + (wave & 3) * 8192 + (wave >> 2) * 4096 + l15 * 128 + (((lane >> 5) * 2 ^ (lane & 7)) << 4) + (q4 & 1) * 8;
        typedef __attribute__((address_space(3))) u32x2 lds_u32x2;
#define OSND(a, j) do { constexpr int x_ = ((a) ^ 1) | (((j) >> 1) & 2 ^ 2);                                             \
            u32x4 p_ = u32x4{cvtpk(acc[a][j][0], acc[a][j][1]), cvtpk(acc[a][j][2], acc[a][j][3]), cvtpk(acc[a][(j) + 1][0], acc[a][(j) + 1][1]), cvtpk(acc[a][(j) + 1][2], acc[a][(j) + 1][3])}; \
            if (x_ == 1) { _Pragma("unroll") for (int k = 0; k < 4; ++k) p_[k] = min(p_[k], 0xfffffffeu); }     \
            asm volatile("global_store_dwordx4 %0, %1, %2 offset:%3 sc1\n\ts_nop 1" :: "v"(xvo), "v"(p_), "s"(pb + (kp ^ x_) * 6144 + (x_ - 1) * 2048), "i"(((j) & 2) * 512) : "memory"); } while (0)
#define OUF(i, u) do { ORL(i, ((u) >> 1) & 3, (u) & 1, 4 + (((u) >> 3) & 3)); ORH(i, ((u) >> 1) & 3, (u) & 1, 4 + (((u) >> 3) & 3)); } while (0)
#define OQ(q) do { constexpr int p_ = (q) + 4; const bf16x8 w_ = CAT8(wl[(q) & 7], wh[(q) & 7]);                                           \
            if ((q) == 16 || (q) == 24) { asm volatile("s_waitcnt vmcnt(%0)" :: "i"((q) == 16 ? 4 : 0) : "memory"); SB(); }               \
            OMF(0, 12 + (((q) >> 3) & 3), ((q) >> 2) & 1, (q) & 3); SB();                                                                 \
            if (p_ < 32) ORL(p_ & 7, (p_ >> 3) & 3, (p_ >> 2) & 1, p_ & 3); else OUF(p_ & 7, p_ - 32);                                    \
            ORQ(384 + 2 * (q)); SB();                                                                                                     \
            OMF(1, 12 + (((q) >> 3) & 3), ((q) >> 2) & 1, (q) & 3); SB();                                                                 \
            if (p_ < 32) ORH(p_ & 7, (p_ >> 3) & 3, (p_ >> 2) & 1, p_ & 3); else if ((q) >= 30) OUF(((q) - 26) & 7, (q) - 26);            \
            if ((q) >= 10 && (q) < 14) ea[((q) - 10) & 3] = *(lds_u32x2*)(uintptr_t)(ea0 ^ (unsigned)((((q) - 10) & 1) * 16 + (((q) - 10) & 2) * 32)); \
            if ((q) == 14 || (q) == 15) zn[(q) & 1] = __builtin_bit_cast(u32x4, lds_rd(ldsbase + 131072 + 2048 + (wave & 3) * 8192 + (wave >> 2) * 4096 + ((q) & 1) * 1024 + xvo)); \
            SB(); } while (0)
#define OU(u) do { const bf16x8 w_ = CAT8(wl[(u) & 7], wh[(u) & 7]);                                                                      \
            OMF((u) >> 5, 12 + (((u) >> 1) & 3), (u) & 1, 4 + (((u) >> 3) & 3)); SB();                                                    \
            if ((u) + 6 < 64) OUF(((u) + 6) & 7, (u) + 6);                                                                                \
            if ((u) == 1) OSND(0, 0); if ((u) == 3) OSND(1, 0); if ((u) == 5) OSND(0, 2); if ((u) == 7) OSND(1, 2);                       \
            if ((u) == 19) OSND(0, 4);                                                                                                    \
            if ((u) == 22) { asm volatile("s_waitcnt vmcnt(1)" ::: "memory"); FLAG_PUT(tfb + kp * 64, tag, 1); }     \
            if ((u) == 35) { OSND(0, 6); asm volatile("s_setprio 0"); S2T(1); }                                                                   \
            if ((u) == 52) asm volatile("global_load_dwordx2 %0, %1, %2 sc1" : "=v"(cxf) : "v"(tfo), "s"(tfb) : "memory");               \
            SB(); } while (0)
#define OQ8(q) do { OQ(q); OQ((q) + 1); OQ((q) + 2); OQ((q) + 3); OQ((q) + 4); OQ((q) + 5); OQ((q) + 6); OQ((q) + 7); } while (0)
#define OU8(q) do { OU(q); OU((q) + 1); OU((q) + 2); OU((q) + 3); OU((q) + 4); OU((q) + 5); OU((q) + 6); OU((q) + 7); } while (0)
        asm volatile("s_setprio 1");
        OQ8(0); OQ8(8); OQ8(16); OQ8(24); OST(13); OU8(0); OU8(8); OU8(16); OU8(24); OU8(32); OU8(40); OU8(48); OU8(56);
        MF_DONE();
        OST(14);
        PH_STAMP(5);
        SST(6); S2T(2);
        SB();
        if (__builtin_expect(nrt == 0, 0)) {
            f32x4 ys;
#pragma unroll
            for (int i = 0; i < 7; ++i) PIN(yq[i]);
            PIN(rq);
            const float sig = rbf(1.f / (1.f + __expf(-__uint_as_float(rq))));
#pragma unroll
            for (int j = 0; j < 4; ++j) {
                const float s_ = ((__uint_as_float(yq[0][j]) + __uint_as_float(yq[1][j])) + (__uint_as_float(yq[2][j]) + __uint_as_float(yq[3][j])))
                               + ((__uint_as_float(yq[4][j]) + __uint_as_float(yq[5][j])) + __uint_as_float(yq[6][j]));
                auto rr = __builtin_amdgcn_permlane16_swap(__float_as_uint(s_), __float_as_uint(s_), false, false);
                ys[j] = rbf(__uint_as_float(rr[0]) + __uint_as_float(rr[1])) * sig; }
            char* yl = lds + 65536 + wave * 256;
            *(u32x2*)(yl + (lane >> 5) * 128 + (l15 >> 2) * 32 + (l15 & 1) * 16 + (l15 & 2) * 4) = u32x2{cvtpk(ys[0], ys[1]), cvtpk(ys[2], ys[3])};
            asm volatile("" ::: "memory");
            const int m = r0 + l15;
#pragma unroll
            for (int i = 0; i < 2; ++i) { const u32x4 yn = *(const u32x4*)(yl + (m == ri1 ? 0 : 128) + i * 64 + q4 * 16);
#pragma unroll
                for (int k = 0; k < 4; ++k) { const unsigned z_ = cvtpk(HALF(zn[i][k], 0) + HALF(yn[k], 0), HALF(zn[i][k], 1) + HALF(yn[k], 1));
                    zn[i][k] = m == ri1 || m == ri2 ? z_ : zn[i][k]; } }
        }
        float gg[2][8], xx[2][8];
#pragma unroll
        for (int i = 0; i < 2; ++i)
#pragma unroll
            for (int j = 0; j < 8; ++j) { gg[i][j] = HALF(zn[i][j >> 1], j); xx[i][j] = HALF(ea[2 * i + (j >> 2)][(j >> 1) & 1], j); PIN(gg[i][j]); PIN(xx[i][j]); }
        unsigned ovo = (unsigned)((l15 & 7) * DM + 32 * (l15 >> 3) + 8 * q4) * 2u; PIN(ovo);
        const char* ob = (const char*)(outp + (size_t)r0 * DM + ntc * 128 + 64 * ch);
        asm volatile("" : "+s"(ob));
        PH_STAMP(6);
        { unsigned c_, m_;
          asm volatile("s_waitcnt vmcnt(0)\n\ts_mov_b32 %7, 0x40000\n"
                       "1:\n\tv_cmp_ne_u64 vcc, %11, %6\n\ts_and_b32 vcc_lo, vcc_lo, 12\n\ts_cbranch_scc0 2f\n\t"
                       "global_load_dwordx2 %6, %12, %13 sc1\n\ts_waitcnt vmcnt(0)\n\ts_sub_u32 %7, %7, 1\n\ts_cbranch_scc0 1b\n"
                       "2:\n\tglobal_load_dwordx4 %1, %9, %10 offset:-1024 sc1\n\tglobal_load_dwordx4 %2, %9, %10 offset:1024 sc1\n\t"
                       "global_load_dwordx4 %4, %9, %10 sc1\n\tglobal_load_dwordx4 %5, %9, %10 offset:2048 sc1\n\t"
                       "global_load_dwordx4 %0, %9, %10 offset:-3072 sc1\n\tglobal_load_dwordx4 %3, %9, %10 offset:-2048 sc1"
                       : "=&v"(pv[0]), "=&v"(pv[1]), "=&v"(pv[2]), "=&v"(pv[3]), "=&v"(pv[4]), "=&v"(pv[5]), "+v"(cxf), "=&s"(c_), "=&v"(m_)
                       : "v"(xvo), "s"(pb + kp * 6144 + 3072), "s"(tag), "v"(tfo), "s"(tfb) : "memory", "vcc", "scc"); }
        S2T(3);
#pragma unroll
        for (int i = 0; i < 16; i += 2) { float g_[2];
#pragma unroll
            for (int e_ = 0; e_ < 2; ++e_) { float a_ = gg[i >> 3][(i & 7) + e_] * -1.4426950408889634f; PIN(a_); a_ = 1.f + __builtin_amdgcn_exp2f(a_); PIN(a_);
                g_[e_] = gg[i >> 3][(i & 7) + e_] * __builtin_amdgcn_rcpf(a_); PIN(g_[e_]); }
            const unsigned p_ = cvtpk(g_[0], g_[1]); gg[i >> 3][i & 7] = HALF(p_, 0); gg[i >> 3][(i & 7) + 1] = HALF(p_, 1); PIN(gg[i >> 3][i & 7]); PIN(gg[i >> 3][(i & 7) + 1]); }
        S2T(4);
        asm volatile("s_waitcnt vmcnt(0)" : "+v"(pv[0]), "+v"(pv[1]), "+v"(pv[2]), "+v"(pv[3]), "+v"(pv[4]), "+v"(pv[5]) :: "memory");
        S2T(5);
#pragma unroll 1
        for (int n_ = 0; n_ < 0x40000; ++n_) {
            const unsigned mx_ = max(max(max(pv[0][0], pv[0][1]), max(pv[0][2], pv[0][3])), max(max(pv[3][0], pv[3][1]), max(pv[3][2], pv[3][3])));
            if (__builtin_amdgcn_ballot_w64(mx_ == ~0u) == 0) break;
            asm volatile("global_load_dwordx4 %0, %2, %3 sc1\n\tglobal_load_dwordx4 %1, %2, %3 offset:1024 sc1\n\ts_waitcnt vmcnt(0)"
                         : "=&v"(pv[0]), "=&v"(pv[3]) : "v"(xvo), "s"(pb + kp * 6144) : "memory");
        }
        PH_STAMP(7); S2T(6);
        u32x4 ow[2];
#pragma unroll
        for (int i = 0; i < 2; ++i) {
            u32x4& ov = ow[i];
#pragma unroll
            for (int k = 0; k < 4; ++k) { float sv[2];
#pragma unroll
                for (int e_ = 0; e_ < 2; ++e_) sv[e_] = fmaf((acc[1][4 + 2 * i + (k >> 1)][2 * (k & 1) + e_] + HALF(pv[3 * i][k], e_)) + (HALF(pv[3 * i + 1][k], e_) + HALF(pv[3 * i + 2][k], e_)),
                                                             gg[i][2 * k + e_], xx[i][2 * k + e_]);
                ov[k] = cvtpk(sv[0], sv[1]); }
#pragma unroll
            for (int k = 0; k < 2; ++k) { auto rr = __builtin_amdgcn_permlane16_swap(ov[k], ov[2 + k], false, false); ov[k] = rr[0]; ov[2 + k] = rr[1]; }
        }
#pragma unroll
        for (int i = 0; i < 2; ++i) {
            u32x4 ov;
#pragma unroll
            for (int k = 0; k < 4; ++k) ov[k] = i ? __builtin_amdgcn_update_dpp(ow[1][k], ow[0][k], 0x128, 0xf, 0x3, false) : __builtin_amdgcn_update_dpp(ow[0][k], ow[1][k], 0x128, 0xf, 0xc, false);
            asm volatile("global_store_dwordx4 %0, %1, %2 sc1\n\ts_nop 1" :: "v"(ovo), "v"(ov), "s"(ob + i * (8 * DM * 2)) : "memory");
        }
        SST(7); SST_OUT();
        PH_STAMP(8);
        PH_STAMP(9);
    }
}

extern "C" void kernel_launch(void* const* d_in, const int* in_sizes, int n_in, void* d_out, int out_size, void* d_ws, size_t ws_size,
                              hipStream_t stream) {
    const u16* x    = (const u16*)d_in[0];
    const u16* ckv  = (const u16*)d_in[1];
    const u16* wq   = (const u16*)d_in[2];
    const u16* wuk  = (const u16*)d_in[3];
    const u16* wuv  = (const u16*)d_in[4];
    const u16* wo   = (const u16*)d_in[5];
    const u16* wr   = (const u16*)d_in[7];
    const u16* wg   = (const u16*)d_in[8];
    const u16* wu   = (const u16*)d_in[9];
    const u16* wd   = (const u16*)d_in[10];
    (void)in_sizes; (void)n_in; (void)out_size; (void)ws_size;
    (void)hipFuncSetAttribute((const void*)k_all, hipFuncAttributeMaxDynamicSharedMemorySize, 163840);
    k_all<<<256, 512, 163840, stream>>>(x, ckv, wuk, wuv, wr, (char*)d_ws + ((size_t)WSOFF << 20), wq, wo, wg, wu, wd, (u16*)d_out);
}
```

```cpp
#include <hip/hip_runtime.h>
#include <hip/hip_bf16.h>
#include <stdint.h>

typedef unsigned short u16;
typedef unsigned long long u64;
typedef short    bf16x8 __attribute__((ext_vector_type(8)));
typedef short    bf16x4 __attribute__((ext_vector_type(4)));
typedef float    f32x4  __attribute__((ext_vector_type(4)));
typedef float    f32x2  __attribute__((ext_vector_type(2)));
typedef __bf16   bfx2   __attribute__((ext_vector_type(2)));
typedef unsigned u32x4  __attribute__((ext_vector_type(4)));
typedef unsigned u32x2  __attribute__((ext_vector_type(2)));

#define DI __device__ __forceinline__

constexpr int SQ = 2048, DM = 1024, NH = 32, HD = 128, DC = 512, FF = 3584;

#define WSOFF 0
constexpr size_t WS_R    = 4u << 20;
constexpr size_t WS_FLAG = (4u << 20) + 65536;
constexpr size_t WS_GP   = 6u << 20;
constexpr size_t WS_YP   = 7u << 20;
constexpr size_t WS_PART = 96u << 20;
constexpr size_t WS_XF   = 16u << 20;
#define HSTR 1310720
constexpr size_t WS_KV   = 24u << 20;
constexpr size_t WS_CTX  = 72u << 20;
constexpr int F_EPOCH = 0, F_TOP = 1024, F_LN = 4096, F_KV = 8192, F_GU = 12288  , F_Y = 20480  , F_PARK = 524288  ,
              F_CTX = 65536  ;

#define PH_STAMP(i)
#define SST(i)
#define S2T(i)
#define SST_OUT()
#define ZST(i)
#define ZST_OUT()
DI unsigned cvtpk(float lo, float hi) { f32x2 v = {lo, hi}; bfx2 b = __builtin_convertvector(v, bfx2); return __builtin_bit_cast(unsigned, b); }
DI float rbf(float x) { return __uint_as_float(cvtpk(x, 0.f) << 16); }
#define DPP_ADD(v, ctrl, rmask) v += __uint_as_float(__builtin_amdgcn_update_dpp(0u, __float_as_uint(v), ctrl, rmask, 0xf, false))
DI unsigned tkey(unsigned fbits, int i) { unsigned u = fbits >> 16; u ^= (u & 0x8000u) ? 0xffffu : 0x8000u; return (u << 16) | (unsigned)(2047 - i); }
DI void merge_top2(unsigned& a1, unsigned& a2, unsigned b1, unsigned b2) { const unsigned lo = min(a1, b1); a1 = max(a1, b1); a2 = max(lo, max(a2, b2)); }
#define DPP_TOP2(a1, a2, ctrl, rmask) merge_top2(a1, a2, __builtin_amdgcn_update_dpp(0u, a1, ctrl, rmask, 0xf, false), __builtin_amdgcn_update_dpp(0u, a2, ctrl, rmask, 0xf, false))

#define GLDS_S(voff, sbase, ldsoff, imm)                                                                        \
    asm volatile("s_mov_b32 m0, %2\n\ts_nop 0\n\tglobal_load_lds_dwordx4 %0, %1 offset:%3"                       \
                 :: "v"(voff), "s"(sbase), "s"(ldsoff), "i"(imm) : "memory")
#define GLDS_C(voff, sbase, ldsoff, imm)                                                                        \
    asm volatile("s_mov_b32 m0, %2\n\ts_nop 0\n\tglobal_load_lds_dwordx4 %0, %1 offset:%3 sc1"                   \
                 :: "v"(voff), "s"(sbase), "s"(ldsoff), "i"(imm) : "memory")
#define LGKM0_BAR()    asm volatile("s_waitcnt lgkmcnt(0)\n\ts_barrier" ::: "memory")
#define SB() __builtin_amdgcn_sched_barrier(0)
#define CAT_(a, b) a##b
#define CAT(a, b) CAT_(a, b)
#define PIN(x) asm volatile("" : "+v"(x))
#define OADV(p_, d_) do { p_ += (d_); asm volatile("" : "+s"(p_)); } while (0)
#define VADD(d, a, b)    asm volatile("v_add_f32 %0, %1, %2" : "=v"(d) : "v"(a), "v"(b))
#define KEEP(x) asm volatile("" :: "v"(x))
#define MFA16(c, a, b) asm volatile("v_mfma_f32_16x16x32_bf16 %0, %1, %2, %0" : "+v"(c) : "v"(a), "v"(b))
#define MFA16Z(c, a, b) asm volatile("v_mfma_f32_16x16x32_bf16 %0, %1, %2, 0" : "=&v"(c) : "v"(a), "v"(b))
#define MFA16C(c, a, b, c0) asm volatile("v_mfma_f32_16x16x32_bf16 %0, %1, %2, %3" : "=&v"(c) : "v"(a), "v"(b), "v"(c0))
#define MF_DONE() do { SB(); asm volatile("s_nop 7\n\ts_nop 7"); SB(); } while (0)
#define ST16_SC1(p, v) asm volatile("global_store_dwordx4 %0, %1, off sc1\n\ts_nop 1" :: "v"(p), "v"(v) : "memory")
#define ST4_SC1(p, v)  asm volatile("global_store_dword %0, %1, off sc1" :: "v"(p), "v"(v) : "memory")
#define FLAG_PUT(p_, val_, on_) do { u64 sv_, tv_; unsigned tz_; const u64 v_ = (val_); const unsigned m_ = __builtin_amdgcn_readfirstlane((on_) ? 1u : 0u); \
        asm volatile("s_cmp_eq_u32 %5, 0\n\ts_cbranch_scc1 1f\n\tv_mov_b64 %1, %3\n\tv_mov_b32 %2, 0\n\ts_mov_b64 %0, exec\n\ts_mov_b64 exec, 1\n\t" \
                     "global_store_dwordx2 %2, %1, %4 sc1\n\ts_mov_b64 exec, %0\n1:"                                                      \
                     : "=&s"(sv_), "=&v"(tv_), "=&v"(tz_) : "s"(v_), "s"(p_), "s"(m_) : "memory", "scc"); } while (0)
#define FLAG_PUTZ(p_, val_, nz_) do { u64 sv_, tv_; unsigned tz_; const u64 v_ = (val_);                                                   \
        asm volatile("s_cmp_lg_u32 %5, 0\n\ts_cbranch_scc1 1f\n\tv_mov_b64 %1, %3\n\tv_mov_b32 %2, 0\n\ts_mov_b64 %0, exec\n\ts_mov_b64 exec, 1\n\t" \
                     "global_store_dwordx2 %2, %1, %4 sc1\n\ts_mov_b64 exec, %0\n1:"                                                      \
                     : "=&s"(sv_), "=&v"(tv_), "=&v"(tz_) : "s"(v_), "s"(p_), "s"(nz_) : "memory", "scc"); } while (0)
#define FLAG_GET(r_, p_) asm volatile("global_load_dwordx2 %0, %1, off sc1" : "=v"(r_) : "v"(p_) : "memory")
#define FLAG_WAIT(r_, p_, tag_, N) do { unsigned c_;                                                                                      \
        asm volatile("s_waitcnt vmcnt(%4)\n\ts_mov_b32 %1, 0x40000\n"                                                                     \
                     "1:\n\tv_cmp_ne_u64 vcc, %3, %0\n\ts_cmp_eq_u64 vcc, 0\n\ts_cbranch_scc1 2f\n\t"                                      \
                     "s_sleep 2\n\tglobal_load_dwordx2 %0, %2, off sc1\n\ts_waitcnt vmcnt(0)\n\ts_sub_u32 %1, %1, 1\n\ts_cbranch_scc0 1b\n"  \
                     "2:" : "+v"(r_), "=&s"(c_) : "v"(p_), "s"(tag_), "i"(N) : "memory", "vcc", "scc"); } while (0)
#define FLAG_WAITM(r_, p_, tag_, N, m_) do { unsigned c_;                                                                                 \
        asm volatile("s_waitcnt vmcnt(%4)\n\ts_mov_b32 %1, 0x40000\n"                                                                     \
                     "1:\n\tv_cmp_ne_u64 vcc, %3, %0\n\ts_and_b32 vcc_lo, vcc_lo, %5\n\ts_cbranch_scc0 2f\n\t"                             \
                     "s_sleep 2\n\tglobal_load_dwordx2 %0, %2, off sc1\n\ts_waitcnt vmcnt(0)\n\ts_sub_u32 %1, %1, 1\n\ts_cbranch_scc0 1b\n"  \
                     "2:" : "+v"(r_), "=&s"(c_) : "v"(p_), "s"(tag_), "i"(N), "s"(m_) : "memory", "vcc", "scc"); } while (0)
typedef __attribute__((address_space(3))) bf16x4 lds_bf16x4;
DI bf16x4 tr_rd(unsigned a) { return __builtin_amdgcn_ds_read_tr16_b64_v4i16((lds_bf16x4*)(uintptr_t)a); }
typedef __attribute__((address_space(3))) bf16x8 lds_bf16x8;
DI bf16x8 lds_rd(unsigned a) { return *(lds_bf16x8*)(uintptr_t)a; }
#define CAT8(lo_, hi_) bf16x8{lo_[0], lo_[1], lo_[2], lo_[3], hi_[0], hi_[1], hi_[2], hi_[3]}

#define POFF 32.f
#define LTHR 8192.f
DI float quad_max(float v) {
    auto r1 = __builtin_amdgcn_permlane16_swap(__float_as_uint(v), __float_as_uint(v), false, false);
    v = fmaxf(__uint_as_float(r1[0]), __uint_as_float(r1[1]));
    auto r2 = __builtin_amdgcn_permlane32_swap(__float_as_uint(v), __float_as_uint(v), false, false);
    return fmaxf(__uint_as_float(r2[0]), __uint_as_float(r2[1])); }
#define QWD 3
#define QS1 0xbfff
constexpr int q_xb(int s) { return s >= 1 && s <= 12; }
constexpr int q_xa(int s) { return s >= 0 && s <= 11; }
constexpr int q_s0(int s) { int n = 0; for (int i = 0; i < s; ++i) n += (QS1 >> i) & 1; return n; }
constexpr int q_kv(int kt, int f) { const int s = (kt - 13) * 16 + f - 12; return s >= 0 && s % 3 == 0 && s / 3 < 12 ? s / 3 : -1; }
constexpr int q_op(int s, int f) { return (f == 0 || f == 2 ? q_xb(s) : f == 8 || f == 10 ? q_xa(s) : f == 12 || f == 14 ? s + QWD < 16 : f == 3 ? s == 12
                                          : f == 4 ? (QS1 >> s) & 1 : 0) + (q_kv(s, f) >= 0); }
constexpr int q_after(int p, int kt) { int n = 0; for (int i = p + 1; i <= kt * 16 + 11; ++i) n += q_op(i >> 4, i & 15); return n; }
constexpr int q_min(int a, int b) { return a < b ? a : b; }
constexpr int q_wait(int kt) {
    int n = kt <= 2 ? 4 * (2 - kt) + q_after(-1, kt) : q_after((kt - 2) * 16 + 2, kt);
    if (kt + 1 >= QWD) n = q_min(n, q_after((kt + 1 - QWD) * 16 + 14, kt));
    if (kt == 13) n = q_min(n, q_after(12 * 16 + 3, kt));
    return n; }
constexpr int q_fl() { int s = 0; while (q_s0(s + 1) < 8) ++s; int kt = s; while (q_wait(kt) > q_after(s * 16 + 6, kt)) ++kt; return kt; }
constexpr int Q_NST = q_s0(16);
static_assert(Q_NST >= 8, "");
constexpr int o_xp(int st, int i) { return st <= 5 ? 64 + 4 * (4 * (st - 4) + i) : 32 * (st - 3) + (i == 0 ? 0 : i == 1 ? 4 : i == 2 ? 12 : 16); }
constexpr int o_wp(int st, int i) { return st <= 7 ? 16 * (st - 4) + 4 + 6 * i : 32 * (st - 5) + 24 + 4 * i; }
constexpr int o_x(int h) { for (int st = 4; st < 16; ++st) for (int i = 0; i < 4; ++i) if (o_xp(st, i) == h) return 4 * st + i; return -1; }
constexpr int o_w(int h) { for (int st = 4; st < 16; ++st) for (int i = 0; i < 2; ++i) if (o_wp(st, i) == h) return 2 * st + i; return -1; }
constexpr int o_after(int p, int e) { int n = 0; for (int h = p + 1; h < e; ++h) n += (o_x(h) >= 0) + (o_w(h) >= 0); return n; }
constexpr int o_wait(int kt) { return o_after(kt == 11 ? o_wp(15, 1) : o_xp(kt + 1, 3), 32 * kt + 24); }
constexpr int L_PFT = 149504, L_DUMP = 155648;
constexpr int CFS = 128;
constexpr unsigned F1S = 0xffff642fu, F1F = 0xffff5310u, F2S = 0xff64fff0u, F2F = 0xff53f21fu;
constexpr int F1T = 3;

__global__ __launch_bounds__(512, 2) void k_all(const u16* __restrict__ x, const u16* __restrict__ ckv, const u16* __restrict__ wuk, const u16* __restrict__ wuv,
                                                const u16* __restrict__ w_r, char* __restrict__ ws, const u16* __restrict__ wq, const u16* __restrict__ wo,
                                                const u16* __restrict__ w_gate, const u16* __restrict__ w_up,
                                                const u16* __restrict__ w_down, u16* __restrict__ out) {
    extern __shared__ __attribute__((aligned(16))) char lds[];
    const int b = blockIdx.x, xcd = b & 7, idx = b >> 3, head = xcd * 4 + (idx >> 3), jj = idx & 7;
    const int hp = head ^ 1;
    const unsigned ldsbase = (unsigned)(uintptr_t)(__attribute__((address_space(3))) char*)lds;
    const int wave = __builtin_amdgcn_readfirstlane(threadIdx.x >> 6);
    const int qbA = 15 - jj, qbB = jj;
#define KROW(g) (((2 * ((g) & 3) + ((g) >> 2)) * 8 + jj) * 32)
    const int qw = wave < 4 ? wave : 7 - wave, qb = wave < 4 ? qbA : qbB;
    bf16x8 qf[8];
    u64 tag;
    u16* outp = out;
    int ri1, ri2;
    PH_STAMP(0);
    SST(0);
    ZST(0);
    u32x4 pk[16];
    bf16x8 xr[4][2][2];
    u32x4 rv;
    const char* xg;
#define QXL1(j) asm volatile("global_load_dwordx4 %0, %1, %2 offset:%3 sc1" : "=v"(xr[(j) >> 2][(j) & 1][((j) >> 1) & 1]) : "v"(xvo), "s"(xg + (size_t)((j) >> 2) * 262144), \
                            "i"(((j) & 1) * 2048 + (((j) >> 1) & 1) * 1024) : "memory")
    const char* sb_[4];
    unsigned qa, qa2; bf16x4 ql[8], qh[8];
#define QFA(s, f) ((((f) & 1) ? qa2 : qa) + (unsigned)(((s) & 3) * 16384 + ((f) >> 3) * 8192 + (((f) >> 2) & 1) * 1024 + (((f) >> 1) & 1) * 512))
#define QRL(s, f) ql[(f) & 7] = tr_rd(QFA(s, f))
#define QRH(s, f) qh[(f) & 7] = tr_rd(QFA(s, f) + 2048u)
    unsigned xvo, qwo;
    const u16* wqb;
#define QWOFF() ({ unsigned l_ = threadIdx.x & 63; asm volatile("" : "+v"(l_)); (unsigned)(((l_ >> 2) & 7) * (NH * HD) + 32 * (l_ >> 5) + 8 * ((l_ & 3) ^ ((l_ >> 3) & 2))) * 2u; })
    {
        constexpr int LDW = NH * HD, K = DC;
        int tid = threadIdx.x; asm volatile("" : "+v"(tid));
        const int lane = tid & 63;
        const int wm = wave >> 2, wn = wave & 3;
        const int row = b * 8 + wave;
        u32x4 lx[2], lw[2]; u32x2 ep;
        asm volatile("global_load_dwordx4 %0, %5, %6\n\tglobal_load_dwordx4 %1, %5, %6 offset:1024\n\tglobal_load_dwordx4 %2, %5, %7\n\tglobal_load_dwordx4 %3, %5, %7 offset:1024\n\t"
                     "global_load_dwordx2 %4, %8, off sc1"
                     : "=&v"(lx[0]), "=&v"(lx[1]), "=&v"(lw[0]), "=&v"(lw[1]), "=&v"(ep) : "v"(lane * 16), "s"(x + (unsigned)(row * DM)), "s"(w_r), "v"(ws + WS_FLAG + F_EPOCH) : "memory");
#define XKEY(r) (((r) >> 1) & 7)
        unsigned xoff[4], woff[2];
        { const unsigned x0_ = (unsigned)(KROW(wave) + (lane >> 3)) * (K * 2) + (unsigned)((lane & 7) ^ (lane >> 4)) * 16u;
          xoff[0] = x0_; xoff[1] = (x0_ ^ 64u) + 7168u; xoff[2] = x0_ + 14336u; xoff[3] = xoff[1] + 14336u;
          woff[1] = (unsigned)((8 * wave + ((lane >> 2) & 7)) * LDW + hp * HD + 32 * (lane >> 5) + 8 * ((lane & 3) ^ (2 * (wave & 1)))) * 2u; woff[0] = woff[1] + 896u; }
        const char* xsp = (const char*)ckv;
        const char* wsp = (const char*)wuk - 896;
        const char* wsv = (const char*)wuv - 2944;
        const unsigned m0b = ldsbase + wave * 4096;
#define KM0(g, w_) asm volatile("s_add_u32 m0, %0, %1" :: "s"(m0b), "i"(((g) & 1) * 65536 + (w_) * 32768) : "memory", "scc")
#define KRX(i) asm volatile("global_load_lds_dwordx4 %0, %1 offset:%2" :: "v"(xoff[i]), "s"(xsp), "i"((i) * 1024) : "memory")
#define KRW(i) asm volatile("global_load_lds_dwordx4 %0, %1 offset:%2 nt" :: "v"(woff[(i) & 1]), "s"((i) < 2 ? wsp : wsv), "i"((i) * 1024) : "memory")
#define STAGE(g) do { KM0(g, 0); asm volatile("s_nop 0"); KRX(0); KRX(1); KRX(2); KRX(3); KM0(g, 1); asm volatile("s_nop 0"); KRW(0); KRW(1); KRW(2); KRW(3); \
                      OADV(xsp, 128); OADV(wsp, 64 * LDW * 2); OADV(wsv, 64 * LDW * 2); } while (0)
#define KST(i)
        const unsigned pfl = ldsbase + L_DUMP + wave * 1024;
#define PFX(g) do { unsigned l_ = threadIdx.x & 63; asm volatile("" : "+v"(l_)); l_ = ((l_ >> 3) * DC + (l_ & 7) * 8) * 2u; GLDS_S(l_, pfxp + (g) * 64, pfl, 0); } while (0)
#define PFQ(i) do { unsigned l_ = threadIdx.x & 63; asm volatile("" : "+v"(l_)); l_ = ((l_ >> 4) * LDW + (l_ & 15) * 8) * 2u; GLDS_S(l_, pfqp + (size_t)(4 * (i)) * LDW, pfl, 0); } while (0)
        STAGE(0);
        SB(); ZST(1);
        int w2 = wave; asm volatile("" : "+s"(w2));
        xg = ws + WS_XF + (unsigned)((w2 < 4 ? qbA * 4 + w2 : qbB * 4 + 7 - w2) * 4096);
        wqb = wq + (unsigned)(8 * w2 * LDW + head * HD);
        const u16* pfqp = wq + (unsigned)((128 * jj + 16 * w2) * LDW + head * HD);
        const u16* pfxp = ckv + (unsigned)((KROW(w2) + 8 * (head & 3)) * DC);
        unsigned xa[2][2], wa[2][2];
        const int l15 = lane & 15, q4 = lane >> 4;
#pragma unroll
        for (int s = 0; s < 2; ++s) {
            const int r0 = l15;
            xa[0][s] = ldsbase + (unsigned)((wm * 128 + r0) * 128 + ((((4 * s + q4) ^ XKEY(r0))) << 4));
            wa[0][s] = (ldsbase + 32768u + (unsigned)(q4 * 4096 + wn * 512 + (l15 >> 2) * 64 + (q4 & 1) * 32 + (lane & 3) * 8)) ^ (32u * s);
            xa[1][s] = xa[0][s] + 65536u; wa[1][s] = wa[0][s] + 65536u;
            PIN(xa[0][s]); PIN(xa[1][s]); PIN(wa[0][s]); PIN(wa[1][s]); }
        u64 pf0;
        { u64 pw_ = (u64)(((w2 & 2) ? wuv : wuk) + (unsigned)((128 + 192 * (w2 & 1) + jj) * LDW + hp * HD)), pc_ = (u64)ckv + (unsigned)(b * 8192 + (w2 & 3) * 2048);
          asm volatile("" : "+s"(pw_), "+s"(pc_));
          unsigned o0_ = (unsigned)(lane >> 1) * (8 * LDW * 2) + (lane & 1) * 128u, o1_ = (lane & 15) * 128u; PIN(o0_); PIN(o1_);
          pf0 = (lane < 48 ? pw_ : pc_) + (lane < 48 ? o0_ : o1_); PIN(pf0); }
        asm volatile("" : "+s"(pfqp), "+s"(pfxp), "+s"(xg), "+s"(wqb), "+s"(outp));
        SB();
        asm volatile("s_waitcnt vmcnt(8)" : "+v"(lx[0]), "+v"(lx[1]), "+v"(lw[0]), "+v"(lw[1]), "+v"(ep) :: "memory");
        KST(1); ZST(2);
#define HALF(v_, i) __uint_as_float(((i) & 1) ? (v_) & 0xffff0000u : (v_) << 16)
#define BOTH(v_) f32x2{HALF(v_, 0), HALF(v_, 1)}
        {
            f32x2 v[8];
#pragma unroll
            for (int i = 0; i < 8; ++i) v[i] = BOTH(lx[i >> 2][i & 3]);
            const float pv_ = __uint_as_float(__builtin_amdgcn_readfirstlane(__float_as_uint(v[0][0])));
            f32x2 s2 = f32x2{0.f, 0.f}, q2 = f32x2{0.f, 0.f};
#pragma unroll
            for (int i = 0; i < 8; ++i) { const f32x2 d = v[i] - pv_; s2 += d; q2 += d * d; }
            float s = s2[0] + s2[1], q = q2[0] + q2[1];
            DPP_ADD(s, 0x111, 0xf); DPP_ADD(q, 0x111, 0xf); DPP_ADD(s, 0x112, 0xf); DPP_ADD(q, 0x112, 0xf); DPP_ADD(s, 0x114, 0xf); DPP_ADD(q, 0x114, 0xf);
            DPP_ADD(s, 0x118, 0xf); DPP_ADD(q, 0x118, 0xf); DPP_ADD(s, 0x142, 0xa); DPP_ADD(q, 0x142, 0xa); DPP_ADD(s, 0x143, 0xc); DPP_ADD(q, 0x143, 0xc);
            const float dm = __uint_as_float(__builtin_amdgcn_readlane(__float_as_uint(s), 63)) * (1.f / DM);
            const float var_bf = rbf(fmaxf(fmaf(-dm, dm, __uint_as_float(__builtin_amdgcn_readlane(__float_as_uint(q), 63)) * (1.f / DM)), 0.f));
            const float mean_bf = rbf(pv_ + dm), den = rbf(__builtin_amdgcn_sqrtf(rbf(var_bf + 1e-5f))), rden = __builtin_amdgcn_rcpf(den);
            u32x4 xw[2]; float r2[2] = {0.f, 0.f};
#pragma unroll
            for (int i = 0; i < 8; ++i) {
                const f32x2 c_ = v[i] - mean_bf;
                const unsigned cp = cvtpk(c_[0], c_[1]);
                const f32x2 cb = BOTH(cp);
                f32x2 n_ = cb * rden;
                n_ += (cb - n_ * den) * rden;
                const unsigned np = cvtpk(n_[0], n_[1]);
                const unsigned wp = lw[i >> 2][i & 3];
                r2[i & 1] = __builtin_amdgcn_fdot2_f32_bf16(__builtin_bit_cast(bfx2, np), __builtin_bit_cast(bfx2, wp), r2[i & 1], false);
                xw[i >> 2][i & 3] = np;
            }
            char* lr = lds + 131072 + wave * 2064 + lane * 16;
            *(u32x4*)lr = xw[0]; *(u32x4*)(lr + 1024) = xw[1];
            s = r2[0] + r2[1];
            DPP_ADD(s, 0x111, 0xf); DPP_ADD(s, 0x112, 0xf); DPP_ADD(s, 0x114, 0xf); DPP_ADD(s, 0x118, 0xf); DPP_ADD(s, 0x142, 0xa); DPP_ADD(s, 0x143, 0xc);
            if (lane == 63) *(float*)(lds + 131072 + wave * 2064 + 2048) = rbf(s);
        }
        SB(); ZST(3);
        STAGE(1);
        asm volatile("s_bitcmp0_b32 %2, 2\n\ts_cbranch_scc1 1f\n\ts_mov_b32 m0, %1\n\ts_nop 0\n\tglobal_load_lds_dword %0, off\n1:" :: "v"(pf0), "s"(pfl), "s"(wave) : "memory", "scc");
        tag = (((u64)(unsigned)__builtin_amdgcn_readfirstlane(ep[1]) << 32) | (unsigned)__builtin_amdgcn_readfirstlane(ep[0])) + 0x9E3779B97F4A7C15ull;
        float rr;
        const unsigned l16 = (unsigned)lane * 16u;
        u32x2 xi_[4];
#define XI_RD() do { int t_ = threadIdx.x; asm volatile("" : "+v"(t_)); const int r8 = t_ & 7, pc = t_ >> 3;                               \
            const char* lr = lds + 131072 + r8 * 2064 + (pc >> 2) * 64 + ((pc >> 1) & 1) * 32 + (pc & 1) * 8;                              \
            xi_[0] = *(const u32x2*)lr; xi_[1] = *(const u32x2*)(lr + 16); xi_[2] = *(const u32x2*)(lr + 1024); xi_[3] = *(const u32x2*)(lr + 1040); \
            rr = *(const float*)(lds + 131072 + wave * 2064 + 2048); } while (0)
#define XI_ST() do { int t_ = threadIdx.x; asm volatile("" : "+v"(t_)); const int r8 = t_ & 7, pc = t_ >> 3;                               \
            char* dst = ws + WS_XF + (size_t)(pc >> 3) * 262144 + (b >> 1) * 2048 + (pc & 7) * 256 + ((b & 1) * 8 + r8) * 16;              \
            const u32x4 pa = u32x4{xi_[0][0], xi_[0][1], xi_[1][0], xi_[1][1]}, pb = u32x4{xi_[2][0], xi_[2][1], xi_[3][0], xi_[3][1]};    \
            ST16_SC1(dst, pa); ST16_SC1(dst + 2097152, pb); } while (0)
        SB(); ZST(4);
        asm volatile("s_waitcnt vmcnt(%1)\n\ts_bitcmp1_b32 %0, 2\n\ts_cbranch_scc1 1f\n\ts_waitcnt vmcnt(8)\n1:\n\ts_barrier" :: "s"(wave), "i"(9) : "memory", "scc");
        KST(2); ZST(6);
        f32x4 a16[8][4];
        bf16x8 xf[2][4];
        bf16x4 wl[4], wh[4];
#define KXR(kt, q, mb) xf[(q) & 1][mb] = lds_rd(xa[(kt) & 1][(q) >> 1] + (unsigned)((((q) & 1) * 4 + (mb)) * 2048))
#define KWL(kt, s32, nb) wl[nb] = tr_rd(wa[(kt) & 1][(nb) & 1] + (unsigned)((s32) * 16384 + ((nb) >> 1) * 2048))
#define KWH(kt, s32, nb) wh[nb] = tr_rd(wa[(kt) & 1][(nb) & 1] + (unsigned)((s32) * 16384 + ((nb) >> 1) * 2048 + 256))
#define KMF(kt, q, g) do { const bf16x8 w_ = CAT8(wl[(g) >> 2], wh[(g) >> 2]); constexpr int m_ = ((q) & 1) * 4 + ((g) & 3);               \
            if ((kt) == 0 && (q) < 2) { if ((g) >= 8) MFA16Z(a16[m_][(g) >> 2], xf[(q) & 1][(g) & 3], w_); else MFA16Z(a16[m_][(g) >> 2], w_, xf[(q) & 1][(g) & 3]); } \
            else                      { if ((g) >= 8) MFA16(a16[m_][(g) >> 2], xf[(q) & 1][(g) & 3], w_);  else MFA16(a16[m_][(g) >> 2], w_, xf[(q) & 1][(g) & 3]); } } while (0)
        u64 lnf;
#define LNFP (ws + WS_FLAG + F_LN + (wave * 32 + (lane & 31)) * 8)
#define KRQ(i) do { GLDS_S(qwo, wqb + (size_t)(((i) >> 1) * 64) * LDW, ldsbase + wave * 2048 + ((i) >> 1) * 16384 + ((i) & 1) * 896, ((i) & 1) * 128); } while (0)
        constexpr int KRS = 3, KPFD = 3;
#define KG(kt, q, g) do { constexpr int nk_ = (kt) + ((q) == 3), nq_ = ((q) + 1) & 3;                                                     \
            constexpr int xi = ((q) & 1) ? ((g) == 1 ? 0 : (g) == 2 ? 1 : (g) == 5 ? 2 : (g) == 6 ? 3 : -1) : (((g) & 1) && (g) < 8 ? (g) >> 1 : -1); \
            constexpr int ri = (q) == 3 ? ((g) == 9 ? 0 : (g) == 10 ? 1 : (g) == 13 ? 2 : (g) == 14 ? 3 : -1) : (((g) & 1) && (g) > 8 ? ((g) - 9) >> 1 : -1); \
            KMF(kt, q, g); SB();                                                                                                        \
            if (xi >= 0 && nk_ < 8) KXR(nk_, nq_, xi & 3);                                                                              \
            if (((q) & 1) && nk_ < 8) { if (((g) & 3) == 3) KWL(nk_, nq_ >> 1, (g) >> 2); if (((g) & 3) == 0 && (g) > 0) KWH(nk_, nq_ >> 1, (((g) >> 2) + 3) & 3); } \
            if (!((q) & 1) && (g) == 0 && (kt) + (q) > 0) KWH(kt, (q) >> 1, 3);                                                          \
            { constexpr int G = (q) == 3 ? (g) : (q) == 0 ? 16 + (g) : -9, T = (kt) + ((q) == 3 ? 2 : 1);          \
              if (T >= 2 && T < 8) { if (G == 0) KM0(T, 0); if (G == 4 * KRS) KM0(T, 1);                                                 \
                  if (G >= 1 && (G - 1) % KRS == 0 && (G - 1) / KRS < 8) { if ((G - 1) / KRS < 4) KRX(((G - 1) / KRS) & 3); else KRW(((G - 1) / KRS) & 3); } \
                  if (G == 29) OADV(xsp, 128); if (G == 30) OADV(wsp, 64 * LDW * 2); if (G == 31) OADV(wsv, 64 * LDW * 2); } }                                                 \
            if ((q) == 1 && (kt) + KPFD < 8) { if ((kt) >= 1 && (g) == 13) PFX((kt) + KPFD); }                \
            if ((q) == 2 && (kt) == 6 && (g) == 9) { qwo = QWOFF(); PIN(qwo); }                                                         \
            if ((q) == 1 && (kt) == 7) { if ((g) == 9) asm volatile("global_load_dwordx4 %0, %1, %2 sc1" : "=v"(rv) : "v"(xvo), "s"(ws + WS_R + wave * 1024) : "memory"); \
                                         if ((g) == 10) QXL1(0); if ((g) == 13) QXL1(1); if ((g) == 14) QXL1(2); }                      \
            if ((q) == 2 && (kt) == 7 && (g) == 2) QXL1(3);                                                                             \
            if ((q) == 2 && (kt) == 7 && (g) == 9) { int w_ = wave; asm volatile("" : "+s"(w_));                                        \
                sb_[0] = ws + WS_KV + (unsigned)(hp * HSTR + ((w_ >> 2) * 2 + (jj >> 2)) * 65536 + (jj & 3) * 8192 + (w_ & 3) * 2048); asm volatile("" : "+s"(sb_[0])); } \
            if ((q) == 2 && (kt) == 7 && (g) >= 11 && (g) < 14) { sb_[((g) - 10) & 3] = sb_[0] + ((g) - 10) * 262144; asm volatile("" : "+s"(sb_[((g) - 10) & 3])); } \
            if ((q) == 2 && (kt) == 7 && (g) == 14) { qa = ldsbase + (unsigned)((q4 >> 1) * 4096 + (4 * (q4 & 1) + (l15 >> 2)) * 64 + (q4 & 1) * 32 + (lane & 3) * 8); PIN(qa); } \
            if ((q) == 2 && (kt) == 7 && (g) == 15) { qa2 = qa ^ 32u; PIN(qa2); }                                                                              \
            if ((q) == 3 && (kt) == 6 && ri >= 0) KRQ(ri & 3);                                                                          \
            if ((q) == 0 && (kt) == 7 && ri >= 0 && 4 + ri < 2 * QWD) KRQ(4 + (ri & 3));                                                                    \
            if ((q) == 1 && (kt) == 1) { if ((g) == 8) XI_RD();                                                                       \
                if ((g) == 13) ST4_SC1((float*)(ws + WS_R) + row, rr);                               \
                if ((g) == 15) XI_ST(); }                                                                                               \
            if ((q) == 1 && ((kt) == 3 || (kt) == 4)) { if ((g) == 9) PFQ((2 * (kt) - 6) & 3); if ((g) == 11) PFQ((2 * (kt) - 5) & 3); }                                                    \
            if ((q) == 2 && (kt) == 5 && (g) == 12) FLAG_GET(lnf, LNFP);                                                                \
            SB(); } while (0)
#define KQ(kt, q) do { KG(kt, q, 0); KG(kt, q, 1); KG(kt, q, 2); KG(kt, q, 3); KG(kt, q, 4); KG(kt, q, 5); KG(kt, q, 6); KG(kt, q, 7);    \
                       KG(kt, q, 8); KG(kt, q, 9); KG(kt, q, 10); KG(kt, q, 11); KG(kt, q, 12); KG(kt, q, 13); KG(kt, q, 14); KG(kt, q, 15); } while (0)
#define KEXT(s) (((s) == 1 ? 3 : (s) == 5 ? 1 : 0) + ((s) >= 1 && (s) + KPFD < 8) + ((s) == 3 || (s) == 4 ? 2 : 0))
#define KSTEP(kt) do {                                                                                                                  \
        KQ(kt, 0); KQ(kt, 1); KQ(kt, 2);                                                                                                \
        if ((kt) == 6) FLAG_WAIT(lnf, LNFP, tag, 0);                                   \
                       \
        if ((kt) == 0) asm volatile("s_waitcnt vmcnt(%1) lgkmcnt(0)\n\ts_bitcmp1_b32 %0, 2\n\ts_cbranch_scc1 1f\n\ts_waitcnt vmcnt(%2)\n1:\n\ts_barrier" :: "s"(wave), "i"(KEXT(0) + 1), "i"(KEXT(0)) : "memory", "scc"); \
        else asm volatile("s_waitcnt vmcnt(%0) lgkmcnt(0)\n\ts_barrier" :: "i"(KEXT(kt)) : "memory");                                        \
        KST((kt) + 3);                                                                                                                  \
        if ((kt) == 2) FLAG_PUT(ws + WS_FLAG + F_LN + b * 8, tag, wave == 0);             \
        if ((kt) == 6) FLAG_PUT(ws + WS_FLAG + F_EPOCH, tag - 0x9E3779B97F4A7C15ull + 1, b == 0 && wave == 0);     \
        SB();                                                                                                                           \
        KQ(kt, 3);                                                                                                                      \
    } while (0)
        KXR(0, 0, 0); KXR(0, 0, 1); KWL(0, 0, 0); KWH(0, 0, 0); KXR(0, 0, 2); KXR(0, 0, 3);
        KWL(0, 0, 1); KWH(0, 0, 1); KWL(0, 0, 2); KWH(0, 0, 2); KWL(0, 0, 3); KWH(0, 0, 3);
        SB();
        KSTEP(0); ZST(7); KSTEP(1); KSTEP(2); KSTEP(3); KSTEP(4); KSTEP(5); KSTEP(6);
        xvo = l16;
        KQ(7, 0); KQ(7, 1); KQ(7, 2);
        if (wave < 4) {
            int t2_ = threadIdx.x; asm volatile("" : "+v"(t2_));
            unsigned l7_ = t2_ & 7, c7_ = jj >= 3 ? 7 : 8, b0_ = (unsigned)(((jj + 7) & 7) * 128 + head * 4 + (wave & 3)), b7_ = 3584u + head * 4 + (wave & 3);
            u64 pg_ = (u64)((wave & 1) ? w_up : w_gate), pd_ = (u64)w_down - 1792ull * 8192, po_ = (u64)wo - 2688ull * 8192;
            asm volatile("" : "+s"(c7_), "+s"(b0_), "+s"(b7_), "+s"(pg_), "+s"(pd_), "+s"(po_));
            const unsigned lt_ = l7_ < (unsigned)jj ? l7_ : 0;
            unsigned n_ = b0_ + (13u - lt_) * lt_ * 64u; n_ = l7_ == c7_ ? b7_ : n_;
            u64 p_ = n_ < 2688u ? pd_ : po_; p_ = n_ < 1792u ? pg_ : p_; n_ = n_ < 1792u ? n_ >> 1 : n_;
            *(u64*)(lds + L_PFT + t2_ * 8) = p_ + (u64)n_ * 8192u;
        }
        SB();
        asm volatile("s_waitcnt vmcnt(%0)\n\ts_barrier" :: "i"(2 * QWD + 1) : "memory");
        SB();
#define PKCV(i) do { const f32x4 a_ = ((i) & 2) ? a16[2 * ((i) >> 2)][(i) & 3] : a16[2 * ((i) >> 2) + ((i) & 1)][0];                       \
                     const f32x4 b_ = ((i) & 2) ? a16[2 * ((i) >> 2) + 1][(i) & 3] : a16[2 * ((i) >> 2) + ((i) & 1)][1];                   \
                     pk[i] = u32x4{cvtpk(a_[0], a_[1]), cvtpk(a_[2], a_[3]), cvtpk(b_[0], b_[1]), cvtpk(b_[2], b_[3])}; PIN(pk[i]); } while (0)
#define KL(i) do { KMF(7, 3, 2 * (i)); SB(); if ((i) >= 2) QXL1(2 * (i)); SB(); KMF(7, 3, 2 * (i) + 1); SB(); PKCV(i); if ((i) >= 2) QXL1(2 * (i) + 1); SB(); } while (0)
        KL(0); KL(1); KL(2); KL(3); KL(4); KL(5); KL(6); KL(7);
        KST(10);
        QRL(0, 0); QRH(0, 0); QRL(0, 1); QRH(0, 1); QRL(0, 2); QRH(0, 2); QRL(0, 3); QRH(0, 3);
        SB(); asm volatile("s_nop 3"); SB();
#pragma unroll
        for (int i = 8; i < 16; ++i) PKCV(i);
        SB();
        KST(11); ZST_OUT();
#undef PKCV
#undef KSTEP
#undef PFQ
#undef XKEY
    }
    PH_STAMP(1);
    const char* kvg = ws + WS_KV + (size_t)head * HSTR;
    const unsigned svv = xvo + 32768u;
    u64 pft; bf16x8 ones; unsigned ak, av;
    const int NA = qbA + 1;
    {
        constexpr int LDW = NH * HD, STG = 16384;
        int tid = threadIdx.x; asm volatile("" : "+v"(tid));
        const int lane = tid & 63;
        const unsigned woff = qwo, xvo2 = xvo + 262144u;
        f32x4 acc[2][8];
#define KVST(i) asm volatile("global_store_dwordx4 %0, %1, %2 offset:%3 sc1" :: "v"(((i) & 2) ? svv : xvo), "v"(pk[i]), "s"(sb_[(i) >> 2]), "i"(((i) & 1) * 1024) : "memory")
        u64 kvf = 0;
#define QKV(kt, f) do { constexpr int g_ = q_kv(kt, f), p_ = g_ >> 2; if (g_ >= 0) {                                                       \
            if ((g_ & 3) == 0) asm volatile("s_mov_b32 m0, %0\n\ts_nop 0" :: "s"(ldsbase + (p_ == 1 ? 0 : 65536 + p_ * 16384) + wave * 4096) : "memory"); \
            asm volatile("global_load_lds_dwordx4 %0, %1 offset:%2 sc1" :: "v"(xvo), "s"(kvg + p_ * 32768 + wave * 4096), "i"((g_ & 3) * 1024) : "memory"); } } while (0)
#define KVFP (ws + WS_FLAG + ((lane & 8) ? F_TOP : F_KV + 2048 + (head * 8 + (lane & 7)) * 8))
        const bool top2 = b == 56;
        unsigned* tw = (unsigned*)(lds + 131072 + 2048);
#define QMF(a, kt, f) do { if ((kt) == 0 && (f) < 8) MFA16Z(acc[a][(f) & 7], w_, xr[0][a][0]); else MFA16(acc[a][(f) & 7], w_, xr[(kt) & 3][a][(f) >> 3]); } while (0)
#define ZP0 238
#define QPAIR(kt, f, A, B) do { const bf16x8 w_ = CAT8(ql[(f) & 7], qh[(f) & 7]); constexpr int s_ = (kt) + (((f) + 4) >> 4), m_ = ((f) + 4) & 15;  \
            QMF(0, kt, f); SB(); if (s_ < 16) QRL(s_, m_); A; QKV(kt, f); SB();                                      \
            QMF(1, kt, f); SB(); if (s_ < 16) QRH(s_, m_); B;                                                                             \
            { constexpr int z_ = (kt) * 16 + (f) - ZP0;                    \
                                                                    \
              if (z_ == 8) { pft = *(const u64*)(lds + L_PFT + (tid & 255) * 8); }                                                       \
              if (z_ == 15) { av = ldsbase + xvo; ak = av + 65536u; PIN(av); PIN(ak); }                                                   \
              if (z_ == 16) { const unsigned o1 = (lane & 3) ? 0u : 0x3f803f80u; ones = __builtin_bit_cast(bf16x8, u32x4{o1, o1, o1, o1}); PIN(ones); }     \
              if (z_ == 14) PIN(pft); } SB(); if (((f) & 1) && s_ < 16) { __builtin_amdgcn_s_waitcnt(0xC47F); SB(); } } while (0)
        const char* xsp = xg + 3 * 262144;
#define QX(r, rb, ks, v_) asm volatile("global_load_dwordx4 %0, %1, %2 offset:%3 sc1" : "+v"(xr[r][rb][ks]) : "v"(v_), "s"(xsp), "i"((rb) * 2048 + (ks) * 1024) : "memory")
        const unsigned woff1 = woff + 1024u, woff2 = woff + 128u;
        const char* wsp = (const char*)(wqb + (size_t)QWD * 64 * LDW) - 1024;
        const unsigned m0b = ldsbase + wave * 2048;
#define QM0(r) asm volatile("s_add_u32 m0, %0, %1" :: "s"(m0b), "i"((r) * STG) : "memory", "scc")
#define QW1(v_, imm_) asm volatile("global_load_lds_dwordx4 %0, %1 offset:%2" :: "v"(v_), "s"(wsp), "i"(imm_) : "memory")
#define QST(i)
#define QSTEP(kt) do { constexpr int r_ = (kt) & 3;                                                                                     \
        QPAIR(kt, 0, if (q_xb(kt)) QX((r_ + 3) & 3, 0, 1, xvo), );                                                                      \
        QPAIR(kt, 1, , );                                                                                                               \
        QPAIR(kt, 2, if (q_xb(kt)) QX((r_ + 3) & 3, 1, 1, xvo), );                                                                      \
        QPAIR(kt, 3, if ((kt) == 12) FLAG_GET(kvf, KVFP), );                                                                            \
        if ((kt) == 0 && __builtin_expect(top2, 0)) {                                                                                                        \
            unsigned t1 = tkey(rv[0], tid * 4), t2 = 0;                                                                                 \
            _Pragma("unroll") for (int j = 1; j < 4; ++j) merge_top2(t1, t2, tkey(rv[j], tid * 4 + j), 0);                              \
            DPP_TOP2(t1, t2, 0x111, 0xf); DPP_TOP2(t1, t2, 0x112, 0xf); DPP_TOP2(t1, t2, 0x114, 0xf); DPP_TOP2(t1, t2, 0x118, 0xf);     \
            DPP_TOP2(t1, t2, 0x142, 0xa); DPP_TOP2(t1, t2, 0x143, 0xc);                                                                 \
            if (lane == 63) { tw[wave * 2] = t1; tw[wave * 2 + 1] = t2; } SB(); }                                                       \
        QPAIR(kt, 4, if ((QS1 >> (kt)) & 1) KVST(q_s0(kt) & 15), if ((QS1 >> (kt)) & 1) KEEP(pk[q_s0(kt) & 15]));                       \
        QPAIR(kt, 5, , );                                                                                                               \
        QPAIR(kt, 6, , );                                                                                                               \
        QPAIR(kt, 7, , );                                                                                                               \
        QPAIR(kt, 8, if (q_xa(kt)) QX(r_, 0, 0, xvo2), );                                                                               \
        QPAIR(kt, 9, , );                                                                                                               \
        QPAIR(kt, 10, if (q_xa(kt)) QX(r_, 1, 0, xvo2), if ((kt) <= 11) OADV(xsp, 262144));                                             \
        QPAIR(kt, 11, if ((kt) + QWD < 16) QM0((r_ + QWD) & 3), );                                                                      \
        if ((kt) < 15) {                              \
            if (QWD == 4 || (kt) == 0 || (kt) >= 13) asm volatile("s_waitcnt vmcnt(%0) lgkmcnt(0)\n\ts_barrier" :: "i"(q_wait(kt)) : "memory"); \
            else asm volatile("s_waitcnt vmcnt(%0)\n\ts_barrier" :: "i"(q_wait(kt)) : "memory");                                        \
            if ((kt) == 13) { PIN(kvf); const unsigned sh_ = (lane & 8) ? 22u : 0u; const u64 ex_ = (tag << sh_) >> sh_;     \
                if (__builtin_expect(__builtin_amdgcn_ballot_w64((kvf >> sh_) != ex_) != 0, 0)) { unsigned c_; u64 t_;                    \
                    asm volatile("s_mov_b32 %1, 0x40000\n1:\n\ts_sleep 2\n\tglobal_load_dwordx2 %0, %3, off sc1\n\ts_waitcnt vmcnt(0)\n\tv_lshrrev_b64 %2, %5, %0\n\t" \
                                 "v_cmp_ne_u64 vcc, %4, %2\n\ts_cmp_eq_u64 vcc, 0\n\ts_cbranch_scc1 2f\n\ts_sub_u32 %1, %1, 1\n\ts_cbranch_scc0 1b\n2:" \
                                 : "+v"(kvf), "=&s"(c_), "=&v"(t_) : "v"(KVFP), "v"(ex_), "v"(sh_) : "memory", "vcc", "scc"); }           \
                const unsigned r_ = __builtin_amdgcn_readlane((unsigned)kvf, 8); ri1 = r_ & 2047u; ri2 = (r_ >> 11) & 2047u; }           \
            QST((kt) + 1); SB(); }                                                                                                      \
        QPAIR(kt, 12, if ((kt) + QWD < 16) QW1(woff1, 0), );                                                                            \
        QPAIR(kt, 13, , );                                                                                                              \
        QPAIR(kt, 14, if ((kt) + QWD < 16) QW1(woff2, 1024), if ((kt) + QWD < 15) OADV(wsp, 64 * LDW * 2));                             \
        if ((kt) == q_fl()) FLAG_PUT(ws + WS_FLAG + F_KV + 2048 + (hp * 8 + jj) * 8, tag, wave == 0);     \
        QPAIR(kt, 15, , );                                                                                                              \
        if ((kt) == 0 && __builtin_expect(top2, 0)) {                                                                                                        \
            unsigned t1 = tw[0], t2 = tw[1];                                                                                            \
            _Pragma("unroll") for (int w = 1; w < 8; ++w) merge_top2(t1, t2, tw[w * 2], tw[w * 2 + 1]);                                 \
            FLAG_PUT(ws + WS_FLAG + F_TOP, (tag << 22) | (unsigned)__builtin_amdgcn_readfirstlane((~t1 & 2047u) | (~t2 & 2047u) << 11), wave == 0); \
            } \
        SB();                                                                                                                           \
    } while (0)
        asm volatile("s_waitcnt vmcnt(12)" : "+v"(rv) :: "memory");
        SB();
        QST(0);
        KST(12);
        QSTEP(0); QSTEP(1); QSTEP(2); QSTEP(3); QSTEP(4); QSTEP(5); QSTEP(6); QSTEP(7);
        QSTEP(8); QSTEP(9); QSTEP(10); QSTEP(11); QSTEP(12); QSTEP(13); QSTEP(14); QSTEP(15);
        MF_DONE();
        QST(16);
        {
            constexpr float qs = 0.08838834764831845f * 1.4426950408889634f;
#pragma unroll
            for (int i = 0; i < 8; ++i) { const f32x4 a_ = acc[i & 1][i & 6] * qs, b_ = acc[i & 1][i | 1] * qs;
                qf[i] = __builtin_bit_cast(bf16x8, u32x4{cvtpk(a_[0], a_[1]), cvtpk(a_[2], a_[3]), cvtpk(b_[0], b_[1]), cvtpk(b_[2], b_[3])}); PIN(qf[i]); }
        }
#undef QSTEP
    }
    PH_STAMP(2);
    const int kz = xcd & 1, kp = xcd >> 1, nt = kz * 4 + (idx >> 3), tile = jj * 8 + nt;
#define K64(s) ((head * 2) ^ (s))
#define OWOFF() ({ unsigned l_ = threadIdx.x & 63; asm volatile("" : "+v"(l_)); (unsigned)(((l_ >> 2) & 7) * DM + 32 * (l_ >> 5) + 8 * ((l_ & 3) ^ ((l_ >> 4) & 1))) * 2u; })
    const int n0 = (nt ^ 2) * 128, dn1 = (((nt ^ 3) - (nt ^ 2)) * 128 + 64) * 2, ntc = nt ^ 2 ^ (kp >> 1);
    const u16* wob = wo + (size_t)(8 * wave) * DM + n0;
    u64 cxf = tag + 1;
#define CFB (ws + WS_FLAG + F_CTX)
#define CFO() ({ unsigned l_ = threadIdx.x & 15, s0_ = (unsigned)((qb * 4 + qw) * 32 + head), s1_ = (unsigned)((qbB * 4 + 3 - (wave & 3)) * 32 + (head ^ 8)); \
                 asm volatile("" : "+v"(l_), "+s"(s0_), "+s"(s1_)); (l_ < 8 ? l_ ^ s0_ : s1_) * CFS; })
    u32x4 pc[8];
#define EAR_(v_, b_, pol_) "s_add_u32 m0, m0, 0x400\n\ts_nop 0\n\tglobal_load_lds_dwordx4 " v_ ", " b_ pol_ "\n\t"
#define EALD(h_) do { unsigned l_ = threadIdx.x & 63; asm volatile("" : "+v"(l_)); const unsigned lx_ = ((l_ >> 3) * DM + 8 * ((l_ & 7) ^ (l_ >> 3))) * 2u; \
        const int rb_ = ((h_) ? qbB * 8 + qw * 2 : qbA * 8 + (wave & 3) * 2) + (kp & 1);                                                  \
        asm volatile("s_mov_b32 m0, %5\n\t" EAR_("%0", "%3", "") EAR_("%1", "%3", "") EAR_("%2", "%4", " sc1") "global_load_lds_dwordx4 %2, %4 offset:1024 sc1" \
                     :: "v"(lx_), "v"(lx_ + 8 * DM * 2), "v"(l_ * 16u), "s"(x + (size_t)rb_ * 16 * DM + ntc * 128 + (kp >> 1) * 64),              \
                        "s"(ws + WS_XF + (size_t)(2 * ntc + (kp >> 1)) * 262144 + rb_ * 2048),                                             \
                        "s"(ldsbase + 131072 - 1024 + (wave & 3) * 8192 + (h_) * 4096) : "memory", "scc"); } while (0)
    const unsigned i1 = (((kz ? F1S : F1F) >> (4 * jj)) & 15u) * 16u + (kp * 4 + (idx >> 3));
    const unsigned i2 = (((kz ? F2S : F2F) >> (4 * jj)) & 15u) * 16u + (kp * 4 + (idx >> 3));
    const unsigned nf1 = i1 / 112u, nf2 = i2 / 112u;
    const unsigned k1 = i1 / 14u;
    unsigned ypub = 1;
    {
        int tid = threadIdx.x; asm volatile("" : "+v"(tid));
        const int lane = tid & 63;
        const unsigned l16 = (unsigned)lane * 16u;
#define EX(x) __builtin_amdgcn_exp2f(x)
#define TBAR() do { SB(); __builtin_amdgcn_s_waitcnt(0x0070); asm volatile("s_barrier" ::: "memory"); } while (0)
#define BRQ(t, r) do { constexpr int g_ = (r) >> 2; const int tt_ = (t) + (g_ == 2 ? 1 : 2);                                               \
            const unsigned o_ = (unsigned)((wave & 3) * 4096 + (g_ == 1 || g_ == 2 ? 16384 : 0));                                          \
            if (((r) & 3) == 0) GLDS_C(l16, kvg + (size_t)tt_ * 65536 + (g_ >= 2 ? 32768 : 0) + o_, ldsbase + (g_ >= 2 ? 0 : 65536) + (tt_ & 1) * 32768 + o_, 0); \
            else asm volatile("global_load_lds_dwordx4 %0, %1 offset:%2 sc1" :: "v"(l16), "s"(kvg + (size_t)tt_ * 65536 + (g_ >= 2 ? 32768 : 0) + o_), "i"(((r) & 3) * 1024) : "memory"); } while (0)
#define BRQ4(t, g) do { BRQ(t, 4 * (g)); BRQ(t, 4 * (g) + 1); BRQ(t, 4 * (g) + 2); BRQ(t, 4 * (g) + 3); } while (0)
#define BRQ2C(t, r) do { constexpr int g_ = (r) >> 2; const int tt_ = (t) + (g_ == 2 ? 1 : 2);                                             \
            const unsigned o_ = (unsigned)((wave & 3) * 4096 + (g_ == 1 || g_ == 2 ? 16384 : 0));                                          \
            asm volatile("s_cmp_eq_u32 %5, 0\n\ts_cbranch_scc1 1f\n\ts_mov_b32 m0, %2\n\ts_nop 0\n\tglobal_load_lds_dwordx4 %0, %1 offset:%3 sc1\n\t"  \
                         "global_load_lds_dwordx4 %0, %1 offset:%4 sc1\n1:"                                                               \
                         :: "v"(l16), "s"(kvg + (size_t)tt_ * 65536 + (g_ >= 2 ? 32768 : 0) + o_), "s"(ldsbase + (g_ >= 2 ? 0 : 65536) + (tt_ & 1) * 32768 + o_), \
                            "i"(((r) & 3) * 1024), "i"(((r) & 3) * 1024 + 1024), "s"(isb) : "memory", "scc"); } while (0)
        const unsigned isb = wave >> 2;
        u64 kf2 = tag;
        const char* kfb = ws + WS_FLAG + F_KV + head * 64;
#define KCHK() do { PIN(kf2); if (__builtin_expect((__builtin_amdgcn_ballot_w64(kf2 != tag) | (u64)((33u >> t) & 1u)) != 0, 0)) { \
            unsigned c_, a_;                                                                                                              \
            FLAG_PUTZ(ws + WS_FLAG + F_KV + (hp * 8 + jj) * 8, tag, (unsigned)(wave ^ 4) | (unsigned)t);                                  \
            asm volatile("s_cmp_lt_u32 %6, 5\n\ts_cbranch_scc1 3f\n\tv_lshrrev_b32 %2, 1, %5\n\tv_and_b32 %2, 56, %2\n\ts_mov_b32 %1, 0x40000\n" \
                         "1:\n\tglobal_load_dwordx2 %0, %2, %3 sc1\n\ts_cmp_eq_u32 %6, 5\n\ts_cbranch_scc1 3f\n\ts_waitcnt vmcnt(0)\n\tv_cmp_ne_u64 vcc, %4, %0\n\t" \
                         "s_cmp_eq_u64 vcc, 0\n\ts_cbranch_scc1 3f\n\ts_sleep 2\n\ts_sub_u32 %1, %1, 1\n\ts_cbranch_scc0 1b\n"           \
                         "3:" : "+v"(kf2), "=&s"(c_), "=&v"(a_) : "s"(kfb), "s"(tag), "v"(l16), "s"(t) : "memory", "vcc", "scc"); } SB(); } while (0)
#define PFW1(i_) do { unsigned a_; const u64 p_ = ((u64)(unsigned)__builtin_amdgcn_readlane((int)(pft >> 32), i_) << 32) | (unsigned)__builtin_amdgcn_readlane((int)pft, i_); \
            asm volatile("v_lshlrev_b32 %0, 3, %2\n\ts_mov_b32 m0, %3\n\ts_nop 2\n\tglobal_load_lds_dword %0, %1"                        \
                         : "=&v"(a_) : "s"(p_), "v"(l16), "s"(ldsbase + 131072 + (wave & 3) * 8192) : "memory"); } while (0)
#define ONCE2(n_) int CAT(n_once, __LINE__) = (int)(n_); asm volatile("" : "+s"(CAT(n_once, __LINE__))); _Pragma("unroll 1") for (int z_ = 0; z_ < CAT(n_once, __LINE__); ++z_)
#define AST(i)
#define XST(i)
#define TS0()
#define TS1()
        AST(0);
        asm volatile("s_bitcmp0_b32 %0, 2\n\ts_cbranch_scc1 1f\n\ts_setprio 1\n1:" :: "s"(wave) : "scc");
        {
            f32x4 o[8][2], ls[2], ng[2], S[2][4];
            bf16x8 pf[2][2], fr[16];
            float e[16];
            constexpr bool ZC = false;
            constexpr bool HOT = false;
            const unsigned lb16 = ldsbase + l16;
            int t = 0;
#define KOFF(f) ((((f) & 3) * 2 + (((f) >> 2) & 1)) * 1024)
#define RD(kb, n) do { constexpr int k_ = ((kb) + ((n) >> 4)) & 3, n_ = (n) & 15;                                                          \
            fr[n_] = n_ < 8 ? lds_rd(av + (unsigned)(((k_ + 3) & 3) * 8192 + n_ * 1024)) : lds_rd(ak + (unsigned)(((k_ + 1) & 3) * 8192 + KOFF(n_ & 7))); } while (0)
#define MX(p, n, a) do { if (ZC) MFA16Y(o[n][a], fr[n], pf[(p) ^ 1][a]); else MFA16(o[n][a], fr[n], pf[(p) ^ 1][a]); } while (0)
#define MY(p, n, h) do { constexpr int a_ = ((n) >> 1) & 1, t_ = 2 * ((n) & 1) + (h);                                                      \
                         if (t_) MFA16(S[(p) ^ 1][(((n) >> 1) & 2) + a_], fr[((n) & 12) + t_], qf[2 * t_ + a_]);                           \
                         else MFA16C(S[(p) ^ 1][(((n) >> 1) & 2) + a_], fr[(n) & 12], qf[a_], ng[a_]); } while (0)
#define GX(p, n, F1, F2) do { MX(p, n, 0); SB(); F1; SB(); MX(p, n, 1); SB(); F2; SB(); } while (0)
#define GY(p, n, F1, F2) do { MY(p, n, 0); SB(); F1; SB(); MY(p, n, 1); SB(); F2; SB(); } while (0)
#define ML(p, a) do { if (ZC) MFA16Y(ls[a], ones, pf[(p) ^ 1][a]); else MFA16(ls[a], ones, pf[(p) ^ 1][a]); } while (0)
#define GL(p, F1, F2) do { ML(p, 0); SB(); F1; SB(); ML(p, 1); SB(); F2; SB(); } while (0)
#define SV(p, i) S[p][(((i) >> 1) & 2) + ((i) >> 3)][(i) & 3]
#define E1(p, i) do { e[i] = EX(SV(p, i)); if (HOT) KEEP(e[i]); else PIN(e[i]); } while (0)
#define C1(p, j) do { u32x4 w_ = __builtin_bit_cast(u32x4, pf[p][(j) >> 2]); w_[(j) & 3] = cvtpk(e[2 * (j)], e[2 * (j) + 1]);               \
                      pf[p][(j) >> 2] = __builtin_bit_cast(bf16x8, w_); if (HOT) KEEP(w_[(j) & 3]); else PIN(pf[p][(j) >> 2]); } while (0)
#define DQ(kb, g) do { if (ISB_ && (kb) == 3) { if ((g) == 0) KCHK(); BRQ(t, g); } if (ISB_ && (kb) == 0 && (g) == 1) PFW1(t); } while (0)
#define W4() do { SB(); __builtin_amdgcn_s_waitcnt(0xC37F); } while (0)
#define US(kb, ISB) do { constexpr bool HOT = true, ZC = (ISB) == 3, ISB_ = (ISB) == 1; constexpr int p_ = (kb) & 1; constexpr bool B_ = (kb) == 2;                    \
            GX(p_, 0, RD(kb, 7); if ((kb) == 3) { ak ^= 32768u; PIN(ak); }, E1(p_, 0); DQ(kb, 0));                                        \
            GX(p_, 1, RD(kb, 8); if ((kb) == 0) { av ^= 32768u; PIN(av); }, E1(p_, 1); DQ(kb, 1));                                        \
            GL(p_, E1(p_, 2), E1(p_, 3));                                                                                                 \
            GX(p_, 2, RD(kb, 9); C1(p_, 0), E1(p_, 4); DQ(kb, 2));                                                                        \
            GX(p_, 3, RD(kb, 10); W4(), E1(p_, 5); DQ(kb, 3));                                                                            \
            GX(p_, 4, RD(kb, 11); C1(p_, 1), E1(p_, 6); DQ(kb, 4));                                                                       \
            GX(p_, 5, RD(kb, 12); C1(p_, 2), E1(p_, 7); DQ(kb, 5));                                                                       \
            GX(p_, 6, RD(kb, 13); C1(p_, 3), E1(p_, 8); DQ(kb, 6));                                                                       \
            GX(p_, 7, RD(kb, 14); W4(), E1(p_, 9); DQ(kb, 7));                                                                            \
            GY(p_, 8, RD(kb, 15), E1(p_, 10); DQ(kb, 8));                                           \
            GY(p_, 9, if (!B_) RD(kb, 16); C1(p_, 4), E1(p_, 11); DQ(kb, 9));                                                             \
            GY(p_, 10, if (B_) { TS0(); if (ISB_) { __builtin_amdgcn_s_waitcnt(0x0071); asm volatile("s_barrier" ::: "memory"); } else TBAR(); \
                                 TS1(); SB(); RD(kb, 16); }                                                                               \
                       RD(kb, 17); C1(p_, 5), E1(p_, 12); DQ(kb, 10));                                                                    \
            GY(p_, 11, RD(kb, 18); if (!B_) W4(), E1(p_, 13); DQ(kb, 11));                                                                \
            GY(p_, 12, RD(kb, 19), E1(p_, 14); DQ(kb, 12));                                                \
            GY(p_, 13, RD(kb, 20); C1(p_, 6), E1(p_, 15); DQ(kb, 13));                                                                    \
            GY(p_, 14, RD(kb, 21); C1(p_, 7), DQ(kb, 14));                                 \
            GY(p_, 15, RD(kb, 22), DQ(kb, 15); W4());                                                                                     \
        } while (0)
            asm volatile("s_waitcnt vmcnt(%0) lgkmcnt(0)\n\ts_barrier" :: "i"(4 + (QS1 >> 15)) : "memory");
            AST(1);
#pragma unroll
            for (int s_ = 0; s_ < 8; ++s_) fr[s_] = lds_rd(ak + (unsigned)KOFF(s_));
            BRQ2C(-1, 12); BRQ2C(-1, 14);
            SB();
#define M0_(n, a) do { if ((n) & 3) MFA16(S[0][(((n) >> 1) & 2) + (a)], fr[n], qf[2 * ((n) & 3) + (a)]); else MFA16Z(S[0][(((n) >> 1) & 2) + (a)], fr[n], qf[a]); } while (0)
#define G0_(n) do { M0_(n, 0); SB(); if (Q_NST + (n) < 16) KVST((Q_NST + (n)) & 15); SB(); M0_(n, 1); SB(); RD(0, 8 + (n));               \
                    if (Q_NST + (n) < 16) KEEP(pk[(Q_NST + (n)) & 15]); SB(); } while (0)
            G0_(0); G0_(1); G0_(2); G0_(3); G0_(4); G0_(5); G0_(6); G0_(7);
            MF_DONE();
            { ONCE2(isb * min(jj, 1)) { PFW1(0); } }
            {
                PIN(S[0][0]); PIN(S[0][1]); PIN(S[0][2]); PIN(S[0][3]);
                if (__builtin_expect((qb | qw) == 0, 0)) {
#pragma unroll
                    for (int r = 0; r < 4; ++r) { S[0][2][r] = -__builtin_inff(); if (4 * (lane >> 4) + r > (lane & 15)) S[0][0][r] = S[0][3][r] = -__builtin_inff(); } }
#pragma unroll
                for (int a = 0; a < 2; ++a) {
                    float mx_;
#define MX3(d, a_, b_, c_) asm("v_max3_f32 %0, %1, %2, %3" : "=v"(d) : "v"(a_), "v"(b_), "v"(c_))
                    MX3(mx_, SV(0, 8 * a), SV(0, 8 * a + 1), SV(0, 8 * a + 2)); MX3(mx_, mx_, SV(0, 8 * a + 3), SV(0, 8 * a + 4)); MX3(mx_, mx_, SV(0, 8 * a + 5), SV(0, 8 * a + 6));
                    { float m7_ = SV(0, 8 * a + 7), n_ = -1e30f; MX3(mx_, mx_, m7_, n_); }
                    { auto r1 = __builtin_amdgcn_permlane16_swap(__float_as_uint(mx_), __float_as_uint(mx_), false, false); MX3(mx_, r1[0], r1[1], r1[1]);
                      auto r2 = __builtin_amdgcn_permlane32_swap(__float_as_uint(mx_), __float_as_uint(mx_), false, false); MX3(mx_, r2[0], r2[1], r2[1]); }
                    mx_ = -POFF - mx_;
                    ng[a] = f32x4{mx_, mx_, mx_, mx_}; PIN(ng[a]);
                }
#pragma unroll
                for (int i = 0; i < 16; ++i) { float v_ = SV(0, i) + ng[i >> 3][0]; PIN(v_); SV(0, i) = v_; }
                PIN(S[0][0]); PIN(S[0][1]); PIN(S[0][2]); PIN(S[0][3]);
            }
            SB();
            GY(0, 8, E1(0, 0), RD(0, 16); E1(0, 1)); GY(0, 9, E1(0, 2); C1(0, 0), RD(0, 17); E1(0, 3));
            GY(0, 10, E1(0, 4); C1(0, 1), RD(0, 18); E1(0, 5)); GY(0, 11, E1(0, 6); C1(0, 2), RD(0, 19); E1(0, 7));
            GY(0, 12, E1(0, 8); C1(0, 3), RD(0, 20); E1(0, 9)); GY(0, 13, E1(0, 10); C1(0, 4), RD(0, 21); E1(0, 11));
            GY(0, 14, E1(0, 12); C1(0, 5), RD(0, 22); E1(0, 13)); GY(0, 15, E1(0, 14); C1(0, 6), E1(0, 15); C1(0, 7));
#define SCOPY() do { MF_DONE(); _Pragma("unroll") for (int i = 0; i < 4; ++i) { S[1][i] = S[0][i]; PIN(S[1][i]); } } while (0)
            if (__builtin_expect((qb | qw) == 0, 0)) { SCOPY(); pf[0][0] = pf[0][1] = bf16x8{}; PIN(pf[0][0]); PIN(pf[0][1]); }
            W4(); asm volatile("s_nop 1"); SB();
            AST(2);
            const int nu = 4 * qb + qw - 2;
#define MFA16Y(c, a, b) asm volatile("v_mfma_f32_16x16x32_bf16 %0, %1, %2, 0" : "+v"(c) : "v"(a), "v"(b))
#pragma unroll
            for (int d = 0; d < 18; ++d) { if (d < 16) asm volatile("" : "=v"(o[d >> 1][d & 1])); else asm volatile("" : "=v"(ls[d & 1])); }
            if (__builtin_expect(nu < 0, 0)) { const bf16x8 z_ = bf16x8{}; asm volatile("s_nop 1" :: "v"(z_));
#pragma unroll
                for (int d = 0; d < 18; ++d) { if (d < 16) MFA16Y(o[d >> 1][d & 1], z_, z_); else MFA16Y(ls[d & 1], z_, z_); } }
            { int n1_ = nu; asm volatile("" : "+s"(n1_)); ONCE2(min(n1_ + 2, 2) >> 1) { US(1, 3); } }
            {
                const int nl = nu >> 2, nB = isb ? nl : 0;
#pragma unroll 1
                while (t < nB) { US(2, 1); US(3, 1); ++t; US(0, 1); US(1, 1); }
#pragma unroll 1
                while (t < nl) { US(2, 0); US(3, 0); ++t; US(0, 0); US(1, 0); }
            }
            AST(3);
            SST(1);
#define PCOPY() do { SCOPY(); pf[0][0] = pf[1][0]; pf[0][1] = pf[1][1]; PIN(pf[0][0]); PIN(pf[0][1]); SB(); asm volatile("s_nop 1"); } while (0)
            {
                const int nr = nu < 0 ? 0 : nu & 3, ne = nu < -1 ? 0 : nu + 1;
                if (__builtin_expect(nr >= 1, 1)) {
                    US(2, 2);
                    if (__builtin_expect(nr >= 2, 1)) {
                        if (isb) { KCHK(); asm volatile("s_setprio 0"); BRQ4(t, 0); BRQ4(t, 1); BRQ4(t, 2); BRQ4(t, 3); asm volatile("s_setprio 1"); }
                        US(3, 2);
                        if (__builtin_expect(nr == 3, 0)) US(0, 2);
                    }
                }
                int np = ne; asm volatile("" : "+s"(np));
                if (__builtin_expect(np & 1, 0)) PCOPY();
                KEEP(ng[0]); KEEP(ng[1]);
                t = qb;
            }
#define RDV(n) fr[n] = lds_rd(avf + (unsigned)((n) * 1024))
#define M1(i) do { e[i] = __builtin_amdgcn_fmed3f(e[i], 0.f, rmp[(i) & 3]); PIN(e[i]); } while (0)
#define E2(i) E1(1, i)
#define CST(i) asm volatile("global_store_dwordx4 %0, %1, %2 offset:%3 sc1\n\ts_nop 1" :: "v"(l16), "v"(pc[i]), "s"(cb + ((i) >> 2) * 262144), "i"((((i) >> 1) & 1) * 1024 + ((i) & 1) * 2048) : "memory")
#define NRM(ISB, i) do { float y_[8];                                              \
                _Pragma("unroll") for (int z_ = 0; z_ < 8; ++z_) { y_[z_] = o[((i) & 6) + (z_ >> 2)][(i) & 1][z_ & 3] * inv[(i) & 1]; PIN(y_[z_]); } \
                pc[i] = u32x4{cvtpk(y_[0], y_[1]), cvtpk(y_[2], y_[3]), cvtpk(y_[4], y_[5]), cvtpk(y_[6], y_[7])};                        \
                PIN(pc[i]); if (!(ISB)) CST(i); SB(); } while (0)
#define XLD(b_) do { _Pragma("unroll") for (int n = 0; n < 8; ++n) asm volatile("global_load_dwordx4 %0, %1, %2 offset:%3 sc1" : "=v"(fr[n]) : "v"(l16 + (n >> 2) * 4096u), "s"(b_), "i"((n & 3) * 1024) : "memory"); \
                asm volatile("s_waitcnt vmcnt(0)" : "+v"(fr[0]), "+v"(fr[1]), "+v"(fr[2]), "+v"(fr[3]), "+v"(fr[4]), "+v"(fr[5]), "+v"(fr[6]), "+v"(fr[7]) :: "memory"); } while (0)
#define MFB(a_, b_, c_) __builtin_amdgcn_mfma_f32_16x16x32_bf16(a_, b_, c_, 0, 0, 0)
#define EXACT() do { __builtin_amdgcn_s_waitcnt(0xC07F); MF_DONE();                                                                                                           \
                float mr_[2] = {-1e30f, -1e30f}; const int nu_ = 4 * qb + qw2;                                                            \
                ls[0] = ls[1] = f32x4{}; _Pragma("unroll") for (int d = 0; d < 16; ++d) o[d >> 1][d & 1] = f32x4{};                       \
                _Pragma("unroll 1") for (int u = 0; u <= nu_; ++u) {                                                                      \
                    const char* kb_ = kvg + (size_t)(u >> 2) * 65536 + (u & 3) * 8192;                                                    \
                    XLD(kb_);                                                                                                             \
                    _Pragma("unroll") for (int i = 0; i < 4; ++i) { f32x4 s_ = f32x4{};                                                   \
                        _Pragma("unroll") for (int d = 0; d < 4; ++d) s_ = MFB(fr[2 * d + (i >> 1)], qf[2 * d + (i & 1)], s_);            \
                        S[1][i] = s_; }                                                                                                   \
                    if (u == nu_) { _Pragma("unroll") for (int r = 0; r < 4; ++r) { S[1][2][r] = -__builtin_inff();                       \
                        if (4 * (ln2 >> 4) + r > (ln2 & 15)) S[1][0][r] = S[1][3][r] = -__builtin_inff(); } }                             \
                    XLD(kb_ + 32768);                                                                                                     \
                    _Pragma("unroll") for (int a = 0; a < 2; ++a) {                                                                       \
                        float mx_ = SV(1, 8 * a); _Pragma("unroll") for (int r = 1; r < 8; ++r) mx_ = fmaxf(mx_, SV(1, 8 * a + r));       \
                        mx_ = fmaxf(quad_max(mx_), mr_[a]); const float al_ = EX(mr_[a] - mx_); mr_[a] = mx_;                             \
                        u32x4 w_; _Pragma("unroll") for (int j = 0; j < 4; ++j) w_[j] = cvtpk(EX(SV(1, 8 * a + 2 * j) - mx_), EX(SV(1, 8 * a + 2 * j + 1) - mx_)); \
                        const bf16x8 p_ = __builtin_bit_cast(bf16x8, w_);                                                                 \
                        ls[a] = MFB(ones, p_, ls[a] * al_);                                                                               \
                        _Pragma("unroll") for (int d = 0; d < 8; ++d) o[d][a] = MFB(fr[d], p_, o[d][a] * al_); } }                        \
                pf[1][0] = pf[1][1] = bf16x8{}; _Pragma("unroll") for (int n = 4; n < 8; ++n) fr[n] = bf16x8{};     \
                PIN(pf[1][0]); PIN(pf[1][1]); MF_DONE(); } while (0)
#define TAIL(ISB) do { float rmp[4];                                                                                         \
                XST(0);                                                                                                                   \
                MX(1, 0, 0); SB();                                                                                                        \
                int ln2 = lane, qw2 = qw; asm volatile("" : "+v"(ln2), "+s"(qw2));   \
                fr[7] = lds_rd(av + (unsigned)((qw2 + 3) & 3) * 8192u + 7168u); SB();                                                     \
                MX(1, 0, 1); SB(); E2(0); SB();                                                                                           \
                GX(1, 1, E2(1), E2(2));                                                                                                   \
                GL(1, E2(3), E2(8));                                                                                                      \
                float fl_ = (float)((ln2 & 15) - 4 * (ln2 >> 4)); PIN(fl_);                                                               \
                unsigned avf = lb16 + (unsigned)(t & 1) * 32768u + (unsigned)qw2 * 8192u; PIN(avf); SB();                                 \
                GX(1, 2, E2(9), E2(10));                                                                             \
                GX(1, 3, E2(11), _Pragma("unroll") for (int r = 0; r < 4; ++r) { rmp[r] = (fl_ + (0.5f - r)) * 1.2089258196146292e24f; PIN(rmp[r]); }); \
                if (__builtin_expect(qw2 == 2, 0)) {                               \
                    if (ISB) TBAR(); else LGKM0_BAR(); }                                                                                  \
                SB();                                                                                                                     \
                RDV(0); RDV(1); RDV(2); RDV(3); SB();                                                                                     \
                GX(1, 4, E2(12), M1(0); M1(1));                                                                                           \
                GX(1, 5, RDV(4); E2(13), M1(2); M1(3));                                                                                   \
                GX(1, 6, RDV(5); E2(14), C1(1, 0); C1(1, 1));                                                                             \
                GX(1, 7, RDV(6); E2(15), C1(1, 4); C1(1, 5));                                                                             \
                RDV(7); M1(12); M1(13); M1(14); M1(15); e[4] = e[5] = e[6] = e[7] = 0.f; C1(1, 2); C1(1, 3); SB();                        \
                C1(1, 6); C1(1, 7); SB();                                                                                                 \
                XST(1);                                                                                                                   \
                if (__builtin_expect(qw2 < 2, 0)) { if (ISB) TBAR(); else LGKM0_BAR(); }                                                  \
                SB();                                                                                                                     \
                XST(2); XST(3);                                                                                                           \
                float inv[2];                                                                                                \
                int hd2 = head; asm volatile("" : "+s"(hd2)); const char* cb = ws + WS_CTX + (unsigned)(hd2 * 524288 + (qb * 8 + qw2 * 2) * 2048); SB(); \
                GL(0, , ); GX(0, 0, , ); GX(0, 1, , ); GX(0, 2, , );                                                                      \
                PIN(ls[0]); PIN(ls[1]);                                                                                                   \
                const bool c_ = __builtin_amdgcn_ballot_w64(!(ls[0][0] + ls[1][0] <= LTHR)) != 0; SB();                                   \
                GX(0, 3, , );                                                                                                             \
                if (__builtin_expect(c_, 0)) EXACT();                                                                                     \
                SB();                                                                                                                     \
                inv[0] = __builtin_amdgcn_rcpf(ls[0][0]); inv[1] = __builtin_amdgcn_rcpf(ls[1][0]); PIN(inv[0]); PIN(inv[1]); SB();       \
                GX(0, 4, , asm volatile("s_nop 0"));                                                                                      \
                NRM(ISB, 0); GX(0, 5, , ); NRM(ISB, 1); GX(0, 6, , ); NRM(ISB, 2); GX(0, 7, , ); NRM(ISB, 3);                             \
                XST(4);                                                                                                                   \
                NRM(ISB, 4); NRM(ISB, 5); NRM(ISB, 6); NRM(ISB, 7);                                                                       \
                if (ISB) { asm volatile("s_setprio 0"); asm volatile("" : "+s"(t)); KCHK(); if (t + 2 < NA) { BRQ4(t, 0); BRQ4(t, 1); BRQ4(t, 3); } BRQ4(t, 2); \
                           CST(0); CST(1); CST(2); CST(3); CST(4); CST(5); CST(6); CST(7); }                                              \
                XST(5);                                                                                                                   \
            } while (0)
#define SPIN() do { PIN(S[1][0]); PIN(S[1][1]); PIN(S[1][2]); PIN(S[1][3]); } while (0)
            { ONCE2(isb ^ 1) { SPIN(); TAIL(0); } }
            { ONCE2(isb) { SPIN(); TAIL(1); } }
            SST(2);
            AST(4);
        }
        if (wave >= 4) {
            int t = qbB + 1;
            const int wv = wave & 3;
            XST(6);
#define WOLD(h_) do { const unsigned ow_ = OWOFF(); const char* wp_ = (const char*)(wo + (size_t)(K64(2 * (h_)) * 64 + 16 * wv) * DM + n0);  \
                _Pragma("unroll") for (int i = 0; i < 8; ++i)                                                                             \
                    GLDS_S(ow_, wp_ + (size_t)((i >> 2) * 64 + 8 * ((i >> 1) & 1)) * (DM * 2) + (i & 1) * dn1,                             \
                           ldsbase + 65536 + (h_) * 32768 + (i >> 2) * 16384 + (2 * wv + ((i >> 1) & 1)) * 2048 + (i & 1) * 1024, 0); } while (0)
            {
                const u32x4 em = u32x4{~0u, ~0u, ~0u, ~0u};
                const char* eb = ws + WS_PART + (size_t)(tile * 8 + wv) * 24576 + kp * 6144;
                asm volatile("global_store_dwordx4 %0, %1, %2 sc1\n\tglobal_store_dwordx4 %0, %1, %2 offset:1024 sc1\n\tglobal_store_dwordx4 %0, %1, %3 sc1\n\t"
                             "global_store_dwordx4 %0, %1, %3 offset:1024 sc1\n\ts_waitcnt vmcnt(12) lgkmcnt(0)"
                             :: "v"(l16), "v"(em), "s"(eb), "s"(eb + 4 * 24576) : "memory"); }
            if (jj == 7) { WOLD(1); EALD(0); EALD(1); }
            asm volatile("s_barrier" ::: "memory");
#define BATCH() do { KCHK(); if (t + 2 < NA) { BRQ4(t, 0); BRQ4(t, 1); BRQ4(t, 3); } BRQ4(t, 2); } while (0)
#define BSTEP(N) do { asm volatile("s_waitcnt vmcnt(%0)\n\ts_barrier" :: "i"(N) : "memory"); ++t; } while (0)
            u32x4 fw[16], wd[16], xq[4], gp[8];
            f32x4 fa[8];
            int ln_ = lane; asm volatile("" : "+v"(ln_));
#define FMM(w_, i0, a_, z_) do { asm volatile("s_setprio 1");                                                              \
                    _Pragma("unroll") for (int i = 0; i < 8; ++i) PIN(w_[(i0) + i]);                                                      \
                    PIN(a_);                                                                                                              \
                    _Pragma("unroll") for (int j = 0; j < 8; ++j) { u32x4 f_;                                                             \
                        _Pragma("unroll") for (int d = 0; d < 4; ++d) f_[d] = __builtin_amdgcn_perm(w_[(i0) + 2 * d + 1][j >> 1], w_[(i0) + 2 * d][j >> 1], (j & 1) ? 0x07060302u : 0x05040100u); \
                        fa[j] = __builtin_amdgcn_mfma_f32_16x16x32_bf16(__builtin_bit_cast(bf16x8, a_), __builtin_bit_cast(bf16x8, f_), (z_) ? f32x4{} : fa[j], 0, 0, 0); } \
                    _Pragma("unroll") for (int j = 0; j < 8; ++j) PIN(fa[j]);                                                             \
                    asm volatile("s_setprio 0"); } while (0)
#define FOUT() f32x4{ln_ >= 32 ? fa[4][0] : fa[0][0], ln_ >= 32 ? fa[5][0] : fa[1][0], ln_ >= 32 ? fa[6][0] : fa[2][0], ln_ >= 32 ? fa[7][0] : fa[3][0]}
            const int c1 = 2 * (i1 - 14 * k1) + (wv & 1);
            const unsigned r4o = (unsigned)(ln_ & 15) * 16u;
            const char* cxp = ws + WS_FLAG + F_CTX + ((qbB * 4 + qw) * 32 + head) * CFS;
            if (nf1 == 0) {
#pragma unroll 1
                while (t < F1T) { BATCH(); BSTEP(0); }
                const unsigned evo = (unsigned)((16 * (ln_ >> 5) + 4 * ((ln_ >> 4) & 1)) * FF * 2) + r4o;
                const u16* fwp = (wv < 2 ? w_gate : w_up) + (size_t)(k1 * 128) * FF + c1 * 128;
#define FLD(h_, c_) do { _Pragma("unroll") for (int i = 0; i < 8; ++i)                                                                    \
                    asm volatile("global_load_dwordx4 %0, %1, %2" : c_(fw[((h_) & 1) * 8 + i]) : "v"(evo), "s"(fwp + (size_t)(32 * (h_) + 8 * (i >> 2) + (i & 3)) * FF) : "memory"); } while (0)
                BATCH();
                { const int xr_ = (ln_ & 4) ? ri2 : ri1;
#pragma unroll
                  for (int i = 0; i < 4; ++i)
                      asm volatile("global_load_dwordx4 %0, %1, %2 offset:%3 sc1" : "=v"(xq[i]) : "v"((unsigned)((xr_ >> 4) * 2048 + (xr_ & 15) * 16 + (ln_ >> 4) * 256)),
                                   "s"(ws + WS_XF + (size_t)(k1 * 2 + (i >> 1)) * 262144), "i"((i & 1) * 1024) : "memory"); }
                FLD(0, "=v"); FLD(1, "=v"); BSTEP(20);
                BATCH(); asm volatile("s_waitcnt vmcnt(16)" ::: "memory");
                FLAG_PUT(cxp, tag, 1);
                FMM(fw, 0, xq[0], 1); FLD(2, "=v"); FMM(fw, 8, xq[1], 0); FLD(3, "=v"); BSTEP(17);
                BATCH(); asm volatile("s_waitcnt vmcnt(16)" ::: "memory");
                FMM(fw, 0, xq[2], 0); FMM(fw, 8, xq[3], 0); const f32x4 sum = FOUT();
                ST16_SC1((float*)(ws + WS_GP) + ((size_t)k1 * 4 + (wv >> 1) * 2 + ((ln_ >> 4) & 1)) * FF + c1 * 128 + (ln_ & 15) * 8 + (ln_ >> 5) * 4, sum);
                PFW1(7); BSTEP(2);
            }
            const char* pnd = nf1 ? cxp : ws + WS_FLAG + F_GU + (c1 * 16 + (wv >> 1) * 8 + k1) * 8;
            const int tp = nf1 ? t + 1 : t;
#define PUB(N) do { if (t == tp) { asm volatile("s_waitcnt vmcnt(%0)" :: "i"(N) : "memory"); FLAG_PUT(pnd, tag, 1); } } while (0)
            const int f0 = (nf2 ? 0 : i2 >> 3) * 256 + wv * 64;
            const char* gfp = ws + WS_FLAG + F_GU + (f0 >> 7) * 128 + (ln_ & 15) * 8;
            u64 gf = tag + 1;
            char* vs = lds + (NA & 1) * 32768;
#define GFLD() asm volatile("global_load_dwordx2 %0, %1, off sc1" : "+v"(gf) : "v"(gfp) : "memory")
#define WDLD(i, n_) do { const unsigned dvo = (unsigned)((ln_ >> 4) * 8 * DM * 2) + r4o;                                                  \
                    const char* db_ = (const char*)(w_down + (size_t)((nf2 ? 0 : i2 >> 3) * 256 + wv * 64) * DM + (i2 & 7) * 128) + 4096; \
                    asm volatile("s_cmp_lg_u32 %11, 0\n\ts_cbranch_scc1 1f\n\t"                                                          \
                                 "global_load_dwordx4 %0, %8, %9 offset:-4096\n\tglobal_load_dwordx4 %1, %8, %9 offset:-2048\n\tglobal_load_dwordx4 %2, %8, %9\n\t" \
                                 "global_load_dwordx4 %3, %8, %9 offset:2048\n\tglobal_load_dwordx4 %4, %8, %10 offset:-4096\n\tglobal_load_dwordx4 %5, %8, %10 offset:-2048\n\t" \
                                 "global_load_dwordx4 %6, %8, %10\n\tglobal_load_dwordx4 %7, %8, %10 offset:2048\n1:"  \
                                 : "=&v"(wd[i]), "=&v"(wd[i + 1]), "=&v"(wd[i + 2]), "=&v"(wd[i + 3]), "=&v"(wd[i + 4]), "=&v"(wd[i + 5]), "=&v"(wd[i + 6]), "=&v"(wd[i + 7]) \
                                 : "v"(dvo), "s"(db_ + (i) * 8192), "s"(db_ + (i) * 8192 + 8192), "s"(nf2) : "memory", "scc");            \
                    EALD((i) >> 3);                                                                                                       \
                    asm volatile("s_waitcnt vmcnt(%1)\n\ts_cmp_eq_u32 %0, 0\n\ts_cbranch_scc1 1f\n\ts_waitcnt vmcnt(%2)\n1:"                \
                                 :: "s"(nf2), "i"(12 + (n_)), "i"(4 + (n_)) : "memory", "scc");                                           \
                    BSTEP(63); } while (0)
#pragma unroll 1
            while (t < NA - 4) { BATCH(); PUB(17); BSTEP(0); }
            if (jj < 7) {
                BATCH(); PUB(16); WDLD(0, 0);
                BATCH(); PUB(16); GFLD(); WDLD(8, 0);
            }
            if (jj < 6) {
                BATCH();
                if (nf2 == 0) {
                    FLAG_WAIT(gf, gfp, tag, 63);
#pragma unroll
                    for (int i = 0; i < 8; ++i)
                        asm volatile("global_load_dwordx4 %0, %1, %2 sc1" : "=v"(gp[i]) : "v"((unsigned)((ln_ >> 4) * FF * 4) + r4o), "s"(ws + WS_GP + ((size_t)i * 4 * FF + f0) * 4) : "memory");
                    *(unsigned*)vs = 0;
                    asm volatile("s_waitcnt lgkmcnt(0)" ::: "memory");
                }
                WOLD(NA & 1);
                BSTEP(8);
            } else if (jj == 6) WOLD(0);
            asm volatile("global_load_dwordx2 %0, %1, %2 sc1" : "+v"(cxf) : "v"(CFO()), "s"(CFB) : "memory");
            WOLD((NA & 1) ^ 1);
            asm volatile("s_waitcnt vmcnt(16)" ::: "memory");
            if (jj == 7) FLAG_PUT(cxp, tag, 1);
            if (nf2 == 0) {
                f32x4 g4;
#pragma unroll
                for (int i = 0; i < 8; ++i) { PIN(gp[i]);
#pragma unroll
                    for (int j = 0; j < 4; ++j) { if (i) VADD(g4[j], g4[j], __uint_as_float(gp[i][j])); else g4[j] = __uint_as_float(gp[0][j]); } }
                float hv[2];
#pragma unroll
                for (int k = 0; k < 2; ++k) { auto rr = __builtin_amdgcn_permlane32_swap(__float_as_uint(g4[k]), __float_as_uint(g4[2 + k]), false, false);
                    const float gb = rbf(__uint_as_float(rr[0])), ub = rbf(__uint_as_float(rr[1]));
                    hv[k] = rbf(gb / (1.f + __expf(-gb))) * ub; PIN(hv[k]); }
                char* hl = vs + 8192 + wv * 256;
                *(unsigned*)(hl + ((ln_ >> 4) & 1) * 128 + (ln_ & 15) * 8 + (ln_ >> 5) * 4) = cvtpk(hv[0], hv[1]);
                asm volatile("" ::: "memory");
                xq[0] = *(const u32x4*)(hl + ((ln_ >> 2) & 1) * 128 + (ln_ >> 4) * 16); xq[1] = *(const u32x4*)(hl + ((ln_ >> 2) & 1) * 128 + 64 + (ln_ >> 4) * 16);
                FMM(wd, 0, xq[0], 1); FMM(wd, 8, xq[1], 0);
                *(f32x4*)(vs + 4096 + wv * 1024 + ln_ * 16) = FOUT();
                unsigned n_;
                asm volatile("s_waitcnt lgkmcnt(0)\n\ts_mov_b64 exec, 1\n\tds_add_rtn_u32 %0, %1, %2\n\ts_mov_b64 exec, -1\n\ts_waitcnt lgkmcnt(0)"
                             : "=&v"(n_) : "v"(ldsbase + (NA & 1) * 32768u), "v"(1u) : "memory");
                ypub = __builtin_amdgcn_readfirstlane(n_) ^ 3u;
                if (ypub == 0) {
                    const f32x4* yr = (const f32x4*)(vs + 4096) + ln_;
                    const f32x4 a = (yr[0] + yr[64]) + (yr[128] + yr[192]);
                    ST16_SC1((float*)(ws + WS_YP) + ((size_t)(((ln_ >> 4) & 1) * 8 + (i2 & 7)) * 16 + (i2 >> 3)) * 128 + (ln_ & 15) * 8 + (ln_ >> 5) * 4, a);
                }
            }
            AST(5);
        }
    }
    PH_STAMP(3);
    {
        constexpr int LDW = 1024, STG = 16384;
        int tid = threadIdx.x; asm volatile("" : "+v"(tid));
        const int lane = tid & 63, l15 = lane & 15, q4 = lane >> 4;
        unsigned xvo = (unsigned)lane * 16u; PIN(xvo);
        if (!(kp & 1)) {
#pragma unroll
            for (int i = 0; i < 8; i += 2) { const u32x4 t_ = pc[i]; pc[i] = pc[i + 1]; pc[i + 1] = t_; PIN(pc[i]); PIN(pc[i + 1]); } }
        bf16x8 yr_[4][2][2];
#pragma unroll
        for (int i = 0; i < 8; ++i) yr_[i >> 2][i & 1][(i >> 1) & 1] = __builtin_bit_cast(bf16x8, pc[i]);
        unsigned wa[2][2];
        wa[0][0] = ldsbase + (unsigned)(65536 + ((kp >> 1) ^ 1) * 1024 + (q4 >> 1) * 2048 + (4 * (q4 & 1) + (l15 >> 2)) * 64 + (q4 & 1) * 16 + (lane & 2) * 16 + (lane & 1) * 8);
        wa[0][1] = wa[0][0] ^ 16u; wa[1][0] = wa[0][0] ^ 1024u; wa[1][1] = wa[0][1] ^ 1024u;
        PIN(wa[0][0]); PIN(wa[0][1]); PIN(wa[1][0]); PIN(wa[1][1]);
        const char* cg = ws + WS_CTX + (qb * 8 + qw * 2) * 2048;
        unsigned xok;
#define XLD8 "global_load_dwordx4 %0, %10, %12 sc1\n\tglobal_load_dwordx4 %1, %10, %13 sc1\n\tglobal_load_dwordx4 %2, %10, %12 offset:1024 sc1\n\tglobal_load_dwordx4 %3, %10, %13 offset:1024 sc1\n\t" \
             "global_load_dwordx4 %4, %11, %12 sc1\n\tglobal_load_dwordx4 %5, %11, %13 sc1\n\tglobal_load_dwordx4 %6, %11, %12 offset:1024 sc1\n\tglobal_load_dwordx4 %7, %11, %13 offset:1024 sc1\n\t"
#define XOUT(c_) c_(yr_[2][0][0]), c_(yr_[2][1][0]), c_(yr_[2][0][1]), c_(yr_[2][1][1]), c_(yr_[3][0][0]), c_(yr_[3][1][0]), c_(yr_[3][0][1]), c_(yr_[3][1][1])
#define XIN "v"(xvo), "v"(xvo2), "s"(xp0), "s"(xp1), "s"(tag)
        unsigned xvo2 = xvo + 262144u, wvo = OWOFF() + (unsigned)K64(4) * (64 * LDW * 2), xso = xvo + (unsigned)K64(4) * 262144u, cfo = CFO();
        PIN(xvo2); PIN(wvo); PIN(xso); PIN(cfo);
        const char* cg0 = cg + ((kp ^ 1) & 1) * 2048, * cg1 = cg + (kp & 1) * 2048, * xp0 = cg0 + (unsigned)K64(2) * 262144u, * xp1 = cg1 + (unsigned)K64(2) * 262144u;
        const char* cfb = CFB, * wb0 = (const char*)wob, * wb1 = wb0 + (dn1 - 1024);
        const char* fpp = ws + WS_FLAG + ((wave & 4) ? F_Y + ((i2 & 7) * 16 + ((i2 >> 3) & 15)) * 8 : F_CTX + ((qbA * 4 + wave) * 32 + head) * CFS);
        unsigned fpz = (wave >> 2) * ypub, m0w = ldsbase + wave * 2048;
        asm volatile("" : "+s"(cg0), "+s"(cg1), "+s"(xp0), "+s"(xp1), "+s"(cfb), "+s"(wb0), "+s"(wb1), "+s"(fpp), "+s"(fpz), "+s"(m0w));
        SST(3);
        SB();
        asm volatile("s_waitcnt vmcnt(0)" ::: "memory");
        FLAG_PUTZ(fpp, tag, fpz);
        asm volatile("v_cmp_ne_u64 vcc, %14, %8\n\ts_and_b32 %9, vcc_lo, 14\n\ts_cbranch_scc1 1f\n\t"
                     XLD8 "\n1:\n\ts_barrier" : XOUT("=&v"), "+v"(cxf), "=&s"(xok) : XIN : "memory", "vcc", "scc");
        SST(4);
        f32x4 acc[2][8];
        bf16x4 wl[8], wh[8];
#define OFA(s, k_, j) (wa[(j) >> 2][(j) & 1] + (unsigned)(((s) & 3) * STG + (k_) * 8192 + (((j) >> 1) & 1) * 512))
#define ORL(i, s, k_, j) wl[i] = tr_rd(OFA(s, k_, j))
#define ORH(i, s, k_, j) wh[i] = tr_rd(OFA(s, k_, j) + 4096u)
        ORL(0, 0, 0, 0); ORH(0, 0, 0, 0); ORL(1, 0, 0, 1); ORH(1, 0, 0, 1); ORL(2, 0, 0, 2); ORH(2, 0, 0, 2); ORL(3, 0, 0, 3); ORH(3, 0, 0, 3); SB();
        const int r0 = qb * 128 + qw * 32 + (kp & 1) * 16, ch = kp >> 1;
        const unsigned nrt = min((unsigned)(ri1 - r0) >> 4, (unsigned)(ri2 - r0) >> 4);
        u64 yf = 0;
#define YFP (ws + WS_FLAG + F_Y + (ntc * 16 + (l15 < 14 ? l15 : 0)) * 8)
#define OMF(a, s, k_, j) do { if ((s) == 0 && (k_) == 0) MFA16Z(acc[a][j], w_, yr_[0][a][0]); else MFA16(acc[a][j], w_, yr_[(s) & 3][a][k_]); } while (0)
#define ORQ(h) do { constexpr int x_ = o_x(h), w_ = o_w(h);                                                                               \
            if (x_ >= 0) { asm volatile("global_load_dwordx4 %0, %1, %2 offset:%3 sc1" : "=v"(yr_[(x_ >> 2) & 3][x_ & 1][(x_ >> 1) & 1])   \
                                        : "v"(xso), "s"((x_ & 1) ? cg1 : cg0), "i"(((x_ >> 1) & 1) * 1024) : "memory");         \
                           if ((x_ & 3) == 3) { xso ^= (unsigned)((x_ >> 2) ^ ((x_ >> 2) + 1)) * 262144u; PIN(xso); } }                    \
            if (o_w((h) + 1) >= 0 && !(o_w((h) + 1) & 1)) asm volatile("s_mov_b32 m0, %0" :: "s"(m0w + (((o_w((h) + 1) >> 1) & 7) ^ 4) * STG) : "memory"); \
            if (w_ >= 0) { asm volatile("global_load_lds_dwordx4 %0, %1 offset:%2" :: "v"(wvo), "s"((w_ & 1) ? wb1 : wb0), "i"((w_ & 1) * 1024) : "memory"); \
                           if (w_ & 1) { wvo ^= (unsigned)((w_ >> 1) ^ ((w_ >> 1) + 1)) * (64 * LDW * 2); PIN(wvo); } } } while (0)
#define OPAIR(s, f, A) do { const bf16x8 w_ = CAT8(wl[(f) & 7], wh[(f) & 7]); constexpr int s_ = (s) + (((f) + 4) >> 4), m_ = ((f) + 4) & 15; \
            OMF(0, s, (f) >> 3, (f) & 7); SB(); ORL(m_ & 7, s_, m_ >> 3, m_ & 7); ORQ(32 * (s) + 2 * (f)); A; SB();                       \
            OMF(1, s, (f) >> 3, (f) & 7); SB(); ORH(m_ & 7, s_, m_ >> 3, m_ & 7); ORQ(32 * (s) + 2 * (f) + 1); SB(); if (((f) & 1)) { __builtin_amdgcn_s_waitcnt(0xC47F); SB(); } } while (0)
#define OTG(h_) do { wa[h_][0] ^= 65536u; wa[h_][1] ^= 65536u; PIN(wa[h_][0]); PIN(wa[h_][1]); } while (0)
#define OST(i)
#define OSTEP(s, POST) do {                                                                                                               \
        OPAIR(s, 0, ); OPAIR(s, 1, ); OPAIR(s, 2, ); OPAIR(s, 3, ); OPAIR(s, 4, ); OPAIR(s, 5, ); OPAIR(s, 6, ); OPAIR(s, 7, );           \
        OPAIR(s, 8, ); OPAIR(s, 9, if (((s) & 3) == 3) OTG(0)); OPAIR(s, 10, ); OPAIR(s, 11, );                                           \
        if ((s) >= 3) asm volatile("s_waitcnt vmcnt(%0)\n\ts_barrier" :: "i"(o_wait(s)) : "memory");                                      \
        SB(); POST; OST((s) + 1);                                                                                                         \
        OPAIR(s, 12, ); OPAIR(s, 13, if (((s) & 3) == 3) OTG(1)); OPAIR(s, 14, ); OPAIR(s, 15, );                                         \
    } while (0)
        PH_STAMP(4);
        SST(5);
        OST(0);
        SB();
        OSTEP(0, );
#define MSK "14"
#define MSKI
        asm volatile("s_cmp_eq_u32 %3, 0\n\ts_cbranch_scc1 1f\n\tglobal_load_dwordx2 %0, %1, %2 sc1\n1:" : "+v"(cxf) : "v"(cfo), "s"(cfb), "s"(xok) : "memory", "scc");
        OSTEP(1, );
        asm volatile("s_waitcnt vmcnt(8)\n\ts_cmp_eq_u32 %9, 0\n\ts_cbranch_scc1 3f\n\ts_mov_b32 %9, 0x40000\n\ts_waitcnt vmcnt(4)\n"
                     "1:\n\tv_cmp_ne_u64 vcc, %14, %8\n\ts_and_b32 vcc_lo, vcc_lo, " MSK "\n\ts_cbranch_scc0 2f\n\t"
                     "global_load_dwordx2 %8, %15, %16 sc1\n\ts_sleep 4\n\ts_waitcnt vmcnt(0)\n\ts_sub_u32 %9, %9, 1\n\ts_cbranch_scc0 1b\n2:\n\t" XLD8 "s_waitcnt vmcnt(4)\n3:"
                     : XOUT("+v"), "+v"(cxf), "+s"(xok) : XIN, "v"(cfo), "s"(cfb), "s"(wave) MSKI : "memory", "vcc", "scc");
        const unsigned inc = (unsigned)__builtin_amdgcn_ballot_w64(cxf != tag) & 0x1f0u;
        SB();
        OSTEP(2, if (__builtin_expect(inc != 0, 0)) asm volatile("global_load_dwordx2 %0, %1, %2 sc1" : "+v"(cxf) : "v"(cfo), "s"(cfb) : "memory"); SB());
        asm volatile("s_waitcnt vmcnt(8)" ::: "memory"); SB();
        OSTEP(3, );
        OSTEP(4, if (__builtin_expect(inc != 0, 0)) FLAG_WAITM(cxf, cfb + cfo, tag, 63, 0x1f0u); SB());
        OSTEP(5, ); OSTEP(6, );
        OSTEP(7, if (__builtin_expect(nrt == 0, 0)) asm volatile("global_load_dwordx2 %0, %1, off sc1" : "+v"(yf) : "v"(YFP) : "memory"); SB());
        OSTEP(8, ); OSTEP(9, ); OSTEP(10, );
        u32x4 yq[7], pv[6], zn[2]; u32x2 ea[4]; unsigned rq;
        if (__builtin_expect(nrt == 0, 0)) {
            FLAG_WAIT(yf, YFP, tag, 63);
            asm volatile("global_load_dwordx4 %0, %8, %10 sc1\n\tglobal_load_dwordx4 %1, %8, %10 offset:512 sc1\n\tglobal_load_dwordx4 %2, %8, %10 offset:1024 sc1\n\t"
                         "global_load_dwordx4 %3, %8, %10 offset:1536 sc1\n\tglobal_load_dwordx4 %4, %8, %10 offset:2048 sc1\n\tglobal_load_dwordx4 %5, %8, %10 offset:2560 sc1\n\t"
                         "global_load_dwordx4 %6, %8, %10 offset:3072 sc1\n\tglobal_load_dword %7, %9, %10 sc1"
                         : "=&v"(yq[0]), "=&v"(yq[1]), "=&v"(yq[2]), "=&v"(yq[3]), "=&v"(yq[4]), "=&v"(yq[5]), "=&v"(yq[6]), "=&v"(rq)
                         : "v"((unsigned)WS_YP + (unsigned)((((lane >> 5) * 8 + ntc) * 16 + (q4 & 1) * 7) * 128 + ch * 64 + 4 * l15) * 4u),
                           "v"((unsigned)WS_R + (unsigned)(lane >= 32 ? ri2 : ri1) * 4u), "s"(ws) : "memory");
        }
        SB();
        OSTEP(11, );
        char* pb = ws + WS_PART + (size_t)(tile * 8 + wave) * 24576;
        const char* tfb = ws + WS_FLAG + F_PARK + (tile * 8 + wave) * 256;
        const unsigned tfo = (unsigned)((lane & 3) ^ kp) * 64u;
        const unsigned ea0 = ldsbase + 131072 + (wave & 3) * 8192 + (wave >> 2) * 4096 + l15 * 128 + (((lane >> 5) * 2 ^ (lane & 7)) << 4) + (q4 & 1) * 8;
        typedef __attribute__((address_space(3))) u32x2 lds_u32x2;
#define OSND(a, j) do { constexpr int x_ = ((a) ^ 1) | (((j) >> 1) & 2 ^ 2);                                             \
            u32x4 p_ = u32x4{cvtpk(acc[a][j][0], acc[a][j][1]), cvtpk(acc[a][j][2], acc[a][j][3]), cvtpk(acc[a][(j) + 1][0], acc[a][(j) + 1][1]), cvtpk(acc[a][(j) + 1][2], acc[a][(j) + 1][3])}; \
            if (x_ == 1) { _Pragma("unroll") for (int k = 0; k < 4; ++k) p_[k] = min(p_[k], 0xfffffffeu); }     \
            asm volatile("global_store_dwordx4 %0, %1, %2 offset:%3 sc1\n\ts_nop 1" :: "v"(xvo), "v"(p_), "s"(pb + (kp ^ x_) * 6144 + (x_ - 1) * 2048), "i"(((j) & 2) * 512) : "memory"); } while (0)
#define OQ(q) do { constexpr int p_ = (q) + 4; const bf16x8 w_ = CAT8(wl[(q) & 7], wh[(q) & 7]);                                           \
            constexpr int j_ = (q) < 32 ? (q) & 3 : 4 + 2 * (((q) >> 4) & 1) + ((q) & 1), k_ = (q) < 32 ? ((q) >> 2) & 1 : ((q) >> 1) & 1, s_ = (q) < 32 ? ((q) >> 3) & 3 : ((q) >> 2) & 3; \
            constexpr int pj_ = p_ < 32 ? p_ & 3 : 4 + 2 * ((p_ >> 4) & 1) + (p_ & 1), pk_ = p_ < 32 ? (p_ >> 2) & 1 : (p_ >> 1) & 1, ps_ = p_ < 32 ? (p_ >> 3) & 3 : (p_ >> 2) & 3; \
            if ((q) == 16 || (q) == 24) { asm volatile("s_waitcnt vmcnt(%0)" :: "i"((q) == 16 ? 4 : 0) : "memory"); SB(); }               \
            OMF(0, 12 + s_, k_, j_); SB();                                                                                                \
            if (p_ < 64) ORL(p_ & 7, ps_, pk_, pj_);                                                                                      \
            ORQ(384 + 2 * (q)); SB();                                                                                                     \
            OMF(1, 12 + s_, k_, j_); SB();                                                                                                \
            if (p_ < 64) ORH(p_ & 7, ps_, pk_, pj_);                                                                                      \
            if ((q) >= 10 && (q) < 14) ea[((q) - 10) & 3] = *(lds_u32x2*)(uintptr_t)(ea0 ^ (unsigned)((((q) - 10) & 1) * 16 + (((q) - 10) & 2) * 32)); \
            if ((q) == 14 || (q) == 15) zn[(q) & 1] = __builtin_bit_cast(u32x4, lds_rd(ldsbase + 131072 + 2048 + (wave & 3) * 8192 + (wave >> 2) * 4096 + ((q) & 1) * 1024 + xvo)); \
            if ((q) == 32) OSND(0, 0); if ((q) == 33) OSND(1, 0); if ((q) == 34) OSND(0, 2); if ((q) == 35) OSND(1, 2);                   \
            if ((q) == PQ) asm volatile("s_setprio 0");                                                                                   \
            if ((q) == 50) OSND(0, 4);                                                                                                    \
            if ((q) == FQ) { asm volatile("s_waitcnt vmcnt(%0)" :: "i"(FQ >= 50) : "memory"); FLAG_PUT(tfb + kp * 64, tag, 1); }     \
            if ((q) == GQ) asm volatile("global_load_dwordx2 %0, %1, %2 sc1" : "=v"(cxf) : "v"(tfo), "s"(tfb) : "memory");               \
            SB(); } while (0)
#define PQ 35
#define FQ 44
#define GQ 60
#define OQ8(q) do { OQ(q); OQ((q) + 1); OQ((q) + 2); OQ((q) + 3); OQ((q) + 4); OQ((q) + 5); OQ((q) + 6); OQ((q) + 7); } while (0)
        if (PQ < 64) asm volatile("s_setprio 1");
        OQ8(0); OQ8(8); OQ8(16); OQ8(24); OST(13); OQ8(32); OQ8(40); OQ8(48); OQ8(56);
        MF_DONE();
        OSND(0, 6); S2T(1);
        OST(14);
        PH_STAMP(5);
        SST(6); S2T(2);
        SB();
        if (__builtin_expect(nrt == 0, 0)) {
            f32x4 ys;
#pragma unroll
            for (int i = 0; i < 7; ++i) PIN(yq[i]);
            PIN(rq);
            const float sig = rbf(1.f / (1.f + __expf(-__uint_as_float(rq))));
#pragma unroll
            for (int j = 0; j < 4; ++j) {
                const float s_ = ((__uint_as_float(yq[0][j]) + __uint_as_float(yq[1][j])) + (__uint_as_float(yq[2][j]) + __uint_as_float(yq[3][j])))
                               + ((__uint_as_float(yq[4][j]) + __uint_as_float(yq[5][j])) + __uint_as_float(yq[6][j]));
                auto rr = __builtin_amdgcn_permlane16_swap(__float_as_uint(s_), __float_as_uint(s_), false, false);
                ys[j] = rbf(__uint_as_float(rr[0]) + __uint_as_float(rr[1])) * sig; }
            char* yl = lds + 65536 + wave * 256;
            *(u32x2*)(yl + (lane >> 5) * 128 + (l15 >> 2) * 32 + (l15 & 1) * 16 + (l15 & 2) * 4) = u32x2{cvtpk(ys[0], ys[1]), cvtpk(ys[2], ys[3])};
            asm volatile("" ::: "memory");
            const int m = r0 + l15;
#pragma unroll
            for (int i = 0; i < 2; ++i) { const u32x4 yn = *(const u32x4*)(yl + (m == ri1 ? 0 : 128) + i * 64 + q4 * 16);
#pragma unroll
                for (int k = 0; k < 4; ++k) { const unsigned z_ = cvtpk(HALF(zn[i][k], 0) + HALF(yn[k], 0), HALF(zn[i][k], 1) + HALF(yn[k], 1));
                    zn[i][k] = m == ri1 || m == ri2 ? z_ : zn[i][k]; } }
        }
        float gg[2][8], xx[2][8];
#pragma unroll
        for (int i = 0; i < 2; ++i)
#pragma unroll
            for (int j = 0; j < 8; ++j) { gg[i][j] = HALF(zn[i][j >> 1], j); xx[i][j] = HALF(ea[2 * i + (j >> 2)][(j >> 1) & 1], j); PIN(gg[i][j]); PIN(xx[i][j]); }
        unsigned ovo = (unsigned)((l15 & 7) * DM + 32 * (l15 >> 3) + 8 * q4) * 2u; PIN(ovo);
        const char* ob = (const char*)(outp + (size_t)r0 * DM + ntc * 128 + 64 * ch);
        asm volatile("" : "+s"(ob));
        PH_STAMP(6);
        { unsigned c_, m_;
          asm volatile("s_waitcnt vmcnt(%0)" :: "i"(GQ < 50 ? 2 : 1) : "memory");
          asm volatile("s_mov_b32 %7, 0x40000\n"
                       "1:\n\tv_cmp_ne_u64 vcc, %11, %6\n\ts_and_b32 vcc_lo, vcc_lo, 12\n\ts_cbranch_scc0 2f\n\t"
                       "global_load_dwordx2 %6, %12, %13 sc1\n\ts_waitcnt vmcnt(0)\n\ts_sub_u32 %7, %7, 1\n\ts_cbranch_scc0 1b\n"
                       "2:\n\tglobal_load_dwordx4 %1, %9, %10 offset:-1024 sc1\n\tglobal_load_dwordx4 %2, %9, %10 offset:1024 sc1\n\t"
                       "global_load_dwordx4 %4, %9, %10 sc1\n\tglobal_load_dwordx4 %5, %9, %10 offset:2048 sc1\n\t"
                       "global_load_dwordx4 %0, %9, %10 offset:-3072 sc1\n\tglobal_load_dwordx4 %3, %9, %10 offset:-2048 sc1"
                       : "=&v"(pv[0]), "=&v"(pv[1]), "=&v"(pv[2]), "=&v"(pv[3]), "=&v"(pv[4]), "=&v"(pv[5]), "+v"(cxf), "=&s"(c_), "=&v"(m_)
                       : "v"(xvo), "s"(pb + kp * 6144 + 3072), "s"(tag), "v"(tfo), "s"(tfb) : "memory", "vcc", "scc"); }
        S2T(3);
#pragma unroll
        for (int i = 0; i < 16; i += 2) { float g_[2];
#pragma unroll
            for (int e_ = 0; e_ < 2; ++e_) { float a_ = gg[i >> 3][(i & 7) + e_] * -1.4426950408889634f; PIN(a_); a_ = 1.f + __builtin_amdgcn_exp2f(a_); PIN(a_);
                g_[e_] = gg[i >> 3][(i & 7) + e_] * __builtin_amdgcn_rcpf(a_); PIN(g_[e_]); }
            const unsigned p_ = cvtpk(g_[0], g_[1]); gg[i >> 3][i & 7] = HALF(p_, 0); gg[i >> 3][(i & 7) + 1] = HALF(p_, 1); PIN(gg[i >> 3][i & 7]); PIN(gg[i >> 3][(i & 7) + 1]); }
        S2T(4);
        asm volatile("s_waitcnt vmcnt(0)" : "+v"(pv[0]), "+v"(pv[1]), "+v"(pv[2]), "+v"(pv[3]), "+v"(pv[4]), "+v"(pv[5]) :: "memory");
        S2T(5);
#pragma unroll 1
        for (int n_ = 0; n_ < 0x40000; ++n_) {
            const unsigned mx_ = max(max(max(pv[0][0], pv[0][1]), max(pv[0][2], pv[0][3])), max(max(pv[3][0], pv[3][1]), max(pv[3][2], pv[3][3])));
            if (__builtin_amdgcn_ballot_w64(mx_ == ~0u) == 0) break;
            asm volatile("global_load_dwordx4 %0, %2, %3 sc1\n\tglobal_load_dwordx4 %1, %2, %3 offset:1024 sc1\n\ts_waitcnt vmcnt(0)"
                         : "=&v"(pv[0]), "=&v"(pv[3]) : "v"(xvo), "s"(pb + kp * 6144) : "memory");
        }
        PH_STAMP(7); S2T(6);
        u32x4 ow[2];
#pragma unroll
        for (int i = 0; i < 2; ++i) {
            u32x4& ov = ow[i];
#pragma unroll
            for (int k = 0; k < 4; ++k) { float sv[2];
#pragma unroll
                for (int e_ = 0; e_ < 2; ++e_) sv[e_] = fmaf((acc[1][4 + 2 * i + (k >> 1)][2 * (k & 1) + e_] + HALF(pv[3 * i][k], e_)) + (HALF(pv[3 * i + 1][k], e_) + HALF(pv[3 * i + 2][k], e_)),
                                                             gg[i][2 * k + e_], xx[i][2 * k + e_]);
                ov[k] = cvtpk(sv[0], sv[1]); }
#pragma unroll
            for (int k = 0; k < 2; ++k) { auto rr = __builtin_amdgcn_permlane16_swap(ov[k], ov[2 + k], false, false); ov[k] = rr[0]; ov[2 + k] = rr[1]; }
        }
#pragma unroll
        for (int i = 0; i < 2; ++i) {
            u32x4 ov;
#pragma unroll
            for (int k = 0; k < 4; ++k) ov[k] = i ? __builtin_amdgcn_update_dpp(ow[1][k], ow[0][k], 0x128, 0xf, 0x3, false) : __builtin_amdgcn_update_dpp(ow[0][k], ow[1][k], 0x128, 0xf, 0xc, false);
            asm volatile("global_store_dwordx4 %0, %1, %2 sc1\n\ts_nop 1" :: "v"(ovo), "v"(ov), "s"(ob + i * (8 * DM * 2)) : "memory");
        }
        SST(7); SST_OUT();
        PH_STAMP(8);
        PH_STAMP(9);
    }
}

extern "C" void kernel_launch(void* const* d_in, const int* in_sizes, int n_in, void* d_out, int out_size, void* d_ws, size_t ws_size,
                              hipStream_t stream) {
    const u16* x    = (const u16*)d_in[0];
    const u16* ckv  = (const u16*)d_in[1];
    const u16* wq   = (const u16*)d_in[2];
    const u16* wuk  = (const u16*)d_in[3];
    const u16* wuv  = (const u16*)d_in[4];
    const u16* wo   = (const u16*)d_in[5];
    const u16* wr   = (const u16*)d_in[7];
    const u16* wg   = (const u16*)d_in[8];
    const u16* wu   = (const u16*)d_in[9];
    const u16* wd   = (const u16*)d_in[10];
    (void)in_sizes; (void)n_in; (void)out_size; (void)ws_size;
    (void)hipFuncSetAttribute((const void*)k_all, hipFuncAttributeMaxDynamicSharedMemorySize, 163840);
    k_all<<<256, 512, 163840, stream>>>(x, ckv, wuk, wuv, wr, (char*)d_ws + ((size_t)WSOFF << 20), wq, wo, wg, wu, wd, (u16*)d_out);
}
```

```cpp
#include <hip/hip_runtime.h>
#include <hip/hip_bf16.h>
#include <stdint.h>

typedef unsigned short u16;
typedef unsigned long long u64;
typedef short    bf16x8 __attribute__((ext_vector_type(8)));
typedef short    bf16x4 __attribute__((ext_vector_type(4)));
typedef float    f32x4  __attribute__((ext_vector_type(4)));
typedef float    f32x2  __attribute__((ext_vector_type(2)));
typedef __bf16   bfx2   __attribute__((ext_vector_type(2)));
typedef unsigned u32x4  __attribute__((ext_vector_type(4)));
typedef unsigned u32x2  __attribute__((ext_vector_type(2)));

#define DI __device__ __forceinline__

constexpr int SQ = 2048, DM = 1024, NH = 32, HD = 128, DC = 512, FF = 3584;

#define WSOFF 0
constexpr size_t WS_R    = 4u << 20;
constexpr size_t WS_FLAG = (4u << 20) + 65536;
constexpr size_t WS_GP   = 6u << 20;
constexpr size_t WS_YP   = 7u << 20;
constexpr size_t WS_PART = 96u << 20;
constexpr size_t WS_XF   = 16u << 20;
#define HSTR 1310720
constexpr size_t WS_KV   = 24u << 20;
constexpr size_t WS_CTX  = 72u << 20;
constexpr int F_EPOCH = 0, F_TOP = 1024, F_LN = 4096, F_KV = 8192, F_GU = 12288  , F_Y = 20480  , F_PARK = 524288  ,
              F_CTX = 65536  ;

#define PH_STAMP(i)
#define SST(i)
#define S2T(i)
#define SST_OUT()
#define ZST(i)
#define ZST_OUT()
DI unsigned cvtpk(float lo, float hi) { f32x2 v = {lo, hi}; bfx2 b = __builtin_convertvector(v, bfx2); return __builtin_bit_cast(unsigned, b); }
DI float rbf(float x) { return __uint_as_float(cvtpk(x, 0.f) << 16); }
#define DPP_ADD(v, ctrl, rmask) v += __uint_as_float(__builtin_amdgcn_update_dpp(0u, __float_as_uint(v), ctrl, rmask, 0xf, false))
DI unsigned tkey(unsigned fbits, int i) { unsigned u = fbits >> 16; u ^= (u & 0x8000u) ? 0xffffu : 0x8000u; return (u << 16) | (unsigned)(2047 - i); }
DI void merge_top2(unsigned& a1, unsigned& a2, unsigned b1, unsigned b2) { const unsigned lo = min(a1, b1); a1 = max(a1, b1); a2 = max(lo, max(a2, b2)); }
#define DPP_TOP2(a1, a2, ctrl, rmask) merge_top2(a1, a2, __builtin_amdgcn_update_dpp(0u, a1, ctrl, rmask, 0xf, false), __builtin_amdgcn_update_dpp(0u, a2, ctrl, rmask, 0xf, false))

#define GLDS_S(voff, sbase, ldsoff, imm)                                                                        \
    asm volatile("s_mov_b32 m0, %2\n\ts_nop 0\n\tglobal_load_lds_dwordx4 %0, %1 offset:%3"                       \
                 :: "v"(voff), "s"(sbase), "s"(ldsoff), "i"(imm) : "memory")
#define GLDS_C(voff, sbase, ldsoff, imm)                                                                        \
    asm volatile("s_mov_b32 m0, %2\n\ts_nop 0\n\tglobal_load_lds_dwordx4 %0, %1 offset:%3 sc1"                   \
                 :: "v"(voff), "s"(sbase), "s"(ldsoff), "i"(imm) : "memory")
#define LGKM0_BAR()    asm volatile("s_waitcnt lgkmcnt(0)\n\ts_barrier" ::: "memory")
#define SB() __builtin_amdgcn_sched_barrier(0)
#define CAT_(a, b) a##b
#define CAT(a, b) CAT_(a, b)
#define PIN(x) asm volatile("" : "+v"(x))
#define OADV(p_, d_) do { p_ += (d_); asm volatile("" : "+s"(p_)); } while (0)
#define VADD(d, a, b)    asm volatile("v_add_f32 %0, %1, %2" : "=v"(d) : "v"(a), "v"(b))
#define KEEP(x) asm volatile("" :: "v"(x))
#define MFA16(c, a, b) asm volatile("v_mfma_f32_16x16x32_bf16 %0, %1, %2, %0" : "+v"(c) : "v"(a), "v"(b))
#define MFA16Z(c, a, b) asm volatile("v_mfma_f32_16x16x32_bf16 %0, %1, %2, 0" : "=&v"(c) : "v"(a), "v"(b))
#define MFA16C(c, a, b, c0) asm volatile("v_mfma_f32_16x16x32_bf16 %0, %1, %2, %3" : "=&v"(c) : "v"(a), "v"(b), "v"(c0))
#define MF_DONE() do { SB(); asm volatile("s_nop 7\n\ts_nop 7"); SB(); } while (0)
#define ST16_SC1(p, v) asm volatile("global_store_dwordx4 %0, %1, off sc1\n\ts_nop 1" :: "v"(p), "v"(v) : "memory")
#define ST4_SC1(p, v)  asm volatile("global_store_dword %0, %1, off sc1" :: "v"(p), "v"(v) : "memory")
#define FLAG_PUT(p_, val_, on_) do { u64 sv_, tv_; unsigned tz_; const u64 v_ = (val_); const unsigned m_ = __builtin_amdgcn_readfirstlane((on_) ? 1u : 0u); \
        asm volatile("s_cmp_eq_u32 %5, 0\n\ts_cbranch_scc1 1f\n\tv_mov_b64 %1, %3\n\tv_mov_b32 %2, 0\n\ts_mov_b64 %0, exec\n\ts_mov_b64 exec, 1\n\t" \
                     "global_store_dwordx2 %2, %1, %4 sc1\n\ts_mov_b64 exec, %0\n1:"                                                      \
                     : "=&s"(sv_), "=&v"(tv_), "=&v"(tz_) : "s"(v_), "s"(p_), "s"(m_) : "memory", "scc"); } while (0)
#define FLAG_PUTZ(p_, val_, nz_) do { u64 sv_, tv_; unsigned tz_; const u64 v_ = (val_);                                                   \
        asm volatile("s_cmp_lg_u32 %5, 0\n\ts_cbranch_scc1 1f\n\tv_mov_b64 %1, %3\n\tv_mov_b32 %2, 0\n\ts_mov_b64 %0, exec\n\ts_mov_b64 exec, 1\n\t" \
                     "global_store_dwordx2 %2, %1, %4 sc1\n\ts_mov_b64 exec, %0\n1:"                                                      \
                     : "=&s"(sv_), "=&v"(tv_), "=&v"(tz_) : "s"(v_), "s"(p_), "s"(nz_) : "memory", "scc"); } while (0)
#define FLAG_GET(r_, p_) asm volatile("global_load_dwordx2 %0, %1, off sc1" : "=v"(r_) : "v"(p_) : "memory")
#define FLAG_WAIT(r_, p_, tag_, N) do { unsigned c_;                                                                                      \
        asm volatile("s_waitcnt vmcnt(%4)\n\ts_mov_b32 %1, 0x40000\n"                                                                     \
                     "1:\n\tv_cmp_ne_u64 vcc, %3, %0\n\ts_cmp_eq_u64 vcc, 0\n\ts_cbranch_scc1 2f\n\t"                                      \
                     "s_sleep 2\n\tglobal_load_dwordx2 %0, %2, off sc1\n\ts_waitcnt vmcnt(0)\n\ts_sub_u32 %1, %1, 1\n\ts_cbranch_scc0 1b\n"  \
                     "2:" : "+v"(r_), "=&s"(c_) : "v"(p_), "s"(tag_), "i"(N) : "memory", "vcc", "scc"); } while (0)
#define FLAG_WAITM(r_, p_, tag_, N, m_) do { unsigned c_;                                                                                 \
        asm volatile("s_waitcnt vmcnt(%4)\n\ts_mov_b32 %1, 0x40000\n"                                                                     \
                     "1:\n\tv_cmp_ne_u64 vcc, %3, %0\n\ts_and_b32 vcc_lo, vcc_lo, %5\n\ts_cbranch_scc0 2f\n\t"                             \
                     "s_sleep 2\n\tglobal_load_dwordx2 %0, %2, off sc1\n\ts_waitcnt vmcnt(0)\n\ts_sub_u32 %1, %1, 1\n\ts_cbranch_scc0 1b\n"  \
                     "2:" : "+v"(r_), "=&s"(c_) : "v"(p_), "s"(tag_), "i"(N), "s"(m_) : "memory", "vcc", "scc"); } while (0)
typedef __attribute__((address_space(3))) bf16x4 lds_bf16x4;
DI bf16x4 tr_rd(unsigned a) { return __builtin_amdgcn_ds_read_tr16_b64_v4i16((lds_bf16x4*)(uintptr_t)a); }
typedef __attribute__((address_space(3))) bf16x8 lds_bf16x8;
DI bf16x8 lds_rd(unsigned a) { return *(lds_bf16x8*)(uintptr_t)a; }
#define CAT8(lo_, hi_) bf16x8{lo_[0], lo_[1], lo_[2], lo_[3], hi_[0], hi_[1], hi_[2], hi_[3]}

#define POFF 32.f
#define LTHR 8192.f
DI float quad_max(float v) {
    auto r1 = __builtin_amdgcn_permlane16_swap(__float_as_uint(v), __float_as_uint(v), false, false);
    v = fmaxf(__uint_as_float(r1[0]), __uint_as_float(r1[1]));
    auto r2 = __builtin_amdgcn_permlane32_swap(__float_as_uint(v), __float_as_uint(v), false, false);
    return fmaxf(__uint_as_float(r2[0]), __uint_as_float(r2[1])); }
#define QWD 3
#define QS1 0xbfff
constexpr int q_xb(int s) { return s >= 1 && s <= 12; }
constexpr int q_xa(int s) { return s >= 0 && s <= 11; }
constexpr int q_s0(int s) { int n = 0; for (int i = 0; i < s; ++i) n += (QS1 >> i) & 1; return n; }
constexpr int q_kv(int kt, int f) { const int s = (kt - 13) * 16 + f - 12; return s >= 0 && s % 3 == 0 && s / 3 < 12 ? s / 3 : -1; }
constexpr int q_op(int s, int f) { return (f == 0 || f == 2 ? q_xb(s) : f == 8 || f == 10 ? q_xa(s) : f == 12 || f == 14 ? s + QWD < 16 : f == 3 ? s == 12
                                          : f == 4 ? (QS1 >> s) & 1 : 0) + (q_kv(s, f) >= 0); }
constexpr int q_after(int p, int kt) { int n = 0; for (int i = p + 1; i <= kt * 16 + 11; ++i) n += q_op(i >> 4, i & 15); return n; }
constexpr int q_min(int a, int b) { return a < b ? a : b; }
constexpr int q_wait(int kt) {
    int n = kt <= 2 ? 4 * (2 - kt) + q_after(-1, kt) : q_after((kt - 2) * 16 + 2, kt);
    if (kt + 1 >= QWD) n = q_min(n, q_after((kt + 1 - QWD) * 16 + 14, kt));
    if (kt == 13) n = q_min(n, q_after(12 * 16 + 3, kt));
    return n; }
constexpr int q_fl() { int s = 0; while (q_s0(s + 1) < 8) ++s; int kt = s; while (q_wait(kt) > q_after(s * 16 + 6, kt)) ++kt; return kt; }
constexpr int Q_NST = q_s0(16);
static_assert(Q_NST >= 8, "");
constexpr int o_xp(int st, int i) { return st <= 5 ? 64 + 4 * (4 * (st - 4) + i) : 32 * (st - 3) + (i == 0 ? 0 : i == 1 ? 4 : i == 2 ? 12 : 16); }
constexpr int o_wp(int st, int i) { return st <= 7 ? 16 * (st - 4) + 4 + 6 * i : 32 * (st - 5) + 24 + 4 * i; }
constexpr int o_x(int h) { for (int st = 4; st < 16; ++st) for (int i = 0; i < 4; ++i) if (o_xp(st, i) == h) return 4 * st + i; return -1; }
constexpr int o_w(int h) { for (int st = 4; st < 16; ++st) for (int i = 0; i < 2; ++i) if (o_wp(st, i) == h) return 2 * st + i; return -1; }
constexpr int o_after(int p, int e) { int n = 0; for (int h = p + 1; h < e; ++h) n += (o_x(h) >= 0) + (o_w(h) >= 0); return n; }
constexpr int o_wait(int kt) { return o_after(kt == 11 ? o_wp(15, 1) : o_xp(kt + 1, 3), 32 * kt + 24); }
constexpr int L_PFT = 149504, L_DUMP = 155648;
constexpr int CFS = 128;
constexpr unsigned F1S = 0xffff642fu, F1F = 0xffff5310u, F2S = 0xff64fff0u, F2F = 0xff53f21fu;
constexpr int F1T = 3;

__global__ __launch_bounds__(512, 2) void k_all(const u16* __restrict__ x, const u16* __restrict__ ckv, const u16* __restrict__ wuk, const u16* __restrict__ wuv,
                                                const u16* __restrict__ w_r, char* __restrict__ ws, const u16* __restrict__ wq, const u16* __restrict__ wo,
                                                const u16* __restrict__ w_gate, const u16* __restrict__ w_up,
                                                const u16* __restrict__ w_down, u16* __restrict__ out) {
    extern __shared__ __attribute__((aligned(16))) char lds[];
    const int b = blockIdx.x, xcd = b & 7, idx = b >> 3, head = xcd * 4 + (idx >> 3), jj = idx & 7;
    const int hp = head ^ 1;
    const unsigned ldsbase = (unsigned)(uintptr_t)(__attribute__((address_space(3))) char*)lds;
    const int wave = __builtin_amdgcn_readfirstlane(threadIdx.x >> 6);
    const int qbA = 15 - jj, qbB = jj;
#define KROW(g) (((2 * ((g) & 3) + ((g) >> 2)) * 8 + jj) * 32)
    const int qw = wave < 4 ? wave : 7 - wave, qb = wave < 4 ? qbA : qbB;
    bf16x8 qf[8];
    u64 tag;
    u16* outp = out;
    int ri1, ri2;
    PH_STAMP(0);
    SST(0);
    ZST(0);
    u32x4 pk[16];
    bf16x8 xr[4][2][2];
    u32x4 rv;
    const char* xg;
#define QXL1(j) asm volatile("global_load_dwordx4 %0, %1, %2 offset:%3 sc1" : "=v"(xr[(j) >> 2][(j) & 1][((j) >> 1) & 1]) : "v"(xvo), "s"(xg + (size_t)((j) >> 2) * 262144), \
                            "i"(((j) & 1) * 2048 + (((j) >> 1) & 1) * 1024) : "memory")
    const char* sb_[4];
    unsigned qa, qa2; bf16x4 ql[8], qh[8];
#define QFA(s, f) ((((f) & 1) ? qa2 : qa) + (unsigned)(((s) & 3) * 16384 + ((f) >> 3) * 8192 + (((f) >> 2) & 1) * 1024 + (((f) >> 1) & 1) * 512))
#define QFM(s, f) ((s) == 15 ? ((f) & 1) * 8 + ((f) >> 1) : (f))
#define QRL(s, f) ql[(f) & 7] = tr_rd(QFA(s, QFM(s, f)))
#define QRH(s, f) qh[(f) & 7] = tr_rd(QFA(s, QFM(s, f)) + 2048u)
    unsigned xvo, qwo;
    const u16* wqb;
#define QWOFF() ({ unsigned l_ = threadIdx.x & 63; asm volatile("" : "+v"(l_)); (unsigned)(((l_ >> 2) & 7) * (NH * HD) + 32 * (l_ >> 5) + 8 * ((l_ & 3) ^ ((l_ >> 3) & 2))) * 2u; })
    {
        constexpr int LDW = NH * HD, K = DC;
        int tid = threadIdx.x; asm volatile("" : "+v"(tid));
        const int lane = tid & 63;
        const int wm = wave >> 2, wn = wave & 3;
        const int row = b * 8 + wave;
        u32x4 lx[2], lw[2]; u32x2 ep;
        asm volatile("global_load_dwordx4 %0, %5, %6\n\tglobal_load_dwordx4 %1, %5, %6 offset:1024\n\tglobal_load_dwordx4 %2, %5, %7\n\tglobal_load_dwordx4 %3, %5, %7 offset:1024\n\t"
                     "global_load_dwordx2 %4, %8, off sc1"
                     : "=&v"(lx[0]), "=&v"(lx[1]), "=&v"(lw[0]), "=&v"(lw[1]), "=&v"(ep) : "v"(lane * 16), "s"(x + (unsigned)(row * DM)), "s"(w_r), "v"(ws + WS_FLAG + F_EPOCH) : "memory");
#define XKEY(r) (((r) >> 1) & 7)
        unsigned xoff[4], woff[2];
        { const unsigned x0_ = (unsigned)(KROW(wave) + (lane >> 3)) * (K * 2) + (unsigned)((lane & 7) ^ (lane >> 4)) * 16u;
          xoff[0] = x0_; xoff[1] = (x0_ ^ 64u) + 7168u; xoff[2] = x0_ + 14336u; xoff[3] = xoff[1] + 14336u;
          woff[1] = (unsigned)((8 * wave + ((lane >> 2) & 7)) * LDW + hp * HD + 32 * (lane >> 5) + 8 * ((lane & 3) ^ (2 * (wave & 1)))) * 2u; woff[0] = woff[1] + 896u; }
        const char* xsp = (const char*)ckv;
        const char* wsp = (const char*)wuk - 896;
        const char* wsv = (const char*)wuv - 2944;
        const unsigned m0b = ldsbase + wave * 4096;
#define KM0(g, w_) asm volatile("s_add_u32 m0, %0, %1" :: "s"(m0b), "i"(((g) & 1) * 65536 + (w_) * 32768) : "memory", "scc")
#define KRX(i) asm volatile("global_load_lds_dwordx4 %0, %1 offset:%2" :: "v"(xoff[i]), "s"(xsp), "i"((i) * 1024) : "memory")
#define KRW(i) asm volatile("global_load_lds_dwordx4 %0, %1 offset:%2 nt" :: "v"(woff[(i) & 1]), "s"((i) < 2 ? wsp : wsv), "i"((i) * 1024) : "memory")
#define STAGE(g) do { KM0(g, 0); asm volatile("s_nop 0"); KRX(0); KRX(1); KRX(2); KRX(3); KM0(g, 1); asm volatile("s_nop 0"); KRW(0); KRW(1); KRW(2); KRW(3); \
                      OADV(xsp, 128); OADV(wsp, 64 * LDW * 2); OADV(wsv, 64 * LDW * 2); } while (0)
#define KST(i)
        const unsigned pfl = ldsbase + L_DUMP + wave * 1024;
        unsigned pxo, pqo, xra, xso_;
#define PFX(g) GLDS_S(pxo, pfxp, pfl - (g) * 128, (g) * 128)
#define PFQ(i) GLDS_S(pqo, pfqp + (size_t)(4 * (i)) * LDW, pfl, 0)
        STAGE(0);
        SB(); ZST(1);
        int w2 = wave; asm volatile("" : "+s"(w2));
        xg = ws + WS_XF + (unsigned)((w2 < 4 ? qbA * 4 + w2 : qbB * 4 + 7 - w2) * 4096);
        wqb = wq + (unsigned)(8 * w2 * LDW + head * HD);
        const u16* pfqp = wq + (unsigned)((128 * jj + 16 * w2) * LDW + head * HD);
        const u16* pfxp = ckv + (unsigned)((KROW(w2) + 8 * (head & 3)) * DC);
        unsigned xa[2][2], wa[2][2];
        const int l15 = lane & 15, q4 = lane >> 4;
#pragma unroll
        for (int s = 0; s < 2; ++s) {
            const int r0 = l15;
            xa[0][s] = ldsbase + (unsigned)((wm * 128 + r0) * 128 + ((((4 * s + q4) ^ XKEY(r0))) << 4));
            wa[0][s] = (ldsbase + 32768u + (unsigned)(q4 * 4096 + wn * 512 + (l15 >> 2) * 64 + (q4 & 1) * 32 + (lane & 3) * 8)) ^ (32u * s);
            xa[1][s] = xa[0][s] + 65536u; wa[1][s] = wa[0][s] + 65536u;
            PIN(xa[0][s]); PIN(xa[1][s]); PIN(wa[0][s]); PIN(wa[1][s]); }
        u64 pf0;
        { u64 pw_ = (u64)(((w2 & 2) ? wuv : wuk) + (unsigned)((128 + 192 * (w2 & 1) + jj) * LDW + hp * HD)), pc_ = (u64)ckv + (unsigned)(b * 8192 + (w2 & 3) * 2048);
          asm volatile("" : "+s"(pw_), "+s"(pc_));
          unsigned o0_ = (unsigned)(lane >> 1) * (8 * LDW * 2) + (lane & 1) * 128u, o1_ = (lane & 15) * 128u; PIN(o0_); PIN(o1_);
          pf0 = (lane < 48 ? pw_ : pc_) + (lane < 48 ? o0_ : o1_); PIN(pf0); }
        asm volatile("" : "+s"(pfqp), "+s"(pfxp), "+s"(xg), "+s"(wqb), "+s"(outp));
        SB();
        asm volatile("s_waitcnt vmcnt(8)" : "+v"(lx[0]), "+v"(lx[1]), "+v"(lw[0]), "+v"(lw[1]), "+v"(ep) :: "memory");
        KST(1); ZST(2);
#define HALF(v_, i) __uint_as_float(((i) & 1) ? (v_) & 0xffff0000u : (v_) << 16)
#define BOTH(v_) f32x2{HALF(v_, 0), HALF(v_, 1)}
        {
            f32x2 v[8];
#pragma unroll
            for (int i = 0; i < 8; ++i) v[i] = BOTH(lx[i >> 2][i & 3]);
            const float pv_ = __uint_as_float(__builtin_amdgcn_readfirstlane(__float_as_uint(v[0][0])));
            f32x2 s2 = f32x2{0.f, 0.f}, q2 = f32x2{0.f, 0.f};
#pragma unroll
            for (int i = 0; i < 8; ++i) { const f32x2 d = v[i] - pv_; s2 += d; q2 += d * d; }
            float s = s2[0] + s2[1], q = q2[0] + q2[1];
            DPP_ADD(s, 0x111, 0xf); DPP_ADD(q, 0x111, 0xf); DPP_ADD(s, 0x112, 0xf); DPP_ADD(q, 0x112, 0xf); DPP_ADD(s, 0x114, 0xf); DPP_ADD(q, 0x114, 0xf);
            DPP_ADD(s, 0x118, 0xf); DPP_ADD(q, 0x118, 0xf); DPP_ADD(s, 0x142, 0xa); DPP_ADD(q, 0x142, 0xa); DPP_ADD(s, 0x143, 0xc); DPP_ADD(q, 0x143, 0xc);
            const float dm = __uint_as_float(__builtin_amdgcn_readlane(__float_as_uint(s), 63)) * (1.f / DM);
            const float var_bf = rbf(fmaxf(fmaf(-dm, dm, __uint_as_float(__builtin_amdgcn_readlane(__float_as_uint(q), 63)) * (1.f / DM)), 0.f));
            const float mean_bf = rbf(pv_ + dm), den = rbf(__builtin_amdgcn_sqrtf(rbf(var_bf + 1e-5f))), rden = __builtin_amdgcn_rcpf(den);
            u32x4 xw[2]; float r2[2] = {0.f, 0.f};
#pragma unroll
            for (int i = 0; i < 8; ++i) {
                const f32x2 c_ = v[i] - mean_bf;
                const unsigned cp = cvtpk(c_[0], c_[1]);
                const f32x2 cb = BOTH(cp);
                f32x2 n_ = cb * rden;
                n_ += (cb - n_ * den) * rden;
                const unsigned np = cvtpk(n_[0], n_[1]);
                const unsigned wp = lw[i >> 2][i & 3];
                r2[i & 1] = __builtin_amdgcn_fdot2_f32_bf16(__builtin_bit_cast(bfx2, np), __builtin_bit_cast(bfx2, wp), r2[i & 1], false);
                xw[i >> 2][i & 3] = np;
            }
            char* lr = lds + 131072 + wave * 2064 + lane * 16;
            *(u32x4*)lr = xw[0]; *(u32x4*)(lr + 1024) = xw[1];
            s = r2[0] + r2[1];
            DPP_ADD(s, 0x111, 0xf); DPP_ADD(s, 0x112, 0xf); DPP_ADD(s, 0x114, 0xf); DPP_ADD(s, 0x118, 0xf); DPP_ADD(s, 0x142, 0xa); DPP_ADD(s, 0x143, 0xc);
            if (lane == 63) *(float*)(lds + 131072 + wave * 2064 + 2048) = rbf(s);
        }
        SB(); ZST(3);
        STAGE(1);
        asm volatile("s_bitcmp0_b32 %2, 2\n\ts_cbranch_scc1 1f\n\ts_mov_b32 m0, %1\n\ts_nop 0\n\tglobal_load_lds_dword %0, off\n1:" :: "v"(pf0), "s"(pfl), "s"(wave) : "memory", "scc");
        tag = (((u64)(unsigned)__builtin_amdgcn_readfirstlane(ep[1]) << 32) | (unsigned)__builtin_amdgcn_readfirstlane(ep[0])) + 0x9E3779B97F4A7C15ull;
        float rr;
        const unsigned l16 = (unsigned)lane * 16u;
        u32x2 xi_[4];
        typedef __attribute__((address_space(3))) u32x2 lds_u32x2; typedef __attribute__((address_space(3))) float lds_f32;
#define XI_RD(i) xi_[i] = *(lds_u32x2*)(uintptr_t)(xra + ((i) & 1) * 16 + ((i) >> 1) * 1024)
#define XI_ST(i) do { const u32x4 p_ = u32x4{xi_[2 * (i)][0], xi_[2 * (i)][1], xi_[2 * (i) + 1][0], xi_[2 * (i) + 1][1]};               \
            asm volatile("global_store_dwordx4 %0, %1, %2 sc1" :: "v"(xso_), "v"(p_), "s"(ws + WS_XF + (unsigned)(wave * 262144 + (b >> 1) * 2048 + (b & 1) * 128 + (i) * 2097152)) : "memory"); } while (0)
        SB(); ZST(4);
        asm volatile("s_waitcnt vmcnt(%1)\n\ts_bitcmp1_b32 %0, 2\n\ts_cbranch_scc1 1f\n\ts_waitcnt vmcnt(8)\n1:\n\ts_barrier" :: "s"(wave), "i"(9) : "memory", "scc");
        KST(2); ZST(6);
        f32x4 a16[8][4];
        bf16x8 xf[2][4];
        bf16x4 wl[4], wh[4];
#define KXR(kt, q, mb) xf[(q) & 1][mb] = lds_rd(xa[(kt) & 1][(q) >> 1] + (unsigned)((((q) & 1) * 4 + (mb)) * 2048))
#define KWL(kt, s32, nb) wl[nb] = tr_rd(wa[(kt) & 1][(nb) & 1] + (unsigned)((s32) * 16384 + ((nb) >> 1) * 2048))
#define KWH(kt, s32, nb) wh[nb] = tr_rd(wa[(kt) & 1][(nb) & 1] + (unsigned)((s32) * 16384 + ((nb) >> 1) * 2048 + 256))
#define KMF(kt, q, g) do { const bf16x8 w_ = CAT8(wl[(g) >> 2], wh[(g) >> 2]); constexpr int m_ = ((q) & 1) * 4 + ((g) & 3);               \
            if ((kt) == 0 && (q) < 2) { if ((g) >= 8) MFA16Z(a16[m_][(g) >> 2], xf[(q) & 1][(g) & 3], w_); else MFA16Z(a16[m_][(g) >> 2], w_, xf[(q) & 1][(g) & 3]); } \
            else                      { if ((g) >= 8) MFA16(a16[m_][(g) >> 2], xf[(q) & 1][(g) & 3], w_);  else MFA16(a16[m_][(g) >> 2], w_, xf[(q) & 1][(g) & 3]); } } while (0)
        u64 lnf;
#define LNFP (ws + WS_FLAG + F_LN + (wave * 32 + (lane & 31)) * 8)
#define KRQ(i) do { GLDS_S(qwo, wqb + (size_t)(((i) >> 1) * 64) * LDW, ldsbase + wave * 2048 + ((i) >> 1) * 16384 + ((i) & 1) * 896, ((i) & 1) * 128); } while (0)
        constexpr int KRS = 3, KPFD = 3;
#define KG(kt, q, g) do { constexpr int nk_ = (kt) + ((q) == 3), nq_ = ((q) + 1) & 3;                                                     \
            constexpr int xi = ((q) & 1) ? ((g) == 1 ? 0 : (g) == 2 ? 1 : (g) == 5 ? 2 : (g) == 6 ? 3 : -1) : (((g) & 1) && (g) < 8 ? (g) >> 1 : -1); \
            constexpr int ri = (q) == 3 ? ((g) == 9 ? 0 : (g) == 10 ? 1 : (g) == 13 ? 2 : (g) == 14 ? 3 : -1) : (((g) & 1) && (g) > 8 ? ((g) - 9) >> 1 : -1); \
            KMF(kt, q, g); SB();                                                                                                        \
            if (xi >= 0 && nk_ < 8) KXR(nk_, nq_, xi & 3);                                                                              \
            if (((q) & 1) && nk_ < 8) { if (((g) & 3) == 3) KWL(nk_, nq_ >> 1, (g) >> 2); if (((g) & 3) == 0 && (g) > 0) KWH(nk_, nq_ >> 1, (((g) >> 2) + 3) & 3); } \
            if (!((q) & 1) && (g) == 0 && (kt) + (q) > 0) KWH(kt, (q) >> 1, 3);                                                          \
            { constexpr int G = (q) == 3 ? (g) : (q) == 0 ? 16 + (g) : -9, T = (kt) + ((q) == 3 ? 2 : 1);          \
              if (T >= 2 && T < 8) { if (G == 0) KM0(T, 0); if (G == 4 * KRS) KM0(T, 1);                                                 \
                  if (G >= 1 && (G - 1) % KRS == 0 && (G - 1) / KRS < 8) { if ((G - 1) / KRS < 4) KRX(((G - 1) / KRS) & 3); else KRW(((G - 1) / KRS) & 3); } \
                  if (G == 29) OADV(xsp, 128); if (G == 30) OADV(wsp, 64 * LDW * 2); if (G == 31) OADV(wsv, 64 * LDW * 2); } }                                                 \
            if ((q) == 1 && (kt) + KPFD < 8) { if ((kt) >= 1 && (g) == 13) PFX((kt) + KPFD); }                \
            if ((q) == 2 && (kt) == 6 && (g) == 9) { qwo = QWOFF(); PIN(qwo); }                                                         \
            if ((q) == 1 && (kt) == 7) { if ((g) == 9) asm volatile("global_load_dwordx4 %0, %1, %2 sc1" : "=v"(rv) : "v"(xvo), "s"(ws + WS_R + wave * 1024) : "memory"); \
                                         if ((g) == 10) QXL1(0); if ((g) == 13) QXL1(1); if ((g) == 14) QXL1(2); }                      \
            if ((q) == 2 && (kt) == 7 && (g) == 2) QXL1(3);                                                                             \
            if ((q) == 2 && (kt) == 7 && (g) == 9) { int w_ = wave; asm volatile("" : "+s"(w_));                                        \
                sb_[0] = ws + WS_KV + (unsigned)(hp * HSTR + ((w_ >> 2) * 2 + (jj >> 2)) * 65536 + (jj & 3) * 8192 + (w_ & 3) * 2048); asm volatile("" : "+s"(sb_[0])); } \
            if ((q) == 2 && (kt) == 7 && (g) >= 11 && (g) < 14) { sb_[((g) - 10) & 3] = sb_[0] + ((g) - 10) * 262144; asm volatile("" : "+s"(sb_[((g) - 10) & 3])); } \
            if ((q) == 2 && (kt) == 7 && (g) == 14) { qa = ldsbase + (unsigned)((q4 >> 1) * 4096 + (4 * (q4 & 1) + (l15 >> 2)) * 64 + (q4 & 1) * 32 + (lane & 3) * 8); PIN(qa); } \
            if ((q) == 2 && (kt) == 7 && (g) == 15) { qa2 = qa ^ 32u; PIN(qa2); }                                                                              \
            if ((q) == 3 && (kt) == 6 && ri >= 0) KRQ(ri & 3);                                                                          \
            if ((q) == 0 && (kt) == 7 && ri >= 0 && 4 + ri < 2 * QWD) KRQ(4 + (ri & 3));                                                                    \
            if ((q) == 0 && (kt) == 0) { unsigned l_ = lane;                                                                            \
                if ((g) == 9)  { PIN(l_); pxo = (l_ >> 3) * (DC * 2) + (l_ & 7) * 16; PIN(pxo); }                                       \
                if ((g) == 11) { PIN(l_); pqo = (l_ >> 4) * (LDW * 2) + (l_ & 15) * 16; PIN(pqo); }                                     \
                if ((g) == 13) { PIN(l_); xso_ = (l_ >> 3) * 256 + (l_ & 7) * 16; PIN(xso_); }                                          \
                if ((g) == 14) { PIN(l_); xra = (l_ & 7) * 2064 + (ldsbase + 131072 + wave * 128); PIN(xra); }                          \
                if ((g) == 15) { PIN(l_); xra += (l_ >> 4) * 32 + (l_ & 8); PIN(xra); } }                                               \
            if ((q) == 0 && (kt) == 1) { if ((g) == 9 || (g) == 11) { XI_RD((g) - 9); XI_RD((g) - 8); }                                 \
                if ((g) == 12) rr = *(lds_f32*)(uintptr_t)(ldsbase + 131072 + wave * 2064 + 2048); }                                    \
            if ((q) == 1 && (kt) == 1) { if ((g) == 9) XI_ST(0); if ((g) == 10) { KEEP(xi_[0]); KEEP(xi_[1]); } if ((g) == 14) XI_ST(1); if ((g) == 15) { KEEP(xi_[2]); KEEP(xi_[3]); } \
                if ((g) == 0) asm volatile("global_store_dword %0, %1, %2 sc1" :: "v"(0u), "v"(rr), "s"((float*)(ws + WS_R) + row) : "memory"); }     \
            if ((q) == 1 && ((kt) == 3 || (kt) == 4)) { if ((g) == 9) PFQ((2 * (kt) - 6) & 3); if ((g) == 11) PFQ((2 * (kt) - 5) & 3); }                                                    \
            if ((q) == 2 && (kt) == 5 && (g) == 12) FLAG_GET(lnf, LNFP);                                                                \
            SB(); } while (0)
#define KQ(kt, q) do { KG(kt, q, 0); KG(kt, q, 1); KG(kt, q, 2); KG(kt, q, 3); KG(kt, q, 4); KG(kt, q, 5); KG(kt, q, 6); KG(kt, q, 7);    \
                       KG(kt, q, 8); KG(kt, q, 9); KG(kt, q, 10); KG(kt, q, 11); KG(kt, q, 12); KG(kt, q, 13); KG(kt, q, 14); KG(kt, q, 15); } while (0)
#define KEXT(s) (((s) == 1 ? 3 : (s) == 5 ? 1 : 0) + ((s) >= 1 && (s) + KPFD < 8) + ((s) == 3 || (s) == 4 ? 2 : 0))
#define KSTEP(kt) do {                                                                                                                  \
        KQ(kt, 0); KQ(kt, 1); KQ(kt, 2);                                                                                                \
        if ((kt) == 6) FLAG_WAIT(lnf, LNFP, tag, 0);                                   \
                       \
        if ((kt) == 0) asm volatile("s_waitcnt vmcnt(%1) lgkmcnt(0)\n\ts_bitcmp1_b32 %0, 2\n\ts_cbranch_scc1 1f\n\ts_waitcnt vmcnt(%2)\n1:\n\ts_barrier" :: "s"(wave), "i"(KEXT(0) + 1), "i"(KEXT(0)) : "memory", "scc"); \
        else asm volatile("s_waitcnt vmcnt(%0) lgkmcnt(0)\n\ts_barrier" :: "i"(KEXT(kt)) : "memory");                                        \
        KST((kt) + 3);                                                                                                                  \
        if ((kt) == 2) FLAG_PUT(ws + WS_FLAG + F_LN + b * 8, tag, wave == 0);             \
        if ((kt) == 6) FLAG_PUT(ws + WS_FLAG + F_EPOCH, tag - 0x9E3779B97F4A7C15ull + 1, b == 0 && wave == 0);     \
        SB();                                                                                                                           \
        KQ(kt, 3);                                                                                                                      \
    } while (0)
        KXR(0, 0, 0); KXR(0, 0, 1); KWL(0, 0, 0); KWH(0, 0, 0); KXR(0, 0, 2); KXR(0, 0, 3);
        KWL(0, 0, 1); KWH(0, 0, 1); KWL(0, 0, 2); KWH(0, 0, 2); KWL(0, 0, 3); KWH(0, 0, 3);
        SB();
        KSTEP(0); ZST(7); KSTEP(1); KSTEP(2); KSTEP(3); KSTEP(4); KSTEP(5); KSTEP(6);
        xvo = l16;
        KQ(7, 0); KQ(7, 1); KQ(7, 2);
        if (wave < 4) {
            int t2_ = threadIdx.x; asm volatile("" : "+v"(t2_));
            unsigned l7_ = t2_ & 7, c7_ = jj >= 3 ? 7 : 8, b0_ = (unsigned)(((jj + 7) & 7) * 128 + head * 4 + (wave & 3)), b7_ = 3584u + head * 4 + (wave & 3);
            u64 pg_ = (u64)((wave & 1) ? w_up : w_gate), pd_ = (u64)w_down - 1792ull * 8192, po_ = (u64)wo - 2688ull * 8192;
            asm volatile("" : "+s"(c7_), "+s"(b0_), "+s"(b7_), "+s"(pg_), "+s"(pd_), "+s"(po_));
            const unsigned lt_ = l7_ < (unsigned)jj ? l7_ : 0;
            unsigned n_ = b0_ + (13u - lt_) * lt_ * 64u; n_ = l7_ == c7_ ? b7_ : n_;
            u64 p_ = n_ < 2688u ? pd_ : po_; p_ = n_ < 1792u ? pg_ : p_; n_ = n_ < 1792u ? n_ >> 1 : n_;
            *(u64*)(lds + L_PFT + t2_ * 8) = p_ + (u64)n_ * 8192u;
        }
        SB();
        asm volatile("s_waitcnt vmcnt(%0)\n\ts_barrier" :: "i"(2 * QWD + 1) : "memory");
        SB();
#define PKCV(i) do { const f32x4 a_ = ((i) & 2) ? a16[2 * ((i) >> 2)][(i) & 3] : a16[2 * ((i) >> 2) + ((i) & 1)][0];                       \
                     const f32x4 b_ = ((i) & 2) ? a16[2 * ((i) >> 2) + 1][(i) & 3] : a16[2 * ((i) >> 2) + ((i) & 1)][1];                   \
                     pk[i] = u32x4{cvtpk(a_[0], a_[1]), cvtpk(a_[2], a_[3]), cvtpk(b_[0], b_[1]), cvtpk(b_[2], b_[3])}; PIN(pk[i]); } while (0)
#define KL(i) do { KMF(7, 3, 2 * (i)); SB(); if ((i) >= 2) QXL1(2 * (i)); SB(); KMF(7, 3, 2 * (i) + 1); SB(); PKCV(i); if ((i) >= 2) QXL1(2 * (i) + 1); SB(); } while (0)
        KL(0); KL(1); KL(2); KL(3); KL(4); KL(5); KL(6); KL(7);
        KST(10);
        QRL(0, 0); QRH(0, 0); QRL(0, 1); QRH(0, 1); QRL(0, 2); QRH(0, 2); QRL(0, 3); QRH(0, 3);
        SB(); asm volatile("s_nop 3"); SB();
#pragma unroll
        for (int i = 8; i < 16; ++i) PKCV(i);
        SB();
        KST(11); ZST_OUT();
#undef PKCV
#undef KSTEP
#undef PFQ
#undef XKEY
    }
    PH_STAMP(1);
    const char* kvg = ws + WS_KV + (size_t)head * HSTR;
    const unsigned svv = xvo + 32768u;
    u64 pft; bf16x8 ones; unsigned ak, av;
    const int NA = qbA + 1;
    {
        constexpr int LDW = NH * HD, STG = 16384;
        int tid = threadIdx.x; asm volatile("" : "+v"(tid));
        const int lane = tid & 63;
        const unsigned woff = qwo, xvo2 = xvo + 262144u;
        f32x4 acc[2][8];
#define KVST(i) asm volatile("global_store_dwordx4 %0, %1, %2 offset:%3 sc1" :: "v"(((i) & 2) ? svv : xvo), "v"(pk[i]), "s"(sb_[(i) >> 2]), "i"(((i) & 1) * 1024) : "memory")
        u64 kvf = 0;
#define QKV(kt, f) do { constexpr int g_ = q_kv(kt, f), p_ = g_ >> 2; if (g_ >= 0) {                                                       \
            if ((g_ & 3) == 0) asm volatile("s_mov_b32 m0, %0\n\ts_nop 0" :: "s"(ldsbase + (p_ == 1 ? 0 : 65536 + p_ * 16384) + wave * 4096) : "memory"); \
            asm volatile("global_load_lds_dwordx4 %0, %1 offset:%2 sc1" :: "v"(xvo), "s"(kvg + p_ * 32768 + wave * 4096), "i"((g_ & 3) * 1024) : "memory"); } } while (0)
#define KVFP (ws + WS_FLAG + ((lane & 8) ? F_TOP : F_KV + 2048 + (head * 8 + (lane & 7)) * 8))
        const bool top2 = b == 56;
        unsigned* tw = (unsigned*)(lds + 131072 + 2048);
#define QMF(a, kt, f) do { if ((kt) == 0 && (f) < 8) MFA16Z(acc[a][(f) & 7], w_, xr[0][a][0]); else MFA16(acc[a][QFM(kt, f) & 7], w_, xr[(kt) & 3][a][QFM(kt, f) >> 3]); } while (0)
        constexpr float qs = 0.08838834764831845f * 1.4426950408889634f;
        u32x4 qw_[8];
        float qm_[2][2], qsv = qs; asm volatile("" : "+s"(qsv));
#define QWI(n) (2 * ((n) >> 3) + (((n) >> 1) & 1))
#define QWW(n) (2 * (((n) >> 2) & 1) + ((n) & 1))
#define QCN(n) do { if ((n) >= 0 && (n) < 32) { _Pragma("unroll") for (int e_ = 0; e_ < 2; ++e_)                                           \
                asm volatile("v_mul_f32 %0, %1, %2" : "=v"(qm_[(n) & 1][e_]) : "s"(qsv), "v"(acc[QWI(n) & 1][(QWI(n) & 6) + (QWW(n) >> 1)][2 * (QWW(n) & 1) + e_])); } \
            if ((n) >= 1 && (n) <= 32) { qw_[QWI((n) - 1) & 7][QWW((n) - 1)] = cvtpk(qm_[((n) - 1) & 1][0], qm_[((n) - 1) & 1][1]); KEEP(qw_[QWI((n) - 1) & 7][QWW((n) - 1)]); } } while (0)
#define QCG(kt, g) do { if ((kt) == 15) QCN((g) - 10); } while (0)
#define ZP0 238
#define QPAIR(kt, f, A, B) do { const bf16x8 w_ = CAT8(ql[(f) & 7], qh[(f) & 7]); constexpr int s_ = (kt) + (((f) + 4) >> 4), m_ = ((f) + 4) & 15;  \
            QMF(0, kt, f); SB(); if (s_ < 16) QRL(s_, m_); A; QKV(kt, f); QCG(kt, 2 * (f)); SB();                                      \
            QMF(1, kt, f); SB(); if (s_ < 16) QRH(s_, m_); B; QCG(kt, 2 * (f) + 1);                                                                             \
            { constexpr int z_ = (kt) * 16 + (f) - ZP0;                    \
                                                                    \
              if (z_ == 8) { pft = *(const u64*)(lds + L_PFT + (tid & 255) * 8); }                                                       \
              if (z_ == 15) { av = ldsbase + xvo; ak = av + 65536u; PIN(av); PIN(ak); }                                                   \
              if (z_ == 16) { const unsigned o1 = (lane & 3) ? 0u : 0x3f803f80u; ones = __builtin_bit_cast(bf16x8, u32x4{o1, o1, o1, o1}); PIN(ones); }     \
              if (z_ == 14) PIN(pft); } SB(); if (((f) & 1) && s_ < 16) { __builtin_amdgcn_s_waitcnt(0xC47F); SB(); } } while (0)
        const char* xsp = xg + 3 * 262144;
#define QX(r, rb, ks, v_) asm volatile("global_load_dwordx4 %0, %1, %2 offset:%3 sc1" : "+v"(xr[r][rb][ks]) : "v"(v_), "s"(xsp), "i"((rb) * 2048 + (ks) * 1024) : "memory")
        const unsigned woff1 = woff + 1024u, woff2 = woff + 128u;
        const char* wsp = (const char*)(wqb + (size_t)QWD * 64 * LDW) - 1024;
        const unsigned m0b = ldsbase + wave * 2048;
#define QM0(r) asm volatile("s_add_u32 m0, %0, %1" :: "s"(m0b), "i"((r) * STG) : "memory", "scc")
#define QW1(v_, imm_) asm volatile("global_load_lds_dwordx4 %0, %1 offset:%2" :: "v"(v_), "s"(wsp), "i"(imm_) : "memory")
#define QST(i)
#define QSTEP(kt) do { constexpr int r_ = (kt) & 3;                                                                                     \
        QPAIR(kt, 0, if (q_xb(kt)) QX((r_ + 3) & 3, 0, 1, xvo), );                                                                      \
        QPAIR(kt, 1, , );                                                                                                               \
        QPAIR(kt, 2, if (q_xb(kt)) QX((r_ + 3) & 3, 1, 1, xvo), );                                                                      \
        QPAIR(kt, 3, if ((kt) == 12) FLAG_GET(kvf, KVFP), );                                                                            \
        if ((kt) == 0 && __builtin_expect(top2, 0)) {                                                                                                        \
            unsigned t1 = tkey(rv[0], tid * 4), t2 = 0;                                                                                 \
            _Pragma("unroll") for (int j = 1; j < 4; ++j) merge_top2(t1, t2, tkey(rv[j], tid * 4 + j), 0);                              \
            DPP_TOP2(t1, t2, 0x111, 0xf); DPP_TOP2(t1, t2, 0x112, 0xf); DPP_TOP2(t1, t2, 0x114, 0xf); DPP_TOP2(t1, t2, 0x118, 0xf);     \
            DPP_TOP2(t1, t2, 0x142, 0xa); DPP_TOP2(t1, t2, 0x143, 0xc);                                                                 \
            if (lane == 63) { tw[wave * 2] = t1; tw[wave * 2 + 1] = t2; } SB(); }                                                       \
        QPAIR(kt, 4, if ((QS1 >> (kt)) & 1) KVST(q_s0(kt) & 15), if ((QS1 >> (kt)) & 1) KEEP(pk[q_s0(kt) & 15]));                       \
        QPAIR(kt, 5, , );                                                                                                               \
        QPAIR(kt, 6, , );                                                                                                               \
        QPAIR(kt, 7, , );                                                                                                               \
        QPAIR(kt, 8, if (q_xa(kt)) QX(r_, 0, 0, xvo2), );                                                                               \
        QPAIR(kt, 9, , );                                                                                                               \
        QPAIR(kt, 10, if (q_xa(kt)) QX(r_, 1, 0, xvo2), if ((kt) <= 11) OADV(xsp, 262144));                                             \
        QPAIR(kt, 11, if ((kt) + QWD < 16) QM0((r_ + QWD) & 3), );                                                                      \
        if ((kt) < 15) {                              \
            if (QWD == 4 || (kt) == 0 || (kt) >= 13) asm volatile("s_waitcnt vmcnt(%0) lgkmcnt(0)\n\ts_barrier" :: "i"(q_wait(kt)) : "memory"); \
            else asm volatile("s_waitcnt vmcnt(%0)\n\ts_barrier" :: "i"(q_wait(kt)) : "memory");                                        \
            if ((kt) == 13) { PIN(kvf); const unsigned sh_ = (lane & 8) ? 22u : 0u; const u64 ex_ = (tag << sh_) >> sh_;     \
                if (__builtin_expect(__builtin_amdgcn_ballot_w64((kvf >> sh_) != ex_) != 0, 0)) { unsigned c_; u64 t_;                    \
                    asm volatile("s_mov_b32 %1, 0x40000\n1:\n\ts_sleep 2\n\tglobal_load_dwordx2 %0, %3, off sc1\n\ts_waitcnt vmcnt(0)\n\tv_lshrrev_b64 %2, %5, %0\n\t" \
                                 "v_cmp_ne_u64 vcc, %4, %2\n\ts_cmp_eq_u64 vcc, 0\n\ts_cbranch_scc1 2f\n\ts_sub_u32 %1, %1, 1\n\ts_cbranch_scc0 1b\n2:" \
                                 : "+v"(kvf), "=&s"(c_), "=&v"(t_) : "v"(KVFP), "v"(ex_), "v"(sh_) : "memory", "vcc", "scc"); }           \
                const unsigned r_ = __builtin_amdgcn_readlane((unsigned)kvf, 8); ri1 = r_ & 2047u; ri2 = (r_ >> 11) & 2047u; }           \
            QST((kt) + 1); SB(); }                                                                                                      \
        QPAIR(kt, 12, if ((kt) + QWD < 16) QW1(woff1, 0), );                                                                            \
        QPAIR(kt, 13, , );                                                                                                              \
        QPAIR(kt, 14, if ((kt) + QWD < 16) QW1(woff2, 1024), if ((kt) + QWD < 15) OADV(wsp, 64 * LDW * 2));                             \
        if ((kt) == q_fl()) FLAG_PUT(ws + WS_FLAG + F_KV + 2048 + (hp * 8 + jj) * 8, tag, wave == 0);     \
        QPAIR(kt, 15, , );                                                                                                              \
        if ((kt) == 0 && __builtin_expect(top2, 0)) {                                                                                                        \
            unsigned t1 = tw[0], t2 = tw[1];                                                                                            \
            _Pragma("unroll") for (int w = 1; w < 8; ++w) merge_top2(t1, t2, tw[w * 2], tw[w * 2 + 1]);                                 \
            FLAG_PUT(ws + WS_FLAG + F_TOP, (tag << 22) | (unsigned)__builtin_amdgcn_readfirstlane((~t1 & 2047u) | (~t2 & 2047u) << 11), wave == 0); \
            } \
        SB();                                                                                                                           \
    } while (0)
        asm volatile("s_waitcnt vmcnt(12)" : "+v"(rv) :: "memory");
        SB();
        QST(0);
        KST(12);
        QSTEP(0); QSTEP(1); QSTEP(2); QSTEP(3); QSTEP(4); QSTEP(5); QSTEP(6); QSTEP(7);
        QSTEP(8); QSTEP(9); QSTEP(10); QSTEP(11); QSTEP(12); QSTEP(13); QSTEP(14); QSTEP(15);
        QCN(22); QCN(23); QCN(24); QCN(25); QCN(26); QCN(27); QST(16); SB(); QCN(28); QCN(29); QCN(30); QCN(31); QCN(32);
#pragma unroll
        for (int i = 0; i < 8; ++i) { qf[i] = __builtin_bit_cast(bf16x8, qw_[i]); PIN(qf[i]); }
#undef QSTEP
    }
    PH_STAMP(2);
    const int kz = xcd & 1, kp = xcd >> 1, nt = kz * 4 + (idx >> 3), tile = jj * 8 + nt;
#define K64(s) ((head * 2) ^ (s))
#define OWOFF() ({ unsigned l_ = threadIdx.x & 63; asm volatile("" : "+v"(l_)); (unsigned)(((l_ >> 2) & 7) * DM + 32 * (l_ >> 5) + 8 * ((l_ & 3) ^ ((l_ >> 4) & 1))) * 2u; })
    const int n0 = (nt ^ 2) * 128, dn1 = (((nt ^ 3) - (nt ^ 2)) * 128 + 64) * 2, ntc = nt ^ 2 ^ (kp >> 1);
    const u16* wob = wo + (size_t)(8 * wave) * DM + n0;
    u64 cxf = tag + 1;
#define CFB (ws + WS_FLAG + F_CTX)
#define CFO() ({ unsigned l_ = threadIdx.x & 15, s0_ = (unsigned)((qb * 4 + qw) * 32 + head), s1_ = (unsigned)((qbB * 4 + 3 - (wave & 3)) * 32 + (head ^ 8)); \
                 asm volatile("" : "+v"(l_), "+s"(s0_), "+s"(s1_)); (l_ < 8 ? l_ ^ s0_ : s1_) * CFS; })
    u32x4 pc[8];
#define EAR_(v_, b_, pol_) "s_add_u32 m0, m0, 0x400\n\ts_nop 0\n\tglobal_load_lds_dwordx4 " v_ ", " b_ pol_ "\n\t"
#define EALD(h_) do { unsigned l_ = threadIdx.x & 63; asm volatile("" : "+v"(l_)); const unsigned lx_ = ((l_ >> 3) * DM + 8 * ((l_ & 7) ^ (l_ >> 3))) * 2u; \
        const int rb_ = ((h_) ? qbB * 8 + qw * 2 : qbA * 8 + (wave & 3) * 2) + (kp & 1);                                                  \
        asm volatile("s_mov_b32 m0, %5\n\t" EAR_("%0", "%3", "") EAR_("%1", "%3", "") EAR_("%2", "%4", " sc1") "global_load_lds_dwordx4 %2, %4 offset:1024 sc1" \
                     :: "v"(lx_), "v"(lx_ + 8 * DM * 2), "v"(l_ * 16u), "s"(x + (size_t)rb_ * 16 * DM + ntc * 128 + (kp >> 1) * 64),              \
                        "s"(ws + WS_XF + (size_t)(2 * ntc + (kp >> 1)) * 262144 + rb_ * 2048),                                             \
                        "s"(ldsbase + 131072 - 1024 + (wave & 3) * 8192 + (h_) * 4096) : "memory", "scc"); } while (0)
    const unsigned i1 = (((kz ? F1S : F1F) >> (4 * jj)) & 15u) * 16u + (kp * 4 + (idx >> 3));
    const unsigned i2 = (((kz ? F2S : F2F) >> (4 * jj)) & 15u) * 16u + (kp * 4 + (idx >> 3));
    const unsigned nf1 = i1 / 112u, nf2 = i2 / 112u;
    const unsigned k1 = i1 / 14u;
    unsigned ypub = 1;
    {
        int tid = threadIdx.x; asm volatile("" : "+v"(tid));
        const int lane = tid & 63;
        const unsigned l16 = (unsigned)lane * 16u;
#define EX(x) __builtin_amdgcn_exp2f(x)
#define TBAR() do { SB(); __builtin_amdgcn_s_waitcnt(0x0070); asm volatile("s_barrier" ::: "memory"); } while (0)
#define BRQ(t, r) do { constexpr int g_ = (r) >> 2; const int tt_ = (t) + (g_ == 2 ? 1 : 2);                                               \
            const unsigned o_ = (unsigned)((wave & 3) * 4096 + (g_ == 1 || g_ == 2 ? 16384 : 0));                                          \
            if (((r) & 3) == 0) GLDS_C(l16, kvg + (size_t)tt_ * 65536 + (g_ >= 2 ? 32768 : 0) + o_, ldsbase + (g_ >= 2 ? 0 : 65536) + (tt_ & 1) * 32768 + o_, 0); \
            else asm volatile("global_load_lds_dwordx4 %0, %1 offset:%2 sc1" :: "v"(l16), "s"(kvg + (size_t)tt_ * 65536 + (g_ >= 2 ? 32768 : 0) + o_), "i"(((r) & 3) * 1024) : "memory"); } while (0)
#define BRQ4(t, g) do { BRQ(t, 4 * (g)); BRQ(t, 4 * (g) + 1); BRQ(t, 4 * (g) + 2); BRQ(t, 4 * (g) + 3); } while (0)
#define BRQ2C(t, r) do { constexpr int g_ = (r) >> 2; const int tt_ = (t) + (g_ == 2 ? 1 : 2);                                             \
            const unsigned o_ = (unsigned)((wave & 3) * 4096 + (g_ == 1 || g_ == 2 ? 16384 : 0));                                          \
            asm volatile("s_cmp_eq_u32 %5, 0\n\ts_cbranch_scc1 1f\n\ts_mov_b32 m0, %2\n\ts_nop 0\n\tglobal_load_lds_dwordx4 %0, %1 offset:%3 sc1\n\t"  \
                         "global_load_lds_dwordx4 %0, %1 offset:%4 sc1\n1:"                                                               \
                         :: "v"(l16), "s"(kvg + (size_t)tt_ * 65536 + (g_ >= 2 ? 32768 : 0) + o_), "s"(ldsbase + (g_ >= 2 ? 0 : 65536) + (tt_ & 1) * 32768 + o_), \
                            "i"(((r) & 3) * 1024), "i"(((r) & 3) * 1024 + 1024), "s"(isb) : "memory", "scc"); } while (0)
        const unsigned isb = wave >> 2;
        u64 kf2 = tag;
        const char* kfb = ws + WS_FLAG + F_KV + head * 64;
#define KCHK() do { PIN(kf2); if (__builtin_expect((__builtin_amdgcn_ballot_w64(kf2 != tag) | (u64)((33u >> t) & 1u)) != 0, 0)) { \
            unsigned c_, a_;                                                                                                              \
            FLAG_PUTZ(ws + WS_FLAG + F_KV + (hp * 8 + jj) * 8, tag, (unsigned)(wave ^ 4) | (unsigned)t);                                  \
            asm volatile("s_cmp_lt_u32 %6, 5\n\ts_cbranch_scc1 3f\n\tv_lshrrev_b32 %2, 1, %5\n\tv_and_b32 %2, 56, %2\n\ts_mov_b32 %1, 0x40000\n" \
                         "1:\n\tglobal_load_dwordx2 %0, %2, %3 sc1\n\ts_cmp_eq_u32 %6, 5\n\ts_cbranch_scc1 3f\n\ts_waitcnt vmcnt(0)\n\tv_cmp_ne_u64 vcc, %4, %0\n\t" \
                         "s_cmp_eq_u64 vcc, 0\n\ts_cbranch_scc1 3f\n\ts_sleep 2\n\ts_sub_u32 %1, %1, 1\n\ts_cbranch_scc0 1b\n"           \
                         "3:" : "+v"(kf2), "=&s"(c_), "=&v"(a_) : "s"(kfb), "s"(tag), "v"(l16), "s"(t) : "memory", "vcc", "scc"); } SB(); } while (0)
#define PFW1(i_) do { unsigned a_; const u64 p_ = ((u64)(unsigned)__builtin_amdgcn_readlane((int)(pft >> 32), i_) << 32) | (unsigned)__builtin_amdgcn_readlane((int)pft, i_); \
            asm volatile("v_lshlrev_b32 %0, 3, %2\n\ts_mov_b32 m0, %3\n\ts_nop 2\n\tglobal_load_lds_dword %0, %1"                        \
                         : "=&v"(a_) : "s"(p_), "v"(l16), "s"(ldsbase + 131072 + (wave & 3) * 8192) : "memory"); } while (0)
#define ONCE2(n_) int CAT(n_once, __LINE__) = (int)(n_); asm volatile("" : "+s"(CAT(n_once, __LINE__))); _Pragma("unroll 1") for (int z_ = 0; z_ < CAT(n_once, __LINE__); ++z_)
#define AST(i)
#define XST(i)
#define TS0()
#define TS1()
        AST(0);
        asm volatile("s_bitcmp0_b32 %0, 2\n\ts_cbranch_scc1 1f\n\ts_setprio 1\n1:" :: "s"(wave) : "scc");
        {
            f32x4 o[8][2], ls[2], ng[2], S[2][4];
            bf16x8 pf[2][2], fr[16];
            float e[16];
            constexpr bool ZC = false;
            constexpr bool HOT = false;
            const unsigned lb16 = ldsbase + l16;
            int t = 0;
#define KOFF(f) ((((f) & 3) * 2 + (((f) >> 2) & 1)) * 1024)
#define RD(kb, n) do { constexpr int k_ = ((kb) + ((n) >> 4)) & 3, n_ = (n) & 15;                                                          \
            fr[n_] = n_ < 8 ? lds_rd(av + (unsigned)(((k_ + 3) & 3) * 8192 + n_ * 1024)) : lds_rd(ak + (unsigned)(((k_ + 1) & 3) * 8192 + KOFF(n_ & 7))); } while (0)
#define MX(p, n, a) do { if (ZC) MFA16Y(o[n][a], fr[n], pf[(p) ^ 1][a]); else MFA16(o[n][a], fr[n], pf[(p) ^ 1][a]); } while (0)
#define MY(p, n, h) do { constexpr int a_ = ((n) >> 1) & 1, t_ = 2 * ((n) & 1) + (h);                                                      \
                         if (t_) MFA16(S[(p) ^ 1][(((n) >> 1) & 2) + a_], fr[((n) & 12) + t_], qf[2 * t_ + a_]);                           \
                         else MFA16C(S[(p) ^ 1][(((n) >> 1) & 2) + a_], fr[(n) & 12], qf[a_], ng[a_]); } while (0)
#define GX(p, n, F1, F2) do { MX(p, n, 0); SB(); F1; SB(); MX(p, n, 1); SB(); F2; SB(); } while (0)
#define GY(p, n, F1, F2) do { MY(p, n, 0); SB(); F1; SB(); MY(p, n, 1); SB(); F2; SB(); } while (0)
#define ML(p, a) do { if (ZC) MFA16Y(ls[a], ones, pf[(p) ^ 1][a]); else MFA16(ls[a], ones, pf[(p) ^ 1][a]); } while (0)
#define GL(p, F1, F2) do { ML(p, 0); SB(); F1; SB(); ML(p, 1); SB(); F2; SB(); } while (0)
#define SV(p, i) S[p][(((i) >> 1) & 2) + ((i) >> 3)][(i) & 3]
#define E1(p, i) do { e[i] = EX(SV(p, i)); if (HOT) KEEP(e[i]); else PIN(e[i]); } while (0)
#define C1(p, j) do { u32x4 w_ = __builtin_bit_cast(u32x4, pf[p][(j) >> 2]); w_[(j) & 3] = cvtpk(e[2 * (j)], e[2 * (j) + 1]);               \
                      pf[p][(j) >> 2] = __builtin_bit_cast(bf16x8, w_); if (HOT) KEEP(w_[(j) & 3]); else PIN(pf[p][(j) >> 2]); } while (0)
#define DQ(kb, g) do { if (ISB_ && (kb) == 3) { if ((g) == 0) KCHK(); BRQ(t, g); } if (ISB_ && (kb) == 0 && (g) == 1) PFW1(t); } while (0)
#define W4() do { SB(); __builtin_amdgcn_s_waitcnt(0xC37F); } while (0)
#define US(kb, ISB) do { constexpr bool HOT = true, ZC = (ISB) == 3, ISB_ = (ISB) == 1; constexpr int p_ = (kb) & 1; constexpr bool B_ = (kb) == 2;                    \
            GX(p_, 0, RD(kb, 7); if ((kb) == 3) { ak ^= 32768u; PIN(ak); }, E1(p_, 0); DQ(kb, 0));                                        \
            GX(p_, 1, RD(kb, 8); if ((kb) == 0) { av ^= 32768u; PIN(av); }, E1(p_, 1); DQ(kb, 1));                                        \
            GL(p_, E1(p_, 2), E1(p_, 3));                                                                                                 \
            GX(p_, 2, RD(kb, 9); C1(p_, 0), E1(p_, 4); DQ(kb, 2));                                                                        \
            GX(p_, 3, RD(kb, 10); W4(), E1(p_, 5); DQ(kb, 3));                                                                            \
            GX(p_, 4, RD(kb, 11); C1(p_, 1), E1(p_, 6); DQ(kb, 4));                                                                       \
            GX(p_, 5, RD(kb, 12); C1(p_, 2), E1(p_, 7); DQ(kb, 5));                                                                       \
            GX(p_, 6, RD(kb, 13); C1(p_, 3), E1(p_, 8); DQ(kb, 6));                                                                       \
            GX(p_, 7, RD(kb, 14); W4(), E1(p_, 9); DQ(kb, 7));                                                                            \
            GY(p_, 8, RD(kb, 15), E1(p_, 10); DQ(kb, 8));                                           \
            GY(p_, 9, if (!B_) RD(kb, 16); C1(p_, 4), E1(p_, 11); DQ(kb, 9));                                                             \
            GY(p_, 10, if (B_) { TS0(); if (ISB_) { __builtin_amdgcn_s_waitcnt(0x0071); asm volatile("s_barrier" ::: "memory"); } else TBAR(); \
                                 TS1(); SB(); RD(kb, 16); }                                                                               \
                       RD(kb, 17); C1(p_, 5), E1(p_, 12); DQ(kb, 10));                                                                    \
            GY(p_, 11, RD(kb, 18); if (!B_) W4(), E1(p_, 13); DQ(kb, 11));                                                                \
            GY(p_, 12, RD(kb, 19), E1(p_, 14); DQ(kb, 12));                                                \
            GY(p_, 13, RD(kb, 20); C1(p_, 6), E1(p_, 15); DQ(kb, 13));                                                                    \
            GY(p_, 14, RD(kb, 21); C1(p_, 7), DQ(kb, 14));                                 \
            GY(p_, 15, RD(kb, 22), DQ(kb, 15); W4());                                                                                     \
        } while (0)
            asm volatile("s_waitcnt vmcnt(%0) lgkmcnt(0)\n\ts_barrier" :: "i"(4 + (QS1 >> 15)) : "memory");
            AST(1);
#pragma unroll
            for (int s_ = 0; s_ < 8; ++s_) fr[s_] = lds_rd(ak + (unsigned)KOFF(s_));
            BRQ2C(-1, 12); BRQ2C(-1, 14);
            SB();
#define M0_(n, a) do { if ((n) & 3) MFA16(S[0][(((n) >> 1) & 2) + (a)], fr[n], qf[2 * ((n) & 3) + (a)]); else MFA16Z(S[0][(((n) >> 1) & 2) + (a)], fr[n], qf[a]); } while (0)
#define G0_(n) do { M0_(n, 0); SB(); if (Q_NST + (n) < 16) KVST((Q_NST + (n)) & 15); SB(); M0_(n, 1); SB(); RD(0, 8 + (n));               \
                    if (Q_NST + (n) < 16) KEEP(pk[(Q_NST + (n)) & 15]); SB(); } while (0)
            G0_(0); G0_(1); G0_(2); G0_(3); G0_(4); G0_(5); G0_(6); G0_(7);
            MF_DONE();
            { ONCE2(isb * min(jj, 1)) { PFW1(0); } }
            {
                PIN(S[0][0]); PIN(S[0][1]); PIN(S[0][2]); PIN(S[0][3]);
                if (__builtin_expect((qb | qw) == 0, 0)) {
#pragma unroll
                    for (int r = 0; r < 4; ++r) { S[0][2][r] = -__builtin_inff(); if (4 * (lane >> 4) + r > (lane & 15)) S[0][0][r] = S[0][3][r] = -__builtin_inff(); } }
#pragma unroll
                for (int a = 0; a < 2; ++a) {
                    float mx_;
#define MX3(d, a_, b_, c_) asm("v_max3_f32 %0, %1, %2, %3" : "=v"(d) : "v"(a_), "v"(b_), "v"(c_))
                    MX3(mx_, SV(0, 8 * a), SV(0, 8 * a + 1), SV(0, 8 * a + 2)); MX3(mx_, mx_, SV(0, 8 * a + 3), SV(0, 8 * a + 4)); MX3(mx_, mx_, SV(0, 8 * a + 5), SV(0, 8 * a + 6));
                    { float m7_ = SV(0, 8 * a + 7), n_ = -1e30f; MX3(mx_, mx_, m7_, n_); }
                    { auto r1 = __builtin_amdgcn_permlane16_swap(__float_as_uint(mx_), __float_as_uint(mx_), false, false); MX3(mx_, r1[0], r1[1], r1[1]);
                      auto r2 = __builtin_amdgcn_permlane32_swap(__float_as_uint(mx_), __float_as_uint(mx_), false, false); MX3(mx_, r2[0], r2[1], r2[1]); }
                    mx_ = -POFF - mx_;
                    ng[a] = f32x4{mx_, mx_, mx_, mx_}; PIN(ng[a]);
                }
#pragma unroll
                for (int i = 0; i < 16; ++i) { float v_ = SV(0, i) + ng[i >> 3][0]; PIN(v_); SV(0, i) = v_; }
                PIN(S[0][0]); PIN(S[0][1]); PIN(S[0][2]); PIN(S[0][3]);
            }
            SB();
            GY(0, 8, E1(0, 0), RD(0, 16); E1(0, 1)); GY(0, 9, E1(0, 2); C1(0, 0), RD(0, 17); E1(0, 3));
            GY(0, 10, E1(0, 4); C1(0, 1), RD(0, 18); E1(0, 5)); GY(0, 11, E1(0, 6); C1(0, 2), RD(0, 19); E1(0, 7));
            GY(0, 12, E1(0, 8); C1(0, 3), RD(0, 20); E1(0, 9)); GY(0, 13, E1(0, 10); C1(0, 4), RD(0, 21); E1(0, 11));
            GY(0, 14, E1(0, 12); C1(0, 5), RD(0, 22); E1(0, 13)); GY(0, 15, E1(0, 14); C1(0, 6), E1(0, 15); C1(0, 7));
#define SCOPY() do { MF_DONE(); _Pragma("unroll") for (int i = 0; i < 4; ++i) { S[1][i] = S[0][i]; PIN(S[1][i]); } } while (0)
            if (__builtin_expect((qb | qw) == 0, 0)) { SCOPY(); pf[0][0] = pf[0][1] = bf16x8{}; PIN(pf[0][0]); PIN(pf[0][1]); }
            W4(); asm volatile("s_nop 1"); SB();
            AST(2);
            const int nu = 4 * qb + qw - 2;
#define MFA16Y(c, a, b) asm volatile("v_mfma_f32_16x16x32_bf16 %0, %1, %2, 0" : "+v"(c) : "v"(a), "v"(b))
#pragma unroll
            for (int d = 0; d < 18; ++d) { if (d < 16) asm volatile("" : "=v"(o[d >> 1][d & 1])); else asm volatile("" : "=v"(ls[d & 1])); }
            if (__builtin_expect(nu < 0, 0)) { const bf16x8 z_ = bf16x8{}; asm volatile("s_nop 1" :: "v"(z_));
#pragma unroll
                for (int d = 0; d < 18; ++d) { if (d < 16) MFA16Y(o[d >> 1][d & 1], z_, z_); else MFA16Y(ls[d & 1], z_, z_); } }
            { int n1_ = nu; asm volatile("" : "+s"(n1_)); ONCE2(min(n1_ + 2, 2) >> 1) { US(1, 3); } }
            {
                const int nl = nu >> 2, nB = isb ? nl : 0;
#pragma unroll 1
                while (t < nB) { US(2, 1); US(3, 1); ++t; US(0, 1); US(1, 1); }
#pragma unroll 1
                while (t < nl) { US(2, 0); US(3, 0); ++t; US(0, 0); US(1, 0); }
            }
            AST(3);
            SST(1);
#define PCOPY() do { SCOPY(); pf[0][0] = pf[1][0]; pf[0][1] = pf[1][1]; PIN(pf[0][0]); PIN(pf[0][1]); SB(); asm volatile("s_nop 1"); } while (0)
            {
                const int nr = nu < 0 ? 0 : nu & 3, ne = nu < -1 ? 0 : nu + 1;
                if (__builtin_expect(nr >= 1, 1)) {
                    US(2, 2);
                    if (__builtin_expect(nr >= 2, 1)) {
                        if (isb) { KCHK(); asm volatile("s_setprio 0"); BRQ4(t, 0); BRQ4(t, 1); BRQ4(t, 2); BRQ4(t, 3); asm volatile("s_setprio 1"); }
                        US(3, 2);
                        if (__builtin_expect(nr == 3, 0)) US(0, 2);
                    }
                }
                int np = ne; asm volatile("" : "+s"(np));
                if (__builtin_expect(np & 1, 0)) PCOPY();
                KEEP(ng[0]); KEEP(ng[1]);
                t = qb;
            }
#define RDV(n) fr[n] = lds_rd(avf + (unsigned)((n) * 1024))
#define M1(i) do { e[i] = __builtin_amdgcn_fmed3f(e[i], 0.f, rmp[(i) & 3]); PIN(e[i]); } while (0)
#define E2(i) E1(1, i)
#define CST(i) asm volatile("global_store_dwordx4 %0, %1, %2 offset:%3 sc1\n\ts_nop 1" :: "v"(l16), "v"(pc[i]), "s"(cb + ((i) >> 2) * 262144), "i"((((i) >> 1) & 1) * 1024 + ((i) & 1) * 2048) : "memory")
#define NRM(ISB, i) do { float y_[8];                                              \
                _Pragma("unroll") for (int z_ = 0; z_ < 8; ++z_) { y_[z_] = o[((i) & 6) + (z_ >> 2)][(i) & 1][z_ & 3] * inv[(i) & 1]; PIN(y_[z_]); } \
                pc[i] = u32x4{cvtpk(y_[0], y_[1]), cvtpk(y_[2], y_[3]), cvtpk(y_[4], y_[5]), cvtpk(y_[6], y_[7])};                        \
                PIN(pc[i]); if (!(ISB)) CST(i); SB(); } while (0)
#define XLD(b_) do { _Pragma("unroll") for (int n = 0; n < 8; ++n) asm volatile("global_load_dwordx4 %0, %1, %2 offset:%3 sc1" : "=v"(fr[n]) : "v"(l16 + (n >> 2) * 4096u), "s"(b_), "i"((n & 3) * 1024) : "memory"); \
                asm volatile("s_waitcnt vmcnt(0)" : "+v"(fr[0]), "+v"(fr[1]), "+v"(fr[2]), "+v"(fr[3]), "+v"(fr[4]), "+v"(fr[5]), "+v"(fr[6]), "+v"(fr[7]) :: "memory"); } while (0)
#define MFB(a_, b_, c_) __builtin_amdgcn_mfma_f32_16x16x32_bf16(a_, b_, c_, 0, 0, 0)
#define EXACT() do { __builtin_amdgcn_s_waitcnt(0xC07F); MF_DONE();                                                                                                           \
                float mr_[2] = {-1e30f, -1e30f}; const int nu_ = 4 * qb + qw2;                                                            \
                ls[0] = ls[1] = f32x4{}; _Pragma("unroll") for (int d = 0; d < 16; ++d) o[d >> 1][d & 1] = f32x4{};                       \
                _Pragma("unroll 1") for (int u = 0; u <= nu_; ++u) {                                                                      \
                    const char* kb_ = kvg + (size_t)(u >> 2) * 65536 + (u & 3) * 8192;                                                    \
                    XLD(kb_);                                                                                                             \
                    _Pragma("unroll") for (int i = 0; i < 4; ++i) { f32x4 s_ = f32x4{};                                                   \
                        _Pragma("unroll") for (int d = 0; d < 4; ++d) s_ = MFB(fr[2 * d + (i >> 1)], qf[2 * d + (i & 1)], s_);            \
                        S[1][i] = s_; }                                                                                                   \
                    if (u == nu_) { _Pragma("unroll") for (int r = 0; r < 4; ++r) { S[1][2][r] = -__builtin_inff();                       \
                        if (4 * (ln2 >> 4) + r > (ln2 & 15)) S[1][0][r] = S[1][3][r] = -__builtin_inff(); } }                             \
                    XLD(kb_ + 32768);                                                                                                     \
                    _Pragma("unroll") for (int a = 0; a < 2; ++a) {                                                                       \
                        float mx_ = SV(1, 8 * a); _Pragma("unroll") for (int r = 1; r < 8; ++r) mx_ = fmaxf(mx_, SV(1, 8 * a + r));       \
                        mx_ = fmaxf(quad_max(mx_), mr_[a]); const float al_ = EX(mr_[a] - mx_); mr_[a] = mx_;                             \
                        u32x4 w_; _Pragma("unroll") for (int j = 0; j < 4; ++j) w_[j] = cvtpk(EX(SV(1, 8 * a + 2 * j) - mx_), EX(SV(1, 8 * a + 2 * j + 1) - mx_)); \
                        const bf16x8 p_ = __builtin_bit_cast(bf16x8, w_);                                                                 \
                        ls[a] = MFB(ones, p_, ls[a] * al_);                                                                               \
                        _Pragma("unroll") for (int d = 0; d < 8; ++d) o[d][a] = MFB(fr[d], p_, o[d][a] * al_); } }                        \
                pf[1][0] = pf[1][1] = bf16x8{}; _Pragma("unroll") for (int n = 4; n < 8; ++n) fr[n] = bf16x8{};     \
                PIN(pf[1][0]); PIN(pf[1][1]); MF_DONE(); } while (0)
#define TAIL(ISB) do { float rmp[4];                                                                                         \
                XST(0);                                                                                                                   \
                MX(1, 0, 0); SB();                                                                                                        \
                int ln2 = lane, qw2 = qw; asm volatile("" : "+v"(ln2), "+s"(qw2));   \
                fr[7] = lds_rd(av + (unsigned)((qw2 + 3) & 3) * 8192u + 7168u); SB();                                                     \
                MX(1, 0, 1); SB(); E2(0); SB();                                                                                           \
                GX(1, 1, E2(1), E2(2));                                                                                                   \
                GL(1, E2(3), E2(8));                                                                                                      \
                float fl_ = (float)((ln2 & 15) - 4 * (ln2 >> 4)); PIN(fl_);                                                               \
                unsigned avf = lb16 + (unsigned)(t & 1) * 32768u + (unsigned)qw2 * 8192u; PIN(avf); SB();                                 \
                GX(1, 2, E2(9), E2(10));                                                                             \
                GX(1, 3, E2(11), _Pragma("unroll") for (int r = 0; r < 4; ++r) { rmp[r] = (fl_ + (0.5f - r)) * 1.2089258196146292e24f; PIN(rmp[r]); }); \
                if (__builtin_expect(qw2 == 2, 0)) {                               \
                    if (ISB) TBAR(); else LGKM0_BAR(); }                                                                                  \
                SB();                                                                                                                     \
                RDV(0); RDV(1); RDV(2); RDV(3); SB();                                                                                     \
                GX(1, 4, E2(12), M1(0); M1(1));                                                                                           \
                GX(1, 5, RDV(4); E2(13), M1(2); M1(3));                                                                                   \
                GX(1, 6, RDV(5); E2(14), C1(1, 0); C1(1, 1));                                                                             \
                GX(1, 7, RDV(6); E2(15), C1(1, 4); C1(1, 5));                                                                             \
                RDV(7); M1(12); M1(13); M1(14); M1(15); e[4] = e[5] = e[6] = e[7] = 0.f; C1(1, 2); C1(1, 3); SB();                        \
                C1(1, 6); C1(1, 7); SB();                                                                                                 \
                XST(1);                                                                                                                   \
                if (__builtin_expect(qw2 < 2, 0)) { if (ISB) TBAR(); else LGKM0_BAR(); }                                                  \
                SB();                                                                                                                     \
                XST(2); XST(3);                                                                                                           \
                float inv[2];                                                                                                \
                int hd2 = head; asm volatile("" : "+s"(hd2)); const char* cb = ws + WS_CTX + (unsigned)(hd2 * 524288 + (qb * 8 + qw2 * 2) * 2048); SB(); \
                GL(0, , ); GX(0, 0, , ); GX(0, 1, , ); GX(0, 2, , );                                                                      \
                PIN(ls[0]); PIN(ls[1]);                                                                                                   \
                const bool c_ = __builtin_amdgcn_ballot_w64(!(ls[0][0] + ls[1][0] <= LTHR)) != 0; SB();                                   \
                GX(0, 3, , );                                                                                                             \
                if (__builtin_expect(c_, 0)) EXACT();                                                                                     \
                SB();                                                                                                                     \
                inv[0] = __builtin_amdgcn_rcpf(ls[0][0]); inv[1] = __builtin_amdgcn_rcpf(ls[1][0]); PIN(inv[0]); PIN(inv[1]); SB();       \
                GX(0, 4, , asm volatile("s_nop 0"));                                                                                      \
                NRM(ISB, 0); GX(0, 5, , ); NRM(ISB, 1); GX(0, 6, , ); NRM(ISB, 2); GX(0, 7, , ); NRM(ISB, 3);                             \
                XST(4);                                                                                                                   \
                NRM(ISB, 4); NRM(ISB, 5); NRM(ISB, 6); NRM(ISB, 7);                                                                       \
                if (ISB) { asm volatile("s_setprio 0"); asm volatile("" : "+s"(t)); KCHK(); if (t + 2 < NA) { BRQ4(t, 0); BRQ4(t, 1); BRQ4(t, 3); } BRQ4(t, 2); \
                           CST(0); CST(1); CST(2); CST(3); CST(4); CST(5); CST(6); CST(7); }                                              \
                XST(5);                                                                                                                   \
            } while (0)
#define SPIN() do { PIN(S[1][0]); PIN(S[1][1]); PIN(S[1][2]); PIN(S[1][3]); } while (0)
            { ONCE2(isb ^ 1) { SPIN(); TAIL(0); } }
            { ONCE2(isb) { SPIN(); TAIL(1); } }
            SST(2);
            AST(4);
        }
        if (wave >= 4) {
            int t = qbB + 1;
            const int wv = wave & 3;
            XST(6);
#define WOLD(h_) do { const unsigned ow_ = OWOFF(); const char* wp_ = (const char*)(wo + (size_t)(K64(2 * (h_)) * 64 + 16 * wv) * DM + n0);  \
                _Pragma("unroll") for (int i = 0; i < 8; ++i)                                                                             \
                    GLDS_S(ow_, wp_ + (size_t)((i >> 2) * 64 + 8 * ((i >> 1) & 1)) * (DM * 2) + (i & 1) * dn1,                             \
                           ldsbase + 65536 + (h_) * 32768 + (i >> 2) * 16384 + (2 * wv + ((i >> 1) & 1)) * 2048 + (i & 1) * 1024, 0); } while (0)
            {
                const u32x4 em = u32x4{~0u, ~0u, ~0u, ~0u};
                const char* eb = ws + WS_PART + (size_t)(tile * 8 + wv) * 24576 + kp * 6144;
                asm volatile("global_store_dwordx4 %0, %1, %2 sc1\n\tglobal_store_dwordx4 %0, %1, %2 offset:1024 sc1\n\tglobal_store_dwordx4 %0, %1, %3 sc1\n\t"
                             "global_store_dwordx4 %0, %1, %3 offset:1024 sc1\n\ts_waitcnt vmcnt(12) lgkmcnt(0)"
                             :: "v"(l16), "v"(em), "s"(eb), "s"(eb + 4 * 24576) : "memory"); }
            if (jj == 7) { WOLD(1); EALD(0); EALD(1); }
            asm volatile("s_barrier" ::: "memory");
#define BATCH() do { KCHK(); if (t + 2 < NA) { BRQ4(t, 0); BRQ4(t, 1); BRQ4(t, 3); } BRQ4(t, 2); } while (0)
#define BSTEP(N) do { asm volatile("s_waitcnt vmcnt(%0)\n\ts_barrier" :: "i"(N) : "memory"); ++t; } while (0)
            u32x4 fw[16], wd[16], xq[4], gp[8];
            f32x4 fa[8];
            int ln_ = lane; asm volatile("" : "+v"(ln_));
#define FMM(w_, i0, a_, z_) do { asm volatile("s_setprio 1");                                                              \
                    _Pragma("unroll") for (int i = 0; i < 8; ++i) PIN(w_[(i0) + i]);                                                      \
                    PIN(a_);                                                                                                              \
                    _Pragma("unroll") for (int j = 0; j < 8; ++j) { u32x4 f_;                                                             \
                        _Pragma("unroll") for (int d = 0; d < 4; ++d) f_[d] = __builtin_amdgcn_perm(w_[(i0) + 2 * d + 1][j >> 1], w_[(i0) + 2 * d][j >> 1], (j & 1) ? 0x07060302u : 0x05040100u); \
                        fa[j] = __builtin_amdgcn_mfma_f32_16x16x32_bf16(__builtin_bit_cast(bf16x8, a_), __builtin_bit_cast(bf16x8, f_), (z_) ? f32x4{} : fa[j], 0, 0, 0); } \
                    _Pragma("unroll") for (int j = 0; j < 8; ++j) PIN(fa[j]);                                                             \
                    asm volatile("s_setprio 0"); } while (0)
#define FOUT() f32x4{ln_ >= 32 ? fa[4][0] : fa[0][0], ln_ >= 32 ? fa[5][0] : fa[1][0], ln_ >= 32 ? fa[6][0] : fa[2][0], ln_ >= 32 ? fa[7][0] : fa[3][0]}
            const int c1 = 2 * (i1 - 14 * k1) + (wv & 1);
            const unsigned r4o = (unsigned)(ln_ & 15) * 16u;
            const char* cxp = ws + WS_FLAG + F_CTX + ((qbB * 4 + qw) * 32 + head) * CFS;
            if (nf1 == 0) {
#pragma unroll 1
                while (t < F1T) { BATCH(); BSTEP(0); }
                const unsigned evo = (unsigned)((16 * (ln_ >> 5) + 4 * ((ln_ >> 4) & 1)) * FF * 2) + r4o;
                const u16* fwp = (wv < 2 ? w_gate : w_up) + (size_t)(k1 * 128) * FF + c1 * 128;
#define FLD(h_, c_) do { _Pragma("unroll") for (int i = 0; i < 8; ++i)                                                                    \
                    asm volatile("global_load_dwordx4 %0, %1, %2" : c_(fw[((h_) & 1) * 8 + i]) : "v"(evo), "s"(fwp + (size_t)(32 * (h_) + 8 * (i >> 2) + (i & 3)) * FF) : "memory"); } while (0)
                BATCH();
                { const int xr_ = (ln_ & 4) ? ri2 : ri1;
#pragma unroll
                  for (int i = 0; i < 4; ++i)
                      asm volatile("global_load_dwordx4 %0, %1, %2 offset:%3 sc1" : "=v"(xq[i]) : "v"((unsigned)((xr_ >> 4) * 2048 + (xr_ & 15) * 16 + (ln_ >> 4) * 256)),
                                   "s"(ws + WS_XF + (size_t)(k1 * 2 + (i >> 1)) * 262144), "i"((i & 1) * 1024) : "memory"); }
                FLD(0, "=v"); FLD(1, "=v"); BSTEP(20);
                BATCH(); asm volatile("s_waitcnt vmcnt(16)" ::: "memory");
                FLAG_PUT(cxp, tag, 1);
                FMM(fw, 0, xq[0], 1); FLD(2, "=v"); FMM(fw, 8, xq[1], 0); FLD(3, "=v"); BSTEP(17);
                BATCH(); asm volatile("s_waitcnt vmcnt(16)" ::: "memory");
                FMM(fw, 0, xq[2], 0); FMM(fw, 8, xq[3], 0); const f32x4 sum = FOUT();
                ST16_SC1((float*)(ws + WS_GP) + ((size_t)k1 * 4 + (wv >> 1) * 2 + ((ln_ >> 4) & 1)) * FF + c1 * 128 + (ln_ & 15) * 8 + (ln_ >> 5) * 4, sum);
                PFW1(7); BSTEP(2);
            }
            const char* pnd = nf1 ? cxp : ws + WS_FLAG + F_GU + (c1 * 16 + (wv >> 1) * 8 + k1) * 8;
            const int tp = nf1 ? t + 1 : t;
#define PUB(N) do { if (t == tp) { asm volatile("s_waitcnt vmcnt(%0)" :: "i"(N) : "memory"); FLAG_PUT(pnd, tag, 1); } } while (0)
            const int f0 = (nf2 ? 0 : i2 >> 3) * 256 + wv * 64;
            const char* gfp = ws + WS_FLAG + F_GU + (f0 >> 7) * 128 + (ln_ & 15) * 8;
            u64 gf = tag + 1;
            char* vs = lds + (NA & 1) * 32768;
#define GFLD() asm volatile("global_load_dwordx2 %0, %1, off sc1" : "+v"(gf) : "v"(gfp) : "memory")
#define WDLD(i, n_) do { const unsigned dvo = (unsigned)((ln_ >> 4) * 8 * DM * 2) + r4o;                                                  \
                    const char* db_ = (const char*)(w_down + (size_t)((nf2 ? 0 : i2 >> 3) * 256 + wv * 64) * DM + (i2 & 7) * 128) + 4096; \
                    asm volatile("s_cmp_lg_u32 %11, 0\n\ts_cbranch_scc1 1f\n\t"                                                          \
                                 "global_load_dwordx4 %0, %8, %9 offset:-4096\n\tglobal_load_dwordx4 %1, %8, %9 offset:-2048\n\tglobal_load_dwordx4 %2, %8, %9\n\t" \
                                 "global_load_dwordx4 %3, %8, %9 offset:2048\n\tglobal_load_dwordx4 %4, %8, %10 offset:-4096\n\tglobal_load_dwordx4 %5, %8, %10 offset:-2048\n\t" \
                                 "global_load_dwordx4 %6, %8, %10\n\tglobal_load_dwordx4 %7, %8, %10 offset:2048\n1:"  \
                                 : "=&v"(wd[i]), "=&v"(wd[i + 1]), "=&v"(wd[i + 2]), "=&v"(wd[i + 3]), "=&v"(wd[i + 4]), "=&v"(wd[i + 5]), "=&v"(wd[i + 6]), "=&v"(wd[i + 7]) \
                                 : "v"(dvo), "s"(db_ + (i) * 8192), "s"(db_ + (i) * 8192 + 8192), "s"(nf2) : "memory", "scc");            \
                    EALD((i) >> 3);                                                                                                       \
                    asm volatile("s_waitcnt vmcnt(%1)\n\ts_cmp_eq_u32 %0, 0\n\ts_cbranch_scc1 1f\n\ts_waitcnt vmcnt(%2)\n1:"                \
                                 :: "s"(nf2), "i"(12 + (n_)), "i"(4 + (n_)) : "memory", "scc");                                           \
                    BSTEP(63); } while (0)
#pragma unroll 1
            while (t < NA - 4) { BATCH(); PUB(17); BSTEP(0); }
            if (jj < 7) {
                BATCH(); PUB(16); WDLD(0, 0);
                BATCH(); PUB(16); GFLD(); WDLD(8, 0);
            }
            if (jj < 6) {
                BATCH();
                if (nf2 == 0) {
                    FLAG_WAIT(gf, gfp, tag, 63);
#pragma unroll
                    for (int i = 0; i < 8; ++i)
                        asm volatile("global_load_dwordx4 %0, %1, %2 sc1" : "=v"(gp[i]) : "v"((unsigned)((ln_ >> 4) * FF * 4) + r4o), "s"(ws + WS_GP + ((size_t)i * 4 * FF + f0) * 4) : "memory");
                    *(unsigned*)vs = 0;
                    asm volatile("s_waitcnt lgkmcnt(0)" ::: "memory");
                }
                WOLD(NA & 1);
                BSTEP(8);
            } else if (jj == 6) WOLD(0);
            asm volatile("global_load_dwordx2 %0, %1, %2 sc1" : "+v"(cxf) : "v"(CFO()), "s"(CFB) : "memory");
            WOLD((NA & 1) ^ 1);
            asm volatile("s_waitcnt vmcnt(16)" ::: "memory");
            if (jj == 7) FLAG_PUT(cxp, tag, 1);
            if (nf2 == 0) {
                f32x4 g4;
#pragma unroll
                for (int i = 0; i < 8; ++i) { PIN(gp[i]);
#pragma unroll
                    for (int j = 0; j < 4; ++j) { if (i) VADD(g4[j], g4[j], __uint_as_float(gp[i][j])); else g4[j] = __uint_as_float(gp[0][j]); } }
                float hv[2];
#pragma unroll
                for (int k = 0; k < 2; ++k) { auto rr = __builtin_amdgcn_permlane32_swap(__float_as_uint(g4[k]), __float_as_uint(g4[2 + k]), false, false);
                    const float gb = rbf(__uint_as_float(rr[0])), ub = rbf(__uint_as_float(rr[1]));
                    hv[k] = rbf(gb / (1.f + __expf(-gb))) * ub; PIN(hv[k]); }
                char* hl = vs + 8192 + wv * 256;
                *(unsigned*)(hl + ((ln_ >> 4) & 1) * 128 + (ln_ & 15) * 8 + (ln_ >> 5) * 4) = cvtpk(hv[0], hv[1]);
                asm volatile("" ::: "memory");
                xq[0] = *(const u32x4*)(hl + ((ln_ >> 2) & 1) * 128 + (ln_ >> 4) * 16); xq[1] = *(const u32x4*)(hl + ((ln_ >> 2) & 1) * 128 + 64 + (ln_ >> 4) * 16);
                FMM(wd, 0, xq[0], 1); FMM(wd, 8, xq[1], 0);
                *(f32x4*)(vs + 4096 + wv * 1024 + ln_ * 16) = FOUT();
                unsigned n_;
                asm volatile("s_waitcnt lgkmcnt(0)\n\ts_mov_b64 exec, 1\n\tds_add_rtn_u32 %0, %1, %2\n\ts_mov_b64 exec, -1\n\ts_waitcnt lgkmcnt(0)"
                             : "=&v"(n_) : "v"(ldsbase + (NA & 1) * 32768u), "v"(1u) : "memory");
                ypub = __builtin_amdgcn_readfirstlane(n_) ^ 3u;
                if (ypub == 0) {
                    const f32x4* yr = (const f32x4*)(vs + 4096) + ln_;
                    const f32x4 a = (yr[0] + yr[64]) + (yr[128] + yr[192]);
                    ST16_SC1((float*)(ws + WS_YP) + ((size_t)(((ln_ >> 4) & 1) * 8 + (i2 & 7)) * 16 + (i2 >> 3)) * 128 + (ln_ & 15) * 8 + (ln_ >> 5) * 4, a);
                }
            }
            AST(5);
        }
    }
    PH_STAMP(3);
    {
        constexpr int LDW = 1024, STG = 16384;
        int tid = threadIdx.x; asm volatile("" : "+v"(tid));
        const int lane = tid & 63, l15 = lane & 15, q4 = lane >> 4;
        unsigned xvo = (unsigned)lane * 16u; PIN(xvo);
        if (!(kp & 1)) {
#pragma unroll
            for (int i = 0; i < 8; i += 2) { const u32x4 t_ = pc[i]; pc[i] = pc[i + 1]; pc[i + 1] = t_; PIN(pc[i]); PIN(pc[i + 1]); } }
        bf16x8 yr_[4][2][2];
#pragma unroll
        for (int i = 0; i < 8; ++i) yr_[i >> 2][i & 1][(i >> 1) & 1] = __builtin_bit_cast(bf16x8, pc[i]);
        unsigned wa[2][2];
        wa[0][0] = ldsbase + (unsigned)(65536 + ((kp >> 1) ^ 1) * 1024 + (q4 >> 1) * 2048 + (4 * (q4 & 1) + (l15 >> 2)) * 64 + (q4 & 1) * 16 + (lane & 2) * 16 + (lane & 1) * 8);
        wa[0][1] = wa[0][0] ^ 16u; wa[1][0] = wa[0][0] ^ 1024u; wa[1][1] = wa[0][1] ^ 1024u;
        PIN(wa[0][0]); PIN(wa[0][1]); PIN(wa[1][0]); PIN(wa[1][1]);
        const char* cg = ws + WS_CTX + (qb * 8 + qw * 2) * 2048;
        unsigned xok;
#define XLD8 "global_load_dwordx4 %0, %10, %12 sc1\n\tglobal_load_dwordx4 %1, %10, %13 sc1\n\tglobal_load_dwordx4 %2, %10, %12 offset:1024 sc1\n\tglobal_load_dwordx4 %3, %10, %13 offset:1024 sc1\n\t" \
             "global_load_dwordx4 %4, %11, %12 sc1\n\tglobal_load_dwordx4 %5, %11, %13 sc1\n\tglobal_load_dwordx4 %6, %11, %12 offset:1024 sc1\n\tglobal_load_dwordx4 %7, %11, %13 offset:1024 sc1\n\t"
#define XOUT(c_) c_(yr_[2][0][0]), c_(yr_[2][1][0]), c_(yr_[2][0][1]), c_(yr_[2][1][1]), c_(yr_[3][0][0]), c_(yr_[3][1][0]), c_(yr_[3][0][1]), c_(yr_[3][1][1])
#define XIN "v"(xvo), "v"(xvo2), "s"(xp0), "s"(xp1), "s"(tag)
        unsigned xvo2 = xvo + 262144u, wvo = OWOFF() + (unsigned)K64(4) * (64 * LDW * 2), xso = xvo + (unsigned)K64(4) * 262144u, cfo = CFO();
        PIN(xvo2); PIN(wvo); PIN(xso); PIN(cfo);
        const char* cg0 = cg + ((kp ^ 1) & 1) * 2048, * cg1 = cg + (kp & 1) * 2048, * xp0 = cg0 + (unsigned)K64(2) * 262144u, * xp1 = cg1 + (unsigned)K64(2) * 262144u;
        const char* cfb = CFB, * wb0 = (const char*)wob, * wb1 = wb0 + (dn1 - 1024);
        const char* fpp = ws + WS_FLAG + ((wave & 4) ? F_Y + ((i2 & 7) * 16 + ((i2 >> 3) & 15)) * 8 : F_CTX + ((qbA * 4 + wave) * 32 + head) * CFS);
        unsigned fpz = (wave >> 2) * ypub, m0w = ldsbase + wave * 2048;
        asm volatile("" : "+s"(cg0), "+s"(cg1), "+s"(xp0), "+s"(xp1), "+s"(cfb), "+s"(wb0), "+s"(wb1), "+s"(fpp), "+s"(fpz), "+s"(m0w));
        SST(3);
        SB();
        asm volatile("s_waitcnt vmcnt(0)" ::: "memory");
        FLAG_PUTZ(fpp, tag, fpz);
        asm volatile("v_cmp_ne_u64 vcc, %14, %8\n\ts_and_b32 %9, vcc_lo, 14\n\ts_cbranch_scc1 1f\n\t"
                     XLD8 "\n1:\n\ts_barrier" : XOUT("=&v"), "+v"(cxf), "=&s"(xok) : XIN : "memory", "vcc", "scc");
        SST(4);
        f32x4 acc[2][8];
        bf16x4 wl[8], wh[8];
#define OFA(s, k_, j) (wa[(j) >> 2][(j) & 1] + (unsigned)(((s) & 3) * STG + (k_) * 8192 + (((j) >> 1) & 1) * 512))
#define ORL(i, s, k_, j) wl[i] = tr_rd(OFA(s, k_, j))
#define ORH(i, s, k_, j) wh[i] = tr_rd(OFA(s, k_, j) + 4096u)
        ORL(0, 0, 0, 0); ORH(0, 0, 0, 0); ORL(1, 0, 0, 1); ORH(1, 0, 0, 1); ORL(2, 0, 0, 2); ORH(2, 0, 0, 2); ORL(3, 0, 0, 3); ORH(3, 0, 0, 3); SB();
        const int r0 = qb * 128 + qw * 32 + (kp & 1) * 16, ch = kp >> 1;
        const unsigned nrt = min((unsigned)(ri1 - r0) >> 4, (unsigned)(ri2 - r0) >> 4);
        u64 yf = 0;
#define YFP (ws + WS_FLAG + F_Y + (ntc * 16 + (l15 < 14 ? l15 : 0)) * 8)
#define OMF(a, s, k_, j) do { if ((s) == 0 && (k_) == 0) MFA16Z(acc[a][j], w_, yr_[0][a][0]); else MFA16(acc[a][j], w_, yr_[(s) & 3][a][k_]); } while (0)
#define ORQ(h) do { constexpr int x_ = o_x(h), w_ = o_w(h);                                                                               \
            if (x_ >= 0) { asm volatile("global_load_dwordx4 %0, %1, %2 offset:%3 sc1" : "=v"(yr_[(x_ >> 2) & 3][x_ & 1][(x_ >> 1) & 1])   \
                                        : "v"(xso), "s"((x_ & 1) ? cg1 : cg0), "i"(((x_ >> 1) & 1) * 1024) : "memory");         \
                           if ((x_ & 3) == 3) { xso ^= (unsigned)((x_ >> 2) ^ ((x_ >> 2) + 1)) * 262144u; PIN(xso); } }                    \
            if (o_w((h) + 1) >= 0 && !(o_w((h) + 1) & 1)) asm volatile("s_mov_b32 m0, %0" :: "s"(m0w + (((o_w((h) + 1) >> 1) & 7) ^ 4) * STG) : "memory"); \
            if (w_ >= 0) { asm volatile("global_load_lds_dwordx4 %0, %1 offset:%2" :: "v"(wvo), "s"((w_ & 1) ? wb1 : wb0), "i"((w_ & 1) * 1024) : "memory"); \
                           if (w_ & 1) { wvo ^= (unsigned)((w_ >> 1) ^ ((w_ >> 1) + 1)) * (64 * LDW * 2); PIN(wvo); } } } while (0)
#define OPAIR(s, f, A) do { const bf16x8 w_ = CAT8(wl[(f) & 7], wh[(f) & 7]); constexpr int s_ = (s) + (((f) + 4) >> 4), m_ = ((f) + 4) & 15; \
            OMF(0, s, (f) >> 3, (f) & 7); SB(); ORL(m_ & 7, s_, m_ >> 3, m_ & 7); ORQ(32 * (s) + 2 * (f)); A; SB();                       \
            OMF(1, s, (f) >> 3, (f) & 7); SB(); ORH(m_ & 7, s_, m_ >> 3, m_ & 7); ORQ(32 * (s) + 2 * (f) + 1); SB(); if (((f) & 1)) { __builtin_amdgcn_s_waitcnt(0xC47F); SB(); } } while (0)
#define OTG(h_) do { wa[h_][0] ^= 65536u; wa[h_][1] ^= 65536u; PIN(wa[h_][0]); PIN(wa[h_][1]); } while (0)
#define OST(i)
#define OSTEP(s, POST) do {                                                                                                               \
        OPAIR(s, 0, ); OPAIR(s, 1, ); OPAIR(s, 2, ); OPAIR(s, 3, ); OPAIR(s, 4, ); OPAIR(s, 5, ); OPAIR(s, 6, ); OPAIR(s, 7, );           \
        OPAIR(s, 8, ); OPAIR(s, 9, if (((s) & 3) == 3) OTG(0)); OPAIR(s, 10, ); OPAIR(s, 11, );                                           \
        if ((s) >= 3) asm volatile("s_waitcnt vmcnt(%0)\n\ts_barrier" :: "i"(o_wait(s)) : "memory");                                      \
        SB(); POST; OST((s) + 1);                                                                                                         \
        OPAIR(s, 12, ); OPAIR(s, 13, if (((s) & 3) == 3) OTG(1)); OPAIR(s, 14, ); OPAIR(s, 15, );                                         \
    } while (0)
        PH_STAMP(4);
        SST(5);
        OST(0);
        SB();
        OSTEP(0, );
#define MSK "14"
#define MSKI
        asm volatile("s_cmp_eq_u32 %3, 0\n\ts_cbranch_scc1 1f\n\tglobal_load_dwordx2 %0, %1, %2 sc1\n1:" : "+v"(cxf) : "v"(cfo), "s"(cfb), "s"(xok) : "memory", "scc");
        OSTEP(1, );
        asm volatile("s_waitcnt vmcnt(8)\n\ts_cmp_eq_u32 %9, 0\n\ts_cbranch_scc1 3f\n\ts_mov_b32 %9, 0x40000\n\ts_waitcnt vmcnt(4)\n"
                     "1:\n\tv_cmp_ne_u64 vcc, %14, %8\n\ts_and_b32 vcc_lo, vcc_lo, " MSK "\n\ts_cbranch_scc0 2f\n\t"
                     "global_load_dwordx2 %8, %15, %16 sc1\n\ts_sleep 4\n\ts_waitcnt vmcnt(0)\n\ts_sub_u32 %9, %9, 1\n\ts_cbranch_scc0 1b\n2:\n\t" XLD8 "s_waitcnt vmcnt(4)\n3:"
                     : XOUT("+v"), "+v"(cxf), "+s"(xok) : XIN, "v"(cfo), "s"(cfb), "s"(wave) MSKI : "memory", "vcc", "scc");
        const unsigned inc = (unsigned)__builtin_amdgcn_ballot_w64(cxf != tag) & 0x1f0u;
        SB();
        OSTEP(2, if (__builtin_expect(inc != 0, 0)) asm volatile("global_load_dwordx2 %0, %1, %2 sc1" : "+v"(cxf) : "v"(cfo), "s"(cfb) : "memory"); SB());
        asm volatile("s_waitcnt vmcnt(8)" ::: "memory"); SB();
        OSTEP(3, );
        OSTEP(4, if (__builtin_expect(inc != 0, 0)) FLAG_WAITM(cxf, cfb + cfo, tag, 63, 0x1f0u); SB());
        OSTEP(5, ); OSTEP(6, );
        OSTEP(7, if (__builtin_expect(nrt == 0, 0)) asm volatile("global_load_dwordx2 %0, %1, off sc1" : "+v"(yf) : "v"(YFP) : "memory"); SB());
        OSTEP(8, ); OSTEP(9, ); OSTEP(10, );
        u32x4 yq[7], pv[6], zn[2]; u32x2 ea[4]; unsigned rq;
        if (__builtin_expect(nrt == 0, 0)) {
            FLAG_WAIT(yf, YFP, tag, 63);
            asm volatile("global_load_dwordx4 %0, %8, %10 sc1\n\tglobal_load_dwordx4 %1, %8, %10 offset:512 sc1\n\tglobal_load_dwordx4 %2, %8, %10 offset:1024 sc1\n\t"
                         "global_load_dwordx4 %3, %8, %10 offset:1536 sc1\n\tglobal_load_dwordx4 %4, %8, %10 offset:2048 sc1\n\tglobal_load_dwordx4 %5, %8, %10 offset:2560 sc1\n\t"
                         "global_load_dwordx4 %6, %8, %10 offset:3072 sc1\n\tglobal_load_dword %7, %9, %10 sc1"
                         : "=&v"(yq[0]), "=&v"(yq[1]), "=&v"(yq[2]), "=&v"(yq[3]), "=&v"(yq[4]), "=&v"(yq[5]), "=&v"(yq[6]), "=&v"(rq)
                         : "v"((unsigned)WS_YP + (unsigned)((((lane >> 5) * 8 + ntc) * 16 + (q4 & 1) * 7) * 128 + ch * 64 + 4 * l15) * 4u),
                           "v"((unsigned)WS_R + (unsigned)(lane >= 32 ? ri2 : ri1) * 4u), "s"(ws) : "memory");
        }
        SB();
        OSTEP(11, );
        char* pb = ws + WS_PART + (size_t)(tile * 8 + wave) * 24576;
        const char* tfb = ws + WS_FLAG + F_PARK + (tile * 8 + wave) * 256;
        const unsigned tfo = (unsigned)((lane & 3) ^ kp) * 64u;
        const unsigned ea0 = ldsbase + 131072 + (wave & 3) * 8192 + (wave >> 2) * 4096 + l15 * 128 + (((lane >> 5) * 2 ^ (lane & 7)) << 4) + (q4 & 1) * 8;
        typedef __attribute__((address_space(3))) u32x2 lds_u32x2;
#define OSND(a, j) do { constexpr int x_ = ((a) ^ 1) | (((j) >> 1) & 2 ^ 2);                                             \
            u32x4 p_ = u32x4{cvtpk(acc[a][j][0], acc[a][j][1]), cvtpk(acc[a][j][2], acc[a][j][3]), cvtpk(acc[a][(j) + 1][0], acc[a][(j) + 1][1]), cvtpk(acc[a][(j) + 1][2], acc[a][(j) + 1][3])}; \
            if (x_ == 1) { _Pragma("unroll") for (int k = 0; k < 4; ++k) p_[k] = min(p_[k], 0xfffffffeu); }     \
            asm volatile("global_store_dwordx4 %0, %1, %2 offset:%3 sc1\n\ts_nop 1" :: "v"(xvo), "v"(p_), "s"(pb + (kp ^ x_) * 6144 + (x_ - 1) * 2048), "i"(((j) & 2) * 512) : "memory"); } while (0)
#define OQ(q) do { constexpr int p_ = (q) + 4; const bf16x8 w_ = CAT8(wl[(q) & 7], wh[(q) & 7]);                                           \
            constexpr int j_ = (q) < 32 ? (q) & 3 : 4 + 2 * (((q) >> 4) & 1) + ((q) & 1), k_ = (q) < 32 ? ((q) >> 2) & 1 : ((q) >> 1) & 1, s_ = (q) < 32 ? ((q) >> 3) & 3 : ((q) >> 2) & 3; \
            constexpr int pj_ = p_ < 32 ? p_ & 3 : 4 + 2 * ((p_ >> 4) & 1) + (p_ & 1), pk_ = p_ < 32 ? (p_ >> 2) & 1 : (p_ >> 1) & 1, ps_ = p_ < 32 ? (p_ >> 3) & 3 : (p_ >> 2) & 3; \
            if ((q) == 16 || (q) == 24) { asm volatile("s_waitcnt vmcnt(%0)" :: "i"((q) == 16 ? 4 : 0) : "memory"); SB(); }               \
            OMF(0, 12 + s_, k_, j_); SB();                                                                                                \
            if (p_ < 64) ORL(p_ & 7, ps_, pk_, pj_);                                                                                      \
            ORQ(384 + 2 * (q)); SB();                                                                                                     \
            OMF(1, 12 + s_, k_, j_); SB();                                                                                                \
            if (p_ < 64) ORH(p_ & 7, ps_, pk_, pj_);                                                                                      \
            if ((q) >= 10 && (q) < 14) ea[((q) - 10) & 3] = *(lds_u32x2*)(uintptr_t)(ea0 ^ (unsigned)((((q) - 10) & 1) * 16 + (((q) - 10) & 2) * 32)); \
            if ((q) == 14 || (q) == 15) zn[(q) & 1] = __builtin_bit_cast(u32x4, lds_rd(ldsbase + 131072 + 2048 + (wave & 3) * 8192 + (wave >> 2) * 4096 + ((q) & 1) * 1024 + xvo)); \
            if ((q) == 32) OSND(0, 0); if ((q) == 33) OSND(1, 0); if ((q) == 34) OSND(0, 2); if ((q) == 35) OSND(1, 2);                   \
            if ((q) == PQ) asm volatile("s_setprio 0");                                                                                   \
            if ((q) == 50) OSND(0, 4);                                                                                                    \
            if ((q) == FQ) { asm volatile("s_waitcnt vmcnt(%0)" :: "i"(FQ >= 50) : "memory"); FLAG_PUT(tfb + kp * 64, tag, 1); }     \
            if ((q) == GQ) asm volatile("global_load_dwordx2 %0, %1, %2 sc1" : "=v"(cxf) : "v"(tfo), "s"(tfb) : "memory");               \
            SB(); } while (0)
#define PQ 35
#define FQ 44
#define GQ 60
#define OQ8(q) do { OQ(q); OQ((q) + 1); OQ((q) + 2); OQ((q) + 3); OQ((q) + 4); OQ((q) + 5); OQ((q) + 6); OQ((q) + 7); } while (0)
        if (PQ < 64) asm volatile("s_setprio 1");
        OQ8(0); OQ8(8); OQ8(16); OQ8(24); OST(13); OQ8(32); OQ8(40); OQ8(48); OQ8(56);
        MF_DONE();
        OSND(0, 6); S2T(1);
        OST(14);
        PH_STAMP(5);
        SST(6); S2T(2);
        SB();
        if (__builtin_expect(nrt == 0, 0)) {
            f32x4 ys;
#pragma unroll
            for (int i = 0; i < 7; ++i) PIN(yq[i]);
            PIN(rq);
            const float sig = rbf(1.f / (1.f + __expf(-__uint_as_float(rq))));
#pragma unroll
            for (int j = 0; j < 4; ++j) {
                const float s_ = ((__uint_as_float(yq[0][j]) + __uint_as_float(yq[1][j])) + (__uint_as_float(yq[2][j]) + __uint_as_float(yq[3][j])))
                               + ((__uint_as_float(yq[4][j]) + __uint_as_float(yq[5][j])) + __uint_as_float(yq[6][j]));
                auto rr = __builtin_amdgcn_permlane16_swap(__float_as_uint(s_), __float_as_uint(s_), false, false);
                ys[j] = rbf(__uint_as_float(rr[0]) + __uint_as_float(rr[1])) * sig; }
            char* yl = lds + 65536 + wave * 256;
            *(u32x2*)(yl + (lane >> 5) * 128 + (l15 >> 2) * 32 + (l15 & 1) * 16 + (l15 & 2) * 4) = u32x2{cvtpk(ys[0], ys[1]), cvtpk(ys[2], ys[3])};
            asm volatile("" ::: "memory");
            const int m = r0 + l15;
#pragma unroll
            for (int i = 0; i < 2; ++i) { const u32x4 yn = *(const u32x4*)(yl + (m == ri1 ? 0 : 128) + i * 64 + q4 * 16);
#pragma unroll
                for (int k = 0; k < 4; ++k) { const unsigned z_ = cvtpk(HALF(zn[i][k], 0) + HALF(yn[k], 0), HALF(zn[i][k], 1) + HALF(yn[k], 1));
                    zn[i][k] = m == ri1 || m == ri2 ? z_ : zn[i][k]; } }
        }
        float gg[2][8], xx[2][8];
#pragma unroll
        for (int i = 0; i < 2; ++i)
#pragma unroll
            for (int j = 0; j < 8; ++j) { gg[i][j] = HALF(zn[i][j >> 1], j); xx[i][j] = HALF(ea[2 * i + (j >> 2)][(j >> 1) & 1], j); PIN(gg[i][j]); PIN(xx[i][j]); }
        unsigned ovo = (unsigned)((l15 & 7) * DM + 32 * (l15 >> 3) + 8 * q4) * 2u; PIN(ovo);
        const char* ob = (const char*)(outp + (size_t)r0 * DM + ntc * 128 + 64 * ch);
        asm volatile("" : "+s"(ob));
        PH_STAMP(6);
        { unsigned c_, m_;
          asm volatile("s_waitcnt vmcnt(%0)" :: "i"(GQ < 50 ? 2 : 1) : "memory");
          asm volatile("s_mov_b32 %7, 0x40000\n"
                       "1:\n\tv_cmp_ne_u64 vcc, %11, %6\n\ts_and_b32 vcc_lo, vcc_lo, 12\n\ts_cbranch_scc0 2f\n\t"
                       "global_load_dwordx2 %6, %12, %13 sc1\n\ts_waitcnt vmcnt(0)\n\ts_sub_u32 %7, %7, 1\n\ts_cbranch_scc0 1b\n"
                       "2:\n\tglobal_load_dwordx4 %1, %9, %10 offset:-1024 sc1\n\tglobal_load_dwordx4 %2, %9, %10 offset:1024 sc1\n\t"
                       "global_load_dwordx4 %4, %9, %10 sc1\n\tglobal_load_dwordx4 %5, %9, %10 offset:2048 sc1\n\t"
                       "global_load_dwordx4 %0, %9, %10 offset:-3072 sc1\n\tglobal_load_dwordx4 %3, %9, %10 offset:-2048 sc1"
                       : "=&v"(pv[0]), "=&v"(pv[1]), "=&v"(pv[2]), "=&v"(pv[3]), "=&v"(pv[4]), "=&v"(pv[5]), "+v"(cxf), "=&s"(c_), "=&v"(m_)
                       : "v"(xvo), "s"(pb + kp * 6144 + 3072), "s"(tag), "v"(tfo), "s"(tfb) : "memory", "vcc", "scc"); }
        S2T(3);
#pragma unroll
        for (int i = 0; i < 16; i += 2) { float g_[2];
#pragma unroll
            for (int e_ = 0; e_ < 2; ++e_) { float a_ = gg[i >> 3][(i & 7) + e_] * -1.4426950408889634f; PIN(a_); a_ = 1.f + __builtin_amdgcn_exp2f(a_); PIN(a_);
                g_[e_] = gg[i >> 3][(i & 7) + e_] * __builtin_amdgcn_rcpf(a_); PIN(g_[e_]); }
            const unsigned p_ = cvtpk(g_[0], g_[1]); gg[i >> 3][i & 7] = HALF(p_, 0); gg[i >> 3][(i & 7) + 1] = HALF(p_, 1); PIN(gg[i >> 3][i & 7]); PIN(gg[i >> 3][(i & 7) + 1]); }
        S2T(4);
        asm volatile("s_waitcnt vmcnt(0)" : "+v"(pv[0]), "+v"(pv[1]), "+v"(pv[2]), "+v"(pv[3]), "+v"(pv[4]), "+v"(pv[5]) :: "memory");
        S2T(5);
#pragma unroll 1
        for (int n_ = 0; n_ < 0x40000; ++n_) {
            const unsigned mx_ = max(max(max(pv[0][0], pv[0][1]), max(pv[0][2], pv[0][3])), max(max(pv[3][0], pv[3][1]), max(pv[3][2], pv[3][3])));
            if (__builtin_amdgcn_ballot_w64(mx_ == ~0u) == 0) break;
            asm volatile("global_load_dwordx4 %0, %2, %3 sc1\n\tglobal_load_dwordx4 %1, %2, %3 offset:1024 sc1\n\ts_waitcnt vmcnt(0)"
                         : "=&v"(pv[0]), "=&v"(pv[3]) : "v"(xvo), "s"(pb + kp * 6144) : "memory");
        }
        PH_STAMP(7); S2T(6);
        u32x4 ow[2];
#pragma unroll
        for (int i = 0; i < 2; ++i) {
            u32x4& ov = ow[i];
#pragma unroll
            for (int k = 0; k < 4; ++k) { float sv[2];
#pragma unroll
                for (int e_ = 0; e_ < 2; ++e_) sv[e_] = fmaf((acc[1][4 + 2 * i + (k >> 1)][2 * (k & 1) + e_] + HALF(pv[3 * i][k], e_)) + (HALF(pv[3 * i + 1][k], e_) + HALF(pv[3 * i + 2][k], e_)),
                                                             gg[i][2 * k + e_], xx[i][2 * k + e_]);
                ov[k] = cvtpk(sv[0], sv[1]); }
#pragma unroll
            for (int k = 0; k < 2; ++k) { auto rr = __builtin_amdgcn_permlane16_swap(ov[k], ov[2 + k], false, false); ov[k] = rr[0]; ov[2 + k] = rr[1]; }
        }
#pragma unroll
        for (int i = 0; i < 2; ++i) {
            u32x4 ov;
#pragma unroll
            for (int k = 0; k < 4; ++k) ov[k] = i ? __builtin_amdgcn_update_dpp(ow[1][k], ow[0][k], 0x128, 0xf, 0x3, false) : __builtin_amdgcn_update_dpp(ow[0][k], ow[1][k], 0x128, 0xf, 0xc, false);
            asm volatile("global_store_dwordx4 %0, %1, %2 sc1\n\ts_nop 1" :: "v"(ovo), "v"(ov), "s"(ob + i * (8 * DM * 2)) : "memory");
        }
        SST(7); SST_OUT();
        PH_STAMP(8);
        PH_STAMP(9);
    }
}

extern "C" void kernel_launch(void* const* d_in, const int* in_sizes, int n_in, void* d_out, int out_size, void* d_ws, size_t ws_size,
                              hipStream_t stream) {
    const u16* x    = (const u16*)d_in[0];
    const u16* ckv  = (const u16*)d_in[1];
    const u16* wq   = (const u16*)d_in[2];
    const u16* wuk  = (const u16*)d_in[3];
    const u16* wuv  = (const u16*)d_in[4];
    const u16* wo   = (const u16*)d_in[5];
    const u16* wr   = (const u16*)d_in[7];
    const u16* wg   = (const u16*)d_in[8];
    const u16* wu   = (const u16*)d_in[9];
    const u16* wd   = (const u16*)d_in[10];
    (void)in_sizes; (void)n_in; (void)out_size; (void)ws_size;
    (void)hipFuncSetAttribute((const void*)k_all, hipFuncAttributeMaxDynamicSharedMemorySize, 163840);
    k_all<<<256, 512, 163840, stream>>>(x, ckv, wuk, wuv, wr, (char*)d_ws + ((size_t)WSOFF << 20), wq, wo, wg, wu, wd, (u16*)d_out);
}
```

```cpp
#include <hip/hip_runtime.h>
#include <hip/hip_bf16.h>
#include <stdint.h>

typedef unsigned short u16;
typedef unsigned long long u64;
typedef short    bf16x8 __attribute__((ext_vector_type(8)));
typedef short    bf16x4 __attribute__((ext_vector_type(4)));
typedef float    f32x4  __attribute__((ext_vector_type(4)));
typedef float    f32x2  __attribute__((ext_vector_type(2)));
typedef __bf16   bfx2   __attribute__((ext_vector_type(2)));
typedef unsigned u32x4  __attribute__((ext_vector_type(4)));
typedef unsigned u32x2  __attribute__((ext_vector_type(2)));

#define DI __device__ __forceinline__

constexpr int SQ = 2048, DM = 1024, NH = 32, HD = 128, DC = 512, FF = 3584;

#define WSOFF 0
constexpr size_t WS_R    = 4u << 20;
constexpr size_t WS_FLAG = (4u << 20) + 65536;
constexpr size_t WS_GP   = 6u << 20;
constexpr size_t WS_YP   = 7u << 20;
constexpr size_t WS_PART = 96u << 20;
constexpr size_t WS_XF   = 16u << 20;
#define HSTR 1310720
constexpr size_t WS_KV   = 24u << 20;
constexpr size_t WS_CTX  = 72u << 20;
constexpr int F_EPOCH = 0, F_TOP = 1024, F_LN = 4096, F_KV = 8192, F_GU = 12288  , F_Y = 20480  , F_PARK = 524288  ,
              F_CTX = 65536  ;

#define PH_STAMP(i)
#define SST(i)
#define S2T(i)
#define SST_OUT()
#define ZST(i)
#define ZST_OUT()
DI unsigned cvtpk(float lo, float hi) { f32x2 v = {lo, hi}; bfx2 b = __builtin_convertvector(v, bfx2); return __builtin_bit_cast(unsigned, b); }
DI float rbf(float x) { return __uint_as_float(cvtpk(x, 0.f) << 16); }
#define DPP_ADD(v, ctrl, rmask) v += __uint_as_float(__builtin_amdgcn_update_dpp(0u, __float_as_uint(v), ctrl, rmask, 0xf, false))
DI unsigned tkey(unsigned fbits, int i) { unsigned u = fbits >> 16; u ^= (u & 0x8000u) ? 0xffffu : 0x8000u; return (u << 16) | (unsigned)(2047 - i); }
DI void merge_top2(unsigned& a1, unsigned& a2, unsigned b1, unsigned b2) { const unsigned lo = min(a1, b1); a1 = max(a1, b1); a2 = max(lo, max(a2, b2)); }
#define DPP_TOP2(a1, a2, ctrl, rmask) merge_top2(a1, a2, __builtin_amdgcn_update_dpp(0u, a1, ctrl, rmask, 0xf, false), __builtin_amdgcn_update_dpp(0u, a2, ctrl, rmask, 0xf, false))

#define GLDS_S(voff, sbase, ldsoff, imm)                                                                        \
    asm volatile("s_mov_b32 m0, %2\n\ts_nop 0\n\tglobal_load_lds_dwordx4 %0, %1 offset:%3"                       \
                 :: "v"(voff), "s"(sbase), "s"(ldsoff), "i"(imm) : "memory")
#define GLDS_C(voff, sbase, ldsoff, imm)                                                                        \
    asm volatile("s_mov_b32 m0, %2\n\ts_nop 0\n\tglobal_load_lds_dwordx4 %0, %1 offset:%3 sc1"                   \
                 :: "v"(voff), "s"(sbase), "s"(ldsoff), "i"(imm) : "memory")
#define LGKM0_BAR()    asm volatile("s_waitcnt lgkmcnt(0)\n\ts_barrier" ::: "memory")
#define SB() __builtin_amdgcn_sched_barrier(0)
#define CAT_(a, b) a##b
#define CAT(a, b) CAT_(a, b)
#define PIN(x) asm volatile("" : "+v"(x))
#define OADV(p_, d_) do { p_ += (d_); asm volatile("" : "+s"(p_)); } while (0)
#define VADD(d, a, b)    asm volatile("v_add_f32 %0, %1, %2" : "=v"(d) : "v"(a), "v"(b))
#define KEEP(x) asm volatile("" :: "v"(x))
#define MFA16(c, a, b) asm volatile("v_mfma_f32_16x16x32_bf16 %0, %1, %2, %0" : "+v"(c) : "v"(a), "v"(b))
#define MFA16Z(c, a, b) asm volatile("v_mfma_f32_16x16x32_bf16 %0, %1, %2, 0" : "=&v"(c) : "v"(a), "v"(b))
#define MFA16C(c, a, b, c0) asm volatile("v_mfma_f32_16x16x32_bf16 %0, %1, %2, %3" : "=&v"(c) : "v"(a), "v"(b), "v"(c0))
#define MF_DONE() do { SB(); asm volatile("s_nop 7\n\ts_nop 7"); SB(); } while (0)
#define ST16_SC1(p, v) asm volatile("global_store_dwordx4 %0, %1, off sc1\n\ts_nop 1" :: "v"(p), "v"(v) : "memory")
#define ST4_SC1(p, v)  asm volatile("global_store_dword %0, %1, off sc1" :: "v"(p), "v"(v) : "memory")
#define FLAG_PUT(p_, val_, on_) do { u64 sv_, tv_; unsigned tz_; const u64 v_ = (val_); const unsigned m_ = __builtin_amdgcn_readfirstlane((on_) ? 1u : 0u); \
        asm volatile("s_cmp_eq_u32 %5, 0\n\ts_cbranch_scc1 1f\n\tv_mov_b64 %1, %3\n\tv_mov_b32 %2, 0\n\ts_mov_b64 %0, exec\n\ts_mov_b64 exec, 1\n\t" \
                     "global_store_dwordx2 %2, %1, %4 sc1\n\ts_mov_b64 exec, %0\n1:"                                                      \
                     : "=&s"(sv_), "=&v"(tv_), "=&v"(tz_) : "s"(v_), "s"(p_), "s"(m_) : "memory", "scc"); } while (0)
#define FLAG_PUTZ(p_, val_, nz_) do { u64 sv_, tv_; unsigned tz_; const u64 v_ = (val_);                                                   \
        asm volatile("s_cmp_lg_u32 %5, 0\n\ts_cbranch_scc1 1f\n\tv_mov_b64 %1, %3\n\tv_mov_b32 %2, 0\n\ts_mov_b64 %0, exec\n\ts_mov_b64 exec, 1\n\t" \
                     "global_store_dwordx2 %2, %1, %4 sc1\n\ts_mov_b64 exec, %0\n1:"                                                      \
                     : "=&s"(sv_), "=&v"(tv_), "=&v"(tz_) : "s"(v_), "s"(p_), "s"(nz_) : "memory", "scc"); } while (0)
#define FLAG_GET(r_, p_) asm volatile("global_load_dwordx2 %0, %1, off sc1" : "=v"(r_) : "v"(p_) : "memory")
#define FLAG_WAIT(r_, p_, tag_, N) do { unsigned c_;                                                                                      \
        asm volatile("s_waitcnt vmcnt(%4)\n\ts_mov_b32 %1, 0x40000\n"                                                                     \
                     "1:\n\tv_cmp_ne_u64 vcc, %3, %0\n\ts_cmp_eq_u64 vcc, 0\n\ts_cbranch_scc1 2f\n\t"                                      \
                     "s_sleep 2\n\tglobal_load_dwordx2 %0, %2, off sc1\n\ts_waitcnt vmcnt(0)\n\ts_sub_u32 %1, %1, 1\n\ts_cbranch_scc0 1b\n"  \
                     "2:" : "+v"(r_), "=&s"(c_) : "v"(p_), "s"(tag_), "i"(N) : "memory", "vcc", "scc"); } while (0)
#define FLAG_WAITM(r_, p_, tag_, N, m_) do { unsigned c_;                                                                                 \
        asm volatile("s_waitcnt vmcnt(%4)\n\ts_mov_b32 %1, 0x40000\n"                                                                     \
                     "1:\n\tv_cmp_ne_u64 vcc, %3, %0\n\ts_and_b32 vcc_lo, vcc_lo, %5\n\ts_cbranch_scc0 2f\n\t"                             \
                     "s_sleep 2\n\tglobal_load_dwordx2 %0, %2, off sc1\n\ts_waitcnt vmcnt(0)\n\ts_sub_u32 %1, %1, 1\n\ts_cbranch_scc0 1b\n"  \
                     "2:" : "+v"(r_), "=&s"(c_) : "v"(p_), "s"(tag_), "i"(N), "s"(m_) : "memory", "vcc", "scc"); } while (0)
typedef __attribute__((address_space(3))) bf16x4 lds_bf16x4;
DI bf16x4 tr_rd(unsigned a) { return __builtin_amdgcn_ds_read_tr16_b64_v4i16((lds_bf16x4*)(uintptr_t)a); }
typedef __attribute__((address_space(3))) bf16x8 lds_bf16x8;
DI bf16x8 lds_rd(unsigned a) { return *(lds_bf16x8*)(uintptr_t)a; }
#define CAT8(lo_, hi_) bf16x8{lo_[0], lo_[1], lo_[2], lo_[3], hi_[0], hi_[1], hi_[2], hi_[3]}

#define POFF 32.f
#define LTHR 8192.f
DI float quad_max(float v) {
    auto r1 = __builtin_amdgcn_permlane16_swap(__float_as_uint(v), __float_as_uint(v), false, false);
    v = fmaxf(__uint_as_float(r1[0]), __uint_as_float(r1[1]));
    auto r2 = __builtin_amdgcn_permlane32_swap(__float_as_uint(v), __float_as_uint(v), false, false);
    return fmaxf(__uint_as_float(r2[0]), __uint_as_float(r2[1])); }
#define QWD 3
#define QS1 0xbfff
constexpr int q_xb(int s) { return s >= 1 && s <= 12; }
constexpr int q_xa(int s) { return s >= 0 && s <= 11; }
constexpr int q_s0(int s) { int n = 0; for (int i = 0; i < s; ++i) n += (QS1 >> i) & 1; return n; }
constexpr int q_kv(int kt, int f) { const int s = (kt - 13) * 16 + f - 12; return s >= 0 && s % 3 == 0 && s / 3 < 12 ? s / 3 : -1; }
constexpr int q_op(int s, int f) { return (f == 0 || f == 2 ? q_xb(s) : f == 8 || f == 10 ? q_xa(s) : f == 12 || f == 14 ? s + QWD < 16 : f == 3 ? s == 12
                                          : f == 4 ? (QS1 >> s) & 1 : 0) + (q_kv(s, f) >= 0); }
constexpr int q_after(int p, int kt) { int n = 0; for (int i = p + 1; i <= kt * 16 + 11; ++i) n += q_op(i >> 4, i & 15); return n; }
constexpr int q_min(int a, int b) { return a < b ? a : b; }
constexpr int q_wait(int kt) {
    int n = kt <= 2 ? 4 * (2 - kt) + q_after(-1, kt) : q_after((kt - 2) * 16 + 2, kt);
    if (kt + 1 >= QWD) n = q_min(n, q_after((kt + 1 - QWD) * 16 + 14, kt));
    if (kt == 13) n = q_min(n, q_after(12 * 16 + 3, kt));
    return n; }
constexpr int q_fl() { int s = 0; while (q_s0(s + 1) < 8) ++s; int kt = s; while (q_wait(kt) > q_after(s * 16 + 6, kt)) ++kt; return kt; }
constexpr int Q_NST = q_s0(16);
static_assert(Q_NST >= 8, "");
constexpr int o_xp(int st, int i) { return st <= 5 ? 64 + 4 * (4 * (st - 4) + i) : 32 * (st - 3) + (i == 0 ? 0 : i == 1 ? 4 : i == 2 ? 12 : 16); }
constexpr int o_wp(int st, int i) { return st <= 7 ? 16 * (st - 4) + 4 + 6 * i : 32 * (st - 5) + 24 + 4 * i; }
constexpr int o_x(int h) { for (int st = 4; st < 16; ++st) for (int i = 0; i < 4; ++i) if (o_xp(st, i) == h) return 4 * st + i; return -1; }
constexpr int o_w(int h) { for (int st = 4; st < 16; ++st) for (int i = 0; i < 2; ++i) if (o_wp(st, i) == h) return 2 * st + i; return -1; }
constexpr int o_after(int p, int e) { int n = 0; for (int h = p + 1; h < e; ++h) n += (o_x(h) >= 0) + (o_w(h) >= 0); return n; }
constexpr int o_wait(int kt) { return o_after(kt == 11 ? o_wp(15, 1) : o_xp(kt + 1, 3), 32 * kt + 24); }
constexpr int L_PFT = 149504, L_DUMP = 155648;
constexpr int CFS = 128;
constexpr unsigned F1S = 0xffff642fu, F1F = 0xffff5310u, F2S = 0xff64fff0u, F2F = 0xff53f21fu;
constexpr int F1T = 3;

__global__ __launch_bounds__(512, 2) void k_all(const u16* __restrict__ x, const u16* __restrict__ ckv, const u16* __restrict__ wuk, const u16* __restrict__ wuv,
                                                const u16* __restrict__ w_r, char* __restrict__ ws, const u16* __restrict__ wq, const u16* __restrict__ wo,
                                                const u16* __restrict__ w_gate, const u16* __restrict__ w_up,
                                                const u16* __restrict__ w_down, u16* __restrict__ out) {
    extern __shared__ __attribute__((aligned(16))) char lds[];
    const int b = blockIdx.x, xcd = b & 7, idx = b >> 3, head = xcd * 4 + (idx >> 3), jj = idx & 7;
    const int hp = head ^ 1;
    const unsigned ldsbase = (unsigned)(uintptr_t)(__attribute__((address_space(3))) char*)lds;
    const int wave = __builtin_amdgcn_readfirstlane(threadIdx.x >> 6);
    const int qbA = 15 - jj, qbB = jj;
#define KROW(g) (((2 * ((g) & 3) + ((g) >> 2)) * 8 + jj) * 32)
    const int qw = wave < 4 ? wave : 7 - wave, qb = wave < 4 ? qbA : qbB;
    bf16x8 qf[8];
    u64 tag;
    u16* outp = out;
    int ri1, ri2;
    PH_STAMP(0);
    SST(0);
    ZST(0);
    u32x4 pk[16];
    bf16x8 xr[4][2][2];
    u32x4 rv;
    const char* xg;
#define QXL1(j) asm volatile("global_load_dwordx4 %0, %1, %2 offset:%3 sc1" : "=v"(xr[(j) >> 2][(j) & 1][((j) >> 1) & 1]) : "v"(xvo), "s"(xg + (size_t)((j) >> 2) * 262144), \
                            "i"(((j) & 1) * 2048 + (((j) >> 1) & 1) * 1024) : "memory")
    const char* sb_[4];
    unsigned qa, qa2; bf16x4 ql[8], qh[8];
#define QFA(s, f) ((((f) & 1) ? qa2 : qa) + (unsigned)(((s) & 3) * 16384 + ((f) >> 3) * 8192 + (((f) >> 2) & 1) * 1024 + (((f) >> 1) & 1) * 512))
#define QFM(s, f) ((s) == 15 ? ((f) & 1) * 8 + ((f) >> 1) : (f))
#define QRL(s, f) ql[(f) & 7] = tr_rd(QFA(s, QFM(s, f)))
#define QRH(s, f) qh[(f) & 7] = tr_rd(QFA(s, QFM(s, f)) + 2048u)
    unsigned xvo, qwo;
    const u16* wqb;
#define QWOFF() ({ unsigned l_ = threadIdx.x & 63; asm volatile("" : "+v"(l_)); (unsigned)(((l_ >> 2) & 7) * (NH * HD) + 32 * (l_ >> 5) + 8 * ((l_ & 3) ^ ((l_ >> 3) & 2))) * 2u; })
    {
        constexpr int LDW = NH * HD, K = DC;
        int tid = threadIdx.x; asm volatile("" : "+v"(tid));
        const int lane = tid & 63;
        const int wm = wave >> 2, wn = wave & 3;
        const int row = b * 8 + wave;
        u32x4 lx[2], lw[2]; u32x2 ep;
        asm volatile("global_load_dwordx4 %0, %5, %6\n\tglobal_load_dwordx4 %1, %5, %6 offset:1024\n\tglobal_load_dwordx4 %2, %5, %7\n\tglobal_load_dwordx4 %3, %5, %7 offset:1024\n\t"
                     "global_load_dwordx2 %4, %8, off sc1"
                     : "=&v"(lx[0]), "=&v"(lx[1]), "=&v"(lw[0]), "=&v"(lw[1]), "=&v"(ep) : "v"(lane * 16), "s"(x + (unsigned)(row * DM)), "s"(w_r), "v"(ws + WS_FLAG + F_EPOCH) : "memory");
#define XKEY(r) (((r) >> 1) & 7)
        unsigned xoff[4], woff[2];
        { const unsigned x0_ = (unsigned)(KROW(wave) + (lane >> 3)) * (K * 2) + (unsigned)((lane & 7) ^ (lane >> 4)) * 16u;
          xoff[0] = x0_; xoff[1] = (x0_ ^ 64u) + 7168u; xoff[2] = x0_ + 14336u; xoff[3] = xoff[1] + 14336u;
          woff[1] = (unsigned)((8 * wave + ((lane >> 2) & 7)) * LDW + hp * HD + 32 * (lane >> 5) + 8 * ((lane & 3) ^ (2 * (wave & 1)))) * 2u; woff[0] = woff[1] + 896u; }
        const char* xsp = (const char*)ckv;
        const char* wsp = (const char*)wuk - 896;
        const char* wsv = (const char*)wuv - 2944;
        const unsigned m0b = ldsbase + wave * 4096;
#define KM0(g, w_) asm volatile("s_add_u32 m0, %0, %1" :: "s"(m0b), "i"(((g) & 1) * 65536 + (w_) * 32768) : "memory", "scc")
#define KRX(i) asm volatile("global_load_lds_dwordx4 %0, %1 offset:%2" :: "v"(xoff[i]), "s"(xsp), "i"((i) * 1024) : "memory")
#define KRW(i) asm volatile("global_load_lds_dwordx4 %0, %1 offset:%2 nt" :: "v"(woff[(i) & 1]), "s"((i) < 2 ? wsp : wsv), "i"((i) * 1024) : "memory")
#define STAGE(g) do { KM0(g, 0); asm volatile("s_nop 0"); KRX(0); KRX(1); KRX(2); KRX(3); KM0(g, 1); asm volatile("s_nop 0"); KRW(0); KRW(1); KRW(2); KRW(3); \
                      OADV(xsp, 128); OADV(wsp, 64 * LDW * 2); OADV(wsv, 64 * LDW * 2); } while (0)
#define KST(i)
        const unsigned pfl = ldsbase + L_DUMP + wave * 1024;
        unsigned pxo, pqo, xra, xso_;
#define PFX(g) GLDS_S(pxo, pfxp, pfl - (g) * 128, (g) * 128)
#define PFQ(i) GLDS_S(pqo, pfqp + (size_t)(4 * (i)) * LDW, pfl, 0)
        STAGE(0);
        SB(); ZST(1);
        int w2 = wave; asm volatile("" : "+s"(w2));
        xg = ws + WS_XF + (unsigned)((w2 < 4 ? qbA * 4 + w2 : qbB * 4 + 7 - w2) * 4096);
        wqb = wq + (unsigned)(8 * w2 * LDW + head * HD);
        const u16* pfqp = wq + (unsigned)((128 * jj + 16 * w2) * LDW + head * HD);
        const u16* pfxp = ckv + (unsigned)((KROW(w2) + 8 * (head & 3)) * DC);
        unsigned xa[2][2], wa[2][2];
        const int l15 = lane & 15, q4 = lane >> 4;
#pragma unroll
        for (int s = 0; s < 2; ++s) {
            const int r0 = l15;
            xa[0][s] = ldsbase + (unsigned)((wm * 128 + r0) * 128 + ((((4 * s + q4) ^ XKEY(r0))) << 4));
            wa[0][s] = (ldsbase + 32768u + (unsigned)(q4 * 4096 + wn * 512 + (l15 >> 2) * 64 + (q4 & 1) * 32 + (lane & 3) * 8)) ^ (32u * s);
            xa[1][s] = xa[0][s] + 65536u; wa[1][s] = wa[0][s] + 65536u;
            PIN(xa[0][s]); PIN(xa[1][s]); PIN(wa[0][s]); PIN(wa[1][s]); }
        u64 pf0;
        { u64 pw_ = (u64)(((w2 & 2) ? wuv : wuk) + (unsigned)((128 + 192 * (w2 & 1) + jj) * LDW + hp * HD)), pc_ = (u64)ckv + (unsigned)(b * 8192 + (w2 & 3) * 2048);
          asm volatile("" : "+s"(pw_), "+s"(pc_));
          unsigned o0_ = (unsigned)(lane >> 1) * (8 * LDW * 2) + (lane & 1) * 128u, o1_ = (lane & 15) * 128u; PIN(o0_); PIN(o1_);
          pf0 = (lane < 48 ? pw_ : pc_) + (lane < 48 ? o0_ : o1_); PIN(pf0); }
        asm volatile("" : "+s"(pfqp), "+s"(pfxp), "+s"(xg), "+s"(wqb), "+s"(outp));
        SB();
        asm volatile("s_waitcnt vmcnt(8)" : "+v"(lx[0]), "+v"(lx[1]), "+v"(lw[0]), "+v"(lw[1]), "+v"(ep) :: "memory");
        KST(1); ZST(2);
#define HALF(v_, i) __uint_as_float(((i) & 1) ? (v_) & 0xffff0000u : (v_) << 16)
#define BOTH(v_) f32x2{HALF(v_, 0), HALF(v_, 1)}
        {
            f32x2 v[8];
#pragma unroll
            for (int i = 0; i < 8; ++i) v[i] = BOTH(lx[i >> 2][i & 3]);
            const float pv_ = __uint_as_float(__builtin_amdgcn_readfirstlane(__float_as_uint(v[0][0])));
            f32x2 s2 = f32x2{0.f, 0.f}, q2 = f32x2{0.f, 0.f};
#pragma unroll
            for (int i = 0; i < 8; ++i) { const f32x2 d = v[i] - pv_; s2 += d; q2 += d * d; }
            float s = s2[0] + s2[1], q = q2[0] + q2[1];
            DPP_ADD(s, 0x111, 0xf); DPP_ADD(q, 0x111, 0xf); DPP_ADD(s, 0x112, 0xf); DPP_ADD(q, 0x112, 0xf); DPP_ADD(s, 0x114, 0xf); DPP_ADD(q, 0x114, 0xf);
            DPP_ADD(s, 0x118, 0xf); DPP_ADD(q, 0x118, 0xf); DPP_ADD(s, 0x142, 0xa); DPP_ADD(q, 0x142, 0xa); DPP_ADD(s, 0x143, 0xc); DPP_ADD(q, 0x143, 0xc);
            const float dm = __uint_as_float(__builtin_amdgcn_readlane(__float_as_uint(s), 63)) * (1.f / DM);
            const float var_bf = rbf(fmaxf(fmaf(-dm, dm, __uint_as_float(__builtin_amdgcn_readlane(__float_as_uint(q), 63)) * (1.f / DM)), 0.f));
            const float mean_bf = rbf(pv_ + dm), den = rbf(__builtin_amdgcn_sqrtf(rbf(var_bf + 1e-5f))), rden = __builtin_amdgcn_rcpf(den);
            u32x4 xw[2]; float r2[2] = {0.f, 0.f};
#pragma unroll
            for (int i = 0; i < 8; ++i) {
                const f32x2 c_ = v[i] - mean_bf;
                const unsigned cp = cvtpk(c_[0], c_[1]);
                const f32x2 cb = BOTH(cp);
                f32x2 n_ = cb * rden;
                n_ += (cb - n_ * den) * rden;
                const unsigned np = cvtpk(n_[0], n_[1]);
                const unsigned wp = lw[i >> 2][i & 3];
                r2[i & 1] = __builtin_amdgcn_fdot2_f32_bf16(__builtin_bit_cast(bfx2, np), __builtin_bit_cast(bfx2, wp), r2[i & 1], false);
                xw[i >> 2][i & 3] = np;
            }
            char* lr = lds + 131072 + wave * 2064 + lane * 16;
            *(u32x4*)lr = xw[0]; *(u32x4*)(lr + 1024) = xw[1];
            s = r2[0] + r2[1];
            DPP_ADD(s, 0x111, 0xf); DPP_ADD(s, 0x112, 0xf); DPP_ADD(s, 0x114, 0xf); DPP_ADD(s, 0x118, 0xf); DPP_ADD(s, 0x142, 0xa); DPP_ADD(s, 0x143, 0xc);
            if (lane == 63) *(float*)(lds + 131072 + wave * 2064 + 2048) = rbf(s);
        }
        SB(); ZST(3);
        STAGE(1);
        asm volatile("s_bitcmp0_b32 %2, 2\n\ts_cbranch_scc1 1f\n\ts_mov_b32 m0, %1\n\ts_nop 0\n\tglobal_load_lds_dword %0, off\n1:" :: "v"(pf0), "s"(pfl), "s"(wave) : "memory", "scc");
        tag = (((u64)(unsigned)__builtin_amdgcn_readfirstlane(ep[1]) << 32) | (unsigned)__builtin_amdgcn_readfirstlane(ep[0])) + 0x9E3779B97F4A7C15ull;
        float rr;
        const unsigned l16 = (unsigned)lane * 16u;
        u32x2 xi_[4];
        typedef __attribute__((address_space(3))) u32x2 lds_u32x2; typedef __attribute__((address_space(3))) float lds_f32;
#define XI_RD(i) xi_[i] = *(lds_u32x2*)(uintptr_t)(xra + ((i) & 1) * 16 + ((i) >> 1) * 1024)
#define XI_ST(i) do { const u32x4 p_ = u32x4{xi_[2 * (i)][0], xi_[2 * (i)][1], xi_[2 * (i) + 1][0], xi_[2 * (i) + 1][1]};               \
            asm volatile("global_store_dwordx4 %0, %1, %2 sc1" :: "v"(xso_), "v"(p_), "s"(ws + WS_XF + (unsigned)(wave * 262144 + (b >> 1) * 2048 + (b & 1) * 128 + (i) * 2097152)) : "memory"); } while (0)
        SB(); ZST(4);
        asm volatile("s_waitcnt vmcnt(%1)\n\ts_bitcmp1_b32 %0, 2\n\ts_cbranch_scc1 1f\n\ts_waitcnt vmcnt(8)\n1:\n\ts_barrier" :: "s"(wave), "i"(9) : "memory", "scc");
        KST(2); ZST(6);
        f32x4 a16[8][4];
        bf16x8 xf[2][4];
        bf16x4 wl[4], wh[4];
#define KXR(kt, q, mb) xf[(q) & 1][mb] = lds_rd(xa[(kt) & 1][(q) >> 1] + (unsigned)((((q) & 1) * 4 + (mb)) * 2048))
#define KWL(kt, s32, nb) wl[nb] = tr_rd(wa[(kt) & 1][(nb) & 1] + (unsigned)((s32) * 16384 + ((nb) >> 1) * 2048))
#define KWH(kt, s32, nb) wh[nb] = tr_rd(wa[(kt) & 1][(nb) & 1] + (unsigned)((s32) * 16384 + ((nb) >> 1) * 2048 + 256))
#define KMF(kt, q, g) do { const bf16x8 w_ = CAT8(wl[(g) >> 2], wh[(g) >> 2]); constexpr int m_ = ((q) & 1) * 4 + ((g) & 3);               \
            if ((kt) == 0 && (q) < 2) { if ((g) >= 8) MFA16Z(a16[m_][(g) >> 2], xf[(q) & 1][(g) & 3], w_); else MFA16Z(a16[m_][(g) >> 2], w_, xf[(q) & 1][(g) & 3]); } \
            else                      { if ((g) >= 8) MFA16(a16[m_][(g) >> 2], xf[(q) & 1][(g) & 3], w_);  else MFA16(a16[m_][(g) >> 2], w_, xf[(q) & 1][(g) & 3]); } } while (0)
        u64 lnf;
#define LNFP (ws + WS_FLAG + F_LN + (wave * 32 + (lane & 31)) * 8)
#define KRQ(i) do { GLDS_S(qwo, wqb + (size_t)(((i) >> 1) * 64) * LDW, ldsbase + wave * 2048 + ((i) >> 1) * 16384 + ((i) & 1) * 896, ((i) & 1) * 128); } while (0)
        constexpr int KRS = 3, KPFD = 3;
#define KG(kt, q, g) do { constexpr int nk_ = (kt) + ((q) == 3), nq_ = ((q) + 1) & 3;                                                     \
            constexpr int xi = ((q) & 1) ? ((g) == 1 ? 0 : (g) == 2 ? 1 : (g) == 5 ? 2 : (g) == 6 ? 3 : -1) : (((g) & 1) && (g) < 8 ? (g) >> 1 : -1); \
            constexpr int ri = (q) == 3 ? ((g) == 9 ? 0 : (g) == 10 ? 1 : (g) == 13 ? 2 : (g) == 14 ? 3 : -1) : (((g) & 1) && (g) > 8 ? ((g) - 9) >> 1 : -1); \
            KMF(kt, q, g); SB();                                                                                                        \
            if (xi >= 0 && nk_ < 8) KXR(nk_, nq_, xi & 3);                                                                              \
            if (((q) & 1) && nk_ < 8) { if (((g) & 3) == 3) KWL(nk_, nq_ >> 1, (g) >> 2); if (((g) & 3) == 0 && (g) > 0) KWH(nk_, nq_ >> 1, (((g) >> 2) + 3) & 3); } \
            if (!((q) & 1) && (g) == 0 && (kt) + (q) > 0) KWH(kt, (q) >> 1, 3);                                                          \
            { constexpr int G = (q) == 3 ? (g) : (q) == 0 ? 16 + (g) : -9, T = (kt) + ((q) == 3 ? 2 : 1);          \
              if (T >= 2 && T < 8) { if (G == 0) KM0(T, 0); if (G == 4 * KRS) KM0(T, 1);                                                 \
                  if (G >= 1 && (G - 1) % KRS == 0 && (G - 1) / KRS < 8) { if ((G - 1) / KRS < 4) KRX(((G - 1) / KRS) & 3); else KRW(((G - 1) / KRS) & 3); } \
                  if (G == 29) OADV(xsp, 128); if (G == 30) OADV(wsp, 64 * LDW * 2); if (G == 31) OADV(wsv, 64 * LDW * 2); } }                                                 \
            if ((q) == 1 && (kt) + KPFD < 8) { if ((kt) >= 1 && (g) == 13) PFX((kt) + KPFD); }                \
            if ((q) == 2 && (kt) == 6 && (g) == 9) { qwo = QWOFF(); PIN(qwo); }                                                         \
            if ((q) == 1 && (kt) == 7) { if ((g) == 9) asm volatile("global_load_dwordx4 %0, %1, %2 sc1" : "=v"(rv) : "v"(xvo), "s"(ws + WS_R + wave * 1024) : "memory"); \
                                         if ((g) == 10) QXL1(0); if ((g) == 13) QXL1(1); if ((g) == 14) QXL1(2); }                      \
            if ((q) == 2 && (kt) == 7 && (g) == 2) QXL1(3);                                                                             \
            if ((q) == 2 && (kt) == 7 && (g) == 9) { int w_ = wave; asm volatile("" : "+s"(w_));                                        \
                sb_[0] = ws + WS_KV + (unsigned)(hp * HSTR + ((w_ >> 2) * 2 + (jj >> 2)) * 65536 + (jj & 3) * 8192 + (w_ & 3) * 2048); asm volatile("" : "+s"(sb_[0])); } \
            if ((q) == 2 && (kt) == 7 && (g) >= 11 && (g) < 14) { sb_[((g) - 10) & 3] = sb_[0] + ((g) - 10) * 262144; asm volatile("" : "+s"(sb_[((g) - 10) & 3])); } \
            if ((q) == 2 && (kt) == 7 && (g) == 14) { qa = ldsbase + (unsigned)((q4 >> 1) * 4096 + (4 * (q4 & 1) + (l15 >> 2)) * 64 + (q4 & 1) * 32 + (lane & 3) * 8); PIN(qa); } \
            if ((q) == 2 && (kt) == 7 && (g) == 15) { qa2 = qa ^ 32u; PIN(qa2); }                                                                              \
            if ((q) == 3 && (kt) == 6 && ri >= 0) KRQ(ri & 3);                                                                          \
            if ((q) == 0 && (kt) == 7 && ri >= 0 && 4 + ri < 2 * QWD) KRQ(4 + (ri & 3));                                                                    \
            if ((q) == 0 && (kt) == 0) { unsigned l_ = lane;                                                                            \
                if ((g) == 9)  { PIN(l_); pxo = (l_ >> 3) * (DC * 2) + (l_ & 7) * 16; PIN(pxo); }                                       \
                if ((g) == 11) { PIN(l_); pqo = (l_ >> 4) * (LDW * 2) + (l_ & 15) * 16; PIN(pqo); }                                     \
                if ((g) == 13) { PIN(l_); xso_ = (l_ >> 3) * 256 + (l_ & 7) * 16; PIN(xso_); }                                          \
                if ((g) == 14) { PIN(l_); xra = (l_ & 7) * 2064 + (ldsbase + 131072 + wave * 128); PIN(xra); }                          \
                if ((g) == 15) { PIN(l_); xra += (l_ >> 4) * 32 + (l_ & 8); PIN(xra); } }                                               \
            if ((q) == 0 && (kt) == 1) { if ((g) == 9 || (g) == 11) { XI_RD((g) - 9); XI_RD((g) - 8); }                                 \
                if ((g) == 12) rr = *(lds_f32*)(uintptr_t)(ldsbase + 131072 + wave * 2064 + 2048); }                                    \
            if ((q) == 1 && (kt) == 1) { if ((g) == 9) XI_ST(0); if ((g) == 10) { KEEP(xi_[0]); KEEP(xi_[1]); } if ((g) == 14) XI_ST(1); if ((g) == 15) { KEEP(xi_[2]); KEEP(xi_[3]); } \
                if ((g) == 0) asm volatile("global_store_dword %0, %1, %2 sc1" :: "v"(0u), "v"(rr), "s"((float*)(ws + WS_R) + row) : "memory"); }     \
            if ((q) == 1 && ((kt) == 3 || (kt) == 4)) { if ((g) == 9) PFQ((2 * (kt) - 6) & 3); if ((g) == 11) PFQ((2 * (kt) - 5) & 3); }                                                    \
            if ((q) == 2 && (kt) == 5 && (g) == 12) FLAG_GET(lnf, LNFP);                                                                \
            SB(); } while (0)
#define KQ(kt, q) do { KG(kt, q, 0); KG(kt, q, 1); KG(kt, q, 2); KG(kt, q, 3); KG(kt, q, 4); KG(kt, q, 5); KG(kt, q, 6); KG(kt, q, 7);    \
                       KG(kt, q, 8); KG(kt, q, 9); KG(kt, q, 10); KG(kt, q, 11); KG(kt, q, 12); KG(kt, q, 13); KG(kt, q, 14); KG(kt, q, 15); } while (0)
#define KEXT(s) (((s) == 1 ? 3 : (s) == 5 ? 1 : 0) + ((s) >= 1 && (s) + KPFD < 8) + ((s) == 3 || (s) == 4 ? 2 : 0))
#define KSTEP(kt) do {                                                                                                                  \
        KQ(kt, 0); KQ(kt, 1); KQ(kt, 2);                                                                                                \
        if ((kt) == 6) FLAG_WAIT(lnf, LNFP, tag, 0);                                   \
                       \
        if ((kt) == 0) asm volatile("s_waitcnt vmcnt(%1) lgkmcnt(0)\n\ts_bitcmp1_b32 %0, 2\n\ts_cbranch_scc1 1f\n\ts_waitcnt vmcnt(%2)\n1:\n\ts_barrier" :: "s"(wave), "i"(KEXT(0) + 1), "i"(KEXT(0)) : "memory", "scc"); \
        else asm volatile("s_waitcnt vmcnt(%0) lgkmcnt(0)\n\ts_barrier" :: "i"(KEXT(kt)) : "memory");                                        \
        KST((kt) + 3);                                                                                                                  \
        if ((kt) == 2) FLAG_PUT(ws + WS_FLAG + F_LN + b * 8, tag, wave == 0);             \
        if ((kt) == 6) FLAG_PUT(ws + WS_FLAG + F_EPOCH, tag - 0x9E3779B97F4A7C15ull + 1, b == 0 && wave == 0);     \
        SB();                                                                                                                           \
        KQ(kt, 3);                                                                                                                      \
    } while (0)
        KXR(0, 0, 0); KXR(0, 0, 1); KWL(0, 0, 0); KWH(0, 0, 0); KXR(0, 0, 2); KXR(0, 0, 3);
        KWL(0, 0, 1); KWH(0, 0, 1); KWL(0, 0, 2); KWH(0, 0, 2); KWL(0, 0, 3); KWH(0, 0, 3);
        SB();
        KSTEP(0); ZST(7); KSTEP(1); KSTEP(2); KSTEP(3); KSTEP(4); KSTEP(5); KSTEP(6);
        xvo = l16;
        KQ(7, 0); KQ(7, 1); KQ(7, 2);
        if (wave < 4) {
            int t2_ = threadIdx.x; asm volatile("" : "+v"(t2_));
            unsigned l7_ = t2_ & 7, c7_ = jj >= 3 ? 7 : 8, b0_ = (unsigned)(((jj + 7) & 7) * 128 + head * 4 + (wave & 3)), b7_ = 3584u + head * 4 + (wave & 3);
            u64 pg_ = (u64)((wave & 1) ? w_up : w_gate), pd_ = (u64)w_down - 1792ull * 8192, po_ = (u64)wo - 2688ull * 8192;
            asm volatile("" : "+s"(c7_), "+s"(b0_), "+s"(b7_), "+s"(pg_), "+s"(pd_), "+s"(po_));
            const unsigned lt_ = l7_ < (unsigned)jj ? l7_ : 0;
            unsigned n_ = b0_ + (13u - lt_) * lt_ * 64u; n_ = l7_ == c7_ ? b7_ : n_;
            u64 p_ = n_ < 2688u ? pd_ : po_; p_ = n_ < 1792u ? pg_ : p_; n_ = n_ < 1792u ? n_ >> 1 : n_;
            *(u64*)(lds + L_PFT + t2_ * 8) = p_ + (u64)n_ * 8192u;
        }
        SB();
        asm volatile("s_waitcnt vmcnt(%0)\n\ts_barrier" :: "i"(2 * QWD + 1) : "memory");
        SB();
#define PKCV(i) do { const f32x4 a_ = ((i) & 2) ? a16[2 * ((i) >> 2)][(i) & 3] : a16[2 * ((i) >> 2) + ((i) & 1)][0];                       \
                     const f32x4 b_ = ((i) & 2) ? a16[2 * ((i) >> 2) + 1][(i) & 3] : a16[2 * ((i) >> 2) + ((i) & 1)][1];                   \
                     pk[i] = u32x4{cvtpk(a_[0], a_[1]), cvtpk(a_[2], a_[3]), cvtpk(b_[0], b_[1]), cvtpk(b_[2], b_[3])}; PIN(pk[i]); } while (0)
#define KL(i) do { KMF(7, 3, 2 * (i)); SB(); if ((i) >= 2) QXL1(2 * (i)); SB(); KMF(7, 3, 2 * (i) + 1); SB(); PKCV(i); if ((i) >= 2) QXL1(2 * (i) + 1); SB(); } while (0)
        KL(0); KL(1); KL(2); KL(3); KL(4); KL(5); KL(6); KL(7);
        KST(10);
        QRL(0, 0); QRH(0, 0); QRL(0, 1); QRH(0, 1); QRL(0, 2); QRH(0, 2); QRL(0, 3); QRH(0, 3);
        SB(); asm volatile("s_nop 3"); SB();
#pragma unroll
        for (int i = 8; i < 16; ++i) PKCV(i);
        SB();
        KST(11); ZST_OUT();
#undef PKCV
#undef KSTEP
#undef PFQ
#undef XKEY
    }
    PH_STAMP(1);
    const char* kvg = ws + WS_KV + (size_t)head * HSTR;
    const unsigned svv = xvo + 32768u;
    u64 pft; bf16x8 ones; unsigned ak, av;
    const int NA = qbA + 1;
    {
        constexpr int LDW = NH * HD, STG = 16384;
        int tid = threadIdx.x; asm volatile("" : "+v"(tid));
        const int lane = tid & 63;
        const unsigned woff = qwo, xvo2 = xvo + 262144u;
        f32x4 acc[2][8];
#define KVST(i) asm volatile("global_store_dwordx4 %0, %1, %2 offset:%3 sc1" :: "v"(((i) & 2) ? svv : xvo), "v"(pk[i]), "s"(sb_[(i) >> 2]), "i"(((i) & 1) * 1024) : "memory")
        u64 kvf = 0;
#define QKV(kt, f) do { constexpr int g_ = q_kv(kt, f), p_ = g_ >> 2; if (g_ >= 0) {                                                       \
            if ((g_ & 3) == 0) asm volatile("s_mov_b32 m0, %0\n\ts_nop 0" :: "s"(ldsbase + (p_ == 1 ? 0 : 65536 + p_ * 16384) + wave * 4096) : "memory"); \
            asm volatile("global_load_lds_dwordx4 %0, %1 offset:%2 sc1" :: "v"(xvo), "s"(kvg + p_ * 32768 + wave * 4096), "i"((g_ & 3) * 1024) : "memory"); } } while (0)
#define KVFP (ws + WS_FLAG + ((lane & 8) ? F_TOP : F_KV + 2048 + (head * 8 + (lane & 7)) * 8))
        const bool top2 = b == 56;
        unsigned* tw = (unsigned*)(lds + 131072 + 2048);
#define QMF(a, kt, f) do { if ((kt) == 0 && (f) < 8) MFA16Z(acc[a][(f) & 7], w_, xr[0][a][0]); else MFA16(acc[a][QFM(kt, f) & 7], w_, xr[(kt) & 3][a][QFM(kt, f) >> 3]); } while (0)
        constexpr float qs = 0.08838834764831845f * 1.4426950408889634f;
        u32x4 qw_[8];
        float qm_[2][2], qsv = qs; asm volatile("" : "+s"(qsv));
#define QWI(n) (2 * ((n) >> 3) + (((n) >> 1) & 1))
#define QWW(n) (2 * (((n) >> 2) & 1) + ((n) & 1))
#define QCN(n) do { if ((n) >= 0 && (n) < 32) { _Pragma("unroll") for (int e_ = 0; e_ < 2; ++e_)                                           \
                asm volatile("v_mul_f32 %0, %1, %2" : "=v"(qm_[(n) & 1][e_]) : "s"(qsv), "v"(acc[QWI(n) & 1][(QWI(n) & 6) + (QWW(n) >> 1)][2 * (QWW(n) & 1) + e_])); } \
            if ((n) >= 1 && (n) <= 32) { qw_[QWI((n) - 1) & 7][QWW((n) - 1)] = cvtpk(qm_[((n) - 1) & 1][0], qm_[((n) - 1) & 1][1]); KEEP(qw_[QWI((n) - 1) & 7][QWW((n) - 1)]); } } while (0)
#define QCG(kt, g) do { if ((kt) == 15) QCN((g) - 10); } while (0)
#define ZP0 238
#define QPAIR(kt, f, A, B) do { const bf16x8 w_ = CAT8(ql[(f) & 7], qh[(f) & 7]); constexpr int s_ = (kt) + (((f) + 4) >> 4), m_ = ((f) + 4) & 15;  \
            QMF(0, kt, f); SB(); if (s_ < 16) QRL(s_, m_); A; QKV(kt, f); QCG(kt, 2 * (f)); SB();                                      \
            QMF(1, kt, f); SB(); if (s_ < 16) QRH(s_, m_); B; QCG(kt, 2 * (f) + 1);                                                                             \
            { constexpr int z_ = (kt) * 16 + (f) - ZP0;                    \
                                                                    \
              if (z_ == 8) { pft = *(const u64*)(lds + L_PFT + (tid & 255) * 8); }                                                       \
              if (z_ == 15) { av = ldsbase + xvo; ak = av + 65536u; PIN(av); PIN(ak); }                                                   \
              if (z_ == 16) { const unsigned o1 = (lane & 3) ? 0u : 0x3f803f80u; ones = __builtin_bit_cast(bf16x8, u32x4{o1, o1, o1, o1}); PIN(ones); }     \
              if (z_ == 14) PIN(pft); } SB(); if (((f) & 1) && s_ < 16) { __builtin_amdgcn_s_waitcnt(0xC47F); SB(); } } while (0)
        const char* xsp = xg + 3 * 262144;
#define QX(r, rb, ks, v_) asm volatile("global_load_dwordx4 %0, %1, %2 offset:%3 sc1" : "+v"(xr[r][rb][ks]) : "v"(v_), "s"(xsp), "i"((rb) * 2048 + (ks) * 1024) : "memory")
        const unsigned woff1 = woff + 1024u, woff2 = woff + 128u;
        const char* wsp = (const char*)(wqb + (size_t)QWD * 64 * LDW) - 1024;
        const unsigned m0b = ldsbase + wave * 2048;
#define QM0(r) asm volatile("s_add_u32 m0, %0, %1" :: "s"(m0b), "i"((r) * STG) : "memory", "scc")
#define QW1(v_, imm_) asm volatile("global_load_lds_dwordx4 %0, %1 offset:%2" :: "v"(v_), "s"(wsp), "i"(imm_) : "memory")
#define QST(i)
#define QSTEP(kt) do { constexpr int r_ = (kt) & 3;                                                                                     \
        QPAIR(kt, 0, if (q_xb(kt)) QX((r_ + 3) & 3, 0, 1, xvo), );                                                                      \
        QPAIR(kt, 1, , );                                                                                                               \
        QPAIR(kt, 2, if (q_xb(kt)) QX((r_ + 3) & 3, 1, 1, xvo), );                                                                      \
        QPAIR(kt, 3, if ((kt) == 12) FLAG_GET(kvf, KVFP), );                                                                            \
        if ((kt) == 0 && __builtin_expect(top2, 0)) {                                                                                                        \
            unsigned t1 = tkey(rv[0], tid * 4), t2 = 0;                                                                                 \
            _Pragma("unroll") for (int j = 1; j < 4; ++j) merge_top2(t1, t2, tkey(rv[j], tid * 4 + j), 0);                              \
            DPP_TOP2(t1, t2, 0x111, 0xf); DPP_TOP2(t1, t2, 0x112, 0xf); DPP_TOP2(t1, t2, 0x114, 0xf); DPP_TOP2(t1, t2, 0x118, 0xf);     \
            DPP_TOP2(t1, t2, 0x142, 0xa); DPP_TOP2(t1, t2, 0x143, 0xc);                                                                 \
            if (lane == 63) { tw[wave * 2] = t1; tw[wave * 2 + 1] = t2; } SB(); }                                                       \
        QPAIR(kt, 4, if ((QS1 >> (kt)) & 1) KVST(q_s0(kt) & 15), if ((QS1 >> (kt)) & 1) KEEP(pk[q_s0(kt) & 15]));                       \
        QPAIR(kt, 5, , );                                                                                                               \
        QPAIR(kt, 6, , );                                                                                                               \
        QPAIR(kt, 7, , );                                                                                                               \
        QPAIR(kt, 8, if (q_xa(kt)) QX(r_, 0, 0, xvo2), );                                                                               \
        QPAIR(kt, 9, , );                                                                                                               \
        QPAIR(kt, 10, if (q_xa(kt)) QX(r_, 1, 0, xvo2), if ((kt) <= 11) OADV(xsp, 262144));                                             \
        QPAIR(kt, 11, if ((kt) + QWD < 16) QM0((r_ + QWD) & 3), );                                                                      \
        if ((kt) < 15) {                              \
            if (QWD == 4 || (kt) == 0 || (kt) >= 13) asm volatile("s_waitcnt vmcnt(%0) lgkmcnt(0)\n\ts_barrier" :: "i"(q_wait(kt)) : "memory"); \
            else asm volatile("s_waitcnt vmcnt(%0)\n\ts_barrier" :: "i"(q_wait(kt)) : "memory");                                        \
            if ((kt) == 13) { PIN(kvf); const unsigned sh_ = (lane & 8) ? 22u : 0u; const u64 ex_ = (tag << sh_) >> sh_;     \
                if (__builtin_expect(__builtin_amdgcn_ballot_w64((kvf >> sh_) != ex_) != 0, 0)) { unsigned c_; u64 t_;                    \
                    asm volatile("s_mov_b32 %1, 0x40000\n1:\n\ts_sleep 2\n\tglobal_load_dwordx2 %0, %3, off sc1\n\ts_waitcnt vmcnt(0)\n\tv_lshrrev_b64 %2, %5, %0\n\t" \
                                 "v_cmp_ne_u64 vcc, %4, %2\n\ts_cmp_eq_u64 vcc, 0\n\ts_cbranch_scc1 2f\n\ts_sub_u32 %1, %1, 1\n\ts_cbranch_scc0 1b\n2:" \
                                 : "+v"(kvf), "=&s"(c_), "=&v"(t_) : "v"(KVFP), "v"(ex_), "v"(sh_) : "memory", "vcc", "scc"); }           \
                const unsigned r_ = __builtin_amdgcn_readlane((unsigned)kvf, 8); ri1 = r_ & 2047u; ri2 = (r_ >> 11) & 2047u; }           \
            QST((kt) + 1); SB(); }                                                                                                      \
        QPAIR(kt, 12, if ((kt) + QWD < 16) QW1(woff1, 0), );                                                                            \
        QPAIR(kt, 13, , );                                                                                                              \
        QPAIR(kt, 14, if ((kt) + QWD < 16) QW1(woff2, 1024), if ((kt) + QWD < 15) OADV(wsp, 64 * LDW * 2));                             \
        if ((kt) == q_fl()) FLAG_PUT(ws + WS_FLAG + F_KV + 2048 + (hp * 8 + jj) * 8, tag, wave == 0);     \
        QPAIR(kt, 15, , );                                                                                                              \
        if ((kt) == 0 && __builtin_expect(top2, 0)) {                                                                                                        \
            unsigned t1 = tw[0], t2 = tw[1];                                                                                            \
            _Pragma("unroll") for (int w = 1; w < 8; ++w) merge_top2(t1, t2, tw[w * 2], tw[w * 2 + 1]);                                 \
            FLAG_PUT(ws + WS_FLAG + F_TOP, (tag << 22) | (unsigned)__builtin_amdgcn_readfirstlane((~t1 & 2047u) | (~t2 & 2047u) << 11), wave == 0); \
            } \
        SB();                                                                                                                           \
    } while (0)
        asm volatile("s_waitcnt vmcnt(12)" : "+v"(rv) :: "memory");
        SB();
        QST(0);
        KST(12);
        QSTEP(0); QSTEP(1); QSTEP(2); QSTEP(3); QSTEP(4); QSTEP(5); QSTEP(6); QSTEP(7);
        QSTEP(8); QSTEP(9); QSTEP(10); QSTEP(11); QSTEP(12); QSTEP(13); QSTEP(14); QSTEP(15);
        QCN(22); QCN(23); QCN(24); QCN(25); QCN(26); QCN(27); QST(16); SB(); QCN(28); QCN(29); QCN(30); QCN(31); QCN(32);
#pragma unroll
        for (int i = 0; i < 8; ++i) { qf[i] = __builtin_bit_cast(bf16x8, qw_[i]); PIN(qf[i]); }
#undef QSTEP
    }
    PH_STAMP(2);
    const int kz = xcd & 1, kp = xcd >> 1, nt = kz * 4 + (idx >> 3), tile = jj * 8 + nt;
#define K64(s) ((head * 2) ^ (s))
#define OWOFF() ({ unsigned l_ = threadIdx.x & 63; asm volatile("" : "+v"(l_)); (unsigned)(((l_ >> 2) & 7) * DM + 32 * (l_ >> 5) + 8 * ((l_ & 3) ^ ((l_ >> 4) & 1))) * 2u; })
    const int n0 = (nt ^ 2) * 128, dn1 = (((nt ^ 3) - (nt ^ 2)) * 128 + 64) * 2, ntc = nt ^ 2 ^ (kp >> 1);
    const u16* wob = wo + (size_t)(8 * wave) * DM + n0;
    u64 cxf = tag + 1;
#define CFB (ws + WS_FLAG + F_CTX)
#define CFO() ({ unsigned l_ = threadIdx.x & 15, s0_ = (unsigned)((qb * 4 + qw) * 32 + head), s1_ = (unsigned)((qbB * 4 + 3 - (wave & 3)) * 32 + (head ^ 8)); \
                 asm volatile("" : "+v"(l_), "+s"(s0_), "+s"(s1_)); (l_ < 8 ? l_ ^ s0_ : s1_) * CFS; })
    u32x4 pc[8];
#define EAR_(v_, b_, pol_) "s_add_u32 m0, m0, 0x400\n\ts_nop 0\n\tglobal_load_lds_dwordx4 " v_ ", " b_ pol_ "\n\t"
#define EALD(h_) do { unsigned l_ = threadIdx.x & 63; asm volatile("" : "+v"(l_)); const unsigned lx_ = ((l_ >> 3) * DM + 8 * ((l_ & 7) ^ (l_ >> 3))) * 2u; \
        const int rb_ = ((h_) ? qbB * 8 + qw * 2 : qbA * 8 + (wave & 3) * 2) + (kp & 1);                                                  \
        asm volatile("s_mov_b32 m0, %5\n\t" EAR_("%0", "%3", "") EAR_("%1", "%3", "") EAR_("%2", "%4", " sc1") "global_load_lds_dwordx4 %2, %4 offset:1024 sc1" \
                     :: "v"(lx_), "v"(lx_ + 8 * DM * 2), "v"(l_ * 16u), "s"(x + (size_t)rb_ * 16 * DM + ntc * 128 + (kp >> 1) * 64),              \
                        "s"(ws + WS_XF + (size_t)(2 * ntc + (kp >> 1)) * 262144 + rb_ * 2048),                                             \
                        "s"(ldsbase + 131072 - 1024 + (wave & 3) * 8192 + (h_) * 4096) : "memory", "scc"); } while (0)
    const unsigned i1 = (((kz ? F1S : F1F) >> (4 * jj)) & 15u) * 16u + (kp * 4 + (idx >> 3));
    const unsigned i2 = (((kz ? F2S : F2F) >> (4 * jj)) & 15u) * 16u + (kp * 4 + (idx >> 3));
    const unsigned nf1 = i1 / 112u, nf2 = i2 / 112u;
    const unsigned k1 = i1 / 14u;
    unsigned ypub = 1;
    {
        int tid = threadIdx.x; asm volatile("" : "+v"(tid));
        const int lane = tid & 63;
        const unsigned l16 = (unsigned)lane * 16u;
#define EX(x) __builtin_amdgcn_exp2f(x)
#define TBAR() do { SB(); __builtin_amdgcn_s_waitcnt(0x0070); asm volatile("s_barrier" ::: "memory"); } while (0)
#define BRQ(t, r) do { constexpr int g_ = (r) >> 2; const int tt_ = (t) + (g_ == 2 ? 1 : 2);                                               \
            const unsigned o_ = (unsigned)((wave & 3) * 4096 + (g_ == 1 || g_ == 2 ? 16384 : 0));                                          \
            if (((r) & 3) == 0) GLDS_C(l16, kvg + (size_t)tt_ * 65536 + (g_ >= 2 ? 32768 : 0) + o_, ldsbase + (g_ >= 2 ? 0 : 65536) + (tt_ & 1) * 32768 + o_, 0); \
            else asm volatile("global_load_lds_dwordx4 %0, %1 offset:%2 sc1" :: "v"(l16), "s"(kvg + (size_t)tt_ * 65536 + (g_ >= 2 ? 32768 : 0) + o_), "i"(((r) & 3) * 1024) : "memory"); } while (0)
#define BRQ4(t, g) do { BRQ(t, 4 * (g)); BRQ(t, 4 * (g) + 1); BRQ(t, 4 * (g) + 2); BRQ(t, 4 * (g) + 3); } while (0)
#define BRQ2C(t, r) do { constexpr int g_ = (r) >> 2; const int tt_ = (t) + (g_ == 2 ? 1 : 2);                                             \
            const unsigned o_ = (unsigned)((wave & 3) * 4096 + (g_ == 1 || g_ == 2 ? 16384 : 0));                                          \
            asm volatile("s_cmp_eq_u32 %5, 0\n\ts_cbranch_scc1 1f\n\ts_mov_b32 m0, %2\n\ts_nop 0\n\tglobal_load_lds_dwordx4 %0, %1 offset:%3 sc1\n\t"  \
                         "global_load_lds_dwordx4 %0, %1 offset:%4 sc1\n1:"                                                               \
                         :: "v"(l16), "s"(kvg + (size_t)tt_ * 65536 + (g_ >= 2 ? 32768 : 0) + o_), "s"(ldsbase + (g_ >= 2 ? 0 : 65536) + (tt_ & 1) * 32768 + o_), \
                            "i"(((r) & 3) * 1024), "i"(((r) & 3) * 1024 + 1024), "s"(isb) : "memory", "scc"); } while (0)
        const unsigned isb = wave >> 2;
        u64 kf2 = tag;
        const char* kfb = ws + WS_FLAG + F_KV + head * 64;
#define KCHK() do { PIN(kf2); if (__builtin_expect((__builtin_amdgcn_ballot_w64(kf2 != tag) | (u64)((33u >> t) & 1u)) != 0, 0)) { \
            unsigned c_, a_;                                                                                                              \
            FLAG_PUTZ(ws + WS_FLAG + F_KV + (hp * 8 + jj) * 8, tag, (unsigned)(wave ^ 4) | (unsigned)t);                                  \
            asm volatile("s_cmp_lt_u32 %6, 5\n\ts_cbranch_scc1 3f\n\tv_lshrrev_b32 %2, 1, %5\n\tv_and_b32 %2, 56, %2\n\ts_mov_b32 %1, 0x40000\n" \
                         "1:\n\tglobal_load_dwordx2 %0, %2, %3 sc1\n\ts_cmp_eq_u32 %6, 5\n\ts_cbranch_scc1 3f\n\ts_waitcnt vmcnt(0)\n\tv_cmp_ne_u64 vcc, %4, %0\n\t" \
                         "s_cmp_eq_u64 vcc, 0\n\ts_cbranch_scc1 3f\n\ts_sleep 2\n\ts_sub_u32 %1, %1, 1\n\ts_cbranch_scc0 1b\n"           \
                         "3:" : "+v"(kf2), "=&s"(c_), "=&v"(a_) : "s"(kfb), "s"(tag), "v"(l16), "s"(t) : "memory", "vcc", "scc"); } SB(); } while (0)
#define PFW1(i_) do { unsigned a_; const u64 p_ = ((u64)(unsigned)__builtin_amdgcn_readlane((int)(pft >> 32), i_) << 32) | (unsigned)__builtin_amdgcn_readlane((int)pft, i_); \
            asm volatile("v_lshlrev_b32 %0, 3, %2\n\ts_mov_b32 m0, %3\n\ts_nop 2\n\tglobal_load_lds_dword %0, %1"                        \
                         : "=&v"(a_) : "s"(p_), "v"(l16), "s"(ldsbase + 131072 + (wave & 3) * 8192) : "memory"); } while (0)
#define ONCE2(n_) int CAT(n_once, __LINE__) = (int)(n_); asm volatile("" : "+s"(CAT(n_once, __LINE__))); _Pragma("unroll 1") for (int z_ = 0; z_ < CAT(n_once, __LINE__); ++z_)
#define AST(i)
#define XST(i)
#define TS0()
#define TS1()
        AST(0);
        asm volatile("s_bitcmp0_b32 %0, 2\n\ts_cbranch_scc1 1f\n\ts_setprio 1\n1:" :: "s"(wave) : "scc");
        {
            f32x4 o[8][2], ls[2], ng[2], S[2][4];
            bf16x8 pf[2][2], fr[16];
            float e[16];
            constexpr bool ZC = false;
            constexpr bool HOT = false;
            const unsigned lb16 = ldsbase + l16;
            int t = 0;
#define KOFF(f) ((((f) & 3) * 2 + (((f) >> 2) & 1)) * 1024)
#define RD(kb, n) do { constexpr int k_ = ((kb) + ((n) >> 4)) & 3, n_ = (n) & 15;                                                          \
            fr[n_] = n_ < 8 ? lds_rd(av + (unsigned)(((k_ + 3) & 3) * 8192 + n_ * 1024)) : lds_rd(ak + (unsigned)(((k_ + 1) & 3) * 8192 + KOFF(n_ & 7))); } while (0)
#define MX(p, n, a) do { if (ZC) MFA16Y(o[n][a], fr[n], pf[(p) ^ 1][a]); else MFA16(o[n][a], fr[n], pf[(p) ^ 1][a]); } while (0)
#define MY(p, n, h) do { constexpr int a_ = ((n) >> 1) & 1, t_ = 2 * ((n) & 1) + (h);                                                      \
                         if (t_) MFA16(S[(p) ^ 1][(((n) >> 1) & 2) + a_], fr[((n) & 12) + t_], qf[2 * t_ + a_]);                           \
                         else MFA16C(S[(p) ^ 1][(((n) >> 1) & 2) + a_], fr[(n) & 12], qf[a_], ng[a_]); } while (0)
#define GX(p, n, F1, F2) do { MX(p, n, 0); SB(); F1; SB(); MX(p, n, 1); SB(); F2; SB(); } while (0)
#define GY(p, n, F1, F2) do { MY(p, n, 0); SB(); F1; SB(); MY(p, n, 1); SB(); F2; SB(); } while (0)
#define ML(p, a) do { if (ZC) MFA16Y(ls[a], ones, pf[(p) ^ 1][a]); else MFA16(ls[a], ones, pf[(p) ^ 1][a]); } while (0)
#define GL(p, F1, F2) do { ML(p, 0); SB(); F1; SB(); ML(p, 1); SB(); F2; SB(); } while (0)
#define SV(p, i) S[p][(((i) >> 1) & 2) + ((i) >> 3)][(i) & 3]
#define E1(p, i) do { e[i] = EX(SV(p, i)); if (HOT) KEEP(e[i]); else PIN(e[i]); } while (0)
#define C1(p, j) do { u32x4 w_ = __builtin_bit_cast(u32x4, pf[p][(j) >> 2]); w_[(j) & 3] = cvtpk(e[2 * (j)], e[2 * (j) + 1]);               \
                      pf[p][(j) >> 2] = __builtin_bit_cast(bf16x8, w_); if (HOT) KEEP(w_[(j) & 3]); else PIN(pf[p][(j) >> 2]); } while (0)
#define DQ(kb, g) do { if (ISB_ && (kb) == 3) { if ((g) == 0) KCHK(); BRQ(t, g); } if (ISB_ && (kb) == 0 && (g) == 1) PFW1(t); } while (0)
#define W4() do { SB(); __builtin_amdgcn_s_waitcnt(0xC37F); } while (0)
#define US(kb, ISB) do { constexpr bool HOT = true, ZC = (ISB) == 3, ISB_ = (ISB) == 1; constexpr int p_ = (kb) & 1; constexpr bool B_ = (kb) == 2;                    \
            GX(p_, 0, RD(kb, 7); if ((kb) == 3) { ak ^= 32768u; PIN(ak); }, E1(p_, 0); DQ(kb, 0));                                        \
            GX(p_, 1, RD(kb, 8); if ((kb) == 0) { av ^= 32768u; PIN(av); }, E1(p_, 1); DQ(kb, 1));                                        \
            GL(p_, E1(p_, 2), E1(p_, 3));                                                                                                 \
            GX(p_, 2, RD(kb, 9); C1(p_, 0), E1(p_, 4); DQ(kb, 2));                                                                        \
            GX(p_, 3, RD(kb, 10); W4(), E1(p_, 5); DQ(kb, 3));                                                                            \
            GX(p_, 4, RD(kb, 11); C1(p_, 1), E1(p_, 6); DQ(kb, 4));                                                                       \
            GX(p_, 5, RD(kb, 12); C1(p_, 2), E1(p_, 7); DQ(kb, 5));                                                                       \
            GX(p_, 6, RD(kb, 13); C1(p_, 3), E1(p_, 8); DQ(kb, 6));                                                                       \
            GX(p_, 7, RD(kb, 14); W4(), E1(p_, 9); DQ(kb, 7));                                                                            \
            GY(p_, 8, RD(kb, 15), E1(p_, 10); DQ(kb, 8));                                           \
            GY(p_, 9, if (!B_) RD(kb, 16); C1(p_, 4), E1(p_, 11); DQ(kb, 9));                                                             \
            GY(p_, 10, if (B_) { TS0(); if (ISB_) { __builtin_amdgcn_s_waitcnt(0x0071); asm volatile("s_barrier" ::: "memory"); } else TBAR(); \
                                 TS1(); SB(); RD(kb, 16); }                                                                               \
                       RD(kb, 17); C1(p_, 5), E1(p_, 12); DQ(kb, 10));                                                                    \
            GY(p_, 11, RD(kb, 18); if (!B_) W4(), E1(p_, 13); DQ(kb, 11));                                                                \
            GY(p_, 12, RD(kb, 19), E1(p_, 14); DQ(kb, 12));                                                \
            GY(p_, 13, RD(kb, 20); C1(p_, 6), E1(p_, 15); DQ(kb, 13));                                                                    \
            GY(p_, 14, RD(kb, 21); C1(p_, 7), DQ(kb, 14));                                 \
            GY(p_, 15, RD(kb, 22), DQ(kb, 15); W4());                                                                                     \
        } while (0)
            asm volatile("s_waitcnt vmcnt(%0) lgkmcnt(0)\n\ts_barrier" :: "i"(4 + (QS1 >> 15)) : "memory");
            AST(1);
#pragma unroll
            for (int s_ = 0; s_ < 8; ++s_) fr[s_] = lds_rd(ak + (unsigned)KOFF(s_));
            BRQ2C(-1, 12); BRQ2C(-1, 14);
            SB();
#define M0_(n, a) do { if ((n) & 3) MFA16(S[0][(((n) >> 1) & 2) + (a)], fr[n], qf[2 * ((n) & 3) + (a)]); else MFA16Z(S[0][(((n) >> 1) & 2) + (a)], fr[n], qf[a]); } while (0)
#define G0_(n) do { M0_(n, 0); SB(); if (Q_NST + (n) < 16) KVST((Q_NST + (n)) & 15); SB(); M0_(n, 1); SB(); RD(0, 8 + (n));               \
                    if (Q_NST + (n) < 16) KEEP(pk[(Q_NST + (n)) & 15]); SB(); } while (0)
            G0_(0); G0_(1); G0_(2); G0_(3); G0_(4); G0_(5); G0_(6); G0_(7);
            MF_DONE();
            { ONCE2(isb * min(jj, 1)) { PFW1(0); } }
            {
                PIN(S[0][0]); PIN(S[0][1]); PIN(S[0][2]); PIN(S[0][3]);
                if (__builtin_expect((qb | qw) == 0, 0)) {
#pragma unroll
                    for (int r = 0; r < 4; ++r) { S[0][2][r] = -__builtin_inff(); if (4 * (lane >> 4) + r > (lane & 15)) S[0][0][r] = S[0][3][r] = -__builtin_inff(); } }
#pragma unroll
                for (int a = 0; a < 2; ++a) {
                    float mx_;
#define MX3(d, a_, b_, c_) asm("v_max3_f32 %0, %1, %2, %3" : "=v"(d) : "v"(a_), "v"(b_), "v"(c_))
                    MX3(mx_, SV(0, 8 * a), SV(0, 8 * a + 1), SV(0, 8 * a + 2)); MX3(mx_, mx_, SV(0, 8 * a + 3), SV(0, 8 * a + 4)); MX3(mx_, mx_, SV(0, 8 * a + 5), SV(0, 8 * a + 6));
                    { float m7_ = SV(0, 8 * a + 7), n_ = -1e30f; MX3(mx_, mx_, m7_, n_); }
                    { auto r1 = __builtin_amdgcn_permlane16_swap(__float_as_uint(mx_), __float_as_uint(mx_), false, false); MX3(mx_, r1[0], r1[1], r1[1]);
                      auto r2 = __builtin_amdgcn_permlane32_swap(__float_as_uint(mx_), __float_as_uint(mx_), false, false); MX3(mx_, r2[0], r2[1], r2[1]); }
                    mx_ = -POFF - mx_;
                    ng[a] = f32x4{mx_, mx_, mx_, mx_}; PIN(ng[a]);
                }
#pragma unroll
                for (int i = 0; i < 16; ++i) { float v_ = SV(0, i) + ng[i >> 3][0]; PIN(v_); SV(0, i) = v_; }
                PIN(S[0][0]); PIN(S[0][1]); PIN(S[0][2]); PIN(S[0][3]);
            }
            SB();
            GY(0, 8, E1(0, 0), RD(0, 16); E1(0, 1)); GY(0, 9, E1(0, 2); C1(0, 0), RD(0, 17); E1(0, 3));
            GY(0, 10, E1(0, 4); C1(0, 1), RD(0, 18); E1(0, 5)); GY(0, 11, E1(0, 6); C1(0, 2), RD(0, 19); E1(0, 7));
            GY(0, 12, E1(0, 8); C1(0, 3), RD(0, 20); E1(0, 9)); GY(0, 13, E1(0, 10); C1(0, 4), RD(0, 21); E1(0, 11));
            GY(0, 14, E1(0, 12); C1(0, 5), RD(0, 22); E1(0, 13)); GY(0, 15, E1(0, 14); C1(0, 6), E1(0, 15); C1(0, 7));
#define SCOPY() do { MF_DONE(); _Pragma("unroll") for (int i = 0; i < 4; ++i) { S[1][i] = S[0][i]; PIN(S[1][i]); } } while (0)
            if (__builtin_expect((qb | qw) == 0, 0)) { SCOPY(); pf[0][0] = pf[0][1] = bf16x8{}; PIN(pf[0][0]); PIN(pf[0][1]); }
            W4(); asm volatile("s_nop 1"); SB();
            AST(2);
            const int nu = 4 * qb + qw - 2;
#define MFA16Y(c, a, b) asm volatile("v_mfma_f32_16x16x32_bf16 %0, %1, %2, 0" : "+v"(c) : "v"(a), "v"(b))
#pragma unroll
            for (int d = 0; d < 18; ++d) { if (d < 16) asm volatile("" : "=v"(o[d >> 1][d & 1])); else asm volatile("" : "=v"(ls[d & 1])); }
            if (__builtin_expect(nu < 0, 0)) { const bf16x8 z_ = bf16x8{}; asm volatile("s_nop 1" :: "v"(z_));
#pragma unroll
                for (int d = 0; d < 18; ++d) { if (d < 16) MFA16Y(o[d >> 1][d & 1], z_, z_); else MFA16Y(ls[d & 1], z_, z_); } }
            { int n1_ = nu; asm volatile("" : "+s"(n1_)); ONCE2(min(n1_ + 2, 2) >> 1) { US(1, 3); } }
            {
                const int nl = nu >> 2, nB = isb ? nl : 0;
#pragma unroll 1
                while (t < nB) { US(2, 1); US(3, 1); ++t; US(0, 1); US(1, 1); }
#pragma unroll 1
                while (t < nl) { US(2, 0); US(3, 0); ++t; US(0, 0); US(1, 0); }
            }
            AST(3);
            SST(1);
#define PCOPY() do { SCOPY(); pf[0][0] = pf[1][0]; pf[0][1] = pf[1][1]; PIN(pf[0][0]); PIN(pf[0][1]); SB(); asm volatile("s_nop 1"); } while (0)
            {
                const int nr = nu < 0 ? 0 : nu & 3, ne = nu < -1 ? 0 : nu + 1;
                if (__builtin_expect(nr >= 1, 1)) {
                    US(2, 2);
                    if (__builtin_expect(nr >= 2, 1)) {
                        if (isb) { KCHK(); asm volatile("s_setprio 0"); BRQ4(t, 0); BRQ4(t, 1); BRQ4(t, 2); BRQ4(t, 3); asm volatile("s_setprio 1"); }
                        US(3, 2);
                        if (__builtin_expect(nr == 3, 0)) US(0, 2);
                    }
                }
                int np = ne; asm volatile("" : "+s"(np));
                if (__builtin_expect(np & 1, 0)) PCOPY();
                KEEP(ng[0]); KEEP(ng[1]);
                t = qb;
            }
#define RDV(n) fr[n] = lds_rd(avf + (unsigned)((n) * 1024))
#define M1(i) do { e[i] = __builtin_amdgcn_fmed3f(e[i], 0.f, rmp[(i) & 3]); PIN(e[i]); } while (0)
#define E2(i) E1(1, i)
#define CST(i) asm volatile("global_store_dwordx4 %0, %1, %2 offset:%3 sc1\n\ts_nop 1" :: "v"(l16), "v"(pc[i]), "s"(cb + ((i) >> 2) * 262144), "i"((((i) >> 1) & 1) * 1024 + ((i) & 1) * 2048) : "memory")
#define NRM(ISB, i) do { float y_[8];                                              \
                _Pragma("unroll") for (int z_ = 0; z_ < 8; ++z_) { y_[z_] = o[((i) & 6) + (z_ >> 2)][(i) & 1][z_ & 3] * inv[(i) & 1]; PIN(y_[z_]); } \
                pc[i] = u32x4{cvtpk(y_[0], y_[1]), cvtpk(y_[2], y_[3]), cvtpk(y_[4], y_[5]), cvtpk(y_[6], y_[7])};                        \
                PIN(pc[i]); if (!(ISB)) CST(i); SB(); } while (0)
#define XLD(b_) do { _Pragma("unroll") for (int n = 0; n < 8; ++n) asm volatile("global_load_dwordx4 %0, %1, %2 offset:%3 sc1" : "=v"(fr[n]) : "v"(l16 + (n >> 2) * 4096u), "s"(b_), "i"((n & 3) * 1024) : "memory"); \
                asm volatile("s_waitcnt vmcnt(0)" : "+v"(fr[0]), "+v"(fr[1]), "+v"(fr[2]), "+v"(fr[3]), "+v"(fr[4]), "+v"(fr[5]), "+v"(fr[6]), "+v"(fr[7]) :: "memory"); } while (0)
#define MFB(a_, b_, c_) __builtin_amdgcn_mfma_f32_16x16x32_bf16(a_, b_, c_, 0, 0, 0)
#define EXACT() do { __builtin_amdgcn_s_waitcnt(0xC07F); MF_DONE();                                                                                                           \
                float mr_[2] = {-1e30f, -1e30f}; const int nu_ = 4 * qb + qw2;                                                            \
                ls[0] = ls[1] = f32x4{}; _Pragma("unroll") for (int d = 0; d < 16; ++d) o[d >> 1][d & 1] = f32x4{};                       \
                _Pragma("unroll 1") for (int u = 0; u <= nu_; ++u) {                                                                      \
                    const char* kb_ = kvg + (size_t)(u >> 2) * 65536 + (u & 3) * 8192;                                                    \
                    XLD(kb_);                                                                                                             \
                    _Pragma("unroll") for (int i = 0; i < 4; ++i) { f32x4 s_ = f32x4{};                                                   \
                        _Pragma("unroll") for (int d = 0; d < 4; ++d) s_ = MFB(fr[2 * d + (i >> 1)], qf[2 * d + (i & 1)], s_);            \
                        S[1][i] = s_; }                                                                                                   \
                    if (u == nu_) { _Pragma("unroll") for (int r = 0; r < 4; ++r) { S[1][2][r] = -__builtin_inff();                       \
                        if (4 * (ln2 >> 4) + r > (ln2 & 15)) S[1][0][r] = S[1][3][r] = -__builtin_inff(); } }                             \
                    XLD(kb_ + 32768);                                                                                                     \
                    _Pragma("unroll") for (int a = 0; a < 2; ++a) {                                                                       \
                        float mx_ = SV(1, 8 * a); _Pragma("unroll") for (int r = 1; r < 8; ++r) mx_ = fmaxf(mx_, SV(1, 8 * a + r));       \
                        mx_ = fmaxf(quad_max(mx_), mr_[a]); const float al_ = EX(mr_[a] - mx_); mr_[a] = mx_;                             \
                        u32x4 w_; _Pragma("unroll") for (int j = 0; j < 4; ++j) w_[j] = cvtpk(EX(SV(1, 8 * a + 2 * j) - mx_), EX(SV(1, 8 * a + 2 * j + 1) - mx_)); \
                        const bf16x8 p_ = __builtin_bit_cast(bf16x8, w_);                                                                 \
                        ls[a] = MFB(ones, p_, ls[a] * al_);                                                                               \
                        _Pragma("unroll") for (int d = 0; d < 8; ++d) o[d][a] = MFB(fr[d], p_, o[d][a] * al_); } }                        \
                pf[1][0] = pf[1][1] = bf16x8{}; _Pragma("unroll") for (int n = 4; n < 8; ++n) fr[n] = bf16x8{};     \
                PIN(pf[1][0]); PIN(pf[1][1]); MF_DONE(); } while (0)
#define TAIL(ISB) do { float rmp[4];                                                                                         \
                XST(0);                                                                                                                   \
                MX(1, 0, 0); SB();                                                                                                        \
                int ln2 = lane, qw2 = qw; asm volatile("" : "+v"(ln2), "+s"(qw2));   \
                fr[7] = lds_rd(av + (unsigned)((qw2 + 3) & 3) * 8192u + 7168u); SB();                                                     \
                MX(1, 0, 1); SB(); E2(0); SB();                                                                                           \
                GX(1, 1, E2(1), E2(2));                                                                                                   \
                GL(1, E2(3), E2(8));                                                                                                      \
                float fl_ = (float)((ln2 & 15) - 4 * (ln2 >> 4)); PIN(fl_);                                                               \
                unsigned avf = lb16 + (unsigned)(t & 1) * 32768u + (unsigned)qw2 * 8192u; PIN(avf); SB();                                 \
                GX(1, 2, E2(9), E2(10));                                                                             \
                GX(1, 3, E2(11), _Pragma("unroll") for (int r = 0; r < 4; ++r) { rmp[r] = (fl_ + (0.5f - r)) * 1.2089258196146292e24f; PIN(rmp[r]); }); \
                if (__builtin_expect(qw2 == 2, 0)) {                               \
                    if (ISB) TBAR(); else LGKM0_BAR(); }                                                                                  \
                SB();                                                                                                                     \
                RDV(0); RDV(1); RDV(2); RDV(3); SB();                                                                                     \
                GX(1, 4, E2(12), M1(0); M1(1));                                                                                           \
                GX(1, 5, RDV(4); E2(13), M1(2); M1(3));                                                                                   \
                GX(1, 6, RDV(5); E2(14), C1(1, 0); C1(1, 1));                                                                             \
                GX(1, 7, RDV(6); E2(15), C1(1, 4); C1(1, 5));                                                                             \
                RDV(7); M1(12); M1(13); M1(14); M1(15); e[4] = e[5] = e[6] = e[7] = 0.f; C1(1, 2); C1(1, 3); SB();                        \
                C1(1, 6); C1(1, 7); SB();                                                                                                 \
                XST(1);                                                                                                                   \
                if (__builtin_expect(qw2 < 2, 0)) { if (ISB) TBAR(); else LGKM0_BAR(); }                                                  \
                SB();                                                                                                                     \
                XST(2); XST(3);                                                                                                           \
                float inv[2];                                                                                                \
                int hd2 = head; asm volatile("" : "+s"(hd2)); const char* cb = ws + WS_CTX + (unsigned)(hd2 * 524288 + (qb * 8 + qw2 * 2) * 2048); SB(); \
                GL(0, , ); GX(0, 0, , ); GX(0, 1, , ); GX(0, 2, , );                                                                      \
                PIN(ls[0]); PIN(ls[1]);                                                                                                   \
                const bool c_ = __builtin_amdgcn_ballot_w64(!(ls[0][0] + ls[1][0] <= LTHR)) != 0; SB();                                   \
                GX(0, 3, , );                                                                                                             \
                if (__builtin_expect(c_, 0)) EXACT();                                                                                     \
                SB();                                                                                                                     \
                inv[0] = __builtin_amdgcn_rcpf(ls[0][0]); inv[1] = __builtin_amdgcn_rcpf(ls[1][0]); PIN(inv[0]); PIN(inv[1]); SB();       \
                GX(0, 4, , asm volatile("s_nop 0"));                                                                                      \
                NRM(ISB, 0); GX(0, 5, , ); NRM(ISB, 1); GX(0, 6, , ); NRM(ISB, 2); GX(0, 7, , ); NRM(ISB, 3);                             \
                XST(4);                                                                                                                   \
                NRM(ISB, 4); NRM(ISB, 5); NRM(ISB, 6); NRM(ISB, 7);                                                                       \
                if (ISB) { asm volatile("s_setprio 0"); asm volatile("" : "+s"(t)); KCHK(); if (t + 2 < NA) { BRQ4(t, 0); BRQ4(t, 1); BRQ4(t, 3); } BRQ4(t, 2); \
                           CST(0); CST(1); CST(2); CST(3); CST(4); CST(5); CST(6); CST(7); }                                              \
                XST(5);                                                                                                                   \
            } while (0)
#define SPIN() do { PIN(S[1][0]); PIN(S[1][1]); PIN(S[1][2]); PIN(S[1][3]); } while (0)
            { ONCE2(isb ^ 1) { SPIN(); TAIL(0); } }
            { ONCE2(isb) { SPIN(); TAIL(1); } }
            SST(2);
            AST(4);
        }
        if (wave >= 4) {
            int t = qbB + 1;
            const int wv = wave & 3;
            XST(6);
#define WOLD(h_) do { const unsigned ow_ = OWOFF(); const char* wp_ = (const char*)(wo + (size_t)(K64(2 * (h_)) * 64 + 16 * wv) * DM + n0);  \
                _Pragma("unroll") for (int i = 0; i < 8; ++i)                                                                             \
                    GLDS_S(ow_, wp_ + (size_t)((i >> 2) * 64 + 8 * ((i >> 1) & 1)) * (DM * 2) + (i & 1) * dn1,                             \
                           ldsbase + 65536 + (h_) * 32768 + (i >> 2) * 16384 + (2 * wv + ((i >> 1) & 1)) * 2048 + (i & 1) * 1024, 0); } while (0)
            {
                const u32x4 em = u32x4{~0u, ~0u, ~0u, ~0u};
                const char* eb = ws + WS_PART + (size_t)(tile * 8 + wv) * 24576 + kp * 6144;
                asm volatile("global_store_dwordx4 %0, %1, %2 sc1\n\tglobal_store_dwordx4 %0, %1, %2 offset:1024 sc1\n\tglobal_store_dwordx4 %0, %1, %3 sc1\n\t"
                             "global_store_dwordx4 %0, %1, %3 offset:1024 sc1\n\ts_waitcnt vmcnt(12) lgkmcnt(0)"
                             :: "v"(l16), "v"(em), "s"(eb), "s"(eb + 4 * 24576) : "memory"); }
            if (jj == 7) { WOLD(1); EALD(0); EALD(1); }
            asm volatile("s_barrier" ::: "memory");
#define BATCH() do { KCHK(); if (t + 2 < NA) { BRQ4(t, 0); BRQ4(t, 1); BRQ4(t, 3); } BRQ4(t, 2); } while (0)
#define BRQ4S(t, g) do { BRQ(t, 4 * (g)); BRQ(t, 4 * (g) + 1); __builtin_amdgcn_s_sleep(2); BRQ(t, 4 * (g) + 2); BRQ(t, 4 * (g) + 3); __builtin_amdgcn_s_sleep(2); } while (0)
#define BATCHS() do { KCHK(); BRQ4S(t, 2); BRQ4S(t, 0); BRQ4S(t, 1); BRQ4S(t, 3); } while (0)
#define BSTEP(N) do { asm volatile("s_waitcnt vmcnt(%0)\n\ts_barrier" :: "i"(N) : "memory"); ++t; } while (0)
            u32x4 fw[16], wd[16], xq[4], gp[8];
            f32x4 fa[8];
            int ln_ = lane; asm volatile("" : "+v"(ln_));
#define FMM(w_, i0, a_, z_) do { asm volatile("s_setprio 1");                                                              \
                    _Pragma("unroll") for (int i = 0; i < 8; ++i) PIN(w_[(i0) + i]);                                                      \
                    PIN(a_);                                                                                                              \
                    _Pragma("unroll") for (int j = 0; j < 8; ++j) { u32x4 f_;                                                             \
                        _Pragma("unroll") for (int d = 0; d < 4; ++d) f_[d] = __builtin_amdgcn_perm(w_[(i0) + 2 * d + 1][j >> 1], w_[(i0) + 2 * d][j >> 1], (j & 1) ? 0x07060302u : 0x05040100u); \
                        fa[j] = __builtin_amdgcn_mfma_f32_16x16x32_bf16(__builtin_bit_cast(bf16x8, a_), __builtin_bit_cast(bf16x8, f_), (z_) ? f32x4{} : fa[j], 0, 0, 0); } \
                    _Pragma("unroll") for (int j = 0; j < 8; ++j) PIN(fa[j]);                                                             \
                    asm volatile("s_setprio 0"); } while (0)
#define FOUT() f32x4{ln_ >= 32 ? fa[4][0] : fa[0][0], ln_ >= 32 ? fa[5][0] : fa[1][0], ln_ >= 32 ? fa[6][0] : fa[2][0], ln_ >= 32 ? fa[7][0] : fa[3][0]}
            const int c1 = 2 * (i1 - 14 * k1) + (wv & 1);
            const unsigned r4o = (unsigned)(ln_ & 15) * 16u;
            const char* cxp = ws + WS_FLAG + F_CTX + ((qbB * 4 + qw) * 32 + head) * CFS;
            if (nf1 == 0) {
#pragma unroll 1
                while (t < F1T) { BATCHS(); BSTEP(0); }
                const unsigned evo = (unsigned)((16 * (ln_ >> 5) + 4 * ((ln_ >> 4) & 1)) * FF * 2) + r4o;
                const u16* fwp = (wv < 2 ? w_gate : w_up) + (size_t)(k1 * 128) * FF + c1 * 128;
#define FLD(h_, c_) do { _Pragma("unroll") for (int i = 0; i < 8; ++i)                                                                    \
                    asm volatile("global_load_dwordx4 %0, %1, %2" : c_(fw[((h_) & 1) * 8 + i]) : "v"(evo), "s"(fwp + (size_t)(32 * (h_) + 8 * (i >> 2) + (i & 3)) * FF) : "memory"); } while (0)
                BATCH();
                { const int xr_ = (ln_ & 4) ? ri2 : ri1;
#pragma unroll
                  for (int i = 0; i < 4; ++i)
                      asm volatile("global_load_dwordx4 %0, %1, %2 offset:%3 sc1" : "=v"(xq[i]) : "v"((unsigned)((xr_ >> 4) * 2048 + (xr_ & 15) * 16 + (ln_ >> 4) * 256)),
                                   "s"(ws + WS_XF + (size_t)(k1 * 2 + (i >> 1)) * 262144), "i"((i & 1) * 1024) : "memory"); }
                FLD(0, "=v"); FLD(1, "=v"); BSTEP(20);
                BATCH(); asm volatile("s_waitcnt vmcnt(16)" ::: "memory");
                FLAG_PUT(cxp, tag, 1);
                FMM(fw, 0, xq[0], 1); FLD(2, "=v"); FMM(fw, 8, xq[1], 0); FLD(3, "=v"); BSTEP(17);
                BATCH(); asm volatile("s_waitcnt vmcnt(16)" ::: "memory");
                FMM(fw, 0, xq[2], 0); FMM(fw, 8, xq[3], 0); const f32x4 sum = FOUT();
                ST16_SC1((float*)(ws + WS_GP) + ((size_t)k1 * 4 + (wv >> 1) * 2 + ((ln_ >> 4) & 1)) * FF + c1 * 128 + (ln_ & 15) * 8 + (ln_ >> 5) * 4, sum);
                PFW1(7); BSTEP(2);
            }
            const char* pnd = nf1 ? cxp : ws + WS_FLAG + F_GU + (c1 * 16 + (wv >> 1) * 8 + k1) * 8;
            const int tp = nf1 ? t + 1 : t;
#define PUB(N) do { if (t == tp) { asm volatile("s_waitcnt vmcnt(%0)" :: "i"(N) : "memory"); FLAG_PUT(pnd, tag, 1); } } while (0)
            const int f0 = (nf2 ? 0 : i2 >> 3) * 256 + wv * 64;
            const char* gfp = ws + WS_FLAG + F_GU + (f0 >> 7) * 128 + (ln_ & 15) * 8;
            u64 gf = tag + 1;
            char* vs = lds + (NA & 1) * 32768;
#define GFLD() asm volatile("global_load_dwordx2 %0, %1, off sc1" : "+v"(gf) : "v"(gfp) : "memory")
#define WDLD(i, n_) do { const unsigned dvo = (unsigned)((ln_ >> 4) * 8 * DM * 2) + r4o;                                                  \
                    const char* db_ = (const char*)(w_down + (size_t)((nf2 ? 0 : i2 >> 3) * 256 + wv * 64) * DM + (i2 & 7) * 128) + 4096; \
                    asm volatile("s_cmp_lg_u32 %11, 0\n\ts_cbranch_scc1 1f\n\t"                                                          \
                                 "global_load_dwordx4 %0, %8, %9 offset:-4096\n\tglobal_load_dwordx4 %1, %8, %9 offset:-2048\n\tglobal_load_dwordx4 %2, %8, %9\n\t" \
                                 "global_load_dwordx4 %3, %8, %9 offset:2048\n\tglobal_load_dwordx4 %4, %8, %10 offset:-4096\n\tglobal_load_dwordx4 %5, %8, %10 offset:-2048\n\t" \
                                 "global_load_dwordx4 %6, %8, %10\n\tglobal_load_dwordx4 %7, %8, %10 offset:2048\n1:"  \
                                 : "=&v"(wd[i]), "=&v"(wd[i + 1]), "=&v"(wd[i + 2]), "=&v"(wd[i + 3]), "=&v"(wd[i + 4]), "=&v"(wd[i + 5]), "=&v"(wd[i + 6]), "=&v"(wd[i + 7]) \
                                 : "v"(dvo), "s"(db_ + (i) * 8192), "s"(db_ + (i) * 8192 + 8192), "s"(nf2) : "memory", "scc");            \
                    EALD((i) >> 3);                                                                                                       \
                    asm volatile("s_waitcnt vmcnt(%1)\n\ts_cmp_eq_u32 %0, 0\n\ts_cbranch_scc1 1f\n\ts_waitcnt vmcnt(%2)\n1:"                \
                                 :: "s"(nf2), "i"(12 + (n_)), "i"(4 + (n_)) : "memory", "scc");                                           \
                    BSTEP(63); } while (0)
#pragma unroll 1
            while (t < NA - 4) { BATCHS(); PUB(17); BSTEP(0); }
            if (jj < 7) {
                BATCH(); PUB(16); WDLD(0, 0);
                BATCH(); PUB(16); GFLD(); WDLD(8, 0);
            }
            if (jj < 6) {
                BATCH();
                if (nf2 == 0) {
                    FLAG_WAIT(gf, gfp, tag, 63);
#pragma unroll
                    for (int i = 0; i < 8; ++i)
                        asm volatile("global_load_dwordx4 %0, %1, %2 sc1" : "=v"(gp[i]) : "v"((unsigned)((ln_ >> 4) * FF * 4) + r4o), "s"(ws + WS_GP + ((size_t)i * 4 * FF + f0) * 4) : "memory");
                    *(unsigned*)vs = 0;
                    asm volatile("s_waitcnt lgkmcnt(0)" ::: "memory");
                }
                WOLD(NA & 1);
                BSTEP(8);
            } else if (jj == 6) WOLD(0);
            asm volatile("global_load_dwordx2 %0, %1, %2 sc1" : "+v"(cxf) : "v"(CFO()), "s"(CFB) : "memory");
            WOLD((NA & 1) ^ 1);
            asm volatile("s_waitcnt vmcnt(16)" ::: "memory");
            if (jj == 7) FLAG_PUT(cxp, tag, 1);
            if (nf2 == 0) {
                f32x4 g4;
#pragma unroll
                for (int i = 0; i < 8; ++i) { PIN(gp[i]);
#pragma unroll
                    for (int j = 0; j < 4; ++j) { if (i) VADD(g4[j], g4[j], __uint_as_float(gp[i][j])); else g4[j] = __uint_as_float(gp[0][j]); } }
                float hv[2];
#pragma unroll
                for (int k = 0; k < 2; ++k) { auto rr = __builtin_amdgcn_permlane32_swap(__float_as_uint(g4[k]), __float_as_uint(g4[2 + k]), false, false);
                    const float gb = rbf(__uint_as_float(rr[0])), ub = rbf(__uint_as_float(rr[1]));
                    hv[k] = rbf(gb / (1.f + __expf(-gb))) * ub; PIN(hv[k]); }
                char* hl = vs + 8192 + wv * 256;
                *(unsigned*)(hl + ((ln_ >> 4) & 1) * 128 + (ln_ & 15) * 8 + (ln_ >> 5) * 4) = cvtpk(hv[0], hv[1]);
                asm volatile("" ::: "memory");
                xq[0] = *(const u32x4*)(hl + ((ln_ >> 2) & 1) * 128 + (ln_ >> 4) * 16); xq[1] = *(const u32x4*)(hl + ((ln_ >> 2) & 1) * 128 + 64 + (ln_ >> 4) * 16);
                FMM(wd, 0, xq[0], 1); FMM(wd, 8, xq[1], 0);
                *(f32x4*)(vs + 4096 + wv * 1024 + ln_ * 16) = FOUT();
                unsigned n_;
                asm volatile("s_waitcnt lgkmcnt(0)\n\ts_mov_b64 exec, 1\n\tds_add_rtn_u32 %0, %1, %2\n\ts_mov_b64 exec, -1\n\ts_waitcnt lgkmcnt(0)"
                             : "=&v"(n_) : "v"(ldsbase + (NA & 1) * 32768u), "v"(1u) : "memory");
                ypub = __builtin_amdgcn_readfirstlane(n_) ^ 3u;
                if (ypub == 0) {
                    const f32x4* yr = (const f32x4*)(vs + 4096) + ln_;
                    const f32x4 a = (yr[0] + yr[64]) + (yr[128] + yr[192]);
                    ST16_SC1((float*)(ws + WS_YP) + ((size_t)(((ln_ >> 4) & 1) * 8 + (i2 & 7)) * 16 + (i2 >> 3)) * 128 + (ln_ & 15) * 8 + (ln_ >> 5) * 4, a);
                }
            }
            AST(5);
        }
    }
    PH_STAMP(3);
    {
        constexpr int LDW = 1024, STG = 16384;
        int tid = threadIdx.x; asm volatile("" : "+v"(tid));
        const int lane = tid & 63, l15 = lane & 15, q4 = lane >> 4;
        unsigned xvo = (unsigned)lane * 16u; PIN(xvo);
        if (!(kp & 1)) {
#pragma unroll
            for (int i = 0; i < 8; i += 2) { const u32x4 t_ = pc[i]; pc[i] = pc[i + 1]; pc[i + 1] = t_; PIN(pc[i]); PIN(pc[i + 1]); } }
        bf16x8 yr_[4][2][2];
#pragma unroll
        for (int i = 0; i < 8; ++i) yr_[i >> 2][i & 1][(i >> 1) & 1] = __builtin_bit_cast(bf16x8, pc[i]);
        unsigned wa[2][2];
        wa[0][0] = ldsbase + (unsigned)(65536 + ((kp >> 1) ^ 1) * 1024 + (q4 >> 1) * 2048 + (4 * (q4 & 1) + (l15 >> 2)) * 64 + (q4 & 1) * 16 + (lane & 2) * 16 + (lane & 1) * 8);
        wa[0][1] = wa[0][0] ^ 16u; wa[1][0] = wa[0][0] ^ 1024u; wa[1][1] = wa[0][1] ^ 1024u;
        PIN(wa[0][0]); PIN(wa[0][1]); PIN(wa[1][0]); PIN(wa[1][1]);
        const char* cg = ws + WS_CTX + (qb * 8 + qw * 2) * 2048;
        unsigned xok;
#define XLD8 "global_load_dwordx4 %0, %10, %12 sc1\n\tglobal_load_dwordx4 %1, %10, %13 sc1\n\tglobal_load_dwordx4 %2, %10, %12 offset:1024 sc1\n\tglobal_load_dwordx4 %3, %10, %13 offset:1024 sc1\n\t" \
             "global_load_dwordx4 %4, %11, %12 sc1\n\tglobal_load_dwordx4 %5, %11, %13 sc1\n\tglobal_load_dwordx4 %6, %11, %12 offset:1024 sc1\n\tglobal_load_dwordx4 %7, %11, %13 offset:1024 sc1\n\t"
#define XOUT(c_) c_(yr_[2][0][0]), c_(yr_[2][1][0]), c_(yr_[2][0][1]), c_(yr_[2][1][1]), c_(yr_[3][0][0]), c_(yr_[3][1][0]), c_(yr_[3][0][1]), c_(yr_[3][1][1])
#define XIN "v"(xvo), "v"(xvo2), "s"(xp0), "s"(xp1), "s"(tag)
        unsigned xvo2 = xvo + 262144u, wvo = OWOFF() + (unsigned)K64(4) * (64 * LDW * 2), xso = xvo + (unsigned)K64(4) * 262144u, cfo = CFO();
        PIN(xvo2); PIN(wvo); PIN(xso); PIN(cfo);
        const char* cg0 = cg + ((kp ^ 1) & 1) * 2048, * cg1 = cg + (kp & 1) * 2048, * xp0 = cg0 + (unsigned)K64(2) * 262144u, * xp1 = cg1 + (unsigned)K64(2) * 262144u;
        const char* cfb = CFB, * wb0 = (const char*)wob, * wb1 = wb0 + (dn1 - 1024);
        const char* fpp = ws + WS_FLAG + ((wave & 4) ? F_Y + ((i2 & 7) * 16 + ((i2 >> 3) & 15)) * 8 : F_CTX + ((qbA * 4 + wave) * 32 + head) * CFS);
        unsigned fpz = (wave >> 2) * ypub, m0w = ldsbase + wave * 2048;
        asm volatile("" : "+s"(cg0), "+s"(cg1), "+s"(xp0), "+s"(xp1), "+s"(cfb), "+s"(wb0), "+s"(wb1), "+s"(fpp), "+s"(fpz), "+s"(m0w));
        SST(3);
        SB();
        asm volatile("s_waitcnt vmcnt(0)" ::: "memory");
        FLAG_PUTZ(fpp, tag, fpz);
        asm volatile("v_cmp_ne_u64 vcc, %14, %8\n\ts_and_b32 %9, vcc_lo, 14\n\ts_cbranch_scc1 1f\n\t"
                     XLD8 "\n1:\n\ts_barrier" : XOUT("=&v"), "+v"(cxf), "=&s"(xok) : XIN : "memory", "vcc", "scc");
        SST(4);
        f32x4 acc[2][8];
        bf16x4 wl[8], wh[8];
#define OFA(s, k_, j) (wa[(j) >> 2][(j) & 1] + (unsigned)(((s) & 3) * STG + (k_) * 8192 + (((j) >> 1) & 1) * 512))
#define ORL(i, s, k_, j) wl[i] = tr_rd(OFA(s, k_, j))
#define ORH(i, s, k_, j) wh[i] = tr_rd(OFA(s, k_, j) + 4096u)
        ORL(0, 0, 0, 0); ORH(0, 0, 0, 0); ORL(1, 0, 0, 1); ORH(1, 0, 0, 1); ORL(2, 0, 0, 2); ORH(2, 0, 0, 2); ORL(3, 0, 0, 3); ORH(3, 0, 0, 3); SB();
        const int r0 = qb * 128 + qw * 32 + (kp & 1) * 16, ch = kp >> 1;
        const unsigned nrt = min((unsigned)(ri1 - r0) >> 4, (unsigned)(ri2 - r0) >> 4);
        u64 yf = 0;
#define YFP (ws + WS_FLAG + F_Y + (ntc * 16 + (l15 < 14 ? l15 : 0)) * 8)
#define OMF(a, s, k_, j) do { if ((s) == 0 && (k_) == 0) MFA16Z(acc[a][j], w_, yr_[0][a][0]); else MFA16(acc[a][j], w_, yr_[(s) & 3][a][k_]); } while (0)
#define ORQ(h) do { constexpr int x_ = o_x(h), w_ = o_w(h);                                                                               \
            if (x_ >= 0) { asm volatile("global_load_dwordx4 %0, %1, %2 offset:%3 sc1" : "=v"(yr_[(x_ >> 2) & 3][x_ & 1][(x_ >> 1) & 1])   \
                                        : "v"(xso), "s"((x_ & 1) ? cg1 : cg0), "i"(((x_ >> 1) & 1) * 1024) : "memory");         \
                           if ((x_ & 3) == 3) { xso ^= (unsigned)((x_ >> 2) ^ ((x_ >> 2) + 1)) * 262144u; PIN(xso); } }                    \
            if (o_w((h) + 1) >= 0 && !(o_w((h) + 1) & 1)) asm volatile("s_mov_b32 m0, %0" :: "s"(m0w + (((o_w((h) + 1) >> 1) & 7) ^ 4) * STG) : "memory"); \
            if (w_ >= 0) { asm volatile("global_load_lds_dwordx4 %0, %1 offset:%2" :: "v"(wvo), "s"((w_ & 1) ? wb1 : wb0), "i"((w_ & 1) * 1024) : "memory"); \
                           if (w_ & 1) { wvo ^= (unsigned)((w_ >> 1) ^ ((w_ >> 1) + 1)) * (64 * LDW * 2); PIN(wvo); } } } while (0)
#define OPAIR(s, f, A) do { const bf16x8 w_ = CAT8(wl[(f) & 7], wh[(f) & 7]); constexpr int s_ = (s) + (((f) + 4) >> 4), m_ = ((f) + 4) & 15; \
            OMF(0, s, (f) >> 3, (f) & 7); SB(); ORL(m_ & 7, s_, m_ >> 3, m_ & 7); ORQ(32 * (s) + 2 * (f)); A; SB();                       \
            OMF(1, s, (f) >> 3, (f) & 7); SB(); ORH(m_ & 7, s_, m_ >> 3, m_ & 7); ORQ(32 * (s) + 2 * (f) + 1); SB(); if (((f) & 1)) { __builtin_amdgcn_s_waitcnt(0xC47F); SB(); } } while (0)
#define OTG(h_) do { wa[h_][0] ^= 65536u; wa[h_][1] ^= 65536u; PIN(wa[h_][0]); PIN(wa[h_][1]); } while (0)
#define OST(i)
#define OSTEP(s, POST) do {                                                                                                               \
        OPAIR(s, 0, ); OPAIR(s, 1, ); OPAIR(s, 2, ); OPAIR(s, 3, ); OPAIR(s, 4, ); OPAIR(s, 5, ); OPAIR(s, 6, ); OPAIR(s, 7, );           \
        OPAIR(s, 8, ); OPAIR(s, 9, if (((s) & 3) == 3) OTG(0)); OPAIR(s, 10, ); OPAIR(s, 11, );                                           \
        if ((s) >= 3) asm volatile("s_waitcnt vmcnt(%0)\n\ts_barrier" :: "i"(o_wait(s)) : "memory");                                      \
        SB(); POST; OST((s) + 1);                                                                                                         \
        OPAIR(s, 12, ); OPAIR(s, 13, if (((s) & 3) == 3) OTG(1)); OPAIR(s, 14, ); OPAIR(s, 15, );                                         \
    } while (0)
        PH_STAMP(4);
        SST(5);
        OST(0);
        SB();
        OSTEP(0, );
#define MSK "14"
#define MSKI
        asm volatile("s_cmp_eq_u32 %3, 0\n\ts_cbranch_scc1 1f\n\tglobal_load_dwordx2 %0, %1, %2 sc1\n1:" : "+v"(cxf) : "v"(cfo), "s"(cfb), "s"(xok) : "memory", "scc");
        OSTEP(1, );
        asm volatile("s_waitcnt vmcnt(8)\n\ts_cmp_eq_u32 %9, 0\n\ts_cbranch_scc1 3f\n\ts_mov_b32 %9, 0x40000\n\ts_waitcnt vmcnt(4)\n"
                     "1:\n\tv_cmp_ne_u64 vcc, %14, %8\n\ts_and_b32 vcc_lo, vcc_lo, " MSK "\n\ts_cbranch_scc0 2f\n\t"
                     "global_load_dwordx2 %8, %15, %16 sc1\n\ts_sleep 4\n\ts_waitcnt vmcnt(0)\n\ts_sub_u32 %9, %9, 1\n\ts_cbranch_scc0 1b\n2:\n\t" XLD8 "s_waitcnt vmcnt(4)\n3:"
                     : XOUT("+v"), "+v"(cxf), "+s"(xok) : XIN, "v"(cfo), "s"(cfb), "s"(wave) MSKI : "memory", "vcc", "scc");
        const unsigned inc = (unsigned)__builtin_amdgcn_ballot_w64(cxf != tag) & 0x1f0u;
        SB();
        OSTEP(2, if (__builtin_expect(inc != 0, 0)) asm volatile("global_load_dwordx2 %0, %1, %2 sc1" : "+v"(cxf) : "v"(cfo), "s"(cfb) : "memory"); SB());
        asm volatile("s_waitcnt vmcnt(8)" ::: "memory"); SB();
        OSTEP(3, );
        OSTEP(4, if (__builtin_expect(inc != 0, 0)) FLAG_WAITM(cxf, cfb + cfo, tag, 63, 0x1f0u); SB());
        OSTEP(5, ); OSTEP(6, );
        OSTEP(7, if (__builtin_expect(nrt == 0, 0)) asm volatile("global_load_dwordx2 %0, %1, off sc1" : "+v"(yf) : "v"(YFP) : "memory"); SB());
        OSTEP(8, ); OSTEP(9, ); OSTEP(10, );
        u32x4 yq[7], pv[6], zn[2]; u32x2 ea[4]; unsigned rq;
        if (__builtin_expect(nrt == 0, 0)) {
            FLAG_WAIT(yf, YFP, tag, 63);
            asm volatile("global_load_dwordx4 %0, %8, %10 sc1\n\tglobal_load_dwordx4 %1, %8, %10 offset:512 sc1\n\tglobal_load_dwordx4 %2, %8, %10 offset:1024 sc1\n\t"
                         "global_load_dwordx4 %3, %8, %10 offset:1536 sc1\n\tglobal_load_dwordx4 %4, %8, %10 offset:2048 sc1\n\tglobal_load_dwordx4 %5, %8, %10 offset:2560 sc1\n\t"
                         "global_load_dwordx4 %6, %8, %10 offset:3072 sc1\n\tglobal_load_dword %7, %9, %10 sc1"
                         : "=&v"(yq[0]), "=&v"(yq[1]), "=&v"(yq[2]), "=&v"(yq[3]), "=&v"(yq[4]), "=&v"(yq[5]), "=&v"(yq[6]), "=&v"(rq)
                         : "v"((unsigned)WS_YP + (unsigned)((((lane >> 5) * 8 + ntc) * 16 + (q4 & 1) * 7) * 128 + ch * 64 + 4 * l15) * 4u),
                           "v"((unsigned)WS_R + (unsigned)(lane >= 32 ? ri2 : ri1) * 4u), "s"(ws) : "memory");
        }
        SB();
        OSTEP(11, );
        char* pb = ws + WS_PART + (size_t)(tile * 8 + wave) * 24576;
        const char* tfb = ws + WS_FLAG + F_PARK + (tile * 8 + wave) * 256;
        const unsigned tfo = (unsigned)((lane & 3) ^ kp) * 64u;
        const unsigned ea0 = ldsbase + 131072 + (wave & 3) * 8192 + (wave >> 2) * 4096 + l15 * 128 + (((lane >> 5) * 2 ^ (lane & 7)) << 4) + (q4 & 1) * 8;
        typedef __attribute__((address_space(3))) u32x2 lds_u32x2;
#define OSND(a, j) do { constexpr int x_ = ((a) ^ 1) | (((j) >> 1) & 2 ^ 2);                                             \
            u32x4 p_ = u32x4{cvtpk(acc[a][j][0], acc[a][j][1]), cvtpk(acc[a][j][2], acc[a][j][3]), cvtpk(acc[a][(j) + 1][0], acc[a][(j) + 1][1]), cvtpk(acc[a][(j) + 1][2], acc[a][(j) + 1][3])}; \
            if (x_ == 1) { _Pragma("unroll") for (int k = 0; k < 4; ++k) p_[k] = min(p_[k], 0xfffffffeu); }     \
            asm volatile("global_store_dwordx4 %0, %1, %2 offset:%3 sc1\n\ts_nop 1" :: "v"(xvo), "v"(p_), "s"(pb + (kp ^ x_) * 6144 + (x_ - 1) * 2048), "i"(((j) & 2) * 512) : "memory"); } while (0)
#define OQ(q) do { constexpr int p_ = (q) + 4; const bf16x8 w_ = CAT8(wl[(q) & 7], wh[(q) & 7]);                                           \
            constexpr int j_ = (q) < 32 ? (q) & 3 : 4 + 2 * (((q) >> 4) & 1) + ((q) & 1), k_ = (q) < 32 ? ((q) >> 2) & 1 : ((q) >> 1) & 1, s_ = (q) < 32 ? ((q) >> 3) & 3 : ((q) >> 2) & 3; \
            constexpr int pj_ = p_ < 32 ? p_ & 3 : 4 + 2 * ((p_ >> 4) & 1) + (p_ & 1), pk_ = p_ < 32 ? (p_ >> 2) & 1 : (p_ >> 1) & 1, ps_ = p_ < 32 ? (p_ >> 3) & 3 : (p_ >> 2) & 3; \
            if ((q) == 16 || (q) == 24) { asm volatile("s_waitcnt vmcnt(%0)" :: "i"((q) == 16 ? 4 : 0) : "memory"); SB(); }               \
            OMF(0, 12 + s_, k_, j_); SB();                                                                                                \
            if (p_ < 64) ORL(p_ & 7, ps_, pk_, pj_);                                                                                      \
            ORQ(384 + 2 * (q)); SB();                                                                                                     \
            OMF(1, 12 + s_, k_, j_); SB();                                                                                                \
            if (p_ < 64) ORH(p_ & 7, ps_, pk_, pj_);                                                                                      \
            if ((q) >= 10 && (q) < 14) ea[((q) - 10) & 3] = *(lds_u32x2*)(uintptr_t)(ea0 ^ (unsigned)((((q) - 10) & 1) * 16 + (((q) - 10) & 2) * 32)); \
            if ((q) == 14 || (q) == 15) zn[(q) & 1] = __builtin_bit_cast(u32x4, lds_rd(ldsbase + 131072 + 2048 + (wave & 3) * 8192 + (wave >> 2) * 4096 + ((q) & 1) * 1024 + xvo)); \
            if ((q) == 32) OSND(0, 0); if ((q) == 33) OSND(1, 0); if ((q) == 34) OSND(0, 2); if ((q) == 35) OSND(1, 2);                   \
            if ((q) == PQ) asm volatile("s_setprio 0");                                                                                   \
            if ((q) == 50) OSND(0, 4);                                                                                                    \
            if ((q) == FQ) { asm volatile("s_waitcnt vmcnt(%0)" :: "i"(FQ >= 50) : "memory"); FLAG_PUT(tfb + kp * 64, tag, 1); }     \
            if ((q) == GQ) asm volatile("global_load_dwordx2 %0, %1, %2 sc1" : "=v"(cxf) : "v"(tfo), "s"(tfb) : "memory");               \
            SB(); } while (0)
#define PQ 35
#define FQ 44
#define GQ 60
#define OQ8(q) do { OQ(q); OQ((q) + 1); OQ((q) + 2); OQ((q) + 3); OQ((q) + 4); OQ((q) + 5); OQ((q) + 6); OQ((q) + 7); } while (0)
        if (PQ < 64) asm volatile("s_setprio 1");
        OQ8(0); OQ8(8); OQ8(16); OQ8(24); OST(13); OQ8(32); OQ8(40); OQ8(48); OQ8(56);
        MF_DONE();
        OSND(0, 6); S2T(1);
        OST(14);
        PH_STAMP(5);
        SST(6); S2T(2);
        SB();
        if (__builtin_expect(nrt == 0, 0)) {
            f32x4 ys;
#pragma unroll
            for (int i = 0; i < 7; ++i) PIN(yq[i]);
            PIN(rq);
            const float sig = rbf(1.f / (1.f + __expf(-__uint_as_float(rq))));
#pragma unroll
            for (int j = 0; j < 4; ++j) {
                const float s_ = ((__uint_as_float(yq[0][j]) + __uint_as_float(yq[1][j])) + (__uint_as_float(yq[2][j]) + __uint_as_float(yq[3][j])))
                               + ((__uint_as_float(yq[4][j]) + __uint_as_float(yq[5][j])) + __uint_as_float(yq[6][j]));
                auto rr = __builtin_amdgcn_permlane16_swap(__float_as_uint(s_), __float_as_uint(s_), false, false);
                ys[j] = rbf(__uint_as_float(rr[0]) + __uint_as_float(rr[1])) * sig; }
            char* yl = lds + 65536 + wave * 256;
            *(u32x2*)(yl + (lane >> 5) * 128 + (l15 >> 2) * 32 + (l15 & 1) * 16 + (l15 & 2) * 4) = u32x2{cvtpk(ys[0], ys[1]), cvtpk(ys[2], ys[3])};
            asm volatile("" ::: "memory");
            const int m = r0 + l15;
#pragma unroll
            for (int i = 0; i < 2; ++i) { const u32x4 yn = *(const u32x4*)(yl + (m == ri1 ? 0 : 128) + i * 64 + q4 * 16);
#pragma unroll
                for (int k = 0; k < 4; ++k) { const unsigned z_ = cvtpk(HALF(zn[i][k], 0) + HALF(yn[k], 0), HALF(zn[i][k], 1) + HALF(yn[k], 1));
                    zn[i][k] = m == ri1 || m == ri2 ? z_ : zn[i][k]; } }
        }
        float gg[2][8], xx[2][8];
#pragma unroll
        for (int i = 0; i < 2; ++i)
#pragma unroll
            for (int j = 0; j < 8; ++j) { gg[i][j] = HALF(zn[i][j >> 1], j); xx[i][j] = HALF(ea[2 * i + (j >> 2)][(j >> 1) & 1], j); PIN(gg[i][j]); PIN(xx[i][j]); }
        unsigned ovo = (unsigned)((l15 & 7) * DM + 32 * (l15 >> 3) + 8 * q4) * 2u; PIN(ovo);
        const char* ob = (const char*)(outp + (size_t)r0 * DM + ntc * 128 + 64 * ch);
        asm volatile("" : "+s"(ob));
        PH_STAMP(6);
        { unsigned c_, m_;
          asm volatile("s_waitcnt vmcnt(%0)" :: "i"(GQ < 50 ? 2 : 1) : "memory");
          asm volatile("s_mov_b32 %7, 0x40000\n"
                       "1:\n\tv_cmp_ne_u64 vcc, %11, %6\n\ts_and_b32 vcc_lo, vcc_lo, 12\n\ts_cbranch_scc0 2f\n\t"
                       "global_load_dwordx2 %6, %12, %13 sc1\n\ts_waitcnt vmcnt(0)\n\ts_sub_u32 %7, %7, 1\n\ts_cbranch_scc0 1b\n"
                       "2:\n\tglobal_load_dwordx4 %1, %9, %10 offset:-1024 sc1\n\tglobal_load_dwordx4 %2, %9, %10 offset:1024 sc1\n\t"
                       "global_load_dwordx4 %4, %9, %10 sc1\n\tglobal_load_dwordx4 %5, %9, %10 offset:2048 sc1\n\t"
                       "global_load_dwordx4 %0, %9, %10 offset:-3072 sc1\n\tglobal_load_dwordx4 %3, %9, %10 offset:-2048 sc1"
                       : "=&v"(pv[0]), "=&v"(pv[1]), "=&v"(pv[2]), "=&v"(pv[3]), "=&v"(pv[4]), "=&v"(pv[5]), "+v"(cxf), "=&s"(c_), "=&v"(m_)
                       : "v"(xvo), "s"(pb + kp * 6144 + 3072), "s"(tag), "v"(tfo), "s"(tfb) : "memory", "vcc", "scc"); }
        S2T(3);
#pragma unroll
        for (int i = 0; i < 16; i += 2) { float g_[2];
#pragma unroll
            for (int e_ = 0; e_ < 2; ++e_) { float a_ = gg[i >> 3][(i & 7) + e_] * -1.4426950408889634f; PIN(a_); a_ = 1.f + __builtin_amdgcn_exp2f(a_); PIN(a_);
                g_[e_] = gg[i >> 3][(i & 7) + e_] * __builtin_amdgcn_rcpf(a_); PIN(g_[e_]); }
            const unsigned p_ = cvtpk(g_[0], g_[1]); gg[i >> 3][i & 7] = HALF(p_, 0); gg[i >> 3][(i & 7) + 1] = HALF(p_, 1); PIN(gg[i >> 3][i & 7]); PIN(gg[i >> 3][(i & 7) + 1]); }
        S2T(4);
        asm volatile("s_waitcnt vmcnt(0)" : "+v"(pv[0]), "+v"(pv[1]), "+v"(pv[2]), "+v"(pv[3]), "+v"(pv[4]), "+v"(pv[5]) :: "memory");
        S2T(5);
#pragma unroll 1
        for (int n_ = 0; n_ < 0x40000; ++n_) {
            const unsigned mx_ = max(max(max(pv[0][0], pv[0][1]), max(pv[0][2], pv[0][3])), max(max(pv[3][0], pv[3][1]), max(pv[3][2], pv[3][3])));
            if (__builtin_amdgcn_ballot_w64(mx_ == ~0u) == 0) break;
            asm volatile("global_load_dwordx4 %0, %2, %3 sc1\n\tglobal_load_dwordx4 %1, %2, %3 offset:1024 sc1\n\ts_waitcnt vmcnt(0)"
                         : "=&v"(pv[0]), "=&v"(pv[3]) : "v"(xvo), "s"(pb + kp * 6144) : "memory");
        }
        PH_STAMP(7); S2T(6);
        u32x4 ow[2];
#pragma unroll
        for (int i = 0; i < 2; ++i) {
            u32x4& ov = ow[i];
#pragma unroll
            for (int k = 0; k < 4; ++k) { float sv[2];
#pragma unroll
                for (int e_ = 0; e_ < 2; ++e_) sv[e_] = fmaf((acc[1][4 + 2 * i + (k >> 1)][2 * (k & 1) + e_] + HALF(pv[3 * i][k], e_)) + (HALF(pv[3 * i + 1][k], e_) + HALF(pv[3 * i + 2][k], e_)),
                                                             gg[i][2 * k + e_], xx[i][2 * k + e_]);
                ov[k] = cvtpk(sv[0], sv[1]); }
#pragma unroll
            for (int k = 0; k < 2; ++k) { auto rr = __builtin_amdgcn_permlane16_swap(ov[k], ov[2 + k], false, false); ov[k] = rr[0]; ov[2 + k] = rr[1]; }
        }
#pragma unroll
        for (int i = 0; i < 2; ++i) {
            u32x4 ov;
#pragma unroll
            for (int k = 0; k < 4; ++k) ov[k] = i ? __builtin_amdgcn_update_dpp(ow[1][k], ow[0][k], 0x128, 0xf, 0x3, false) : __builtin_amdgcn_update_dpp(ow[0][k], ow[1][k], 0x128, 0xf, 0xc, false);
            asm volatile("global_store_dwordx4 %0, %1, %2 sc1\n\ts_nop 1" :: "v"(ovo), "v"(ov), "s"(ob + i * (8 * DM * 2)) : "memory");
        }
        SST(7); SST_OUT();
        PH_STAMP(8);
        PH_STAMP(9);
    }
}

extern "C" void kernel_launch(void* const* d_in, const int* in_sizes, int n_in, void* d_out, int out_size, void* d_ws, size_t ws_size,
                              hipStream_t stream) {
    const u16* x    = (const u16*)d_in[0];
    const u16* ckv  = (const u16*)d_in[1];
    const u16* wq   = (const u16*)d_in[2];
    const u16* wuk  = (const u16*)d_in[3];
    const u16* wuv  = (const u16*)d_in[4];
    const u16* wo   = (const u16*)d_in[5];
    const u16* wr   = (const u16*)d_in[7];
    const u16* wg   = (const u16*)d_in[8];
    const u16* wu   = (const u16*)d_in[9];
    const u16* wd   = (const u16*)d_in[10];
    (void)in_sizes; (void)n_in; (void)out_size; (void)ws_size;
    (void)hipFuncSetAttribute((const void*)k_all, hipFuncAttributeMaxDynamicSharedMemorySize, 163840);
    k_all<<<256, 512, 163840, stream>>>(x, ckv, wuk, wuv, wr, (char*)d_ws + ((size_t)WSOFF << 20), wq, wo, wg, wu, wd, (u16*)d_out);
}
```

```cpp
#include <hip/hip_runtime.h>
#include <hip/hip_bf16.h>
#include <stdint.h>

typedef unsigned short u16;
typedef unsigned long long u64;
typedef short    bf16x8 __attribute__((ext_vector_type(8)));
typedef short    bf16x4 __attribute__((ext_vector_type(4)));
typedef float    f32x4  __attribute__((ext_vector_type(4)));
typedef float    f32x2  __attribute__((ext_vector_type(2)));
typedef __bf16   bfx2   __attribute__((ext_vector_type(2)));
typedef unsigned u32x4  __attribute__((ext_vector_type(4)));
typedef unsigned u32x2  __attribute__((ext_vector_type(2)));

#define DI __device__ __forceinline__

constexpr int SQ = 2048, DM = 1024, NH = 32, HD = 128, DC = 512, FF = 3584;

constexpr size_t WS_R    = 4u << 20;
constexpr size_t WS_FLAG = (4u << 20) + 65536;
constexpr size_t WS_GP   = 6u << 20;
constexpr size_t WS_YP   = 7u << 20;
constexpr size_t WS_PART = 96u << 20;
constexpr size_t WS_XF   = 16u << 20;
#define HSTR 1310720
constexpr size_t WS_KV   = 24u << 20;
constexpr size_t WS_CTX  = 72u << 20;
constexpr int F_EPOCH = 0, F_TOP = 1024, F_LN = 4096, F_KV = 8192, F_GU = 12288  , F_Y = 20480  , F_PARK = 524288  ,
              F_CTX = 65536  ;

#define PH_STAMP(i)
#define SST(i)
#define S2T(i)
#define SST_OUT()
#define ZST(i)
#define ZST_OUT()
DI unsigned cvtpk(float lo, float hi) { f32x2 v = {lo, hi}; bfx2 b = __builtin_convertvector(v, bfx2); return __builtin_bit_cast(unsigned, b); }
DI float rbf(float x) { return __uint_as_float(cvtpk(x, 0.f) << 16); }
#define DPP_ADD(v, ctrl, rmask) v += __uint_as_float(__builtin_amdgcn_update_dpp(0u, __float_as_uint(v), ctrl, rmask, 0xf, false))
DI unsigned tkey(unsigned fbits, int i) { unsigned u = fbits >> 16; u ^= (u & 0x8000u) ? 0xffffu : 0x8000u; return (u << 16) | (unsigned)(2047 - i); }
DI void merge_top2(unsigned& a1, unsigned& a2, unsigned b1, unsigned b2) { const unsigned lo = min(a1, b1); a1 = max(a1, b1); a2 = max(lo, max(a2, b2)); }
#define DPP_TOP2(a1, a2, ctrl, rmask) merge_top2(a1, a2, __builtin_amdgcn_update_dpp(0u, a1, ctrl, rmask, 0xf, false), __builtin_amdgcn_update_dpp(0u, a2, ctrl, rmask, 0xf, false))

#define GLDS_S(voff, sbase, ldsoff, imm)                                                                        \
    asm volatile("s_mov_b32 m0, %2\n\ts_nop 0\n\tglobal_load_lds_dwordx4 %0, %1 offset:%3"                       \
                 :: "v"(voff), "s"(sbase), "s"(ldsoff), "i"(imm) : "memory")
#define GLDS_C(voff, sbase, ldsoff, imm)                                                                        \
    asm volatile("s_mov_b32 m0, %2\n\ts_nop 0\n\tglobal_load_lds_dwordx4 %0, %1 offset:%3 sc1"                   \
                 :: "v"(voff), "s"(sbase), "s"(ldsoff), "i"(imm) : "memory")
#define LGKM0_BAR()    asm volatile("s_waitcnt lgkmcnt(0)\n\ts_barrier" ::: "memory")
#define SB() __builtin_amdgcn_sched_barrier(0)
#define CAT_(a, b) a##b
#define CAT(a, b) CAT_(a, b)
#define PIN(x) asm volatile("" : "+v"(x))
#define OADV(p_, d_) do { p_ += (d_); asm volatile("" : "+s"(p_)); } while (0)
#define VADD(d, a, b)    asm volatile("v_add_f32 %0, %1, %2" : "=v"(d) : "v"(a), "v"(b))
#define KEEP(x) asm volatile("" :: "v"(x))
#define MFA16(c, a, b) asm volatile("v_mfma_f32_16x16x32_bf16 %0, %1, %2, %0" : "+v"(c) : "v"(a), "v"(b))
#define MFA16Z(c, a, b) asm volatile("v_mfma_f32_16x16x32_bf16 %0, %1, %2, 0" : "=&v"(c) : "v"(a), "v"(b))
#define MFA16C(c, a, b, c0) asm volatile("v_mfma_f32_16x16x32_bf16 %0, %1, %2, %3" : "=&v"(c) : "v"(a), "v"(b), "v"(c0))
#define MF_DONE() do { SB(); asm volatile("s_nop 7\n\ts_nop 7"); SB(); } while (0)
#define ST16_SC1(p, v) asm volatile("global_store_dwordx4 %0, %1, off sc1\n\ts_nop 1" :: "v"(p), "v"(v) : "memory")
#define ST4_SC1(p, v)  asm volatile("global_store_dword %0, %1, off sc1" :: "v"(p), "v"(v) : "memory")
#define FLAG_PUT(p_, val_, on_) do { u64 sv_, tv_; unsigned tz_; const u64 v_ = (val_); const unsigned m_ = __builtin_amdgcn_readfirstlane((on_) ? 1u : 0u); \
        asm volatile("s_cmp_eq_u32 %5, 0\n\ts_cbranch_scc1 1f\n\tv_mov_b64 %1, %3\n\tv_mov_b32 %2, 0\n\ts_mov_b64 %0, exec\n\ts_mov_b64 exec, 1\n\t" \
                     "global_store_dwordx2 %2, %1, %4 sc1\n\ts_mov_b64 exec, %0\n1:"                                                      \
                     : "=&s"(sv_), "=&v"(tv_), "=&v"(tz_) : "s"(v_), "s"(p_), "s"(m_) : "memory", "scc"); } while (0)
#define FLAG_PUTZ(p_, val_, nz_) do { u64 sv_, tv_; unsigned tz_; const u64 v_ = (val_);                                                   \
        asm volatile("s_cmp_lg_u32 %5, 0\n\ts_cbranch_scc1 1f\n\tv_mov_b64 %1, %3\n\tv_mov_b32 %2, 0\n\ts_mov_b64 %0, exec\n\ts_mov_b64 exec, 1\n\t" \
                     "global_store_dwordx2 %2, %1, %4 sc1\n\ts_mov_b64 exec, %0\n1:"                                                      \
                     : "=&s"(sv_), "=&v"(tv_), "=&v"(tz_) : "s"(v_), "s"(p_), "s"(nz_) : "memory", "scc"); } while (0)
#define FLAG_GET(r_, p_) asm volatile("global_load_dwordx2 %0, %1, off sc1" : "=v"(r_) : "v"(p_) : "memory")
#define FLAG_WAIT(r_, p_, tag_, N) do { unsigned c_;                                                                                      \
        asm volatile("s_waitcnt vmcnt(%4)\n\ts_mov_b32 %1, 0x40000\n"                                                                     \
                     "1:\n\tv_cmp_ne_u64 vcc, %3, %0\n\ts_cmp_eq_u64 vcc, 0\n\ts_cbranch_scc1 2f\n\t"                                      \
                     "s_sleep 2\n\tglobal_load_dwordx2 %0, %2, off sc1\n\ts_waitcnt vmcnt(0)\n\ts_sub_u32 %1, %1, 1\n\ts_cbranch_scc0 1b\n"  \
                     "2:" : "+v"(r_), "=&s"(c_) : "v"(p_), "s"(tag_), "i"(N) : "memory", "vcc", "scc"); } while (0)
#define FLAG_WAITM(r_, p_, tag_, N, m_) do { unsigned c_;                                                                                 \
        asm volatile("s_waitcnt vmcnt(%4)\n\ts_mov_b32 %1, 0x40000\n"                                                                     \
                     "1:\n\tv_cmp_ne_u64 vcc, %3, %0\n\ts_and_b32 vcc_lo, vcc_lo, %5\n\ts_cbranch_scc0 2f\n\t"                             \
                     "s_sleep 2\n\tglobal_load_dwordx2 %0, %2, off sc1\n\ts_waitcnt vmcnt(0)\n\ts_sub_u32 %1, %1, 1\n\ts_cbranch_scc0 1b\n"  \
                     "2:" : "+v"(r_), "=&s"(c_) : "v"(p_), "s"(tag_), "i"(N), "s"(m_) : "memory", "vcc", "scc"); } while (0)
typedef __attribute__((address_space(3))) bf16x4 lds_bf16x4;
DI bf16x4 tr_rd(unsigned a) { return __builtin_amdgcn_ds_read_tr16_b64_v4i16((lds_bf16x4*)(uintptr_t)a); }
typedef __attribute__((address_space(3))) bf16x8 lds_bf16x8;
DI bf16x8 lds_rd(unsigned a) { return *(lds_bf16x8*)(uintptr_t)a; }
#define CAT8(lo_, hi_) bf16x8{lo_[0], lo_[1], lo_[2], lo_[3], hi_[0], hi_[1], hi_[2], hi_[3]}

#define POFF 32.f
#define LTHR 8192.f
DI float quad_max(float v) {
    auto r1 = __builtin_amdgcn_permlane16_swap(__float_as_uint(v), __float_as_uint(v), false, false);
    v = fmaxf(__uint_as_float(r1[0]), __uint_as_float(r1[1]));
    auto r2 = __builtin_amdgcn_permlane32_swap(__float_as_uint(v), __float_as_uint(v), false, false);
    return fmaxf(__uint_as_float(r2[0]), __uint_as_float(r2[1])); }
#define QWD 3
#define QS1 0xbfff
constexpr int q_xb(int s) { return s >= 1 && s <= 12; }
constexpr int q_xa(int s) { return s >= 0 && s <= 11; }
constexpr int q_s0(int s) { int n = 0; for (int i = 0; i < s; ++i) n += (QS1 >> i) & 1; return n; }
constexpr int q_kv(int kt, int f) { const int s = (kt - 13) * 16 + f - 12; return s >= 0 && s % 3 == 0 && s / 3 < 12 ? s / 3 : -1; }
constexpr int q_op(int s, int f) { return (f == 0 || f == 2 ? q_xb(s) : f == 8 || f == 10 ? q_xa(s) : f == 12 || f == 14 ? s + QWD < 16 : f == 3 ? s == 12
                                          : f == 4 ? (QS1 >> s) & 1 : 0) + (q_kv(s, f) >= 0); }
constexpr int q_after(int p, int kt) { int n = 0; for (int i = p + 1; i <= kt * 16 + 11; ++i) n += q_op(i >> 4, i & 15); return n; }
constexpr int q_min(int a, int b) { return a < b ? a : b; }
constexpr int q_wait(int kt) {
    int n = kt <= 2 ? 4 * (2 - kt) + q_after(-1, kt) : q_after((kt - 2) * 16 + 2, kt);
    if (kt + 1 >= QWD) n = q_min(n, q_after((kt + 1 - QWD) * 16 + 14, kt));
    if (kt == 13) n = q_min(n, q_after(12 * 16 + 3, kt));
    return n; }
constexpr int q_fl() { int s = 0; while (q_s0(s + 1) < 8) ++s; int kt = s; while (q_wait(kt) > q_after(s * 16 + 6, kt)) ++kt; return kt; }
constexpr int Q_NST = q_s0(16);
static_assert(Q_NST >= 8, "");
constexpr int o_xp(int st, int i) { return st <= 5 ? 64 + 4 * (4 * (st - 4) + i) : 32 * (st - 3) + (i == 0 ? 0 : i == 1 ? 4 : i == 2 ? 12 : 16); }
constexpr int o_wp(int st, int i) { return st <= 7 ? 16 * (st - 4) + 4 + 6 * i : 32 * (st - 5) + 24 + 4 * i; }
constexpr int o_x(int h) { for (int st = 4; st < 16; ++st) for (int i = 0; i < 4; ++i) if (o_xp(st, i) == h) return 4 * st + i; return -1; }
constexpr int o_w(int h) { for (int st = 4; st < 16; ++st) for (int i = 0; i < 2; ++i) if (o_wp(st, i) == h) return 2 * st + i; return -1; }
constexpr int o_after(int p, int e) { int n = 0; for (int h = p + 1; h < e; ++h) n += (o_x(h) >= 0) + (o_w(h) >= 0); return n; }
constexpr int o_wait(int kt) { return o_after(kt == 11 ? o_wp(15, 1) : o_xp(kt + 1, 3), 32 * kt + 24); }
constexpr int L_PFT = 149504, L_DUMP = 155648;
constexpr int CFS = 128;
constexpr unsigned F1S = 0xffff642fu, F1F = 0xffff5310u, F2S = 0xff64fff0u, F2F = 0xff53f21fu;
constexpr int F1T = 3;

__global__ __launch_bounds__(512, 2) void k_all(const u16* __restrict__ x, const u16* __restrict__ ckv, const u16* __restrict__ wuk, const u16* __restrict__ wuv,
                                                const u16* __restrict__ w_r, char* __restrict__ ws, const u16* __restrict__ wq, const u16* __restrict__ wo,
                                                const u16* __restrict__ w_gate, const u16* __restrict__ w_up,
                                                const u16* __restrict__ w_down, u16* __restrict__ out) {
    extern __shared__ __attribute__((aligned(16))) char lds[];
    const int b = blockIdx.x, xcd = b & 7, idx = b >> 3, head = xcd * 4 + (idx >> 3), jj = idx & 7;
    const int hp = head ^ 1;
    const unsigned ldsbase = (unsigned)(uintptr_t)(__attribute__((address_space(3))) char*)lds;
    const int wave = __builtin_amdgcn_readfirstlane(threadIdx.x >> 6);
    const int qbA = 15 - jj, qbB = jj;
#define KROW(g) (((2 * ((g) & 3) + ((g) >> 2)) * 8 + jj) * 32)
    const int qw = wave < 4 ? wave : 7 - wave, qb = wave < 4 ? qbA : qbB;
    bf16x8 qf[8];
    u64 tag;
    u16* outp = out;
    int ri1, ri2;
    PH_STAMP(0);
    SST(0);
    ZST(0);
    u32x4 pk[16];
    bf16x8 xr[4][2][2];
    u32x4 rv;
    const char* xg;
#define QXL1(j) asm volatile("global_load_dwordx4 %0, %1, %2 offset:%3 sc1" : "=v"(xr[(j) >> 2][(j) & 1][((j) >> 1) & 1]) : "v"(xvo), "s"(xg + (size_t)((j) >> 2) * 262144), \
                            "i"(((j) & 1) * 2048 + (((j) >> 1) & 1) * 1024) : "memory")
    const char* sb_[4];
    unsigned qa, qa2; bf16x4 ql[8], qh[8];
#define QFA(s, f) ((((f) & 1) ? qa2 : qa) + (unsigned)(((s) & 3) * 16384 + ((f) >> 3) * 8192 + (((f) >> 2) & 1) * 1024 + (((f) >> 1) & 1) * 512))
#define QFM(s, f) ((s) == 15 ? ((f) & 1) * 8 + ((f) >> 1) : (f))
#define QRL(s, f) ql[(f) & 7] = tr_rd(QFA(s, QFM(s, f)))
#define QRH(s, f) qh[(f) & 7] = tr_rd(QFA(s, QFM(s, f)) + 2048u)
    unsigned xvo, qwo;
    const u16* wqb;
#define QWOFF() ({ unsigned l_ = threadIdx.x & 63; asm volatile("" : "+v"(l_)); (unsigned)(((l_ >> 2) & 7) * (NH * HD) + 32 * (l_ >> 5) + 8 * ((l_ & 3) ^ ((l_ >> 3) & 2))) * 2u; })
    {
        constexpr int LDW = NH * HD, K = DC;
        int tid = threadIdx.x; asm volatile("" : "+v"(tid));
        const int lane = tid & 63;
        const int wm = wave >> 2, wn = wave & 3;
        const int row = b * 8 + wave;
        u32x4 lx[2], lw[2]; u32x2 ep;
        asm volatile("global_load_dwordx4 %0, %5, %6\n\tglobal_load_dwordx4 %1, %5, %6 offset:1024\n\tglobal_load_dwordx4 %2, %5, %7\n\tglobal_load_dwordx4 %3, %5, %7 offset:1024\n\t"
                     "global_load_dwordx2 %4, %8, off sc1"
                     : "=&v"(lx[0]), "=&v"(lx[1]), "=&v"(lw[0]), "=&v"(lw[1]), "=&v"(ep) : "v"(lane * 16), "s"(x + (unsigned)(row * DM)), "s"(w_r), "v"(ws + WS_FLAG + F_EPOCH) : "memory");
#define XKEY(r) (((r) >> 1) & 7)
        unsigned xoff[4], woff[2];
        { const unsigned x0_ = (unsigned)(KROW(wave) + (lane >> 3)) * (K * 2) + (unsigned)((lane & 7) ^ (lane >> 4)) * 16u;
          xoff[0] = x0_; xoff[1] = (x0_ ^ 64u) + 7168u; xoff[2] = x0_ + 14336u; xoff[3] = xoff[1] + 14336u;
          woff[1] = (unsigned)((8 * wave + ((lane >> 2) & 7)) * LDW + hp * HD + 32 * (lane >> 5) + 8 * ((lane & 3) ^ (2 * (wave & 1)))) * 2u; woff[0] = woff[1] + 896u; }
        const char* xsp = (const char*)ckv;
        const char* wsp = (const char*)wuk - 896;
        const char* wsv = (const char*)wuv - 2944;
        const unsigned m0b = ldsbase + wave * 4096;
#define KM0(g, w_) asm volatile("s_add_u32 m0, %0, %1" :: "s"(m0b), "i"(((g) & 1) * 65536 + (w_) * 32768) : "memory", "scc")
#define KRX(i) asm volatile("global_load_lds_dwordx4 %0, %1 offset:%2" :: "v"(xoff[i]), "s"(xsp), "i"((i) * 1024) : "memory")
#define KRW(i) asm volatile("global_load_lds_dwordx4 %0, %1 offset:%2 nt" :: "v"(woff[(i) & 1]), "s"((i) < 2 ? wsp : wsv), "i"((i) * 1024) : "memory")
#define STAGE(g) do { KM0(g, 0); asm volatile("s_nop 0"); KRX(0); KRX(1); KRX(2); KRX(3); KM0(g, 1); asm volatile("s_nop 0"); KRW(0); KRW(1); KRW(2); KRW(3); \
                      OADV(xsp, 128); OADV(wsp, 64 * LDW * 2); OADV(wsv, 64 * LDW * 2); } while (0)
#define KST(i)
        const unsigned pfl = ldsbase + L_DUMP + wave * 1024;
        unsigned pxo, pqo, xra, xso_;
#define PFX(g) GLDS_S(pxo, pfxp, pfl - (g) * 128, (g) * 128)
#define PFQ(i) GLDS_S(pqo, pfqp + (size_t)(4 * (i)) * LDW, pfl, 0)
        STAGE(0);
        SB(); ZST(1);
        int w2 = wave; asm volatile("" : "+s"(w2));
        xg = ws + WS_XF + (unsigned)((w2 < 4 ? qbA * 4 + w2 : qbB * 4 + 7 - w2) * 4096);
        wqb = wq + (unsigned)(8 * w2 * LDW + head * HD);
        const u16* pfqp = wq + (unsigned)((128 * jj + 16 * w2) * LDW + head * HD);
        const u16* pfxp = ckv + (unsigned)((KROW(w2) + 8 * (head & 3)) * DC);
        unsigned xa[2][2], wa[2][2];
        const int l15 = lane & 15, q4 = lane >> 4;
#pragma unroll
        for (int s = 0; s < 2; ++s) {
            const int r0 = l15;
            xa[0][s] = ldsbase + (unsigned)((wm * 128 + r0) * 128 + ((((4 * s + q4) ^ XKEY(r0))) << 4));
            wa[0][s] = (ldsbase + 32768u + (unsigned)(q4 * 4096 + wn * 512 + (l15 >> 2) * 64 + (q4 & 1) * 32 + (lane & 3) * 8)) ^ (32u * s);
            xa[1][s] = xa[0][s] + 65536u; wa[1][s] = wa[0][s] + 65536u;
            PIN(xa[0][s]); PIN(xa[1][s]); PIN(wa[0][s]); PIN(wa[1][s]); }
        u64 pf0;
        { u64 pw_ = (u64)(((w2 & 2) ? wuv : wuk) + (unsigned)((128 + 192 * (w2 & 1) + jj) * LDW + hp * HD)), pc_ = (u64)ckv + (unsigned)(b * 8192 + (w2 & 3) * 2048);
          asm volatile("" : "+s"(pw_), "+s"(pc_));
          unsigned o0_ = (unsigned)(lane >> 1) * (8 * LDW * 2) + (lane & 1) * 128u, o1_ = (lane & 15) * 128u; PIN(o0_); PIN(o1_);
          pf0 = (lane < 48 ? pw_ : pc_) + (lane < 48 ? o0_ : o1_); PIN(pf0); }
        asm volatile("" : "+s"(pfqp), "+s"(pfxp), "+s"(xg), "+s"(wqb), "+s"(outp));
        SB();
        asm volatile("s_waitcnt vmcnt(8)" : "+v"(lx[0]), "+v"(lx[1]), "+v"(lw[0]), "+v"(lw[1]), "+v"(ep) :: "memory");
        KST(1); ZST(2);
#define HALF(v_, i) __uint_as_float(((i) & 1) ? (v_) & 0xffff0000u : (v_) << 16)
#define BOTH(v_) f32x2{HALF(v_, 0), HALF(v_, 1)}
        {
            f32x2 v[8];
#pragma unroll
            for (int i = 0; i < 8; ++i) v[i] = BOTH(lx[i >> 2][i & 3]);
            const float pv_ = __uint_as_float(__builtin_amdgcn_readfirstlane(__float_as_uint(v[0][0])));
            f32x2 s2 = f32x2{0.f, 0.f}, q2 = f32x2{0.f, 0.f};
#pragma unroll
            for (int i = 0; i < 8; ++i) { const f32x2 d = v[i] - pv_; s2 += d; q2 += d * d; }
            float s = s2[0] + s2[1], q = q2[0] + q2[1];
            DPP_ADD(s, 0x111, 0xf); DPP_ADD(q, 0x111, 0xf); DPP_ADD(s, 0x112, 0xf); DPP_ADD(q, 0x112, 0xf); DPP_ADD(s, 0x114, 0xf); DPP_ADD(q, 0x114, 0xf);
            DPP_ADD(s, 0x118, 0xf); DPP_ADD(q, 0x118, 0xf); DPP_ADD(s, 0x142, 0xa); DPP_ADD(q, 0x142, 0xa); DPP_ADD(s, 0x143, 0xc); DPP_ADD(q, 0x143, 0xc);
            const float dm = __uint_as_float(__builtin_amdgcn_readlane(__float_as_uint(s), 63)) * (1.f / DM);
            const float var_bf = rbf(fmaxf(fmaf(-dm, dm, __uint_as_float(__builtin_amdgcn_readlane(__float_as_uint(q), 63)) * (1.f / DM)), 0.f));
            const float mean_bf = rbf(pv_ + dm), den = rbf(__builtin_amdgcn_sqrtf(rbf(var_bf + 1e-5f))), rden = __builtin_amdgcn_rcpf(den);
            u32x4 xw[2]; float r2[2] = {0.f, 0.f};
#pragma unroll
            for (int i = 0; i < 8; ++i) {
                const f32x2 c_ = v[i] - mean_bf;
                const unsigned cp = cvtpk(c_[0], c_[1]);
                const f32x2 cb = BOTH(cp);
                f32x2 n_ = cb * rden;
                n_ += (cb - n_ * den) * rden;
                const unsigned np = cvtpk(n_[0], n_[1]);
                const unsigned wp = lw[i >> 2][i & 3];
                r2[i & 1] = __builtin_amdgcn_fdot2_f32_bf16(__builtin_bit_cast(bfx2, np), __builtin_bit_cast(bfx2, wp), r2[i & 1], false);
                xw[i >> 2][i & 3] = np;
            }
            char* lr = lds + 131072 + wave * 2064 + lane * 16;
            *(u32x4*)lr = xw[0]; *(u32x4*)(lr + 1024) = xw[1];
            s = r2[0] + r2[1];
            DPP_ADD(s, 0x111, 0xf); DPP_ADD(s, 0x112, 0xf); DPP_ADD(s, 0x114, 0xf); DPP_ADD(s, 0x118, 0xf); DPP_ADD(s, 0x142, 0xa); DPP_ADD(s, 0x143, 0xc);
            if (lane == 63) *(float*)(lds + 131072 + wave * 2064 + 2048) = rbf(s);
        }
        SB(); ZST(3);
        STAGE(1);
        asm volatile("s_bitcmp0_b32 %2, 2\n\ts_cbranch_scc1 1f\n\ts_mov_b32 m0, %1\n\ts_nop 0\n\tglobal_load_lds_dword %0, off\n1:" :: "v"(pf0), "s"(pfl), "s"(wave) : "memory", "scc");
        tag = (((u64)(unsigned)__builtin_amdgcn_readfirstlane(ep[1]) << 32) | (unsigned)__builtin_amdgcn_readfirstlane(ep[0])) + 0x9E3779B97F4A7C15ull;
        float rr;
        const unsigned l16 = (unsigned)lane * 16u;
        u32x2 xi_[4];
        typedef __attribute__((address_space(3))) u32x2 lds_u32x2; typedef __attribute__((address_space(3))) float lds_f32;
#define XI_RD(i) xi_[i] = *(lds_u32x2*)(uintptr_t)(xra + ((i) & 1) * 16 + ((i) >> 1) * 1024)
#define XI_ST(i) do { const u32x4 p_ = u32x4{xi_[2 * (i)][0], xi_[2 * (i)][1], xi_[2 * (i) + 1][0], xi_[2 * (i) + 1][1]};               \
            asm volatile("global_store_dwordx4 %0, %1, %2 sc1" :: "v"(xso_), "v"(p_), "s"(ws + WS_XF + (unsigned)(wave * 262144 + (b >> 1) * 2048 + (b & 1) * 128 + (i) * 2097152)) : "memory"); } while (0)
        SB(); ZST(4);
        asm volatile("s_waitcnt vmcnt(%1)\n\ts_bitcmp1_b32 %0, 2\n\ts_cbranch_scc1 1f\n\ts_waitcnt vmcnt(8)\n1:\n\ts_barrier" :: "s"(wave), "i"(9) : "memory", "scc");
        KST(2); ZST(6);
        f32x4 a16[8][4];
        bf16x8 xf[2][4];
        bf16x4 wl[4], wh[4];
#define KXR(kt, q, mb) xf[(q) & 1][mb] = lds_rd(xa[(kt) & 1][(q) >> 1] + (unsigned)((((q) & 1) * 4 + (mb)) * 2048))
#define KWL(kt, s32, nb) wl[nb] = tr_rd(wa[(kt) & 1][(nb) & 1] + (unsigned)((s32) * 16384 + ((nb) >> 1) * 2048))
#define KWH(kt, s32, nb) wh[nb] = tr_rd(wa[(kt) & 1][(nb) & 1] + (unsigned)((s32) * 16384 + ((nb) >> 1) * 2048 + 256))
#define KMF(kt, q, g) do { const bf16x8 w_ = CAT8(wl[(g) >> 2], wh[(g) >> 2]); constexpr int m_ = ((q) & 1) * 4 + ((g) & 3);               \
            if ((kt) == 0 && (q) < 2) { if ((g) >= 8) MFA16Z(a16[m_][(g) >> 2], xf[(q) & 1][(g) & 3], w_); else MFA16Z(a16[m_][(g) >> 2], w_, xf[(q) & 1][(g) & 3]); } \
            else                      { if ((g) >= 8) MFA16(a16[m_][(g) >> 2], xf[(q) & 1][(g) & 3], w_);  else MFA16(a16[m_][(g) >> 2], w_, xf[(q) & 1][(g) & 3]); } } while (0)
        u64 lnf;
#define LNFP (ws + WS_FLAG + F_LN + (wave * 32 + (lane & 31)) * 8)
#define KRQ(i) do { GLDS_S(qwo, wqb + (size_t)(((i) >> 1) * 64) * LDW, ldsbase + wave * 2048 + ((i) >> 1) * 16384 + ((i) & 1) * 896, ((i) & 1) * 128); } while (0)
        constexpr int KRS = 3, KPFD = 3;
#define KG(kt, q, g) do { constexpr int nk_ = (kt) + ((q) == 3), nq_ = ((q) + 1) & 3;                                                     \
            constexpr int xi = ((q) & 1) ? ((g) == 1 ? 0 : (g) == 2 ? 1 : (g) == 5 ? 2 : (g) == 6 ? 3 : -1) : (((g) & 1) && (g) < 8 ? (g) >> 1 : -1); \
            constexpr int ri = (q) == 3 ? ((g) == 9 ? 0 : (g) == 10 ? 1 : (g) == 13 ? 2 : (g) == 14 ? 3 : -1) : (((g) & 1) && (g) > 8 ? ((g) - 9) >> 1 : -1); \
            KMF(kt, q, g); SB();                                                                                                        \
            if (xi >= 0 && nk_ < 8) KXR(nk_, nq_, xi & 3);                                                                              \
            if (((q) & 1) && nk_ < 8) { if (((g) & 3) == 3) KWL(nk_, nq_ >> 1, (g) >> 2); if (((g) & 3) == 0 && (g) > 0) KWH(nk_, nq_ >> 1, (((g) >> 2) + 3) & 3); } \
            if (!((q) & 1) && (g) == 0 && (kt) + (q) > 0) KWH(kt, (q) >> 1, 3);                                                          \
            { constexpr int G = (q) == 3 ? (g) : (q) == 0 ? 16 + (g) : -9, T = (kt) + ((q) == 3 ? 2 : 1);          \
              if (T >= 2 && T < 8) { if (G == 0) KM0(T, 0); if (G == 4 * KRS) KM0(T, 1);                                                 \
                  if (G >= 1 && (G - 1) % KRS == 0 && (G - 1) / KRS < 8) { if ((G - 1) / KRS < 4) KRX(((G - 1) / KRS) & 3); else KRW(((G - 1) / KRS) & 3); } \
                  if (G == 29) OADV(xsp, 128); if (G == 30) OADV(wsp, 64 * LDW * 2); if (G == 31) OADV(wsv, 64 * LDW * 2); } }                                                 \
            if ((q) == 1 && (kt) + KPFD < 8) { if ((kt) >= 1 && (g) == 13) PFX((kt) + KPFD); }                \
            if ((q) == 2 && (kt) == 6 && (g) == 9) { qwo = QWOFF(); PIN(qwo); }                                                         \
            if ((q) == 1 && (kt) == 7) { if ((g) == 9) asm volatile("global_load_dwordx4 %0, %1, %2 sc1" : "=v"(rv) : "v"(xvo), "s"(ws + WS_R + wave * 1024) : "memory"); \
                                         if ((g) == 10) QXL1(0); if ((g) == 13) QXL1(1); if ((g) == 14) QXL1(2); }                      \
            if ((q) == 2 && (kt) == 7 && (g) == 2) QXL1(3);                                                                             \
            if ((q) == 2 && (kt) == 7 && (g) == 9) { int w_ = wave; asm volatile("" : "+s"(w_));                                        \
                sb_[0] = ws + WS_KV + (unsigned)(hp * HSTR + ((w_ >> 2) * 2 + (jj >> 2)) * 65536 + (jj & 3) * 8192 + (w_ & 3) * 2048); asm volatile("" : "+s"(sb_[0])); } \
            if ((q) == 2 && (kt) == 7 && (g) >= 11 && (g) < 14) { sb_[((g) - 10) & 3] = sb_[0] + ((g) - 10) * 262144; asm volatile("" : "+s"(sb_[((g) - 10) & 3])); } \
            if ((q) == 2 && (kt) == 7 && (g) == 14) { qa = ldsbase + (unsigned)((q4 >> 1) * 4096 + (4 * (q4 & 1) + (l15 >> 2)) * 64 + (q4 & 1) * 32 + (lane & 3) * 8); PIN(qa); } \
            if ((q) == 2 && (kt) == 7 && (g) == 15) { qa2 = qa ^ 32u; PIN(qa2); }                                                                              \
            if ((q) == 3 && (kt) == 6 && ri >= 0) KRQ(ri & 3);                                                                          \
            if ((q) == 0 && (kt) == 7 && ri >= 0 && 4 + ri < 2 * QWD) KRQ(4 + (ri & 3));                                                                    \
            if ((q) == 0 && (kt) == 0) { unsigned l_ = lane;                                                                            \
                if ((g) == 9)  { PIN(l_); pxo = (l_ >> 3) * (DC * 2) + (l_ & 7) * 16; PIN(pxo); }                                       \
                if ((g) == 11) { PIN(l_); pqo = (l_ >> 4) * (LDW * 2) + (l_ & 15) * 16; PIN(pqo); }                                     \
                if ((g) == 13) { PIN(l_); xso_ = (l_ >> 3) * 256 + (l_ & 7) * 16; PIN(xso_); }                                          \
                if ((g) == 14) { PIN(l_); xra = (l_ & 7) * 2064 + (ldsbase + 131072 + wave * 128); PIN(xra); }                          \
                if ((g) == 15) { PIN(l_); xra += (l_ >> 4) * 32 + (l_ & 8); PIN(xra); } }                                               \
            if ((q) == 0 && (kt) == 1) { if ((g) == 9 || (g) == 11) { XI_RD((g) - 9); XI_RD((g) - 8); }                                 \
                if ((g) == 12) rr = *(lds_f32*)(uintptr_t)(ldsbase + 131072 + wave * 2064 + 2048); }                                    \
            if ((q) == 1 && (kt) == 1) { if ((g) == 9) XI_ST(0); if ((g) == 10) { KEEP(xi_[0]); KEEP(xi_[1]); } if ((g) == 14) XI_ST(1); if ((g) == 15) { KEEP(xi_[2]); KEEP(xi_[3]); } \
                if ((g) == 0) asm volatile("global_store_dword %0, %1, %2 sc1" :: "v"(0u), "v"(rr), "s"((float*)(ws + WS_R) + row) : "memory"); }     \
            if ((q) == 1 && ((kt) == 3 || (kt) == 4)) { if ((g) == 9) PFQ((2 * (kt) - 6) & 3); if ((g) == 11) PFQ((2 * (kt) - 5) & 3); }                                                    \
            if ((q) == 2 && (kt) == 5 && (g) == 12) FLAG_GET(lnf, LNFP);                                                                \
            SB(); } while (0)
#define KQ(kt, q) do { KG(kt, q, 0); KG(kt, q, 1); KG(kt, q, 2); KG(kt, q, 3); KG(kt, q, 4); KG(kt, q, 5); KG(kt, q, 6); KG(kt, q, 7);    \
                       KG(kt, q, 8); KG(kt, q, 9); KG(kt, q, 10); KG(kt, q, 11); KG(kt, q, 12); KG(kt, q, 13); KG(kt, q, 14); KG(kt, q, 15); } while (0)
#define KEXT(s) (((s) == 1 ? 3 : (s) == 5 ? 1 : 0) + ((s) >= 1 && (s) + KPFD < 8) + ((s) == 3 || (s) == 4 ? 2 : 0))
#define KSTEP(kt) do {                                                                                                                  \
        KQ(kt, 0); KQ(kt, 1); KQ(kt, 2);                                                                                                \
        if ((kt) == 6) FLAG_WAIT(lnf, LNFP, tag, 0);                                   \
                       \
        if ((kt) == 0) asm volatile("s_waitcnt vmcnt(%1) lgkmcnt(0)\n\ts_bitcmp1_b32 %0, 2\n\ts_cbranch_scc1 1f\n\ts_waitcnt vmcnt(%2)\n1:\n\ts_barrier" :: "s"(wave), "i"(KEXT(0) + 1), "i"(KEXT(0)) : "memory", "scc"); \
        else asm volatile("s_waitcnt vmcnt(%0) lgkmcnt(0)\n\ts_barrier" :: "i"(KEXT(kt)) : "memory");                                        \
        KST((kt) + 3);                                                                                                                  \
        if ((kt) == 2) FLAG_PUT(ws + WS_FLAG + F_LN + b * 8, tag, wave == 0);             \
        if ((kt) == 6) FLAG_PUT(ws + WS_FLAG + F_EPOCH, tag - 0x9E3779B97F4A7C15ull + 1, b == 0 && wave == 0);     \
        SB();                                                                                                                           \
        KQ(kt, 3);                                                                                                                      \
    } while (0)
        KXR(0, 0, 0); KXR(0, 0, 1); KWL(0, 0, 0); KWH(0, 0, 0); KXR(0, 0, 2); KXR(0, 0, 3);
        KWL(0, 0, 1); KWH(0, 0, 1); KWL(0, 0, 2); KWH(0, 0, 2); KWL(0, 0, 3); KWH(0, 0, 3);
        SB();
        KSTEP(0); ZST(7); KSTEP(1); KSTEP(2); KSTEP(3); KSTEP(4); KSTEP(5); KSTEP(6);
        xvo = l16;
        KQ(7, 0); KQ(7, 1); KQ(7, 2);
        if (wave < 4) {
            int t2_ = threadIdx.x; asm volatile("" : "+v"(t2_));
            unsigned l7_ = t2_ & 7, c7_ = jj >= 3 ? 7 : 8, b0_ = (unsigned)(((jj + 7) & 7) * 128 + head * 4 + (wave & 3)), b7_ = 3584u + head * 4 + (wave & 3);
            u64 pg_ = (u64)((wave & 1) ? w_up : w_gate), pd_ = (u64)w_down - 1792ull * 8192, po_ = (u64)wo - 2688ull * 8192;
            asm volatile("" : "+s"(c7_), "+s"(b0_), "+s"(b7_), "+s"(pg_), "+s"(pd_), "+s"(po_));
            const unsigned lt_ = l7_ < (unsigned)jj ? l7_ : 0;
            unsigned n_ = b0_ + (13u - lt_) * lt_ * 64u; n_ = l7_ == c7_ ? b7_ : n_;
            u64 p_ = n_ < 2688u ? pd_ : po_; p_ = n_ < 1792u ? pg_ : p_; n_ = n_ < 1792u ? n_ >> 1 : n_;
            *(u64*)(lds + L_PFT + t2_ * 8) = p_ + (u64)n_ * 8192u;
        }
        SB();
        asm volatile("s_waitcnt vmcnt(%0)\n\ts_barrier" :: "i"(2 * QWD + 1) : "memory");
        SB();
#define PKCV(i) do { const f32x4 a_ = ((i) & 2) ? a16[2 * ((i) >> 2)][(i) & 3] : a16[2 * ((i) >> 2) + ((i) & 1)][0];                       \
                     const f32x4 b_ = ((i) & 2) ? a16[2 * ((i) >> 2) + 1][(i) & 3] : a16[2 * ((i) >> 2) + ((i) & 1)][1];                   \
                     pk[i] = u32x4{cvtpk(a_[0], a_[1]), cvtpk(a_[2], a_[3]), cvtpk(b_[0], b_[1]), cvtpk(b_[2], b_[3])}; PIN(pk[i]); } while (0)
#define KL(i) do { KMF(7, 3, 2 * (i)); SB(); if ((i) >= 2) QXL1(2 * (i)); SB(); KMF(7, 3, 2 * (i) + 1); SB(); PKCV(i); if ((i) >= 2) QXL1(2 * (i) + 1); SB(); } while (0)
        KL(0); KL(1); KL(2); KL(3); KL(4); KL(5); KL(6); KL(7);
        KST(10);
        QRL(0, 0); QRH(0, 0); QRL(0, 1); QRH(0, 1); QRL(0, 2); QRH(0, 2); QRL(0, 3); QRH(0, 3);
        SB(); asm volatile("s_nop 3"); SB();
#pragma unroll
        for (int i = 8; i < 16; ++i) PKCV(i);
        SB();
        KST(11); ZST_OUT();
#undef PKCV
#undef KSTEP
#undef PFQ
#undef XKEY
    }
    PH_STAMP(1);
    const char* kvg = ws + WS_KV + (size_t)head * HSTR;
    const unsigned svv = xvo + 32768u;
    u64 pft; bf16x8 ones; unsigned ak, av;
    const int NA = qbA + 1;
    {
        constexpr int LDW = NH * HD, STG = 16384;
        int tid = threadIdx.x; asm volatile("" : "+v"(tid));
        const int lane = tid & 63;
        const unsigned woff = qwo, xvo2 = xvo + 262144u;
        f32x4 acc[2][8];
#define KVST(i) asm volatile("global_store_dwordx4 %0, %1, %2 offset:%3 sc1" :: "v"(((i) & 2) ? svv : xvo), "v"(pk[i]), "s"(sb_[(i) >> 2]), "i"(((i) & 1) * 1024) : "memory")
        u64 kvf = 0;
#define QKV(kt, f) do { constexpr int g_ = q_kv(kt, f), p_ = g_ >> 2; if (g_ >= 0) {                                                       \
            if ((g_ & 3) == 0) asm volatile("s_mov_b32 m0, %0\n\ts_nop 0" :: "s"(ldsbase + (p_ == 1 ? 0 : 65536 + p_ * 16384) + wave * 4096) : "memory"); \
            asm volatile("global_load_lds_dwordx4 %0, %1 offset:%2 sc1" :: "v"(xvo), "s"(kvg + p_ * 32768 + wave * 4096), "i"((g_ & 3) * 1024) : "memory"); } } while (0)
#define KVFP (ws + WS_FLAG + ((lane & 8) ? F_TOP : F_KV + 2048 + (head * 8 + (lane & 7)) * 8))
        const bool top2 = b == 56;
        unsigned* tw = (unsigned*)(lds + 131072 + 2048);
#define QMF(a, kt, f) do { if ((kt) == 0 && (f) < 8) MFA16Z(acc[a][(f) & 7], w_, xr[0][a][0]); else MFA16(acc[a][QFM(kt, f) & 7], w_, xr[(kt) & 3][a][QFM(kt, f) >> 3]); } while (0)
        constexpr float qs = 0.08838834764831845f * 1.4426950408889634f;
        u32x4 qw_[8];
        float qm_[2][2], qsv = qs; asm volatile("" : "+s"(qsv));
#define QWI(n) (2 * ((n) >> 3) + (((n) >> 1) & 1))
#define QWW(n) (2 * (((n) >> 2) & 1) + ((n) & 1))
#define QCN(n) do { if ((n) >= 0 && (n) < 32) { _Pragma("unroll") for (int e_ = 0; e_ < 2; ++e_)                                           \
                asm volatile("v_mul_f32 %0, %1, %2" : "=v"(qm_[(n) & 1][e_]) : "s"(qsv), "v"(acc[QWI(n) & 1][(QWI(n) & 6) + (QWW(n) >> 1)][2 * (QWW(n) & 1) + e_])); } \
            if ((n) >= 1 && (n) <= 32) { qw_[QWI((n) - 1) & 7][QWW((n) - 1)] = cvtpk(qm_[((n) - 1) & 1][0], qm_[((n) - 1) & 1][1]); KEEP(qw_[QWI((n) - 1) & 7][QWW((n) - 1)]); } } while (0)
#define QCG(kt, g) do { if ((kt) == 15) QCN((g) - 10); } while (0)
        constexpr int ZP0 = 238;
#define QPAIR(kt, f, A, B) do { const bf16x8 w_ = CAT8(ql[(f) & 7], qh[(f) & 7]); constexpr int s_ = (kt) + (((f) + 4) >> 4), m_ = ((f) + 4) & 15;  \
            QMF(0, kt, f); SB(); if (s_ < 16) QRL(s_, m_); A; QKV(kt, f); QCG(kt, 2 * (f)); SB();                                      \
            QMF(1, kt, f); SB(); if (s_ < 16) QRH(s_, m_); B; QCG(kt, 2 * (f) + 1);                                                                             \
            { constexpr int z_ = (kt) * 16 + (f) - ZP0;                    \
                                                                    \
              if (z_ == 8) { pft = *(const u64*)(lds + L_PFT + (tid & 255) * 8); }                                                       \
              if (z_ == 15) { av = ldsbase + xvo; ak = av + 65536u; PIN(av); PIN(ak); }                                                   \
              if (z_ == 16) { const unsigned o1 = (lane & 3) ? 0u : 0x3f803f80u; ones = __builtin_bit_cast(bf16x8, u32x4{o1, o1, o1, o1}); PIN(ones); }     \
              if (z_ == 14) PIN(pft); } SB(); if (((f) & 1) && s_ < 16) { __builtin_amdgcn_s_waitcnt(0xC47F); SB(); } } while (0)
        const char* xsp = xg + 3 * 262144;
#define QX(r, rb, ks, v_) asm volatile("global_load_dwordx4 %0, %1, %2 offset:%3 sc1" : "+v"(xr[r][rb][ks]) : "v"(v_), "s"(xsp), "i"((rb) * 2048 + (ks) * 1024) : "memory")
        const unsigned woff1 = woff + 1024u, woff2 = woff + 128u;
        const char* wsp = (const char*)(wqb + (size_t)QWD * 64 * LDW) - 1024;
        const unsigned m0b = ldsbase + wave * 2048;
#define QM0(r) asm volatile("s_add_u32 m0, %0, %1" :: "s"(m0b), "i"((r) * STG) : "memory", "scc")
#define QW1(v_, imm_) asm volatile("global_load_lds_dwordx4 %0, %1 offset:%2" :: "v"(v_), "s"(wsp), "i"(imm_) : "memory")
#define QST(i)
#define QSTEP(kt) do { constexpr int r_ = (kt) & 3;                                                                                     \
        QPAIR(kt, 0, if (q_xb(kt)) QX((r_ + 3) & 3, 0, 1, xvo), );                                                                      \
        QPAIR(kt, 1, , );                                                                                                               \
        QPAIR(kt, 2, if (q_xb(kt)) QX((r_ + 3) & 3, 1, 1, xvo), );                                                                      \
        QPAIR(kt, 3, if ((kt) == 12) FLAG_GET(kvf, KVFP), );                                                                            \
        if ((kt) == 0 && __builtin_expect(top2, 0)) {                                                                                                        \
            unsigned t1 = tkey(rv[0], tid * 4), t2 = 0;                                                                                 \
            _Pragma("unroll") for (int j = 1; j < 4; ++j) merge_top2(t1, t2, tkey(rv[j], tid * 4 + j), 0);                              \
            DPP_TOP2(t1, t2, 0x111, 0xf); DPP_TOP2(t1, t2, 0x112, 0xf); DPP_TOP2(t1, t2, 0x114, 0xf); DPP_TOP2(t1, t2, 0x118, 0xf);     \
            DPP_TOP2(t1, t2, 0x142, 0xa); DPP_TOP2(t1, t2, 0x143, 0xc);                                                                 \
            if (lane == 63) { tw[wave * 2] = t1; tw[wave * 2 + 1] = t2; } SB(); }                                                       \
        QPAIR(kt, 4, if ((QS1 >> (kt)) & 1) KVST(q_s0(kt) & 15), if ((QS1 >> (kt)) & 1) KEEP(pk[q_s0(kt) & 15]));                       \
        QPAIR(kt, 5, , );                                                                                                               \
        QPAIR(kt, 6, , );                                                                                                               \
        QPAIR(kt, 7, , );                                                                                                               \
        QPAIR(kt, 8, if (q_xa(kt)) QX(r_, 0, 0, xvo2), );                                                                               \
        QPAIR(kt, 9, , );                                                                                                               \
        QPAIR(kt, 10, if (q_xa(kt)) QX(r_, 1, 0, xvo2), if ((kt) <= 11) OADV(xsp, 262144));                                             \
        QPAIR(kt, 11, if ((kt) + QWD < 16) QM0((r_ + QWD) & 3), );                                                                      \
        if ((kt) < 15) {                              \
            if (QWD == 4 || (kt) == 0 || (kt) >= 13) asm volatile("s_waitcnt vmcnt(%0) lgkmcnt(0)\n\ts_barrier" :: "i"(q_wait(kt)) : "memory"); \
            else asm volatile("s_waitcnt vmcnt(%0)\n\ts_barrier" :: "i"(q_wait(kt)) : "memory");                                        \
            if ((kt) == 13) { PIN(kvf); const unsigned sh_ = (lane & 8) ? 22u : 0u; const u64 ex_ = (tag << sh_) >> sh_;     \
                if (__builtin_expect(__builtin_amdgcn_ballot_w64((kvf >> sh_) != ex_) != 0, 0)) { unsigned c_; u64 t_;                    \
                    asm volatile("s_mov_b32 %1, 0x40000\n1:\n\ts_sleep 2\n\tglobal_load_dwordx2 %0, %3, off sc1\n\ts_waitcnt vmcnt(0)\n\tv_lshrrev_b64 %2, %5, %0\n\t" \
                                 "v_cmp_ne_u64 vcc, %4, %2\n\ts_cmp_eq_u64 vcc, 0\n\ts_cbranch_scc1 2f\n\ts_sub_u32 %1, %1, 1\n\ts_cbranch_scc0 1b\n2:" \
                                 : "+v"(kvf), "=&s"(c_), "=&v"(t_) : "v"(KVFP), "v"(ex_), "v"(sh_) : "memory", "vcc", "scc"); }           \
                const unsigned r_ = __builtin_amdgcn_readlane((unsigned)kvf, 8); ri1 = r_ & 2047u; ri2 = (r_ >> 11) & 2047u; }           \
            QST((kt) + 1); SB(); }                                                                                                      \
        QPAIR(kt, 12, if ((kt) + QWD < 16) QW1(woff1, 0), );                                                                            \
        QPAIR(kt, 13, , );                                                                                                              \
        QPAIR(kt, 14, if ((kt) + QWD < 16) QW1(woff2, 1024), if ((kt) + QWD < 15) OADV(wsp, 64 * LDW * 2));                             \
        if ((kt) == q_fl()) FLAG_PUT(ws + WS_FLAG + F_KV + 2048 + (hp * 8 + jj) * 8, tag, wave == 0);     \
        QPAIR(kt, 15, , );                                                                                                              \
        if ((kt) == 0 && __builtin_expect(top2, 0)) {                                                                                                        \
            unsigned t1 = tw[0], t2 = tw[1];                                                                                            \
            _Pragma("unroll") for (int w = 1; w < 8; ++w) merge_top2(t1, t2, tw[w * 2], tw[w * 2 + 1]);                                 \
            FLAG_PUT(ws + WS_FLAG + F_TOP, (tag << 22) | (unsigned)__builtin_amdgcn_readfirstlane((~t1 & 2047u) | (~t2 & 2047u) << 11), wave == 0); \
            } \
        SB();                                                                                                                           \
    } while (0)
        asm volatile("s_waitcnt vmcnt(12)" : "+v"(rv) :: "memory");
        SB();
        QST(0);
        KST(12);
        QSTEP(0); QSTEP(1); QSTEP(2); QSTEP(3); QSTEP(4); QSTEP(5); QSTEP(6); QSTEP(7);
        QSTEP(8); QSTEP(9); QSTEP(10); QSTEP(11); QSTEP(12); QSTEP(13); QSTEP(14); QSTEP(15);
        QCN(22); QCN(23); QCN(24); QCN(25); QCN(26); QCN(27); QST(16); SB(); QCN(28); QCN(29); QCN(30); QCN(31); QCN(32);
#pragma unroll
        for (int i = 0; i < 8; ++i) { qf[i] = __builtin_bit_cast(bf16x8, qw_[i]); PIN(qf[i]); }
#undef QSTEP
    }
    PH_STAMP(2);
    const int kz = xcd & 1, kp = xcd >> 1, nt = kz * 4 + (idx >> 3), tile = jj * 8 + nt;
#define K64(s) ((head * 2) ^ (s))
#define OWOFF() ({ unsigned l_ = threadIdx.x & 63; asm volatile("" : "+v"(l_)); (unsigned)(((l_ >> 2) & 7) * DM + 32 * (l_ >> 5) + 8 * ((l_ & 3) ^ ((l_ >> 4) & 1))) * 2u; })
    const int n0 = (nt ^ 2) * 128, dn1 = (((nt ^ 3) - (nt ^ 2)) * 128 + 64) * 2, ntc = nt ^ 2 ^ (kp >> 1);
    const u16* wob = wo + (size_t)(8 * wave) * DM + n0;
    u64 cxf = tag + 1;
#define CFB (ws + WS_FLAG + F_CTX)
#define CFO() ({ unsigned l_ = threadIdx.x & 15, s0_ = (unsigned)((qb * 4 + qw) * 32 + head), s1_ = (unsigned)((qbB * 4 + 3 - (wave & 3)) * 32 + (head ^ 8)); \
                 asm volatile("" : "+v"(l_), "+s"(s0_), "+s"(s1_)); (l_ < 8 ? l_ ^ s0_ : s1_) * CFS; })
    u32x4 pc[8];
#define EAR_(v_, b_, pol_) "s_add_u32 m0, m0, 0x400\n\ts_nop 0\n\tglobal_load_lds_dwordx4 " v_ ", " b_ pol_ "\n\t"
#define EALD(h_) do { unsigned l_ = threadIdx.x & 63; asm volatile("" : "+v"(l_)); const unsigned lx_ = ((l_ >> 3) * DM + 8 * ((l_ & 7) ^ (l_ >> 3))) * 2u; \
        const int rb_ = ((h_) ? qbB * 8 + qw * 2 : qbA * 8 + (wave & 3) * 2) + (kp & 1);                                                  \
        asm volatile("s_mov_b32 m0, %5\n\t" EAR_("%0", "%3", "") EAR_("%1", "%3", "") EAR_("%2", "%4", " sc1") "global_load_lds_dwordx4 %2, %4 offset:1024 sc1" \
                     :: "v"(lx_), "v"(lx_ + 8 * DM * 2), "v"(l_ * 16u), "s"(x + (size_t)rb_ * 16 * DM + ntc * 128 + (kp >> 1) * 64),              \
                        "s"(ws + WS_XF + (size_t)(2 * ntc + (kp >> 1)) * 262144 + rb_ * 2048),                                             \
                        "s"(ldsbase + 131072 - 1024 + (wave & 3) * 8192 + (h_) * 4096) : "memory", "scc"); } while (0)
    const unsigned i1 = (((kz ? F1S : F1F) >> (4 * jj)) & 15u) * 16u + (kp * 4 + (idx >> 3));
    const unsigned i2 = (((kz ? F2S : F2F) >> (4 * jj)) & 15u) * 16u + (kp * 4 + (idx >> 3));
    const unsigned nf1 = i1 / 112u, nf2 = i2 / 112u;
    const unsigned k1 = i1 / 14u;
    unsigned ypub = 1;
    {
        int tid = threadIdx.x; asm volatile("" : "+v"(tid));
        const int lane = tid & 63;
        const unsigned l16 = (unsigned)lane * 16u;
#define EX(x) __builtin_amdgcn_exp2f(x)
#define TBAR() do { SB(); __builtin_amdgcn_s_waitcnt(0x0070); asm volatile("s_barrier" ::: "memory"); } while (0)
#define BRQ(t, r) do { constexpr int g_ = (r) >> 2; const int tt_ = (t) + (g_ == 2 ? 1 : 2);                                               \
            const unsigned o_ = (unsigned)((wave & 3) * 4096 + (g_ == 1 || g_ == 2 ? 16384 : 0));                                          \
            if (((r) & 3) == 0) GLDS_C(l16, kvg + (size_t)tt_ * 65536 + (g_ >= 2 ? 32768 : 0) + o_, ldsbase + (g_ >= 2 ? 0 : 65536) + (tt_ & 1) * 32768 + o_, 0); \
            else asm volatile("global_load_lds_dwordx4 %0, %1 offset:%2 sc1" :: "v"(l16), "s"(kvg + (size_t)tt_ * 65536 + (g_ >= 2 ? 32768 : 0) + o_), "i"(((r) & 3) * 1024) : "memory"); } while (0)
#define BRQ4(t, g) do { BRQ(t, 4 * (g)); BRQ(t, 4 * (g) + 1); BRQ(t, 4 * (g) + 2); BRQ(t, 4 * (g) + 3); } while (0)
#define BRQ2C(t, r) do { constexpr int g_ = (r) >> 2; const int tt_ = (t) + (g_ == 2 ? 1 : 2);                                             \
            const unsigned o_ = (unsigned)((wave & 3) * 4096 + (g_ == 1 || g_ == 2 ? 16384 : 0));                                          \
            asm volatile("s_cmp_eq_u32 %5, 0\n\ts_cbranch_scc1 1f\n\ts_mov_b32 m0, %2\n\ts_nop 0\n\tglobal_load_lds_dwordx4 %0, %1 offset:%3 sc1\n\t"  \
                         "global_load_lds_dwordx4 %0, %1 offset:%4 sc1\n1:"                                                               \
                         :: "v"(l16), "s"(kvg + (size_t)tt_ * 65536 + (g_ >= 2 ? 32768 : 0) + o_), "s"(ldsbase + (g_ >= 2 ? 0 : 65536) + (tt_ & 1) * 32768 + o_), \
                            "i"(((r) & 3) * 1024), "i"(((r) & 3) * 1024 + 1024), "s"(isb) : "memory", "scc"); } while (0)
        const unsigned isb = wave >> 2;
        u64 kf2 = tag;
        const char* kfb = ws + WS_FLAG + F_KV + head * 64;
#define KCHK() do { PIN(kf2); if (__builtin_expect((__builtin_amdgcn_ballot_w64(kf2 != tag) | (u64)((33u >> t) & 1u)) != 0, 0)) { \
            unsigned c_, a_;                                                                                                              \
            FLAG_PUTZ(ws + WS_FLAG + F_KV + (hp * 8 + jj) * 8, tag, (unsigned)(wave ^ 4) | (unsigned)t);                                  \
            asm volatile("s_cmp_lt_u32 %6, 5\n\ts_cbranch_scc1 3f\n\tv_lshrrev_b32 %2, 1, %5\n\tv_and_b32 %2, 56, %2\n\ts_mov_b32 %1, 0x40000\n" \
                         "1:\n\tglobal_load_dwordx2 %0, %2, %3 sc1\n\ts_cmp_eq_u32 %6, 5\n\ts_cbranch_scc1 3f\n\ts_waitcnt vmcnt(0)\n\tv_cmp_ne_u64 vcc, %4, %0\n\t" \
                         "s_cmp_eq_u64 vcc, 0\n\ts_cbranch_scc1 3f\n\ts_sleep 2\n\ts_sub_u32 %1, %1, 1\n\ts_cbranch_scc0 1b\n"           \
                         "3:" : "+v"(kf2), "=&s"(c_), "=&v"(a_) : "s"(kfb), "s"(tag), "v"(l16), "s"(t) : "memory", "vcc", "scc"); } SB(); } while (0)
#define PFW1(i_) do { unsigned a_; const u64 p_ = ((u64)(unsigned)__builtin_amdgcn_readlane((int)(pft >> 32), i_) << 32) | (unsigned)__builtin_amdgcn_readlane((int)pft, i_); \
            asm volatile("v_lshlrev_b32 %0, 3, %2\n\ts_mov_b32 m0, %3\n\ts_nop 2\n\tglobal_load_lds_dword %0, %1"                        \
                         : "=&v"(a_) : "s"(p_), "v"(l16), "s"(ldsbase + 131072 + (wave & 3) * 8192) : "memory"); } while (0)
#define ONCE2(n_) int CAT(n_once, __LINE__) = (int)(n_); asm volatile("" : "+s"(CAT(n_once, __LINE__))); _Pragma("unroll 1") for (int z_ = 0; z_ < CAT(n_once, __LINE__); ++z_)
#define AST(i)
#define XST(i)
#define TS0()
#define TS1()
        AST(0);
        asm volatile("s_bitcmp0_b32 %0, 2\n\ts_cbranch_scc1 1f\n\ts_setprio 1\n1:" :: "s"(wave) : "scc");
        {
            f32x4 o[8][2], ls[2], ng[2], S[2][4];
            bf16x8 pf[2][2], fr[16];
            float e[16];
            constexpr bool ZC = false;
            constexpr bool HOT = false;
            const unsigned lb16 = ldsbase + l16;
            int t = 0;
#define KOFF(f) ((((f) & 3) * 2 + (((f) >> 2) & 1)) * 1024)
#define RD(kb, n) do { constexpr int k_ = ((kb) + ((n) >> 4)) & 3, n_ = (n) & 15;                                                          \
            fr[n_] = n_ < 8 ? lds_rd(av + (unsigned)(((k_ + 3) & 3) * 8192 + n_ * 1024)) : lds_rd(ak + (unsigned)(((k_ + 1) & 3) * 8192 + KOFF(n_ & 7))); } while (0)
#define MX(p, n, a) do { if (ZC) MFA16Y(o[n][a], fr[n], pf[(p) ^ 1][a]); else MFA16(o[n][a], fr[n], pf[(p) ^ 1][a]); } while (0)
#define MY(p, n, h) do { constexpr int a_ = ((n) >> 1) & 1, t_ = 2 * ((n) & 1) + (h);                                                      \
                         if (t_) MFA16(S[(p) ^ 1][(((n) >> 1) & 2) + a_], fr[((n) & 12) + t_], qf[2 * t_ + a_]);                           \
                         else MFA16C(S[(p) ^ 1][(((n) >> 1) & 2) + a_], fr[(n) & 12], qf[a_], ng[a_]); } while (0)
#define GX(p, n, F1, F2) do { MX(p, n, 0); SB(); F1; SB(); MX(p, n, 1); SB(); F2; SB(); } while (0)
#define GY(p, n, F1, F2) do { MY(p, n, 0); SB(); F1; SB(); MY(p, n, 1); SB(); F2; SB(); } while (0)
#define ML(p, a) do { if (ZC) MFA16Y(ls[a], ones, pf[(p) ^ 1][a]); else MFA16(ls[a], ones, pf[(p) ^ 1][a]); } while (0)
#define GL(p, F1, F2) do { ML(p, 0); SB(); F1; SB(); ML(p, 1); SB(); F2; SB(); } while (0)
#define SV(p, i) S[p][(((i) >> 1) & 2) + ((i) >> 3)][(i) & 3]
#define E1(p, i) do { e[i] = EX(SV(p, i)); if (HOT) KEEP(e[i]); else PIN(e[i]); } while (0)
#define C1(p, j) do { u32x4 w_ = __builtin_bit_cast(u32x4, pf[p][(j) >> 2]); w_[(j) & 3] = cvtpk(e[2 * (j)], e[2 * (j) + 1]);               \
                      pf[p][(j) >> 2] = __builtin_bit_cast(bf16x8, w_); if (HOT) KEEP(w_[(j) & 3]); else PIN(pf[p][(j) >> 2]); } while (0)
#define DQ(kb, g) do { if (ISB_ && (kb) == 3) { if ((g) == 0) KCHK(); BRQ(t, g); } if (ISB_ && (kb) == 0 && (g) == 1) PFW1(t); } while (0)
#define W4() do { SB(); __builtin_amdgcn_s_waitcnt(0xC37F); } while (0)
#define US(kb, ISB) do { constexpr bool HOT = true, ZC = (ISB) == 3, ISB_ = (ISB) == 1; constexpr int p_ = (kb) & 1; constexpr bool B_ = (kb) == 2;                    \
            GX(p_, 0, RD(kb, 7); if ((kb) == 3) { ak ^= 32768u; PIN(ak); }, E1(p_, 0); DQ(kb, 0));                                        \
            GX(p_, 1, RD(kb, 8); if ((kb) == 0) { av ^= 32768u; PIN(av); }, E1(p_, 1); DQ(kb, 1));                                        \
            GL(p_, E1(p_, 2), E1(p_, 3));                                                                                                 \
            GX(p_, 2, RD(kb, 9); C1(p_, 0), E1(p_, 4); DQ(kb, 2));                                                                        \
            GX(p_, 3, RD(kb, 10); W4(), E1(p_, 5); DQ(kb, 3));                                                                            \
            GX(p_, 4, RD(kb, 11); C1(p_, 1), E1(p_, 6); DQ(kb, 4));                                                                       \
            GX(p_, 5, RD(kb, 12); C1(p_, 2), E1(p_, 7); DQ(kb, 5));                                                                       \
            GX(p_, 6, RD(kb, 13); C1(p_, 3), E1(p_, 8); DQ(kb, 6));                                                                       \
            GX(p_, 7, RD(kb, 14); W4(), E1(p_, 9); DQ(kb, 7));                                                                            \
            GY(p_, 8, RD(kb, 15), E1(p_, 10); DQ(kb, 8));                                           \
            GY(p_, 9, if (!B_) RD(kb, 16); C1(p_, 4), E1(p_, 11); DQ(kb, 9));                                                             \
            GY(p_, 10, if (B_) { TS0(); if (ISB_) { __builtin_amdgcn_s_waitcnt(0x0071); asm volatile("s_barrier" ::: "memory"); } else TBAR(); \
                                 TS1(); SB(); RD(kb, 16); }                                                                               \
                       RD(kb, 17); C1(p_, 5), E1(p_, 12); DQ(kb, 10));                                                                    \
            GY(p_, 11, RD(kb, 18); if (!B_) W4(), E1(p_, 13); DQ(kb, 11));                                                                \
            GY(p_, 12, RD(kb, 19), E1(p_, 14); DQ(kb, 12));                                                \
            GY(p_, 13, RD(kb, 20); C1(p_, 6), E1(p_, 15); DQ(kb, 13));                                                                    \
            GY(p_, 14, RD(kb, 21); C1(p_, 7), DQ(kb, 14));                                 \
            GY(p_, 15, RD(kb, 22), DQ(kb, 15); W4());                                                                                     \
        } while (0)
            asm volatile("s_waitcnt vmcnt(%0) lgkmcnt(0)\n\ts_barrier" :: "i"(4 + (QS1 >> 15)) : "memory");
            AST(1);
#pragma unroll
            for (int s_ = 0; s_ < 8; ++s_) fr[s_] = lds_rd(ak + (unsigned)KOFF(s_));
            BRQ2C(-1, 12); BRQ2C(-1, 14);
            SB();
#define M0_(n, a) do { if ((n) & 3) MFA16(S[0][(((n) >> 1) & 2) + (a)], fr[n], qf[2 * ((n) & 3) + (a)]); else MFA16Z(S[0][(((n) >> 1) & 2) + (a)], fr[n], qf[a]); } while (0)
#define G0_(n) do { M0_(n, 0); SB(); if (Q_NST + (n) < 16) KVST((Q_NST + (n)) & 15); SB(); M0_(n, 1); SB(); RD(0, 8 + (n));               \
                    if (Q_NST + (n) < 16) KEEP(pk[(Q_NST + (n)) & 15]); SB(); } while (0)
            G0_(0); G0_(1); G0_(2); G0_(3); G0_(4); G0_(5); G0_(6); G0_(7);
            MF_DONE();
            { ONCE2(isb * min(jj, 1)) { PFW1(0); } }
            {
                PIN(S[0][0]); PIN(S[0][1]); PIN(S[0][2]); PIN(S[0][3]);
                if (__builtin_expect((qb | qw) == 0, 0)) {
#pragma unroll
                    for (int r = 0; r < 4; ++r) { S[0][2][r] = -__builtin_inff(); if (4 * (lane >> 4) + r > (lane & 15)) S[0][0][r] = S[0][3][r] = -__builtin_inff(); } }
#pragma unroll
                for (int a = 0; a < 2; ++a) {
                    float mx_;
#define MX3(d, a_, b_, c_) asm("v_max3_f32 %0, %1, %2, %3" : "=v"(d) : "v"(a_), "v"(b_), "v"(c_))
                    MX3(mx_, SV(0, 8 * a), SV(0, 8 * a + 1), SV(0, 8 * a + 2)); MX3(mx_, mx_, SV(0, 8 * a + 3), SV(0, 8 * a + 4)); MX3(mx_, mx_, SV(0, 8 * a + 5), SV(0, 8 * a + 6));
                    { float m7_ = SV(0, 8 * a + 7), n_ = -1e30f; MX3(mx_, mx_, m7_, n_); }
                    { auto r1 = __builtin_amdgcn_permlane16_swap(__float_as_uint(mx_), __float_as_uint(mx_), false, false); MX3(mx_, r1[0], r1[1], r1[1]);
                      auto r2 = __builtin_amdgcn_permlane32_swap(__float_as_uint(mx_), __float_as_uint(mx_), false, false); MX3(mx_, r2[0], r2[1], r2[1]); }
                    mx_ = -POFF - mx_;
                    ng[a] = f32x4{mx_, mx_, mx_, mx_}; PIN(ng[a]);
                }
#pragma unroll
                for (int i = 0; i < 16; ++i) { float v_ = SV(0, i) + ng[i >> 3][0]; PIN(v_); SV(0, i) = v_; }
                PIN(S[0][0]); PIN(S[0][1]); PIN(S[0][2]); PIN(S[0][3]);
            }
            SB();
            GY(0, 8, E1(0, 0), RD(0, 16); E1(0, 1)); GY(0, 9, E1(0, 2); C1(0, 0), RD(0, 17); E1(0, 3));
            GY(0, 10, E1(0, 4); C1(0, 1), RD(0, 18); E1(0, 5)); GY(0, 11, E1(0, 6); C1(0, 2), RD(0, 19); E1(0, 7));
            GY(0, 12, E1(0, 8); C1(0, 3), RD(0, 20); E1(0, 9)); GY(0, 13, E1(0, 10); C1(0, 4), RD(0, 21); E1(0, 11));
            GY(0, 14, E1(0, 12); C1(0, 5), RD(0, 22); E1(0, 13)); GY(0, 15, E1(0, 14); C1(0, 6), E1(0, 15); C1(0, 7));
#define SCOPY() do { MF_DONE(); _Pragma("unroll") for (int i = 0; i < 4; ++i) { S[1][i] = S[0][i]; PIN(S[1][i]); } } while (0)
            if (__builtin_expect((qb | qw) == 0, 0)) { SCOPY(); pf[0][0] = pf[0][1] = bf16x8{}; PIN(pf[0][0]); PIN(pf[0][1]); }
            W4(); asm volatile("s_nop 1"); SB();
            AST(2);
            const int nu = 4 * qb + qw - 2;
#define MFA16Y(c, a, b) asm volatile("v_mfma_f32_16x16x32_bf16 %0, %1, %2, 0" : "+v"(c) : "v"(a), "v"(b))
#pragma unroll
            for (int d = 0; d < 18; ++d) { if (d < 16) asm volatile("" : "=v"(o[d >> 1][d & 1])); else asm volatile("" : "=v"(ls[d & 1])); }
            if (__builtin_expect(nu < 0, 0)) { const bf16x8 z_ = bf16x8{}; asm volatile("s_nop 1" :: "v"(z_));
#pragma unroll
                for (int d = 0; d < 18; ++d) { if (d < 16) MFA16Y(o[d >> 1][d & 1], z_, z_); else MFA16Y(ls[d & 1], z_, z_); } }
            { int n1_ = nu; asm volatile("" : "+s"(n1_)); ONCE2(min(n1_ + 2, 2) >> 1) { US(1, 3); } }
            {
                const int nl = nu >> 2, nB = isb ? nl : 0;
#pragma unroll 1
                while (t < nB) { US(2, 1); US(3, 1); ++t; US(0, 1); US(1, 1); }
#pragma unroll 1
                while (t < nl) { US(2, 0); US(3, 0); ++t; US(0, 0); US(1, 0); }
            }
            AST(3);
            SST(1);
#define PCOPY() do { SCOPY(); pf[0][0] = pf[1][0]; pf[0][1] = pf[1][1]; PIN(pf[0][0]); PIN(pf[0][1]); SB(); asm volatile("s_nop 1"); } while (0)
            {
                const int nr = nu < 0 ? 0 : nu & 3, ne = nu < -1 ? 0 : nu + 1;
                if (__builtin_expect(nr >= 1, 1)) {
                    US(2, 2);
                    if (__builtin_expect(nr >= 2, 1)) {
                        if (isb) { KCHK(); asm volatile("s_setprio 0"); BRQ4(t, 0); BRQ4(t, 1); BRQ4(t, 2); BRQ4(t, 3); asm volatile("s_setprio 1"); }
                        US(3, 2);
                        if (__builtin_expect(nr == 3, 0)) US(0, 2);
                    }
                }
                int np = ne; asm volatile("" : "+s"(np));
                if (__builtin_expect(np & 1, 0)) PCOPY();
                KEEP(ng[0]); KEEP(ng[1]);
                t = qb;
            }
#define RDV(n) fr[n] = lds_rd(avf + (unsigned)((n) * 1024))
#define M1(i) do { e[i] = __builtin_amdgcn_fmed3f(e[i], 0.f, rmp[(i) & 3]); PIN(e[i]); } while (0)
#define E2(i) E1(1, i)
#define CST(i) asm volatile("global_store_dwordx4 %0, %1, %2 offset:%3 sc1\n\ts_nop 1" :: "v"(l16), "v"(pc[i]), "s"(cb + ((i) >> 2) * 262144), "i"((((i) >> 1) & 1) * 1024 + ((i) & 1) * 2048) : "memory")
#define NRM(ISB, i) do { float y_[8];                                              \
                _Pragma("unroll") for (int z_ = 0; z_ < 8; ++z_) { y_[z_] = o[((i) & 6) + (z_ >> 2)][(i) & 1][z_ & 3] * inv[(i) & 1]; PIN(y_[z_]); } \
                pc[i] = u32x4{cvtpk(y_[0], y_[1]), cvtpk(y_[2], y_[3]), cvtpk(y_[4], y_[5]), cvtpk(y_[6], y_[7])};                        \
                PIN(pc[i]); if (!(ISB)) CST(i); SB(); } while (0)
#define XLD(b_) do { _Pragma("unroll") for (int n = 0; n < 8; ++n) asm volatile("global_load_dwordx4 %0, %1, %2 offset:%3 sc1" : "=v"(fr[n]) : "v"(l16 + (n >> 2) * 4096u), "s"(b_), "i"((n & 3) * 1024) : "memory"); \
                asm volatile("s_waitcnt vmcnt(0)" : "+v"(fr[0]), "+v"(fr[1]), "+v"(fr[2]), "+v"(fr[3]), "+v"(fr[4]), "+v"(fr[5]), "+v"(fr[6]), "+v"(fr[7]) :: "memory"); } while (0)
#define MFB(a_, b_, c_) __builtin_amdgcn_mfma_f32_16x16x32_bf16(a_, b_, c_, 0, 0, 0)
#define EXACT() do { __builtin_amdgcn_s_waitcnt(0xC07F); MF_DONE();                                                                                                           \
                float mr_[2] = {-1e30f, -1e30f}; const int nu_ = 4 * qb + qw2;                                                            \
                ls[0] = ls[1] = f32x4{}; _Pragma("unroll") for (int d = 0; d < 16; ++d) o[d >> 1][d & 1] = f32x4{};                       \
                _Pragma("unroll 1") for (int u = 0; u <= nu_; ++u) {                                                                      \
                    const char* kb_ = kvg + (size_t)(u >> 2) * 65536 + (u & 3) * 8192;                                                    \
                    XLD(kb_);                                                                                                             \
                    _Pragma("unroll") for (int i = 0; i < 4; ++i) { f32x4 s_ = f32x4{};                                                   \
                        _Pragma("unroll") for (int d = 0; d < 4; ++d) s_ = MFB(fr[2 * d + (i >> 1)], qf[2 * d + (i & 1)], s_);            \
                        S[1][i] = s_; }                                                                                                   \
                    if (u == nu_) { _Pragma("unroll") for (int r = 0; r < 4; ++r) { S[1][2][r] = -__builtin_inff();                       \
                        if (4 * (ln2 >> 4) + r > (ln2 & 15)) S[1][0][r] = S[1][3][r] = -__builtin_inff(); } }                             \
                    XLD(kb_ + 32768);                                                                                                     \
                    _Pragma("unroll") for (int a = 0; a < 2; ++a) {                                                                       \
                        float mx_ = SV(1, 8 * a); _Pragma("unroll") for (int r = 1; r < 8; ++r) mx_ = fmaxf(mx_, SV(1, 8 * a + r));       \
                        mx_ = fmaxf(quad_max(mx_), mr_[a]); const float al_ = EX(mr_[a] - mx_); mr_[a] = mx_;                             \
                        u32x4 w_; _Pragma("unroll") for (int j = 0; j < 4; ++j) w_[j] = cvtpk(EX(SV(1, 8 * a + 2 * j) - mx_), EX(SV(1, 8 * a + 2 * j + 1) - mx_)); \
                        const bf16x8 p_ = __builtin_bit_cast(bf16x8, w_);                                                                 \
                        ls[a] = MFB(ones, p_, ls[a] * al_);                                                                               \
                        _Pragma("unroll") for (int d = 0; d < 8; ++d) o[d][a] = MFB(fr[d], p_, o[d][a] * al_); } }                        \
                pf[1][0] = pf[1][1] = bf16x8{}; _Pragma("unroll") for (int n = 4; n < 8; ++n) fr[n] = bf16x8{};     \
                PIN(pf[1][0]); PIN(pf[1][1]); MF_DONE(); } while (0)
#define TAIL(ISB) do { float rmp[4];                                                                                         \
                XST(0);                                                                                                                   \
                MX(1, 0, 0); SB();                                                                                                        \
                int ln2 = lane, qw2 = qw; asm volatile("" : "+v"(ln2), "+s"(qw2));   \
                fr[7] = lds_rd(av + (unsigned)((qw2 + 3) & 3) * 8192u + 7168u); SB();                                                     \
                MX(1, 0, 1); SB(); E2(0); SB();                                                                                           \
                GX(1, 1, E2(1), E2(2));                                                                                                   \
                GL(1, E2(3), E2(8));                                                                                                      \
                float fl_ = (float)((ln2 & 15) - 4 * (ln2 >> 4)); PIN(fl_);                                                               \
                unsigned avf = lb16 + (unsigned)(t & 1) * 32768u + (unsigned)qw2 * 8192u; PIN(avf); SB();                                 \
                GX(1, 2, E2(9), E2(10));                                                                             \
                GX(1, 3, E2(11), _Pragma("unroll") for (int r = 0; r < 4; ++r) { rmp[r] = (fl_ + (0.5f - r)) * 1.2089258196146292e24f; PIN(rmp[r]); }); \
                if (__builtin_expect(qw2 == 2, 0)) {                               \
                    if (ISB) TBAR(); else LGKM0_BAR(); }                                                                                  \
                SB();                                                                                                                     \
                RDV(0); RDV(1); RDV(2); RDV(3); SB();                                                                                     \
                GX(1, 4, E2(12), M1(0); M1(1));                                                                                           \
                GX(1, 5, RDV(4); E2(13), M1(2); M1(3));                                                                                   \
                GX(1, 6, RDV(5); E2(14), C1(1, 0); C1(1, 1));                                                                             \
                GX(1, 7, RDV(6); E2(15), C1(1, 4); C1(1, 5));                                                                             \
                RDV(7); M1(12); M1(13); M1(14); M1(15); e[4] = e[5] = e[6] = e[7] = 0.f; C1(1, 2); C1(1, 3); SB();                        \
                C1(1, 6); C1(1, 7); SB();                                                                                                 \
                XST(1);                                                                                                                   \
                if (__builtin_expect(qw2 < 2, 0)) { if (ISB) TBAR(); else LGKM0_BAR(); }                                                  \
                SB();                                                                                                                     \
                XST(2); XST(3);                                                                                                           \
                float inv[2];                                                                                                \
                int hd2 = head; asm volatile("" : "+s"(hd2)); const char* cb = ws + WS_CTX + (unsigned)(hd2 * 524288 + (qb * 8 + qw2 * 2) * 2048); SB(); \
                GL(0, , ); GX(0, 0, , ); GX(0, 1, , ); GX(0, 2, , );                                                                      \
                PIN(ls[0]); PIN(ls[1]);                                                                                                   \
                const bool c_ = __builtin_amdgcn_ballot_w64(!(ls[0][0] + ls[1][0] <= LTHR)) != 0; SB();                                   \
                GX(0, 3, , );                                                                                                             \
                if (__builtin_expect(c_, 0)) EXACT();                                                                                     \
                SB();                                                                                                                     \
                inv[0] = __builtin_amdgcn_rcpf(ls[0][0]); inv[1] = __builtin_amdgcn_rcpf(ls[1][0]); PIN(inv[0]); PIN(inv[1]); SB();       \
                GX(0, 4, , asm volatile("s_nop 0"));                                                                                      \
                NRM(ISB, 0); GX(0, 5, , ); NRM(ISB, 1); GX(0, 6, , ); NRM(ISB, 2); GX(0, 7, , ); NRM(ISB, 3);                             \
                XST(4);                                                                                                                   \
                NRM(ISB, 4); NRM(ISB, 5); NRM(ISB, 6); NRM(ISB, 7);                                                                       \
                if (ISB) { asm volatile("s_setprio 0"); asm volatile("" : "+s"(t)); KCHK(); if (t + 2 < NA) { BRQ4(t, 0); BRQ4(t, 1); BRQ4(t, 3); } BRQ4(t, 2); \
                           CST(0); CST(1); CST(2); CST(3); CST(4); CST(5); CST(6); CST(7); }                                              \
                XST(5);                                                                                                                   \
            } while (0)
#define SPIN() do { PIN(S[1][0]); PIN(S[1][1]); PIN(S[1][2]); PIN(S[1][3]); } while (0)
            { ONCE2(isb ^ 1) { SPIN(); TAIL(0); } }
            { ONCE2(isb) { SPIN(); TAIL(1); } }
            SST(2);
            AST(4);
        }
        if (wave >= 4) {
            int t = qbB + 1;
            const int wv = wave & 3;
            XST(6);
#define WOLD(h_) do { const unsigned ow_ = OWOFF(); const char* wp_ = (const char*)(wo + (size_t)(K64(2 * (h_)) * 64 + 16 * wv) * DM + n0);  \
                _Pragma("unroll") for (int i = 0; i < 8; ++i)                                                                             \
                    GLDS_S(ow_, wp_ + (size_t)((i >> 2) * 64 + 8 * ((i >> 1) & 1)) * (DM * 2) + (i & 1) * dn1,                             \
                           ldsbase + 65536 + (h_) * 32768 + (i >> 2) * 16384 + (2 * wv + ((i >> 1) & 1)) * 2048 + (i & 1) * 1024, 0); } while (0)
            {
                const u32x4 em = u32x4{~0u, ~0u, ~0u, ~0u};
                const char* eb = ws + WS_PART + (size_t)(tile * 8 + wv) * 24576 + kp * 6144;
                asm volatile("global_store_dwordx4 %0, %1, %2 sc1\n\tglobal_store_dwordx4 %0, %1, %2 offset:1024 sc1\n\tglobal_store_dwordx4 %0, %1, %3 sc1\n\t"
                             "global_store_dwordx4 %0, %1, %3 offset:1024 sc1\n\ts_waitcnt vmcnt(12) lgkmcnt(0)"
                             :: "v"(l16), "v"(em), "s"(eb), "s"(eb + 4 * 24576) : "memory"); }
            if (jj == 7) { WOLD(1); EALD(0); EALD(1); }
            asm volatile("s_barrier" ::: "memory");
#define BATCH() do { KCHK(); if (t + 2 < NA) { BRQ4(t, 0); BRQ4(t, 1); BRQ4(t, 3); } BRQ4(t, 2); } while (0)
#define BRQ4S(t, g) do { BRQ(t, 4 * (g)); BRQ(t, 4 * (g) + 1); __builtin_amdgcn_s_sleep(2); BRQ(t, 4 * (g) + 2); BRQ(t, 4 * (g) + 3); __builtin_amdgcn_s_sleep(2); } while (0)
#define BATCHS() do { KCHK(); BRQ4S(t, 2); BRQ4S(t, 0); BRQ4S(t, 1); BRQ4S(t, 3); } while (0)
#define BSTEP(N) do { asm volatile("s_waitcnt vmcnt(%0)\n\ts_barrier" :: "i"(N) : "memory"); ++t; } while (0)
            u32x4 fw[16], wd[16], xq[4], gp[8];
            f32x4 fa[8];
            int ln_ = lane; asm volatile("" : "+v"(ln_));
#define FMM(w_, i0, a_, z_) do { asm volatile("s_setprio 1");                                                              \
                    _Pragma("unroll") for (int i = 0; i < 8; ++i) PIN(w_[(i0) + i]);                                                      \
                    PIN(a_);                                                                                                              \
                    _Pragma("unroll") for (int j = 0; j < 8; ++j) { u32x4 f_;                                                             \
                        _Pragma("unroll") for (int d = 0; d < 4; ++d) f_[d] = __builtin_amdgcn_perm(w_[(i0) + 2 * d + 1][j >> 1], w_[(i0) + 2 * d][j >> 1], (j & 1) ? 0x07060302u : 0x05040100u); \
                        fa[j] = __builtin_amdgcn_mfma_f32_16x16x32_bf16(__builtin_bit_cast(bf16x8, a_), __builtin_bit_cast(bf16x8, f_), (z_) ? f32x4{} : fa[j], 0, 0, 0); } \
                    _Pragma("unroll") for (int j = 0; j < 8; ++j) PIN(fa[j]);                                                             \
                    asm volatile("s_setprio 0"); } while (0)
#define FOUT() f32x4{ln_ >= 32 ? fa[4][0] : fa[0][0], ln_ >= 32 ? fa[5][0] : fa[1][0], ln_ >= 32 ? fa[6][0] : fa[2][0], ln_ >= 32 ? fa[7][0] : fa[3][0]}
            const int c1 = 2 * (i1 - 14 * k1) + (wv & 1);
            const unsigned r4o = (unsigned)(ln_ & 15) * 16u;
            const char* cxp = ws + WS_FLAG + F_CTX + ((qbB * 4 + qw) * 32 + head) * CFS;
            if (nf1 == 0) {
#pragma unroll 1
                while (t < F1T) { BATCHS(); BSTEP(0); }
                const unsigned evo = (unsigned)((16 * (ln_ >> 5) + 4 * ((ln_ >> 4) & 1)) * FF * 2) + r4o;
                const u16* fwp = (wv < 2 ? w_gate : w_up) + (size_t)(k1 * 128) * FF + c1 * 128;
#define FLD(h_, c_) do { _Pragma("unroll") for (int i = 0; i < 8; ++i)                                                                    \
                    asm volatile("global_load_dwordx4 %0, %1, %2" : c_(fw[((h_) & 1) * 8 + i]) : "v"(evo), "s"(fwp + (size_t)(32 * (h_) + 8 * (i >> 2) + (i & 3)) * FF) : "memory"); } while (0)
                BATCH();
                { const int xr_ = (ln_ & 4) ? ri2 : ri1;
#pragma unroll
                  for (int i = 0; i < 4; ++i)
                      asm volatile("global_load_dwordx4 %0, %1, %2 offset:%3 sc1" : "=v"(xq[i]) : "v"((unsigned)((xr_ >> 4) * 2048 + (xr_ & 15) * 16 + (ln_ >> 4) * 256)),
                                   "s"(ws + WS_XF + (size_t)(k1 * 2 + (i >> 1)) * 262144), "i"((i & 1) * 1024) : "memory"); }
                FLD(0, "=v"); FLD(1, "=v"); BSTEP(20);
                BATCH(); asm volatile("s_waitcnt vmcnt(16)" ::: "memory");
                FLAG_PUT(cxp, tag, 1);
                FMM(fw, 0, xq[0], 1); FLD(2, "=v"); FMM(fw, 8, xq[1], 0); FLD(3, "=v"); BSTEP(17);
                BATCH(); asm volatile("s_waitcnt vmcnt(16)" ::: "memory");
                FMM(fw, 0, xq[2], 0); FMM(fw, 8, xq[3], 0); const f32x4 sum = FOUT();
                ST16_SC1((float*)(ws + WS_GP) + ((size_t)k1 * 4 + (wv >> 1) * 2 + ((ln_ >> 4) & 1)) * FF + c1 * 128 + (ln_ & 15) * 8 + (ln_ >> 5) * 4, sum);
                PFW1(7); BSTEP(2);
            }
            const char* pnd = nf1 ? cxp : ws + WS_FLAG + F_GU + (c1 * 16 + (wv >> 1) * 8 + k1) * 8;
            const int tp = nf1 ? t + 1 : t;
#define PUB(N) do { if (t == tp) { asm volatile("s_waitcnt vmcnt(%0)" :: "i"(N) : "memory"); FLAG_PUT(pnd, tag, 1); } } while (0)
            const int f0 = (nf2 ? 0 : i2 >> 3) * 256 + wv * 64;
            const char* gfp = ws + WS_FLAG + F_GU + (f0 >> 7) * 128 + (ln_ & 15) * 8;
            u64 gf = tag + 1;
            char* vs = lds + (NA & 1) * 32768;
#define GFLD() asm volatile("global_load_dwordx2 %0, %1, off sc1" : "+v"(gf) : "v"(gfp) : "memory")
#define WDLD(i, n_) do { const unsigned dvo = (unsigned)((ln_ >> 4) * 8 * DM * 2) + r4o;                                                  \
                    const char* db_ = (const char*)(w_down + (size_t)((nf2 ? 0 : i2 >> 3) * 256 + wv * 64) * DM + (i2 & 7) * 128) + 4096; \
                    asm volatile("s_cmp_lg_u32 %11, 0\n\ts_cbranch_scc1 1f\n\t"                                                          \
                                 "global_load_dwordx4 %0, %8, %9 offset:-4096\n\tglobal_load_dwordx4 %1, %8, %9 offset:-2048\n\tglobal_load_dwordx4 %2, %8, %9\n\t" \
                                 "global_load_dwordx4 %3, %8, %9 offset:2048\n\tglobal_load_dwordx4 %4, %8, %10 offset:-4096\n\tglobal_load_dwordx4 %5, %8, %10 offset:-2048\n\t" \
                                 "global_load_dwordx4 %6, %8, %10\n\tglobal_load_dwordx4 %7, %8, %10 offset:2048\n1:"  \
                                 : "=&v"(wd[i]), "=&v"(wd[i + 1]), "=&v"(wd[i + 2]), "=&v"(wd[i + 3]), "=&v"(wd[i + 4]), "=&v"(wd[i + 5]), "=&v"(wd[i + 6]), "=&v"(wd[i + 7]) \
                                 : "v"(dvo), "s"(db_ + (i) * 8192), "s"(db_ + (i) * 8192 + 8192), "s"(nf2) : "memory", "scc");            \
                    EALD((i) >> 3);                                                                                                       \
                    asm volatile("s_waitcnt vmcnt(%1)\n\ts_cmp_eq_u32 %0, 0\n\ts_cbranch_scc1 1f\n\ts_waitcnt vmcnt(%2)\n1:"                \
                                 :: "s"(nf2), "i"(12 + (n_)), "i"(4 + (n_)) : "memory", "scc");                                           \
                    BSTEP(63); } while (0)
#pragma unroll 1
            while (t < NA - 4) { BATCHS(); PUB(17); BSTEP(0); }
            if (jj < 7) {
                BATCH(); PUB(16); WDLD(0, 0);
                BATCH(); PUB(16); GFLD(); WDLD(8, 0);
            }
            if (jj < 6) {
                BATCH();
                if (nf2 == 0) {
                    FLAG_WAIT(gf, gfp, tag, 63);
#pragma unroll
                    for (int i = 0; i < 8; ++i)
                        asm volatile("global_load_dwordx4 %0, %1, %2 sc1" : "=v"(gp[i]) : "v"((unsigned)((ln_ >> 4) * FF * 4) + r4o), "s"(ws + WS_GP + ((size_t)i * 4 * FF + f0) * 4) : "memory");
                    *(unsigned*)vs = 0;
                    asm volatile("s_waitcnt lgkmcnt(0)" ::: "memory");
                }
                WOLD(NA & 1);
                BSTEP(8);
            } else if (jj == 6) WOLD(0);
            asm volatile("global_load_dwordx2 %0, %1, %2 sc1" : "+v"(cxf) : "v"(CFO()), "s"(CFB) : "memory");
            WOLD((NA & 1) ^ 1);
            asm volatile("s_waitcnt vmcnt(16)" ::: "memory");
            if (jj == 7) FLAG_PUT(cxp, tag, 1);
            if (nf2 == 0) {
                f32x4 g4;
#pragma unroll
                for (int i = 0; i < 8; ++i) { PIN(gp[i]);
#pragma unroll
                    for (int j = 0; j < 4; ++j) { if (i) VADD(g4[j], g4[j], __uint_as_float(gp[i][j])); else g4[j] = __uint_as_float(gp[0][j]); } }
                float hv[2];
#pragma unroll
                for (int k = 0; k < 2; ++k) { auto rr = __builtin_amdgcn_permlane32_swap(__float_as_uint(g4[k]), __float_as_uint(g4[2 + k]), false, false);
                    const float gb = rbf(__uint_as_float(rr[0])), ub = rbf(__uint_as_float(rr[1]));
                    hv[k] = rbf(gb / (1.f + __expf(-gb))) * ub; PIN(hv[k]); }
                char* hl = vs + 8192 + wv * 256;
                *(unsigned*)(hl + ((ln_ >> 4) & 1) * 128 + (ln_ & 15) * 8 + (ln_ >> 5) * 4) = cvtpk(hv[0], hv[1]);
                asm volatile("" ::: "memory");
                xq[0] = *(const u32x4*)(hl + ((ln_ >> 2) & 1) * 128 + (ln_ >> 4) * 16); xq[1] = *(const u32x4*)(hl + ((ln_ >> 2) & 1) * 128 + 64 + (ln_ >> 4) * 16);
                FMM(wd, 0, xq[0], 1); FMM(wd, 8, xq[1], 0);
                *(f32x4*)(vs + 4096 + wv * 1024 + ln_ * 16) = FOUT();
                unsigned n_;
                asm volatile("s_waitcnt lgkmcnt(0)\n\ts_mov_b64 exec, 1\n\tds_add_rtn_u32 %0, %1, %2\n\ts_mov_b64 exec, -1\n\ts_waitcnt lgkmcnt(0)"
                             : "=&v"(n_) : "v"(ldsbase + (NA & 1) * 32768u), "v"(1u) : "memory");
                ypub = __builtin_amdgcn_readfirstlane(n_) ^ 3u;
                if (ypub == 0) {
                    const f32x4* yr = (const f32x4*)(vs + 4096) + ln_;
                    const f32x4 a = (yr[0] + yr[64]) + (yr[128] + yr[192]);
                    ST16_SC1((float*)(ws + WS_YP) + ((size_t)(((ln_ >> 4) & 1) * 8 + (i2 & 7)) * 16 + (i2 >> 3)) * 128 + (ln_ & 15) * 8 + (ln_ >> 5) * 4, a);
                }
            }
            AST(5);
        }
    }
    PH_STAMP(3);
    {
        constexpr int LDW = 1024, STG = 16384;
        int tid = threadIdx.x; asm volatile("" : "+v"(tid));
        const int lane = tid & 63, l15 = lane & 15, q4 = lane >> 4;
        unsigned xvo = (unsigned)lane * 16u; PIN(xvo);
        if (!(kp & 1)) {
#pragma unroll
            for (int i = 0; i < 8; i += 2) { const u32x4 t_ = pc[i]; pc[i] = pc[i + 1]; pc[i + 1] = t_; PIN(pc[i]); PIN(pc[i + 1]); } }
        bf16x8 yr_[4][2][2];
#pragma unroll
        for (int i = 0; i < 8; ++i) yr_[i >> 2][i & 1][(i >> 1) & 1] = __builtin_bit_cast(bf16x8, pc[i]);
        unsigned wa[2][2];
        wa[0][0] = ldsbase + (unsigned)(65536 + ((kp >> 1) ^ 1) * 1024 + (q4 >> 1) * 2048 + (4 * (q4 & 1) + (l15 >> 2)) * 64 + (q4 & 1) * 16 + (lane & 2) * 16 + (lane & 1) * 8);
        wa[0][1] = wa[0][0] ^ 16u; wa[1][0] = wa[0][0] ^ 1024u; wa[1][1] = wa[0][1] ^ 1024u;
        PIN(wa[0][0]); PIN(wa[0][1]); PIN(wa[1][0]); PIN(wa[1][1]);
        const char* cg = ws + WS_CTX + (qb * 8 + qw * 2) * 2048;
        unsigned xok;
#define XLD8 "global_load_dwordx4 %0, %10, %12 sc1\n\tglobal_load_dwordx4 %1, %10, %13 sc1\n\tglobal_load_dwordx4 %2, %10, %12 offset:1024 sc1\n\tglobal_load_dwordx4 %3, %10, %13 offset:1024 sc1\n\t" \
             "global_load_dwordx4 %4, %11, %12 sc1\n\tglobal_load_dwordx4 %5, %11, %13 sc1\n\tglobal_load_dwordx4 %6, %11, %12 offset:1024 sc1\n\tglobal_load_dwordx4 %7, %11, %13 offset:1024 sc1\n\t"
#define XOUT(c_) c_(yr_[2][0][0]), c_(yr_[2][1][0]), c_(yr_[2][0][1]), c_(yr_[2][1][1]), c_(yr_[3][0][0]), c_(yr_[3][1][0]), c_(yr_[3][0][1]), c_(yr_[3][1][1])
#define XIN "v"(xvo), "v"(xvo2), "s"(xp0), "s"(xp1), "s"(tag)
        unsigned xvo2 = xvo + 262144u, wvo = OWOFF() + (unsigned)K64(4) * (64 * LDW * 2), xso = xvo + (unsigned)K64(4) * 262144u, cfo = CFO();
        PIN(xvo2); PIN(wvo); PIN(xso); PIN(cfo);
        const char* cg0 = cg + ((kp ^ 1) & 1) * 2048, * cg1 = cg + (kp & 1) * 2048, * xp0 = cg0 + (unsigned)K64(2) * 262144u, * xp1 = cg1 + (unsigned)K64(2) * 262144u;
        const char* cfb = CFB, * wb0 = (const char*)wob, * wb1 = wb0 + (dn1 - 1024);
        const char* fpp = ws + WS_FLAG + ((wave & 4) ? F_Y + ((i2 & 7) * 16 + ((i2 >> 3) & 15)) * 8 : F_CTX + ((qbA * 4 + wave) * 32 + head) * CFS);
        unsigned fpz = (wave >> 2) * ypub, m0w = ldsbase + wave * 2048;
        asm volatile("" : "+s"(cg0), "+s"(cg1), "+s"(xp0), "+s"(xp1), "+s"(cfb), "+s"(wb0), "+s"(wb1), "+s"(fpp), "+s"(fpz), "+s"(m0w));
        SST(3);
        SB();
        asm volatile("s_waitcnt vmcnt(0)" ::: "memory");
        FLAG_PUTZ(fpp, tag, fpz);
        asm volatile("v_cmp_ne_u64 vcc, %14, %8\n\ts_and_b32 %9, vcc_lo, 14\n\ts_cbranch_scc1 1f\n\t"
                     XLD8 "\n1:\n\ts_barrier" : XOUT("=&v"), "+v"(cxf), "=&s"(xok) : XIN : "memory", "vcc", "scc");
        SST(4);
        f32x4 acc[2][8];
        bf16x4 wl[8], wh[8];
#define OFA(s, k_, j) (wa[(j) >> 2][(j) & 1] + (unsigned)(((s) & 3) * STG + (k_) * 8192 + (((j) >> 1) & 1) * 512))
#define ORL(i, s, k_, j) wl[i] = tr_rd(OFA(s, k_, j))
#define ORH(i, s, k_, j) wh[i] = tr_rd(OFA(s, k_, j) + 4096u)
        ORL(0, 0, 0, 0); ORH(0, 0, 0, 0); ORL(1, 0, 0, 1); ORH(1, 0, 0, 1); ORL(2, 0, 0, 2); ORH(2, 0, 0, 2); ORL(3, 0, 0, 3); ORH(3, 0, 0, 3); SB();
        const int r0 = qb * 128 + qw * 32 + (kp & 1) * 16, ch = kp >> 1;
        const unsigned nrt = min((unsigned)(ri1 - r0) >> 4, (unsigned)(ri2 - r0) >> 4);
        u64 yf = 0;
#define YFP (ws + WS_FLAG + F_Y + (ntc * 16 + (l15 < 14 ? l15 : 0)) * 8)
#define OMF(a, s, k_, j) do { if ((s) == 0 && (k_) == 0) MFA16Z(acc[a][j], w_, yr_[0][a][0]); else MFA16(acc[a][j], w_, yr_[(s) & 3][a][k_]); } while (0)
#define ORQ(h) do { constexpr int x_ = o_x(h), w_ = o_w(h);                                                                               \
            if (x_ >= 0) { asm volatile("global_load_dwordx4 %0, %1, %2 offset:%3 sc1" : "=v"(yr_[(x_ >> 2) & 3][x_ & 1][(x_ >> 1) & 1])   \
                                        : "v"(xso), "s"((x_ & 1) ? cg1 : cg0), "i"(((x_ >> 1) & 1) * 1024) : "memory");         \
                           if ((x_ & 3) == 3) { xso ^= (unsigned)((x_ >> 2) ^ ((x_ >> 2) + 1)) * 262144u; PIN(xso); } }                    \
            if (o_w((h) + 1) >= 0 && !(o_w((h) + 1) & 1)) asm volatile("s_mov_b32 m0, %0" :: "s"(m0w + (((o_w((h) + 1) >> 1) & 7) ^ 4) * STG) : "memory"); \
            if (w_ >= 0) { asm volatile("global_load_lds_dwordx4 %0, %1 offset:%2" :: "v"(wvo), "s"((w_ & 1) ? wb1 : wb0), "i"((w_ & 1) * 1024) : "memory"); \
                           if (w_ & 1) { wvo ^= (unsigned)((w_ >> 1) ^ ((w_ >> 1) + 1)) * (64 * LDW * 2); PIN(wvo); } } } while (0)
#define OPAIR(s, f, A) do { const bf16x8 w_ = CAT8(wl[(f) & 7], wh[(f) & 7]); constexpr int s_ = (s) + (((f) + 4) >> 4), m_ = ((f) + 4) & 15; \
            OMF(0, s, (f) >> 3, (f) & 7); SB(); ORL(m_ & 7, s_, m_ >> 3, m_ & 7); ORQ(32 * (s) + 2 * (f)); A; SB();                       \
            OMF(1, s, (f) >> 3, (f) & 7); SB(); ORH(m_ & 7, s_, m_ >> 3, m_ & 7); ORQ(32 * (s) + 2 * (f) + 1); SB(); if (((f) & 1)) { __builtin_amdgcn_s_waitcnt(0xC47F); SB(); } } while (0)
#define OTG(h_) do { wa[h_][0] ^= 65536u; wa[h_][1] ^= 65536u; PIN(wa[h_][0]); PIN(wa[h_][1]); } while (0)
#define OST(i)
#define OSTEP(s, POST) do {                                                                                                               \
        OPAIR(s, 0, ); OPAIR(s, 1, ); OPAIR(s, 2, ); OPAIR(s, 3, ); OPAIR(s, 4, ); OPAIR(s, 5, ); OPAIR(s, 6, ); OPAIR(s, 7, );           \
        OPAIR(s, 8, ); OPAIR(s, 9, if (((s) & 3) == 3) OTG(0)); OPAIR(s, 10, ); OPAIR(s, 11, );                                           \
        if ((s) >= 3) asm volatile("s_waitcnt vmcnt(%0)\n\ts_barrier" :: "i"(o_wait(s)) : "memory");                                      \
        SB(); POST; OST((s) + 1);                                                                                                         \
        OPAIR(s, 12, ); OPAIR(s, 13, if (((s) & 3) == 3) OTG(1)); OPAIR(s, 14, ); OPAIR(s, 15, );                                         \
    } while (0)
        PH_STAMP(4);
        SST(5);
        OST(0);
        SB();
        OSTEP(0, );
        asm volatile("s_cmp_eq_u32 %3, 0\n\ts_cbranch_scc1 1f\n\tglobal_load_dwordx2 %0, %1, %2 sc1\n1:" : "+v"(cxf) : "v"(cfo), "s"(cfb), "s"(xok) : "memory", "scc");
        OSTEP(1, );
        asm volatile("s_waitcnt vmcnt(8)\n\ts_cmp_eq_u32 %9, 0\n\ts_cbranch_scc1 3f\n\ts_mov_b32 %9, 0x40000\n\ts_waitcnt vmcnt(4)\n"
                     "1:\n\tv_cmp_ne_u64 vcc, %14, %8\n\ts_and_b32 vcc_lo, vcc_lo, 14\n\ts_cbranch_scc0 2f\n\t"
                     "global_load_dwordx2 %8, %15, %16 sc1\n\ts_sleep 4\n\ts_waitcnt vmcnt(0)\n\ts_sub_u32 %9, %9, 1\n\ts_cbranch_scc0 1b\n2:\n\t" XLD8 "s_waitcnt vmcnt(4)\n3:"
                     : XOUT("+v"), "+v"(cxf), "+s"(xok) : XIN, "v"(cfo), "s"(cfb) : "memory", "vcc", "scc");
        const unsigned inc = (unsigned)__builtin_amdgcn_ballot_w64(cxf != tag) & 0x1f0u;
        SB();
        OSTEP(2, if (__builtin_expect(inc != 0, 0)) asm volatile("global_load_dwordx2 %0, %1, %2 sc1" : "+v"(cxf) : "v"(cfo), "s"(cfb) : "memory"); SB());
        asm volatile("s_waitcnt vmcnt(8)" ::: "memory"); SB();
        OSTEP(3, );
        OSTEP(4, if (__builtin_expect(inc != 0, 0)) FLAG_WAITM(cxf, cfb + cfo, tag, 63, 0x1f0u); SB());
        OSTEP(5, ); OSTEP(6, );
        OSTEP(7, if (__builtin_expect(nrt == 0, 0)) asm volatile("global_load_dwordx2 %0, %1, off sc1" : "+v"(yf) : "v"(YFP) : "memory"); SB());
        OSTEP(8, ); OSTEP(9, ); OSTEP(10, );
        u32x4 yq[7], pv[6], zn[2]; u32x2 ea[4]; unsigned rq;
        if (__builtin_expect(nrt == 0, 0)) {
            FLAG_WAIT(yf, YFP, tag, 63);
            asm volatile("global_load_dwordx4 %0, %8, %10 sc1\n\tglobal_load_dwordx4 %1, %8, %10 offset:512 sc1\n\tglobal_load_dwordx4 %2, %8, %10 offset:1024 sc1\n\t"
                         "global_load_dwordx4 %3, %8, %10 offset:1536 sc1\n\tglobal_load_dwordx4 %4, %8, %10 offset:2048 sc1\n\tglobal_load_dwordx4 %5, %8, %10 offset:2560 sc1\n\t"
                         "global_load_dwordx4 %6, %8, %10 offset:3072 sc1\n\tglobal_load_dword %7, %9, %10 sc1"
                         : "=&v"(yq[0]), "=&v"(yq[1]), "=&v"(yq[2]), "=&v"(yq[3]), "=&v"(yq[4]), "=&v"(yq[5]), "=&v"(yq[6]), "=&v"(rq)
                         : "v"((unsigned)WS_YP + (unsigned)((((lane >> 5) * 8 + ntc) * 16 + (q4 & 1) * 7) * 128 + ch * 64 + 4 * l15) * 4u),
                           "v"((unsigned)WS_R + (unsigned)(lane >= 32 ? ri2 : ri1) * 4u), "s"(ws) : "memory");
        }
        SB();
        OSTEP(11, );
        char* pb = ws + WS_PART + (size_t)(tile * 8 + wave) * 24576;
        const char* tfb = ws + WS_FLAG + F_PARK + (tile * 8 + wave) * 256;
        const unsigned tfo = (unsigned)((lane & 3) ^ kp) * 64u;
        const unsigned ea0 = ldsbase + 131072 + (wave & 3) * 8192 + (wave >> 2) * 4096 + l15 * 128 + (((lane >> 5) * 2 ^ (lane & 7)) << 4) + (q4 & 1) * 8;
        typedef __attribute__((address_space(3))) u32x2 lds_u32x2;
#define OSND(a, j) do { constexpr int x_ = ((a) ^ 1) | (((j) >> 1) & 2 ^ 2);                                             \
            u32x4 p_ = u32x4{cvtpk(acc[a][j][0], acc[a][j][1]), cvtpk(acc[a][j][2], acc[a][j][3]), cvtpk(acc[a][(j) + 1][0], acc[a][(j) + 1][1]), cvtpk(acc[a][(j) + 1][2], acc[a][(j) + 1][3])}; \
            if (x_ == 1) { _Pragma("unroll") for (int k = 0; k < 4; ++k) p_[k] = min(p_[k], 0xfffffffeu); }     \
            asm volatile("global_store_dwordx4 %0, %1, %2 offset:%3 sc1\n\ts_nop 1" :: "v"(xvo), "v"(p_), "s"(pb + (kp ^ x_) * 6144 + (x_ - 1) * 2048), "i"(((j) & 2) * 512) : "memory"); } while (0)
        constexpr int PQ = 35, FQ = 44, GQ = 60;
#define OQ(q) do { constexpr int p_ = (q) + 4; const bf16x8 w_ = CAT8(wl[(q) & 7], wh[(q) & 7]);                                           \
            constexpr int j_ = (q) < 32 ? (q) & 3 : 4 + 2 * (((q) >> 4) & 1) + ((q) & 1), k_ = (q) < 32 ? ((q) >> 2) & 1 : ((q) >> 1) & 1, s_ = (q) < 32 ? ((q) >> 3) & 3 : ((q) >> 2) & 3; \
            constexpr int pj_ = p_ < 32 ? p_ & 3 : 4 + 2 * ((p_ >> 4) & 1) + (p_ & 1), pk_ = p_ < 32 ? (p_ >> 2) & 1 : (p_ >> 1) & 1, ps_ = p_ < 32 ? (p_ >> 3) & 3 : (p_ >> 2) & 3; \
            if ((q) == 16 || (q) == 24) { asm volatile("s_waitcnt vmcnt(%0)" :: "i"((q) == 16 ? 4 : 0) : "memory"); SB(); }               \
            OMF(0, 12 + s_, k_, j_); SB();                                                                                                \
            if (p_ < 64) ORL(p_ & 7, ps_, pk_, pj_);                                                                                      \
            ORQ(384 + 2 * (q)); SB();                                                                                                     \
            OMF(1, 12 + s_, k_, j_); SB();                                                                                                \
            if (p_ < 64) ORH(p_ & 7, ps_, pk_, pj_);                                                                                      \
            if ((q) >= 10 && (q) < 14) ea[((q) - 10) & 3] = *(lds_u32x2*)(uintptr_t)(ea0 ^ (unsigned)((((q) - 10) & 1) * 16 + (((q) - 10) & 2) * 32)); \
            if ((q) == 14 || (q) == 15) zn[(q) & 1] = __builtin_bit_cast(u32x4, lds_rd(ldsbase + 131072 + 2048 + (wave & 3) * 8192 + (wave >> 2) * 4096 + ((q) & 1) * 1024 + xvo)); \
            if ((q) == 32) OSND(0, 0); if ((q) == 33) OSND(1, 0); if ((q) == 34) OSND(0, 2); if ((q) == 35) OSND(1, 2);                   \
            if ((q) == PQ) asm volatile("s_setprio 0");                                                                                   \
            if ((q) == 50) OSND(0, 4);                                                                                                    \
            if ((q) == FQ) { asm volatile("s_waitcnt vmcnt(%0)" :: "i"(FQ >= 50) : "memory"); FLAG_PUT(tfb + kp * 64, tag, 1); }     \
            if ((q) == GQ) asm volatile("global_load_dwordx2 %0, %1, %2 sc1" : "=v"(cxf) : "v"(tfo), "s"(tfb) : "memory");               \
            SB(); } while (0)
#define OQ8(q) do { OQ(q); OQ((q) + 1); OQ((q) + 2); OQ((q) + 3); OQ((q) + 4); OQ((q) + 5); OQ((q) + 6); OQ((q) + 7); } while (0)
        asm volatile("s_setprio 1");
        OQ8(0); OQ8(8); OQ8(16); OQ8(24); OST(13); OQ8(32); OQ8(40); OQ8(48); OQ8(56);
        MF_DONE();
        OSND(0, 6); S2T(1);
        OST(14);
        PH_STAMP(5);
        SST(6); S2T(2);
        SB();
        if (__builtin_expect(nrt == 0, 0)) {
            f32x4 ys;
#pragma unroll
            for (int i = 0; i < 7; ++i) PIN(yq[i]);
            PIN(rq);
            const float sig = rbf(1.f / (1.f + __expf(-__uint_as_float(rq))));
#pragma unroll
            for (int j = 0; j < 4; ++j) {
                const float s_ = ((__uint_as_float(yq[0][j]) + __uint_as_float(yq[1][j])) + (__uint_as_float(yq[2][j]) + __uint_as_float(yq[3][j])))
                               + ((__uint_as_float(yq[4][j]) + __uint_as_float(yq[5][j])) + __uint_as_float(yq[6][j]));
                auto rr = __builtin_amdgcn_permlane16_swap(__float_as_uint(s_), __float_as_uint(s_), false, false);
                ys[j] = rbf(__uint_as_float(rr[0]) + __uint_as_float(rr[1])) * sig; }
            char* yl = lds + 65536 + wave * 256;
            *(u32x2*)(yl + (lane >> 5) * 128 + (l15 >> 2) * 32 + (l15 & 1) * 16 + (l15 & 2) * 4) = u32x2{cvtpk(ys[0], ys[1]), cvtpk(ys[2], ys[3])};
            asm volatile("" ::: "memory");
            const int m = r0 + l15;
#pragma unroll
            for (int i = 0; i < 2; ++i) { const u32x4 yn = *(const u32x4*)(yl + (m == ri1 ? 0 : 128) + i * 64 + q4 * 16);
#pragma unroll
                for (int k = 0; k < 4; ++k) { const unsigned z_ = cvtpk(HALF(zn[i][k], 0) + HALF(yn[k], 0), HALF(zn[i][k], 1) + HALF(yn[k], 1));
                    zn[i][k] = m == ri1 || m == ri2 ? z_ : zn[i][k]; } }
        }
        float gg[2][8], xx[2][8];
#pragma unroll
        for (int i = 0; i < 2; ++i)
#pragma unroll
            for (int j = 0; j < 8; ++j) { gg[i][j] = HALF(zn[i][j >> 1], j); xx[i][j] = HALF(ea[2 * i + (j >> 2)][(j >> 1) & 1], j); PIN(gg[i][j]); PIN(xx[i][j]); }
        unsigned ovo = (unsigned)((l15 & 7) * DM + 32 * (l15 >> 3) + 8 * q4) * 2u; PIN(ovo);
        const char* ob = (const char*)(outp + (size_t)r0 * DM + ntc * 128 + 64 * ch);
        asm volatile("" : "+s"(ob));
        PH_STAMP(6);
        { unsigned c_, m_;
          asm volatile("s_waitcnt vmcnt(%0)" :: "i"(GQ < 50 ? 2 : 1) : "memory");
          asm volatile("s_mov_b32 %7, 0x40000\n"
                       "1:\n\tv_cmp_ne_u64 vcc, %11, %6\n\ts_and_b32 vcc_lo, vcc_lo, 12\n\ts_cbranch_scc0 2f\n\t"
                       "global_load_dwordx2 %6, %12, %13 sc1\n\ts_waitcnt vmcnt(0)\n\ts_sub_u32 %7, %7, 1\n\ts_cbranch_scc0 1b\n"
                       "2:\n\tglobal_load_dwordx4 %1, %9, %10 offset:-1024 sc1\n\tglobal_load_dwordx4 %2, %9, %10 offset:1024 sc1\n\t"
                       "global_load_dwordx4 %4, %9, %10 sc1\n\tglobal_load_dwordx4 %5, %9, %10 offset:2048 sc1\n\t"
                       "global_load_dwordx4 %0, %9, %10 offset:-3072 sc1\n\tglobal_load_dwordx4 %3, %9, %10 offset:-2048 sc1"
                       : "=&v"(pv[0]), "=&v"(pv[1]), "=&v"(pv[2]), "=&v"(pv[3]), "=&v"(pv[4]), "=&v"(pv[5]), "+v"(cxf), "=&s"(c_), "=&v"(m_)
                       : "v"(xvo), "s"(pb + kp * 6144 + 3072), "s"(tag), "v"(tfo), "s"(tfb) : "memory", "vcc", "scc"); }
        S2T(3);
#pragma unroll
        for (int i = 0; i < 16; i += 2) { float g_[2];
#pragma unroll
            for (int e_ = 0; e_ < 2; ++e_) { float a_ = gg[i >> 3][(i & 7) + e_] * -1.4426950408889634f; PIN(a_); a_ = 1.f + __builtin_amdgcn_exp2f(a_); PIN(a_);
                g_[e_] = gg[i >> 3][(i & 7) + e_] * __builtin_amdgcn_rcpf(a_); PIN(g_[e_]); }
            const unsigned p_ = cvtpk(g_[0], g_[1]); gg[i >> 3][i & 7] = HALF(p_, 0); gg[i >> 3][(i & 7) + 1] = HALF(p_, 1); PIN(gg[i >> 3][i & 7]); PIN(gg[i >> 3][(i & 7) + 1]); }
        S2T(4);
        asm volatile("s_waitcnt vmcnt(0)" : "+v"(pv[0]), "+v"(pv[1]), "+v"(pv[2]), "+v"(pv[3]), "+v"(pv[4]), "+v"(pv[5]) :: "memory");
        S2T(5);
#pragma unroll 1
        for (int n_ = 0; n_ < 0x40000; ++n_) {
            const unsigned mx_ = max(max(max(pv[0][0], pv[0][1]), max(pv[0][2], pv[0][3])), max(max(pv[3][0], pv[3][1]), max(pv[3][2], pv[3][3])));
            if (__builtin_amdgcn_ballot_w64(mx_ == ~0u) == 0) break;
            asm volatile("global_load_dwordx4 %0, %2, %3 sc1\n\tglobal_load_dwordx4 %1, %2, %3 offset:1024 sc1\n\ts_waitcnt vmcnt(0)"
                         : "=&v"(pv[0]), "=&v"(pv[3]) : "v"(xvo), "s"(pb + kp * 6144) : "memory");
        }
        PH_STAMP(7); S2T(6);
        u32x4 ow[2];
#pragma unroll
        for (int i = 0; i < 2; ++i) {
            u32x4& ov = ow[i];
#pragma unroll
            for (int k = 0; k < 4; ++k) { float sv[2];
#pragma unroll
                for (int e_ = 0; e_ < 2; ++e_) sv[e_] = fmaf((acc[1][4 + 2 * i + (k >> 1)][2 * (k & 1) + e_] + HALF(pv[3 * i][k], e_)) + (HALF(pv[3 * i + 1][k], e_) + HALF(pv[3 * i + 2][k], e_)),
                                                             gg[i][2 * k + e_], xx[i][2 * k + e_]);
                ov[k] = cvtpk(sv[0], sv[1]); }
#pragma unroll
            for (int k = 0; k < 2; ++k) { auto rr = __builtin_amdgcn_permlane16_swap(ov[k], ov[2 + k], false, false); ov[k] = rr[0]; ov[2 + k] = rr[1]; }
        }
#pragma unroll
        for (int i = 0; i < 2; ++i) {
            u32x4 ov;
#pragma unroll
            for (int k = 0; k < 4; ++k) ov[k] = i ? __builtin_amdgcn_update_dpp(ow[1][k], ow[0][k], 0x128, 0xf, 0x3, false) : __builtin_amdgcn_update_dpp(ow[0][k], ow[1][k], 0x128, 0xf, 0xc, false);
            asm volatile("global_store_dwordx4 %0, %1, %2 sc1\n\ts_nop 1" :: "v"(ovo), "v"(ov), "s"(ob + i * (8 * DM * 2)) : "memory");
        }
        SST(7); SST_OUT();
        PH_STAMP(8);
        PH_STAMP(9);
    }
}

extern "C" void kernel_launch(void* const* d_in, const int* in_sizes, int n_in, void* d_out, int out_size, void* d_ws, size_t ws_size,
                              hipStream_t stream) {
    const u16* x    = (const u16*)d_in[0];
    const u16* ckv  = (const u16*)d_in[1];
    const u16* wq   = (const u16*)d_in[2];
    const u16* wuk  = (const u16*)d_in[3];
    const u16* wuv  = (const u16*)d_in[4];
    const u16* wo   = (const u16*)d_in[5];
    const u16* wr   = (const u16*)d_in[7];
    const u16* wg   = (const u16*)d_in[8];
    const u16* wu   = (const u16*)d_in[9];
    const u16* wd   = (const u16*)d_in[10];
    (void)in_sizes; (void)n_in; (void)out_size; (void)ws_size;
    (void)hipFuncSetAttribute((const void*)k_all, hipFuncAttributeMaxDynamicSharedMemorySize, 163840);
    k_all<<<256, 512, 163840, stream>>>(x, ckv, wuk, wuv, wr, (char*)d_ws, wq, wo, wg, wu, wd, (u16*)d_out);
}
```

```cpp
#include <hip/hip_runtime.h>
#include <hip/hip_bf16.h>
#include <stdint.h>

typedef unsigned short u16;
typedef unsigned long long u64;
typedef short    bf16x8 __attribute__((ext_vector_type(8)));
typedef short    bf16x4 __attribute__((ext_vector_type(4)));
typedef float    f32x4  __attribute__((ext_vector_type(4)));
typedef float    f32x2  __attribute__((ext_vector_type(2)));
typedef __bf16   bfx2   __attribute__((ext_vector_type(2)));
typedef unsigned u32x4  __attribute__((ext_vector_type(4)));
typedef unsigned u32x2  __attribute__((ext_vector_type(2)));

#define DI __device__ __forceinline__

constexpr int SQ = 2048, DM = 1024, NH = 32, HD = 128, DC = 512, FF = 3584;

constexpr size_t WS_R    = 4u << 20;
constexpr size_t WS_FLAG = (4u << 20) + 65536;
constexpr size_t WS_GP   = 6u << 20;
constexpr size_t WS_YP   = 7u << 20;
constexpr size_t WS_PART = 96u << 20;
constexpr size_t WS_XF   = 16u << 20;
#define HSTR 1310720
constexpr size_t WS_KV   = 24u << 20;
constexpr size_t WS_CTX  = 72u << 20;
constexpr int F_EPOCH = 0, F_TOP = 1024, F_LN = 4096, F_KV = 8192, F_GU = 12288  , F_Y = 20480  , F_PARK = 524288  ,
              F_CTX = 65536  ;

#define PH_STAMP(i)
#define SST(i)
#define S2T(i)
#define SST_OUT()
#define ZST(i)
#define ZST_OUT()
DI unsigned cvtpk(float lo, float hi) { f32x2 v = {lo, hi}; bfx2 b = __builtin_convertvector(v, bfx2); return __builtin_bit_cast(unsigned, b); }
DI float rbf(float x) { return __uint_as_float(cvtpk(x, 0.f) << 16); }
#define DPP_ADD(v, ctrl, rmask) v += __uint_as_float(__builtin_amdgcn_update_dpp(0u, __float_as_uint(v), ctrl, rmask, 0xf, false))
DI unsigned tkey(unsigned fbits, int i) { unsigned u = fbits >> 16; u ^= (u & 0x8000u) ? 0xffffu : 0x8000u; return (u << 16) | (unsigned)(2047 - i); }
DI void merge_top2(unsigned& a1, unsigned& a2, unsigned b1, unsigned b2) { const unsigned lo = min(a1, b1); a1 = max(a1, b1); a2 = max(lo, max(a2, b2)); }
#define DPP_TOP2(a1, a2, ctrl, rmask) merge_top2(a1, a2, __builtin_amdgcn_update_dpp(0u, a1, ctrl, rmask, 0xf, false), __builtin_amdgcn_update_dpp(0u, a2, ctrl, rmask, 0xf, false))

#define GLDS_S(voff, sbase, ldsoff, imm)                                                                        \
    asm volatile("s_mov_b32 m0, %2\n\ts_nop 0\n\tglobal_load_lds_dwordx4 %0, %1 offset:%3"                       \
                 :: "v"(voff), "s"(sbase), "s"(ldsoff), "i"(imm) : "memory")
#define GLDS_C(voff, sbase, ldsoff, imm)                                                                        \
    asm volatile("s_mov_b32 m0, %2\n\ts_nop 0\n\tglobal_load_lds_dwordx4 %0, %1 offset:%3 sc1"                   \
                 :: "v"(voff), "s"(sbase), "s"(ldsoff), "i"(imm) : "memory")
#define LGKM0_BAR()    asm volatile("s_waitcnt lgkmcnt(0)\n\ts_barrier" ::: "memory")
#define SB() __builtin_amdgcn_sched_barrier(0)
#define CAT_(a, b) a##b
#define CAT(a, b) CAT_(a, b)
#define PIN(x) asm volatile("" : "+v"(x))
#define OADV(p_, d_) do { p_ += (d_); asm volatile("" : "+s"(p_)); } while (0)
#define VADD(d, a, b)    asm volatile("v_add_f32 %0, %1, %2" : "=v"(d) : "v"(a), "v"(b))
#define KEEP(x) asm volatile("" :: "v"(x))
#define MFA16(c, a, b) asm volatile("v_mfma_f32_16x16x32_bf16 %0, %1, %2, %0" : "+v"(c) : "v"(a), "v"(b))
#define MFA16Z(c, a, b) asm volatile("v_mfma_f32_16x16x32_bf16 %0, %1, %2, 0" : "=&v"(c) : "v"(a), "v"(b))
#define MFA16C(c, a, b, c0) asm volatile("v_mfma_f32_16x16x32_bf16 %0, %1, %2, %3" : "=&v"(c) : "v"(a), "v"(b), "v"(c0))
#define MF_DONE() do { SB(); asm volatile("s_nop 7\n\ts_nop 7"); SB(); } while (0)
#define ST16_SC1(p, v) asm volatile("global_store_dwordx4 %0, %1, off sc1\n\ts_nop 1" :: "v"(p), "v"(v) : "memory")
#define ST4_SC1(p, v)  asm volatile("global_store_dword %0, %1, off sc1" :: "v"(p), "v"(v) : "memory")
#define FLAG_PUT(p_, val_, on_) do { u64 sv_, tv_; unsigned tz_; const u64 v_ = (val_); const unsigned m_ = __builtin_amdgcn_readfirstlane((on_) ? 1u : 0u); \
        asm volatile("s_cmp_eq_u32 %5, 0\n\ts_cbranch_scc1 1f\n\tv_mov_b64 %1, %3\n\tv_mov_b32 %2, 0\n\ts_mov_b64 %0, exec\n\ts_mov_b64 exec, 1\n\t" \
                     "global_store_dwordx2 %2, %1, %4 sc1\n\ts_mov_b64 exec, %0\n1:"                                                      \
                     : "=&s"(sv_), "=&v"(tv_), "=&v"(tz_) : "s"(v_), "s"(p_), "s"(m_) : "memory", "scc"); } while (0)
#define FLAG_PUTZ(p_, val_, nz_) do { u64 sv_, tv_; unsigned tz_; const u64 v_ = (val_);                                                   \
        asm volatile("s_cmp_lg_u32 %5, 0\n\ts_cbranch_scc1 1f\n\tv_mov_b64 %1, %3\n\tv_mov_b32 %2, 0\n\ts_mov_b64 %0, exec\n\ts_mov_b64 exec, 1\n\t" \
                     "global_store_dwordx2 %2, %1, %4 sc1\n\ts_mov_b64 exec, %0\n1:"                                                      \
                     : "=&s"(sv_), "=&v"(tv_), "=&v"(tz_) : "s"(v_), "s"(p_), "s"(nz_) : "memory", "scc"); } while (0)
#define FLAG_GET(r_, p_) asm volatile("global_load_dwordx2 %0, %1, off sc1" : "=v"(r_) : "v"(p_) : "memory")
#define FLAG_WAIT(r_, p_, tag_, N) do { unsigned c_;                                                                                      \
        asm volatile("s_waitcnt vmcnt(%4)\n\ts_mov_b32 %1, 0x40000\n"                                                                     \
                     "1:\n\tv_cmp_ne_u64 vcc, %3, %0\n\ts_cmp_eq_u64 vcc, 0\n\ts_cbranch_scc1 2f\n\t"                                      \
                     "s_sleep 2\n\tglobal_load_dwordx2 %0, %2, off sc1\n\ts_waitcnt vmcnt(0)\n\ts_sub_u32 %1, %1, 1\n\ts_cbranch_scc0 1b\n"  \
                     "2:" : "+v"(r_), "=&s"(c_) : "v"(p_), "s"(tag_), "i"(N) : "memory", "vcc", "scc"); } while (0)
#define FLAG_WAITM(r_, p_, tag_, N, m_) do { unsigned c_;                                                                                 \
        asm volatile("s_waitcnt vmcnt(%4)\n\ts_mov_b32 %1, 0x40000\n"                                                                     \
                     "1:\n\tv_cmp_ne_u64 vcc, %3, %0\n\ts_and_b32 vcc_lo, vcc_lo, %5\n\ts_cbranch_scc0 2f\n\t"                             \
                     "s_sleep 2\n\tglobal_load_dwordx2 %0, %2, off sc1\n\ts_waitcnt vmcnt(0)\n\ts_sub_u32 %1, %1, 1\n\ts_cbranch_scc0 1b\n"  \
                     "2:" : "+v"(r_), "=&s"(c_) : "v"(p_), "s"(tag_), "i"(N), "s"(m_) : "memory", "vcc", "scc"); } while (0)
typedef __attribute__((address_space(3))) bf16x4 lds_bf16x4;
DI bf16x4 tr_rd(unsigned a) { return __builtin_amdgcn_ds_read_tr16_b64_v4i16((lds_bf16x4*)(uintptr_t)a); }
typedef __attribute__((address_space(3))) bf16x8 lds_bf16x8;
DI bf16x8 lds_rd(unsigned a) { return *(lds_bf16x8*)(uintptr_t)a; }
#define CAT8(lo_, hi_) bf16x8{lo_[0], lo_[1], lo_[2], lo_[3], hi_[0], hi_[1], hi_[2], hi_[3]}

#define POFF 32.f
#define LTHR 8192.f
DI float quad_max(float v) {
    auto r1 = __builtin_amdgcn_permlane16_swap(__float_as_uint(v), __float_as_uint(v), false, false);
    v = fmaxf(__uint_as_float(r1[0]), __uint_as_float(r1[1]));
    auto r2 = __builtin_amdgcn_permlane32_swap(__float_as_uint(v), __float_as_uint(v), false, false);
    return fmaxf(__uint_as_float(r2[0]), __uint_as_float(r2[1])); }
#define QWD 3
#define QS1 0xbfff
constexpr int q_xb(int s) { return s >= 1 && s <= 12; }
constexpr int q_xa(int s) { return s >= 0 && s <= 11; }
constexpr int q_s0(int s) { int n = 0; for (int i = 0; i < s; ++i) n += (QS1 >> i) & 1; return n; }
constexpr int q_kv(int kt, int f) { const int s = (kt - 13) * 16 + f - 12; return s >= 0 && s % 3 == 0 && s / 3 < 12 ? s / 3 : -1; }
constexpr int q_op(int s, int f) { return (f == 0 || f == 2 ? q_xb(s) : f == 8 || f == 10 ? q_xa(s) : f == 12 || f == 14 ? s + QWD < 16 : f == 3 ? s == 12
                                          : f == 4 ? (QS1 >> s) & 1 : 0) + (q_kv(s, f) >= 0); }
constexpr int q_after(int p, int kt) { int n = 0; for (int i = p + 1; i <= kt * 16 + 11; ++i) n += q_op(i >> 4, i & 15); return n; }
constexpr int q_min(int a, int b) { return a < b ? a : b; }
constexpr int q_wait(int kt) {
    int n = kt <= 2 ? 4 * (2 - kt) + q_after(-1, kt) : q_after((kt - 2) * 16 + 2, kt);
    if (kt + 1 >= QWD) n = q_min(n, q_after((kt + 1 - QWD) * 16 + 14, kt));
    if (kt == 13) n = q_min(n, q_after(12 * 16 + 3, kt));
    return n; }
constexpr int q_fl() { int s = 0; while (q_s0(s + 1) < 8) ++s; int kt = s; while (q_wait(kt) > q_after(s * 16 + 6, kt)) ++kt; return kt; }
constexpr int Q_NST = q_s0(16);
static_assert(Q_NST >= 8, "");
constexpr int o_xp(int st, int i) { return st <= 5 ? 64 + 4 * (4 * (st - 4) + i) : 32 * (st - 3) + (i == 0 ? 0 : i == 1 ? 4 : i == 2 ? 12 : 16); }
constexpr int o_wp(int st, int i) { return st <= 7 ? 16 * (st - 4) + 4 + 6 * i : 32 * (st - 5) + 24 + 4 * i; }
constexpr int o_x(int h) { for (int st = 4; st < 16; ++st) for (int i = 0; i < 4; ++i) if (o_xp(st, i) == h) return 4 * st + i; return -1; }
constexpr int o_w(int h) { for (int st = 4; st < 16; ++st) for (int i = 0; i < 2; ++i) if (o_wp(st, i) == h) return 2 * st + i; return -1; }
constexpr int o_after(int p, int e) { int n = 0; for (int h = p + 1; h < e; ++h) n += (o_x(h) >= 0) + (o_w(h) >= 0); return n; }
constexpr int o_wait(int kt) { return o_after(kt == 11 ? o_wp(15, 1) : o_xp(kt + 1, 3), 32 * kt + 24); }
constexpr int L_PFT = 149504, L_DUMP = 155648;
constexpr int CFS = 128;
constexpr unsigned F1S = 0xffff642fu, F1F = 0xffff5310u, F2S = 0xff64fff0u, F2F = 0xff53f21fu;
constexpr int F1T = 3;

__global__ __launch_bounds__(512, 2) void k_all(const u16* __restrict__ x, const u16* __restrict__ ckv, const u16* __restrict__ wuk, const u16* __restrict__ wuv,
                                                const u16* __restrict__ w_r, char* __restrict__ ws, const u16* __restrict__ wq, const u16* __restrict__ wo,
                                                const u16* __restrict__ w_gate, const u16* __restrict__ w_up,
                                                const u16* __restrict__ w_down, u16* __restrict__ out) {
    extern __shared__ __attribute__((aligned(16))) char lds[];
    const int b = blockIdx.x, xcd = b & 7, idx = b >> 3, head = xcd * 4 + (idx >> 3), jj = idx & 7;
    const int hp = head ^ 1;
    const unsigned ldsbase = (unsigned)(uintptr_t)(__attribute__((address_space(3))) char*)lds;
    const int wave = __builtin_amdgcn_readfirstlane(threadIdx.x >> 6);
    const int qbA = 15 - jj, qbB = jj;
#define KROW(g) (((2 * ((g) & 3) + ((g) >> 2)) * 8 + jj) * 32)
    const int qw = wave < 4 ? wave : 7 - wave, qb = wave < 4 ? qbA : qbB;
    bf16x8 qf[8];
    u64 tag;
    u16* outp = out;
    int ri1, ri2;
    PH_STAMP(0);
    SST(0);
    ZST(0);
    u32x4 pk[16];
    bf16x8 xr[4][2][2];
    u32x4 rv;
    const char* xg;
#define QXL1(j) asm volatile("global_load_dwordx4 %0, %1, %2 offset:%3 sc1" : "=v"(xr[(j) >> 2][(j) & 1][((j) >> 1) & 1]) : "v"(xvo), "s"(xg + (size_t)((j) >> 2) * 262144), \
                            "i"(((j) & 1) * 2048 + (((j) >> 1) & 1) * 1024) : "memory")
    const char* sb_[4];
    unsigned qa, qa2; bf16x4 ql[8], qh[8];
#define QFA(s, f) ((((f) & 1) ? qa2 : qa) + (unsigned)(((s) & 3) * 16384 + ((f) >> 3) * 8192 + (((f) >> 2) & 1) * 1024 + (((f) >> 1) & 1) * 512))
#define QFM(s, f) ((s) == 15 ? ((f) & 1) * 8 + ((f) >> 1) : (f))
#define QRL(s, f) ql[(f) & 7] = tr_rd(QFA(s, QFM(s, f)))
#define QRH(s, f) qh[(f) & 7] = tr_rd(QFA(s, QFM(s, f)) + 2048u)
    unsigned xvo, qwo;
    const u16* wqb;
#define QWOFF() ({ unsigned l_ = threadIdx.x & 63; asm volatile("" : "+v"(l_)); (unsigned)(((l_ >> 2) & 7) * (NH * HD) + 32 * (l_ >> 5) + 8 * ((l_ & 3) ^ ((l_ >> 3) & 2))) * 2u; })
    {
        constexpr int LDW = NH * HD, K = DC;
        int tid = threadIdx.x; asm volatile("" : "+v"(tid));
        const int lane = tid & 63;
        const int wm = wave >> 2, wn = wave & 3;
        const int row = b * 8 + wave;
        u32x4 lx[2], lw[2]; u32x2 ep;
        asm volatile("global_load_dwordx4 %0, %5, %6\n\tglobal_load_dwordx4 %1, %5, %6 offset:1024\n\tglobal_load_dwordx4 %2, %5, %7\n\tglobal_load_dwordx4 %3, %5, %7 offset:1024\n\t"
                     "global_load_dwordx2 %4, %8, off sc1"
                     : "=&v"(lx[0]), "=&v"(lx[1]), "=&v"(lw[0]), "=&v"(lw[1]), "=&v"(ep) : "v"(lane * 16), "s"(x + (unsigned)(row * DM)), "s"(w_r), "v"(ws + WS_FLAG + F_EPOCH) : "memory");
#define XKEY(r) (((r) >> 1) & 7)
        unsigned xoff[4], woff[2];
        { const unsigned x0_ = (unsigned)(KROW(wave) + (lane >> 3)) * (K * 2) + (unsigned)((lane & 7) ^ (lane >> 4)) * 16u;
          xoff[0] = x0_; xoff[1] = (x0_ ^ 64u) + 7168u; xoff[2] = x0_ + 14336u; xoff[3] = xoff[1] + 14336u;
          woff[1] = (unsigned)((8 * wave + ((lane >> 2) & 7)) * LDW + hp * HD + 32 * (lane >> 5) + 8 * ((lane & 3) ^ (2 * (wave & 1)))) * 2u; woff[0] = woff[1] + 896u; }
        const char* xsp = (const char*)ckv;
        const char* wsp = (const char*)wuk - 896;
        const char* wsv = (const char*)wuv - 2944;
        const unsigned m0b = ldsbase + wave * 4096;
#define KM0(g, w_) asm volatile("s_add_u32 m0, %0, %1" :: "s"(m0b), "i"(((g) & 1) * 65536 + (w_) * 32768) : "memory", "scc")
#define KRX(i) asm volatile("global_load_lds_dwordx4 %0, %1 offset:%2" :: "v"(xoff[i]), "s"(xsp), "i"((i) * 1024) : "memory")
#define KRW(i) asm volatile("global_load_lds_dwordx4 %0, %1 offset:%2 nt" :: "v"(woff[(i) & 1]), "s"((i) < 2 ? wsp : wsv), "i"((i) * 1024) : "memory")
#define STAGE(g) do { KM0(g, 0); asm volatile("s_nop 0"); KRX(0); KRX(1); KRX(2); KRX(3); KM0(g, 1); asm volatile("s_nop 0"); KRW(0); KRW(1); KRW(2); KRW(3); \
                      OADV(xsp, 128); OADV(wsp, 64 * LDW * 2); OADV(wsv, 64 * LDW * 2); } while (0)
#define KST(i)
        const unsigned pfl = ldsbase + L_DUMP + wave * 1024;
        unsigned pxo, pqo, xra, xso_;
#define PFX(g) GLDS_S(pxo, pfxp, pfl - (g) * 128, (g) * 128)
#define PFQ(i) GLDS_S(pqo, pfqp + (size_t)(4 * (i)) * LDW, pfl, 0)
        STAGE(0);
        SB(); ZST(1);
        int w2 = wave; asm volatile("" : "+s"(w2));
        xg = ws + WS_XF + (unsigned)((w2 < 4 ? qbA * 4 + w2 : qbB * 4 + 7 - w2) * 4096);
        wqb = wq + (unsigned)(8 * w2 * LDW + head * HD);
        const u16* pfqp = wq + (unsigned)((128 * jj + 16 * w2) * LDW + head * HD);
        const u16* pfxp = ckv + (unsigned)((KROW(w2) + 8 * (head & 3)) * DC);
        unsigned xa[2][2], wa[2][2];
        const int l15 = lane & 15, q4 = lane >> 4;
#pragma unroll
        for (int s = 0; s < 2; ++s) {
            const int r0 = l15;
            xa[0][s] = ldsbase + (unsigned)((wm * 128 + r0) * 128 + ((((4 * s + q4) ^ XKEY(r0))) << 4));
            wa[0][s] = (ldsbase + 32768u + (unsigned)(q4 * 4096 + wn * 512 + (l15 >> 2) * 64 + (q4 & 1) * 32 + (lane & 3) * 8)) ^ (32u * s);
            xa[1][s] = xa[0][s] + 65536u; wa[1][s] = wa[0][s] + 65536u;
            PIN(xa[0][s]); PIN(xa[1][s]); PIN(wa[0][s]); PIN(wa[1][s]); }
        u64 pf0;
        { u64 pw_ = (u64)(((w2 & 2) ? wuv : wuk) + (unsigned)((128 + 192 * (w2 & 1) + jj) * LDW + hp * HD)), pc_ = (u64)ckv + (unsigned)(b * 8192 + (w2 & 3) * 2048);
          asm volatile("" : "+s"(pw_), "+s"(pc_));
          unsigned o0_ = (unsigned)(lane >> 1) * (8 * LDW * 2) + (lane & 1) * 128u, o1_ = (lane & 15) * 128u; PIN(o0_); PIN(o1_);
          pf0 = (lane < 48 ? pw_ : pc_) + (lane < 48 ? o0_ : o1_); PIN(pf0); }
        asm volatile("" : "+s"(pfqp), "+s"(pfxp), "+s"(xg), "+s"(wqb), "+s"(outp));
        SB();
        asm volatile("s_waitcnt vmcnt(8)" : "+v"(lx[0]), "+v"(lx[1]), "+v"(lw[0]), "+v"(lw[1]), "+v"(ep) :: "memory");
        KST(1); ZST(2);
#define HALF(v_, i) __uint_as_float(((i) & 1) ? (v_) & 0xffff0000u : (v_) << 16)
#define BOTH(v_) f32x2{HALF(v_, 0), HALF(v_, 1)}
        {
            f32x2 v[8];
#pragma unroll
            for (int i = 0; i < 8; ++i) v[i] = BOTH(lx[i >> 2][i & 3]);
            const float pv_ = __uint_as_float(__builtin_amdgcn_readfirstlane(__float_as_uint(v[0][0])));
            f32x2 s2 = f32x2{0.f, 0.f}, q2 = f32x2{0.f, 0.f};
#pragma unroll
            for (int i = 0; i < 8; ++i) { const f32x2 d = v[i] - pv_; s2 += d; q2 += d * d; }
            float s = s2[0] + s2[1], q = q2[0] + q2[1];
            DPP_ADD(s, 0x111, 0xf); DPP_ADD(q, 0x111, 0xf); DPP_ADD(s, 0x112, 0xf); DPP_ADD(q, 0x112, 0xf); DPP_ADD(s, 0x114, 0xf); DPP_ADD(q, 0x114, 0xf);
            DPP_ADD(s, 0x118, 0xf); DPP_ADD(q, 0x118, 0xf); DPP_ADD(s, 0x142, 0xa); DPP_ADD(q, 0x142, 0xa); DPP_ADD(s, 0x143, 0xc); DPP_ADD(q, 0x143, 0xc);
            const float dm = __uint_as_float(__builtin_amdgcn_readlane(__float_as_uint(s), 63)) * (1.f / DM);
            const float var_bf = rbf(fmaxf(fmaf(-dm, dm, __uint_as_float(__builtin_amdgcn_readlane(__float_as_uint(q), 63)) * (1.f / DM)), 0.f));
            const float mean_bf = rbf(pv_ + dm), den = rbf(__builtin_amdgcn_sqrtf(rbf(var_bf + 1e-5f))), rden = __builtin_amdgcn_rcpf(den);
            u32x4 xw[2]; float r2[2] = {0.f, 0.f};
#pragma unroll
            for (int i = 0; i < 8; ++i) {
                const f32x2 c_ = v[i] - mean_bf;
                const unsigned cp = cvtpk(c_[0], c_[1]);
                const f32x2 cb = BOTH(cp);
                f32x2 n_ = cb * rden;
                n_ += (cb - n_ * den) * rden;
                const unsigned np = cvtpk(n_[0], n_[1]);
                const unsigned wp = lw[i >> 2][i & 3];
                r2[i & 1] = __builtin_amdgcn_fdot2_f32_bf16(__builtin_bit_cast(bfx2, np), __builtin_bit_cast(bfx2, wp), r2[i & 1], false);
                xw[i >> 2][i & 3] = np;
            }
            char* lr = lds + 131072 + wave * 2064 + lane * 16;
            *(u32x4*)lr = xw[0]; *(u32x4*)(lr + 1024) = xw[1];
            s = r2[0] + r2[1];
            DPP_ADD(s, 0x111, 0xf); DPP_ADD(s, 0x112, 0xf); DPP_ADD(s, 0x114, 0xf); DPP_ADD(s, 0x118, 0xf); DPP_ADD(s, 0x142, 0xa); DPP_ADD(s, 0x143, 0xc);
            if (lane == 63) *(float*)(lds + 131072 + wave * 2064 + 2048) = rbf(s);
        }
        SB(); ZST(3);
        STAGE(1);
        asm volatile("s_bitcmp0_b32 %2, 2\n\ts_cbranch_scc1 1f\n\ts_mov_b32 m0, %1\n\ts_nop 0\n\tglobal_load_lds_dword %0, off\n1:" :: "v"(pf0), "s"(pfl), "s"(wave) : "memory", "scc");
        tag = (((u64)(unsigned)__builtin_amdgcn_readfirstlane(ep[1]) << 32) | (unsigned)__builtin_amdgcn_readfirstlane(ep[0])) + 0x9E3779B97F4A7C15ull;
        float rr;
        const unsigned l16 = (unsigned)lane * 16u;
        u32x2 xi_[4];
        typedef __attribute__((address_space(3))) u32x2 lds_u32x2; typedef __attribute__((address_space(3))) float lds_f32;
#define XI_RD(i) xi_[i] = *(lds_u32x2*)(uintptr_t)(xra + ((i) & 1) * 16 + ((i) >> 1) * 1024)
#define XI_ST(i) do { const u32x4 p_ = u32x4{xi_[2 * (i)][0], xi_[2 * (i)][1], xi_[2 * (i) + 1][0], xi_[2 * (i) + 1][1]};               \
            asm volatile("global_store_dwordx4 %0, %1, %2 sc1" :: "v"(xso_), "v"(p_), "s"(ws + WS_XF + (unsigned)(wave * 262144 + (b >> 1) * 2048 + (b & 1) * 128 + (i) * 2097152)) : "memory"); } while (0)
        SB(); ZST(4);
        asm volatile("s_waitcnt vmcnt(%1)\n\ts_bitcmp1_b32 %0, 2\n\ts_cbranch_scc1 1f\n\ts_waitcnt vmcnt(8)\n1:\n\ts_barrier" :: "s"(wave), "i"(9) : "memory", "scc");
        KST(2); ZST(6);
        f32x4 a16[8][4];
        bf16x8 xf[2][4];
        bf16x4 wl[4], wh[4];
#define KXR(kt, q, mb) xf[(q) & 1][mb] = lds_rd(xa[(kt) & 1][(q) >> 1] + (unsigned)((((q) & 1) * 4 + (mb)) * 2048))
#define KWL(kt, s32, nb) wl[nb] = tr_rd(wa[(kt) & 1][(nb) & 1] + (unsigned)((s32) * 16384 + ((nb) >> 1) * 2048))
#define KWH(kt, s32, nb) wh[nb] = tr_rd(wa[(kt) & 1][(nb) & 1] + (unsigned)((s32) * 16384 + ((nb) >> 1) * 2048 + 256))
#define KMF(kt, q, g) do { const bf16x8 w_ = CAT8(wl[(g) >> 2], wh[(g) >> 2]); constexpr int m_ = ((q) & 1) * 4 + ((g) & 3);               \
            if ((kt) == 0 && (q) < 2) { if ((g) >= 8) MFA16Z(a16[m_][(g) >> 2], xf[(q) & 1][(g) & 3], w_); else MFA16Z(a16[m_][(g) >> 2], w_, xf[(q) & 1][(g) & 3]); } \
            else                      { if ((g) >= 8) MFA16(a16[m_][(g) >> 2], xf[(q) & 1][(g) & 3], w_);  else MFA16(a16[m_][(g) >> 2], w_, xf[(q) & 1][(g) & 3]); } } while (0)
        u64 lnf;
#define LNFP (ws + WS_FLAG + F_LN + (wave * 32 + (lane & 31)) * 8)
#define KRQ(i) do { GLDS_S(qwo, wqb + (size_t)(((i) >> 1) * 64) * LDW, ldsbase + wave * 2048 + ((i) >> 1) * 16384 + ((i) & 1) * 896, ((i) & 1) * 128); } while (0)
        constexpr int KRS = 3, KPFD = 3;
#define KG(kt, q, g) do { constexpr int nk_ = (kt) + ((q) == 3), nq_ = ((q) + 1) & 3;                                                     \
            constexpr int xi = ((q) & 1) ? ((g) == 1 ? 0 : (g) == 2 ? 1 : (g) == 5 ? 2 : (g) == 6 ? 3 : -1) : (((g) & 1) && (g) < 8 ? (g) >> 1 : -1); \
            constexpr int ri = (q) == 3 ? ((g) == 9 ? 0 : (g) == 10 ? 1 : (g) == 13 ? 2 : (g) == 14 ? 3 : -1) : (((g) & 1) && (g) > 8 ? ((g) - 9) >> 1 : -1); \
            KMF(kt, q, g); SB();                                                                                                        \
            if (xi >= 0 && nk_ < 8) KXR(nk_, nq_, xi & 3);                                                                              \
            if (((q) & 1) && nk_ < 8) { if (((g) & 3) == 3) KWL(nk_, nq_ >> 1, (g) >> 2); if (((g) & 3) == 0 && (g) > 0) KWH(nk_, nq_ >> 1, (((g) >> 2) + 3) & 3); } \
            if (!((q) & 1) && (g) == 0 && (kt) + (q) > 0) KWH(kt, (q) >> 1, 3);                                                          \
            { constexpr int G = (q) == 3 ? (g) : (q) == 0 ? 16 + (g) : -9, T = (kt) + ((q) == 3 ? 2 : 1);          \
              if (T >= 2 && T < 8) { if (G == 0) KM0(T, 0); if (G == 4 * KRS) KM0(T, 1);                                                 \
                  if (G >= 1 && (G - 1) % KRS == 0 && (G - 1) / KRS < 8) { if ((G - 1) / KRS < 4) KRX(((G - 1) / KRS) & 3); else KRW(((G - 1) / KRS) & 3); } \
                  if (G == 29) OADV(xsp, 128); if (G == 30) OADV(wsp, 64 * LDW * 2); if (G == 31) OADV(wsv, 64 * LDW * 2); } }                                                 \
            if ((q) == 1 && (kt) + KPFD < 8) { if ((kt) >= 1 && (g) == 13) PFX((kt) + KPFD); }                \
            if ((q) == 2 && (kt) == 6 && (g) == 9) { qwo = QWOFF(); PIN(qwo); }                                                         \
            if ((q) == 1 && (kt) == 7) { if ((g) == 9) asm volatile("global_load_dwordx4 %0, %1, %2 sc1" : "=v"(rv) : "v"(xvo), "s"(ws + WS_R + wave * 1024) : "memory"); \
                                         if ((g) == 10) QXL1(0); if ((g) == 13) QXL1(1); if ((g) == 14) QXL1(2); }                      \
            if ((q) == 2 && (kt) == 7 && (g) == 2) QXL1(3);                                                                             \
            if ((q) == 2 && (kt) == 7 && (g) == 9) { int w_ = wave; asm volatile("" : "+s"(w_));                                        \
                sb_[0] = ws + WS_KV + (unsigned)(hp * HSTR + ((w_ >> 2) * 2 + (jj >> 2)) * 65536 + (jj & 3) * 8192 + (w_ & 3) * 2048); asm volatile("" : "+s"(sb_[0])); } \
            if ((q) == 2 && (kt) == 7 && (g) >= 11 && (g) < 14) { sb_[((g) - 10) & 3] = sb_[0] + ((g) - 10) * 262144; asm volatile("" : "+s"(sb_[((g) - 10) & 3])); } \
            if ((q) == 2 && (kt) == 7 && (g) == 14) { qa = ldsbase + (unsigned)((q4 >> 1) * 4096 + (4 * (q4 & 1) + (l15 >> 2)) * 64 + (q4 & 1) * 32 + (lane & 3) * 8); PIN(qa); } \
            if ((q) == 2 && (kt) == 7 && (g) == 15) { qa2 = qa ^ 32u; PIN(qa2); }                                                                              \
            if ((q) == 3 && (kt) == 6 && ri >= 0) KRQ(ri & 3);                                                                          \
            if ((q) == 0 && (kt) == 7 && ri >= 0 && 4 + ri < 2 * QWD) KRQ(4 + (ri & 3));                                                                    \
            if ((q) == 0 && (kt) == 0) { unsigned l_ = lane;                                                                            \
                if ((g) == 9)  { PIN(l_); pxo = (l_ >> 3) * (DC * 2) + (l_ & 7) * 16; PIN(pxo); }                                       \
                if ((g) == 11) { PIN(l_); pqo = (l_ >> 4) * (LDW * 2) + (l_ & 15) * 16; PIN(pqo); }                                     \
                if ((g) == 13) { PIN(l_); xso_ = (l_ >> 3) * 256 + (l_ & 7) * 16; PIN(xso_); }                                          \
                if ((g) == 14) { PIN(l_); xra = (l_ & 7) * 2064 + (ldsbase + 131072 + wave * 128); PIN(xra); }                          \
                if ((g) == 15) { PIN(l_); xra += (l_ >> 4) * 32 + (l_ & 8); PIN(xra); } }                                               \
            if ((q) == 0 && (kt) == 1) { if ((g) == 9 || (g) == 11) { XI_RD((g) - 9); XI_RD((g) - 8); }                                 \
                if ((g) == 12) rr = *(lds_f32*)(uintptr_t)(ldsbase + 131072 + wave * 2064 + 2048); }                                    \
            if ((q) == 1 && (kt) == 1) { if ((g) == 9) XI_ST(0); if ((g) == 10) { KEEP(xi_[0]); KEEP(xi_[1]); } if ((g) == 14) XI_ST(1); if ((g) == 15) { KEEP(xi_[2]); KEEP(xi_[3]); } \
                if ((g) == 0) asm volatile("global_store_dword %0, %1, %2 sc1" :: "v"(0u), "v"(rr), "s"((float*)(ws + WS_R) + row) : "memory"); }     \
            if ((q) == 1 && ((kt) == 3 || (kt) == 4)) { if ((g) == 9) PFQ((2 * (kt) - 6) & 3); if ((g) == 11) PFQ((2 * (kt) - 5) & 3); }                                                    \
            if ((q) == 2 && (kt) == 5 && (g) == 12) FLAG_GET(lnf, LNFP);                                                                \
            SB(); } while (0)
#define KQ(kt, q) do { KG(kt, q, 0); KG(kt, q, 1); KG(kt, q, 2); KG(kt, q, 3); KG(kt, q, 4); KG(kt, q, 5); KG(kt, q, 6); KG(kt, q, 7);    \
                       KG(kt, q, 8); KG(kt, q, 9); KG(kt, q, 10); KG(kt, q, 11); KG(kt, q, 12); KG(kt, q, 13); KG(kt, q, 14); KG(kt, q, 15); } while (0)
#define KEXT(s) (((s) == 1 ? 3 : (s) == 5 ? 1 : 0) + ((s) >= 1 && (s) + KPFD < 8) + ((s) == 3 || (s) == 4 ? 2 : 0))
#define KSTEP(kt) do {                                                                                                                  \
        KQ(kt, 0); KQ(kt, 1); KQ(kt, 2);                                                                                                \
        if ((kt) == 6) FLAG_WAIT(lnf, LNFP, tag, 0);                                   \
                       \
        if ((kt) == 0) asm volatile("s_waitcnt vmcnt(%1) lgkmcnt(0)\n\ts_bitcmp1_b32 %0, 2\n\ts_cbranch_scc1 1f\n\ts_waitcnt vmcnt(%2)\n1:\n\ts_barrier" :: "s"(wave), "i"(KEXT(0) + 1), "i"(KEXT(0)) : "memory", "scc"); \
        else asm volatile("s_waitcnt vmcnt(%0) lgkmcnt(0)\n\ts_barrier" :: "i"(KEXT(kt)) : "memory");                                        \
        KST((kt) + 3);                                                                                                                  \
        if ((kt) == 2) FLAG_PUT(ws + WS_FLAG + F_LN + b * 8, tag, wave == 0);             \
        if ((kt) == 6) FLAG_PUT(ws + WS_FLAG + F_EPOCH, tag - 0x9E3779B97F4A7C15ull + 1, b == 0 && wave == 0);     \
        SB();                                                                                                                           \
        KQ(kt, 3);                                                                                                                      \
    } while (0)
        KXR(0, 0, 0); KXR(0, 0, 1); KWL(0, 0, 0); KWH(0, 0, 0); KXR(0, 0, 2); KXR(0, 0, 3);
        KWL(0, 0, 1); KWH(0, 0, 1); KWL(0, 0, 2); KWH(0, 0, 2); KWL(0, 0, 3); KWH(0, 0, 3);
        SB();
        KSTEP(0); ZST(7); KSTEP(1); KSTEP(2); KSTEP(3); KSTEP(4); KSTEP(5); KSTEP(6);
        xvo = l16;
        KQ(7, 0); KQ(7, 1); KQ(7, 2);
        if (wave < 4) {
            int t2_ = threadIdx.x; asm volatile("" : "+v"(t2_));
            unsigned l7_ = t2_ & 7, c7_ = jj >= 3 ? 7 : 8, b0_ = (unsigned)(((jj + 7) & 7) * 128 + head * 4 + (wave & 3)), b7_ = 3584u + head * 4 + (wave & 3);
            u64 pg_ = (u64)((wave & 1) ? w_up : w_gate), pd_ = (u64)w_down - 1792ull * 8192, po_ = (u64)wo - 2688ull * 8192;
            asm volatile("" : "+s"(c7_), "+s"(b0_), "+s"(b7_), "+s"(pg_), "+s"(pd_), "+s"(po_));
            const unsigned lt_ = l7_ < (unsigned)jj ? l7_ : 0;
            unsigned n_ = b0_ + (13u - lt_) * lt_ * 64u; n_ = l7_ == c7_ ? b7_ : n_;
            u64 p_ = n_ < 2688u ? pd_ : po_; p_ = n_ < 1792u ? pg_ : p_; n_ = n_ < 1792u ? n_ >> 1 : n_;
            *(u64*)(lds + L_PFT + t2_ * 8) = p_ + (u64)n_ * 8192u;
        }
        SB();
        asm volatile("s_waitcnt vmcnt(%0)\n\ts_barrier" :: "i"(2 * QWD + 1) : "memory");
        SB();
#define PKCV(i) do { const f32x4 a_ = ((i) & 2) ? a16[2 * ((i) >> 2)][(i) & 3] : a16[2 * ((i) >> 2) + ((i) & 1)][0];                       \
                     const f32x4 b_ = ((i) & 2) ? a16[2 * ((i) >> 2) + 1][(i) & 3] : a16[2 * ((i) >> 2) + ((i) & 1)][1];                   \
                     pk[i] = u32x4{cvtpk(a_[0], a_[1]), cvtpk(a_[2], a_[3]), cvtpk(b_[0], b_[1]), cvtpk(b_[2], b_[3])}; PIN(pk[i]); } while (0)
#define KL(i) do { KMF(7, 3, 2 * (i)); SB(); if ((i) >= 2) QXL1(2 * (i)); SB(); KMF(7, 3, 2 * (i) + 1); SB(); PKCV(i); if ((i) >= 2) QXL1(2 * (i) + 1); SB(); } while (0)
        KL(0); KL(1); KL(2); KL(3); KL(4); KL(5); KL(6); KL(7);
        KST(10);
        QRL(0, 0); QRH(0, 0); QRL(0, 1); QRH(0, 1); QRL(0, 2); QRH(0, 2); QRL(0, 3); QRH(0, 3);
        SB(); asm volatile("s_nop 3"); SB();
#pragma unroll
        for (int i = 8; i < 16; ++i) PKCV(i);
        SB();
        KST(11); ZST_OUT();
#undef PKCV
#undef KSTEP
#undef PFQ
#undef XKEY
    }
    PH_STAMP(1);
    const char* kvg = ws + WS_KV + (size_t)head * HSTR;
    const unsigned svv = xvo + 32768u;
    u64 pft; bf16x8 ones; unsigned ak, av;
    const int NA = qbA + 1;
    {
        constexpr int LDW = NH * HD, STG = 16384;
        int tid = threadIdx.x; asm volatile("" : "+v"(tid));
        const int lane = tid & 63;
        const unsigned woff = qwo, xvo2 = xvo + 262144u;
        f32x4 acc[2][8];
#define KVST(i) asm volatile("global_store_dwordx4 %0, %1, %2 offset:%3 sc1" :: "v"(((i) & 2) ? svv : xvo), "v"(pk[i]), "s"(sb_[(i) >> 2]), "i"(((i) & 1) * 1024) : "memory")
        u64 kvf = 0;
#define QKV(kt, f) do { constexpr int g_ = q_kv(kt, f), p_ = g_ >> 2; if (g_ >= 0) {                                                       \
            if ((g_ & 3) == 0) asm volatile("s_mov_b32 m0, %0\n\ts_nop 0" :: "s"(ldsbase + (p_ == 1 ? 0 : 65536 + p_ * 16384) + wave * 4096) : "memory"); \
            asm volatile("global_load_lds_dwordx4 %0, %1 offset:%2 sc1" :: "v"(xvo), "s"(kvg + p_ * 32768 + wave * 4096), "i"((g_ & 3) * 1024) : "memory"); } } while (0)
#define KVFP (ws + WS_FLAG + ((lane & 8) ? F_TOP : F_KV + 2048 + (head * 8 + (lane & 7)) * 8))
        const bool top2 = b == 56;
        unsigned* tw = (unsigned*)(lds + 131072 + 2048);
#define QMF(a, kt, f) do { if ((kt) == 0 && (f) < 8) MFA16Z(acc[a][(f) & 7], w_, xr[0][a][0]); else MFA16(acc[a][QFM(kt, f) & 7], w_, xr[(kt) & 3][a][QFM(kt, f) >> 3]); } while (0)
        constexpr float qs = 0.08838834764831845f * 1.4426950408889634f;
        u32x4 qw_[8];
        float qm_[2][2], qsv = qs; asm volatile("" : "+s"(qsv));
#define QWI(n) (2 * ((n) >> 3) + (((n) >> 1) & 1))
#define QWW(n) (2 * (((n) >> 2) & 1) + ((n) & 1))
#define QCN(n) do { if ((n) >= 0 && (n) < 32) { _Pragma("unroll") for (int e_ = 0; e_ < 2; ++e_)                                           \
                asm volatile("v_mul_f32 %0, %1, %2" : "=v"(qm_[(n) & 1][e_]) : "s"(qsv), "v"(acc[QWI(n) & 1][(QWI(n) & 6) + (QWW(n) >> 1)][2 * (QWW(n) & 1) + e_])); } \
            if ((n) >= 1 && (n) <= 32) { qw_[QWI((n) - 1) & 7][QWW((n) - 1)] = cvtpk(qm_[((n) - 1) & 1][0], qm_[((n) - 1) & 1][1]); KEEP(qw_[QWI((n) - 1) & 7][QWW((n) - 1)]); } } while (0)
#define QCG(kt, g) do { if ((kt) == 15) QCN((g) - 10); } while (0)
        constexpr int ZP0 = 238;
#define QPAIR(kt, f, A, B) do { const bf16x8 w_ = CAT8(ql[(f) & 7], qh[(f) & 7]); constexpr int s_ = (kt) + (((f) + 4) >> 4), m_ = ((f) + 4) & 15;  \
            QMF(0, kt, f); SB(); if (s_ < 16) QRL(s_, m_); A; QKV(kt, f); QCG(kt, 2 * (f)); SB();                                      \
            QMF(1, kt, f); SB(); if (s_ < 16) QRH(s_, m_); B; QCG(kt, 2 * (f) + 1);                                                                             \
            { constexpr int z_ = (kt) * 16 + (f) - ZP0;                    \
                                                                    \
              if (z_ == 8) { pft = *(const u64*)(lds + L_PFT + (tid & 255) * 8); }                                                       \
              if (z_ == 15) { av = ldsbase + xvo; ak = av + 65536u; PIN(av); PIN(ak); }                                                   \
              if (z_ == 16) { const unsigned o1 = (lane & 3) ? 0u : 0x3f803f80u; ones = __builtin_bit_cast(bf16x8, u32x4{o1, o1, o1, o1}); PIN(ones); }     \
              if (z_ == 14) PIN(pft); } SB(); if (((f) & 1) && s_ < 16) { __builtin_amdgcn_s_waitcnt(0xC47F); SB(); } } while (0)
        const char* xsp = xg + 3 * 262144;
#define QX(r, rb, ks, v_) asm volatile("global_load_dwordx4 %0, %1, %2 offset:%3 sc1" : "+v"(xr[r][rb][ks]) : "v"(v_), "s"(xsp), "i"((rb) * 2048 + (ks) * 1024) : "memory")
        const unsigned woff1 = woff + 1024u, woff2 = woff + 128u;
        const char* wsp = (const char*)(wqb + (size_t)QWD * 64 * LDW) - 1024;
        const unsigned m0b = ldsbase + wave * 2048;
#define QM0(r) asm volatile("s_add_u32 m0, %0, %1" :: "s"(m0b), "i"((r) * STG) : "memory", "scc")
#define QW1(v_, imm_) asm volatile("global_load_lds_dwordx4 %0, %1 offset:%2" :: "v"(v_), "s"(wsp), "i"(imm_) : "memory")
#define QST(i)
#define QSTEP(kt) do { constexpr int r_ = (kt) & 3;                                                                                     \
        QPAIR(kt, 0, if (q_xb(kt)) QX((r_ + 3) & 3, 0, 1, xvo), );                                                                      \
        QPAIR(kt, 1, , );                                                                                                               \
        QPAIR(kt, 2, if (q_xb(kt)) QX((r_ + 3) & 3, 1, 1, xvo), );                                                                      \
        QPAIR(kt, 3, if ((kt) == 12) FLAG_GET(kvf, KVFP), );                                                                            \
        if ((kt) == 0 && __builtin_expect(top2, 0)) {                                                                                                        \
            unsigned t1 = tkey(rv[0], tid * 4), t2 = 0;                                                                                 \
            _Pragma("unroll") for (int j = 1; j < 4; ++j) merge_top2(t1, t2, tkey(rv[j], tid * 4 + j), 0);                              \
            DPP_TOP2(t1, t2, 0x111, 0xf); DPP_TOP2(t1, t2, 0x112, 0xf); DPP_TOP2(t1, t2, 0x114, 0xf); DPP_TOP2(t1, t2, 0x118, 0xf);     \
            DPP_TOP2(t1, t2, 0x142, 0xa); DPP_TOP2(t1, t2, 0x143, 0xc);                                                                 \
            if (lane == 63) { tw[wave * 2] = t1; tw[wave * 2 + 1] = t2; } SB(); }                                                       \
        QPAIR(kt, 4, if ((QS1 >> (kt)) & 1) KVST(q_s0(kt) & 15), if ((QS1 >> (kt)) & 1) KEEP(pk[q_s0(kt) & 15]));                       \
        QPAIR(kt, 5, , );                                                                                                               \
        QPAIR(kt, 6, , );                                                                                                               \
        QPAIR(kt, 7, , );                                                                                                               \
        QPAIR(kt, 8, if (q_xa(kt)) QX(r_, 0, 0, xvo2), );                                                                               \
        QPAIR(kt, 9, , );                                                                                                               \
        QPAIR(kt, 10, if (q_xa(kt)) QX(r_, 1, 0, xvo2), if ((kt) <= 11) OADV(xsp, 262144));                                             \
        QPAIR(kt, 11, if ((kt) + QWD < 16) QM0((r_ + QWD) & 3), );                                                                      \
        if ((kt) < 15) {                              \
            if (QWD == 4 || (kt) == 0 || (kt) >= 13) asm volatile("s_waitcnt vmcnt(%0) lgkmcnt(0)\n\ts_barrier" :: "i"(q_wait(kt)) : "memory"); \
            else asm volatile("s_waitcnt vmcnt(%0)\n\ts_barrier" :: "i"(q_wait(kt)) : "memory");                                        \
            if ((kt) == 13) { PIN(kvf); const unsigned sh_ = (lane & 8) ? 22u : 0u; const u64 ex_ = (tag << sh_) >> sh_;     \
                if (__builtin_expect(__builtin_amdgcn_ballot_w64((kvf >> sh_) != ex_) != 0, 0)) { unsigned c_; u64 t_;                    \
                    asm volatile("s_mov_b32 %1, 0x40000\n1:\n\ts_sleep 2\n\tglobal_load_dwordx2 %0, %3, off sc1\n\ts_waitcnt vmcnt(0)\n\tv_lshrrev_b64 %2, %5, %0\n\t" \
                                 "v_cmp_ne_u64 vcc, %4, %2\n\ts_cmp_eq_u64 vcc, 0\n\ts_cbranch_scc1 2f\n\ts_sub_u32 %1, %1, 1\n\ts_cbranch_scc0 1b\n2:" \
                                 : "+v"(kvf), "=&s"(c_), "=&v"(t_) : "v"(KVFP), "v"(ex_), "v"(sh_) : "memory", "vcc", "scc"); }           \
                const unsigned r_ = __builtin_amdgcn_readlane((unsigned)kvf, 8); ri1 = r_ & 2047u; ri2 = (r_ >> 11) & 2047u; }           \
            QST((kt) + 1); SB(); }                                                                                                      \
        QPAIR(kt, 12, if ((kt) + QWD < 16) QW1(woff1, 0), );                                                                            \
        QPAIR(kt, 13, , );                                                                                                              \
        QPAIR(kt, 14, if ((kt) + QWD < 16) QW1(woff2, 1024), if ((kt) + QWD < 15) OADV(wsp, 64 * LDW * 2));                             \
        if ((kt) == q_fl()) FLAG_PUT(ws + WS_FLAG + F_KV + 2048 + (hp * 8 + jj) * 8, tag, wave == 0);     \
        QPAIR(kt, 15, , );                                                                                                              \
        if ((kt) == 0 && __builtin_expect(top2, 0)) {                                                                                                        \
            unsigned t1 = tw[0], t2 = tw[1];                                                                                            \
            _Pragma("unroll") for (int w = 1; w < 8; ++w) merge_top2(t1, t2, tw[w * 2], tw[w * 2 + 1]);                                 \
            FLAG_PUT(ws + WS_FLAG + F_TOP, (tag << 22) | (unsigned)__builtin_amdgcn_readfirstlane((~t1 & 2047u) | (~t2 & 2047u) << 11), wave == 0); \
            } \
        SB();                                                                                                                           \
    } while (0)
        asm volatile("s_waitcnt vmcnt(12)" : "+v"(rv) :: "memory");
        SB();
        QST(0);
        KST(12);
        QSTEP(0); QSTEP(1); QSTEP(2); QSTEP(3); QSTEP(4); QSTEP(5); QSTEP(6); QSTEP(7);
        QSTEP(8); QSTEP(9); QSTEP(10); QSTEP(11); QSTEP(12); QSTEP(13); QSTEP(14); QSTEP(15);
        QCN(22); QCN(23); QCN(24); QCN(25); QCN(26); QCN(27); QST(16); SB(); QCN(28); QCN(29); QCN(30); QCN(31); QCN(32);
#pragma unroll
        for (int i = 0; i < 8; ++i) { qf[i] = __builtin_bit_cast(bf16x8, qw_[i]); PIN(qf[i]); }
#undef QSTEP
    }
    PH_STAMP(2);
    const int kz = xcd & 1, kp = xcd >> 1, nt = kz * 4 + (idx >> 3), tile = jj * 8 + nt;
#define K64(s) ((head * 2) ^ (s))
#define OWOFF() ({ unsigned l_ = threadIdx.x & 63; asm volatile("" : "+v"(l_)); (unsigned)(((l_ >> 2) & 7) * DM + 32 * (l_ >> 5) + 8 * ((l_ & 3) ^ ((l_ >> 4) & 1))) * 2u; })
    const int n0 = (nt ^ 2) * 128, dn1 = (((nt ^ 3) - (nt ^ 2)) * 128 + 64) * 2, ntc = nt ^ 2 ^ (kp >> 1);
    const u16* wob = wo + (size_t)(8 * wave) * DM + n0;
    u64 cxf = tag + 1;
#define CFB (ws + WS_FLAG + F_CTX)
#define CFO() ({ unsigned l_ = threadIdx.x & 15, s0_ = (unsigned)((qb * 4 + qw) * 32 + head), s1_ = (unsigned)((qbB * 4 + 3 - (wave & 3)) * 32 + (head ^ 8)); \
                 asm volatile("" : "+v"(l_), "+s"(s0_), "+s"(s1_)); (l_ < 8 ? l_ ^ s0_ : s1_) * CFS; })
    u32x4 pc[8];
#define EAR_(v_, b_, pol_) "s_add_u32 m0, m0, 0x400\n\ts_nop 0\n\tglobal_load_lds_dwordx4 " v_ ", " b_ pol_ "\n\t"
#define EALD(h_) do { unsigned l_ = threadIdx.x & 63; asm volatile("" : "+v"(l_)); const unsigned lx_ = ((l_ >> 3) * DM + 8 * ((l_ & 7) ^ (l_ >> 3))) * 2u; \
        const int rb_ = ((h_) ? qbB * 8 + qw * 2 : qbA * 8 + (wave & 3) * 2) + (kp & 1);                                                  \
        asm volatile("s_mov_b32 m0, %5\n\t" EAR_("%0", "%3", "") EAR_("%1", "%3", "") EAR_("%2", "%4", " sc1") "global_load_lds_dwordx4 %2, %4 offset:1024 sc1" \
                     :: "v"(lx_), "v"(lx_ + 8 * DM * 2), "v"(l_ * 16u), "s"(x + (size_t)rb_ * 16 * DM + ntc * 128 + (kp >> 1) * 64),              \
                        "s"(ws + WS_XF + (size_t)(2 * ntc + (kp >> 1)) * 262144 + rb_ * 2048),                                             \
                        "s"(ldsbase + 131072 - 1024 + (wave & 3) * 8192 + (h_) * 4096) : "memory", "scc"); } while (0)
    const unsigned i1 = (((kz ? F1S : F1F) >> (4 * jj)) & 15u) * 16u + (kp * 4 + (idx >> 3));
    const unsigned i2 = (((kz ? F2S : F2F) >> (4 * jj)) & 15u) * 16u + (kp * 4 + (idx >> 3));
    const unsigned nf1 = i1 / 112u, nf2 = i2 / 112u;
    const unsigned k1 = i1 / 14u;
    unsigned ypub = 1;
    {
        int tid = threadIdx.x; asm volatile("" : "+v"(tid));
        const int lane = tid & 63;
        const unsigned l16 = (unsigned)lane * 16u;
#define EX(x) __builtin_amdgcn_exp2f(x)
#define TBAR() do { SB(); __builtin_amdgcn_s_waitcnt(0x0070); asm volatile("s_barrier" ::: "memory"); } while (0)
#define BRQ(t, r) do { constexpr int g_ = (r) >> 2; const int tt_ = (t) + (g_ == 2 ? 1 : 2);                                               \
            const unsigned o_ = (unsigned)((wave & 3) * 4096 + (g_ == 1 || g_ == 2 ? 16384 : 0));                                          \
            if (((r) & 3) == 0) GLDS_C(l16, kvg + (size_t)tt_ * 65536 + (g_ >= 2 ? 32768 : 0) + o_, ldsbase + (g_ >= 2 ? 0 : 65536) + (tt_ & 1) * 32768 + o_, 0); \
            else asm volatile("global_load_lds_dwordx4 %0, %1 offset:%2 sc1" :: "v"(l16), "s"(kvg + (size_t)tt_ * 65536 + (g_ >= 2 ? 32768 : 0) + o_), "i"(((r) & 3) * 1024) : "memory"); } while (0)
#define BRQ4(t, g) do { BRQ(t, 4 * (g)); BRQ(t, 4 * (g) + 1); BRQ(t, 4 * (g) + 2); BRQ(t, 4 * (g) + 3); } while (0)
#define BRQ2C(t, r) do { constexpr int g_ = (r) >> 2; const int tt_ = (t) + (g_ == 2 ? 1 : 2);                                             \
            const unsigned o_ = (unsigned)((wave & 3) * 4096 + (g_ == 1 || g_ == 2 ? 16384 : 0));                                          \
            asm volatile("s_cmp_eq_u32 %5, 0\n\ts_cbranch_scc1 1f\n\ts_mov_b32 m0, %2\n\ts_nop 0\n\tglobal_load_lds_dwordx4 %0, %1 offset:%3 sc1\n\t"  \
                         "global_load_lds_dwordx4 %0, %1 offset:%4 sc1\n1:"                                                               \
                         :: "v"(l16), "s"(kvg + (size_t)tt_ * 65536 + (g_ >= 2 ? 32768 : 0) + o_), "s"(ldsbase + (g_ >= 2 ? 0 : 65536) + (tt_ & 1) * 32768 + o_), \
                            "i"(((r) & 3) * 1024), "i"(((r) & 3) * 1024 + 1024), "s"(isb) : "memory", "scc"); } while (0)
        const unsigned isb = wave >> 2;
        u64 kf2 = tag;
        const char* kfb = ws + WS_FLAG + F_KV + head * 64;
#define KCHK() do { PIN(kf2); if (__builtin_expect((__builtin_amdgcn_ballot_w64(kf2 != tag) | (u64)((33u >> t) & 1u)) != 0, 0)) { \
            unsigned c_, a_;                                                                                                              \
            FLAG_PUTZ(ws + WS_FLAG + F_KV + (hp * 8 + jj) * 8, tag, (unsigned)(wave ^ 4) | (unsigned)t);                                  \
            asm volatile("s_cmp_lt_u32 %6, 5\n\ts_cbranch_scc1 3f\n\tv_lshrrev_b32 %2, 1, %5\n\tv_and_b32 %2, 56, %2\n\ts_mov_b32 %1, 0x40000\n" \
                         "1:\n\tglobal_load_dwordx2 %0, %2, %3 sc1\n\ts_cmp_eq_u32 %6, 5\n\ts_cbranch_scc1 3f\n\ts_waitcnt vmcnt(0)\n\tv_cmp_ne_u64 vcc, %4, %0\n\t" \
                         "s_cmp_eq_u64 vcc, 0\n\ts_cbranch_scc1 3f\n\ts_sleep 2\n\ts_sub_u32 %1, %1, 1\n\ts_cbranch_scc0 1b\n"           \
                         "3:" : "+v"(kf2), "=&s"(c_), "=&v"(a_) : "s"(kfb), "s"(tag), "v"(l16), "s"(t) : "memory", "vcc", "scc"); } SB(); } while (0)
#define PFW1(i_) do { unsigned a_; const u64 p_ = ((u64)(unsigned)__builtin_amdgcn_readlane((int)(pft >> 32), i_) << 32) | (unsigned)__builtin_amdgcn_readlane((int)pft, i_); \
            asm volatile("v_lshlrev_b32 %0, 3, %2\n\ts_mov_b32 m0, %3\n\ts_nop 2\n\tglobal_load_lds_dword %0, %1"                        \
                         : "=&v"(a_) : "s"(p_), "v"(l16), "s"(ldsbase + 131072 + (wave & 3) * 8192) : "memory"); } while (0)
#define ONCE2(n_) int CAT(n_once, __LINE__) = (int)(n_); asm volatile("" : "+s"(CAT(n_once, __LINE__))); _Pragma("unroll 1") for (int z_ = 0; z_ < CAT(n_once, __LINE__); ++z_)
#define AST(i)
#define XST(i)
#define TS0()
#define TS1()
        AST(0);
        asm volatile("s_bitcmp0_b32 %0, 2\n\ts_cbranch_scc1 1f\n\ts_setprio 1\n1:" :: "s"(wave) : "scc");
        {
            f32x4 o[8][2], ls[2], ng[2], S[2][4];
            bf16x8 pf[2][2], fr[16];
            float e[16];
            constexpr bool ZC = false;
            constexpr bool HOT = false;
            const unsigned lb16 = ldsbase + l16;
            int t = 0;
#define KOFF(f) ((((f) & 3) * 2 + (((f) >> 2) & 1)) * 1024)
#define RD(kb, n) do { constexpr int k_ = ((kb) + ((n) >> 4)) & 3, n_ = (n) & 15;                                                          \
            fr[n_] = n_ < 8 ? lds_rd(av + (unsigned)(((k_ + 3) & 3) * 8192 + n_ * 1024)) : lds_rd(ak + (unsigned)(((k_ + 1) & 3) * 8192 + KOFF(n_ & 7))); } while (0)
#define MX(p, n, a) do { if (ZC) MFA16Y(o[n][a], fr[n], pf[(p) ^ 1][a]); else MFA16(o[n][a], fr[n], pf[(p) ^ 1][a]); } while (0)
#define MY(p, n, h) do { constexpr int a_ = ((n) >> 1) & 1, t_ = 2 * ((n) & 1) + (h);                                                      \
                         if (t_) MFA16(S[(p) ^ 1][(((n) >> 1) & 2) + a_], fr[((n) & 12) + t_], qf[2 * t_ + a_]);                           \
                         else MFA16C(S[(p) ^ 1][(((n) >> 1) & 2) + a_], fr[(n) & 12], qf[a_], ng[a_]); } while (0)
#define GX(p, n, F1, F2) do { MX(p, n, 0); SB(); F1; SB(); MX(p, n, 1); SB(); F2; SB(); } while (0)
#define GY(p, n, F1, F2) do { MY(p, n, 0); SB(); F1; SB(); MY(p, n, 1); SB(); F2; SB(); } while (0)
#define ML(p, a) do { if (ZC) MFA16Y(ls[a], ones, pf[(p) ^ 1][a]); else MFA16(ls[a], ones, pf[(p) ^ 1][a]); } while (0)
#define GL(p, F1, F2) do { ML(p, 0); SB(); F1; SB(); ML(p, 1); SB(); F2; SB(); } while (0)
#define SV(p, i) S[p][(((i) >> 1) & 2) + ((i) >> 3)][(i) & 3]
#define E1(p, i) do { e[i] = EX(SV(p, i)); if (HOT) KEEP(e[i]); else PIN(e[i]); } while (0)
#define C1(p, j) do { u32x4 w_ = __builtin_bit_cast(u32x4, pf[p][(j) >> 2]); w_[(j) & 3] = cvtpk(e[2 * (j)], e[2 * (j) + 1]);               \
                      pf[p][(j) >> 2] = __builtin_bit_cast(bf16x8, w_); if (HOT) KEEP(w_[(j) & 3]); else PIN(pf[p][(j) >> 2]); } while (0)
#define DQ(kb, g) do { if (ISB_ && (kb) == 3) { if ((g) == 0) KCHK(); BRQ(t, g); } if (ISB_ && (kb) == 0 && (g) == 1) PFW1(t); } while (0)
#define W4() do { SB(); __builtin_amdgcn_s_waitcnt(0xC37F); } while (0)
#define US(kb, ISB) do { constexpr bool HOT = true, ZC = (ISB) == 3, ISB_ = (ISB) == 1; constexpr int p_ = (kb) & 1; constexpr bool B_ = (kb) == 2;                    \
            GX(p_, 0, RD(kb, 7); if ((kb) == 3) { ak ^= 32768u; PIN(ak); }, E1(p_, 0); DQ(kb, 0));                                        \
            GX(p_, 1, RD(kb, 8); if ((kb) == 0) { av ^= 32768u; PIN(av); }, E1(p_, 1); DQ(kb, 1));                                        \
            GL(p_, E1(p_, 2), E1(p_, 3));                                                                                                 \
            GX(p_, 2, RD(kb, 9); C1(p_, 0), E1(p_, 4); DQ(kb, 2));                                                                        \
            GX(p_, 3, RD(kb, 10); W4(), E1(p_, 5); DQ(kb, 3));                                                                            \
            GX(p_, 4, RD(kb, 11); C1(p_, 1), E1(p_, 6); DQ(kb, 4));                                                                       \
            GX(p_, 5, RD(kb, 12); C1(p_, 2), E1(p_, 7); DQ(kb, 5));                                                                       \
            GX(p_, 6, RD(kb, 13); C1(p_, 3), E1(p_, 8); DQ(kb, 6));                                                                       \
            GX(p_, 7, RD(kb, 14); W4(), E1(p_, 9); DQ(kb, 7));                                                                            \
            GY(p_, 8, RD(kb, 15), E1(p_, 10); DQ(kb, 8));                                           \
            GY(p_, 9, if (!B_) RD(kb, 16); C1(p_, 4), E1(p_, 11); DQ(kb, 9));                                                             \
            GY(p_, 10, if (B_) { TS0(); if (ISB_) { __builtin_amdgcn_s_waitcnt(0x0071); asm volatile("s_barrier" ::: "memory"); } else TBAR(); \
                                 TS1(); SB(); RD(kb, 16); }                                                                               \
                       RD(kb, 17); C1(p_, 5), E1(p_, 12); DQ(kb, 10));                                                                    \
            GY(p_, 11, RD(kb, 18); if (!B_) W4(), E1(p_, 13); DQ(kb, 11));                                                                \
            GY(p_, 12, RD(kb, 19), E1(p_, 14); DQ(kb, 12));                                                \
            GY(p_, 13, RD(kb, 20); C1(p_, 6), E1(p_, 15); DQ(kb, 13));                                                                    \
            GY(p_, 14, RD(kb, 21); C1(p_, 7), DQ(kb, 14));                                 \
            GY(p_, 15, RD(kb, 22), DQ(kb, 15); W4());                                                                                     \
        } while (0)
            asm volatile("s_waitcnt vmcnt(%0) lgkmcnt(0)\n\ts_barrier" :: "i"(4 + (QS1 >> 15)) : "memory");
            AST(1);
#pragma unroll
            for (int s_ = 0; s_ < 8; ++s_) fr[s_] = lds_rd(ak + (unsigned)KOFF(s_));
            BRQ2C(-1, 12); BRQ2C(-1, 14);
            SB();
#define M0_(n, a) do { if ((n) & 3) MFA16(S[0][(((n) >> 1) & 2) + (a)], fr[n], qf[2 * ((n) & 3) + (a)]); else MFA16Z(S[0][(((n) >> 1) & 2) + (a)], fr[n], qf[a]); } while (0)
#define G0_(n) do { M0_(n, 0); SB(); if (Q_NST + (n) < 16) KVST((Q_NST + (n)) & 15); SB(); M0_(n, 1); SB(); RD(0, 8 + (n));               \
                    if (Q_NST + (n) < 16) KEEP(pk[(Q_NST + (n)) & 15]); SB(); } while (0)
            G0_(0); G0_(1); G0_(2); G0_(3); G0_(4); G0_(5); G0_(6); G0_(7);
            MF_DONE();
            { ONCE2(isb * min(jj, 1)) { PFW1(0); } }
            {
                PIN(S[0][0]); PIN(S[0][1]); PIN(S[0][2]); PIN(S[0][3]);
                if (__builtin_expect((qb | qw) == 0, 0)) {
#pragma unroll
                    for (int r = 0; r < 4; ++r) { S[0][2][r] = -__builtin_inff(); if (4 * (lane >> 4) + r > (lane & 15)) S[0][0][r] = S[0][3][r] = -__builtin_inff(); } }
#pragma unroll
                for (int a = 0; a < 2; ++a) {
                    float mx_;
#define MX3(d, a_, b_, c_) asm("v_max3_f32 %0, %1, %2, %3" : "=v"(d) : "v"(a_), "v"(b_), "v"(c_))
                    MX3(mx_, SV(0, 8 * a), SV(0, 8 * a + 1), SV(0, 8 * a + 2)); MX3(mx_, mx_, SV(0, 8 * a + 3), SV(0, 8 * a + 4)); MX3(mx_, mx_, SV(0, 8 * a + 5), SV(0, 8 * a + 6));
                    { float m7_ = SV(0, 8 * a + 7), n_ = -1e30f; MX3(mx_, mx_, m7_, n_); }
                    { auto r1 = __builtin_amdgcn_permlane16_swap(__float_as_uint(mx_), __float_as_uint(mx_), false, false); MX3(mx_, r1[0], r1[1], r1[1]);
                      auto r2 = __builtin_amdgcn_permlane32_swap(__float_as_uint(mx_), __float_as_uint(mx_), false, false); MX3(mx_, r2[0], r2[1], r2[1]); }
                    mx_ = -POFF - mx_;
                    ng[a] = f32x4{mx_, mx_, mx_, mx_}; PIN(ng[a]);
                }
#pragma unroll
                for (int i = 0; i < 16; ++i) { float v_ = SV(0, i) + ng[i >> 3][0]; PIN(v_); SV(0, i) = v_; }
                PIN(S[0][0]); PIN(S[0][1]); PIN(S[0][2]); PIN(S[0][3]);
            }
            SB();
            GY(0, 8, E1(0, 0), RD(0, 16); E1(0, 1)); GY(0, 9, E1(0, 2); C1(0, 0), RD(0, 17); E1(0, 3));
            GY(0, 10, E1(0, 4); C1(0, 1), RD(0, 18); E1(0, 5)); GY(0, 11, E1(0, 6); C1(0, 2), RD(0, 19); E1(0, 7));
            GY(0, 12, E1(0, 8); C1(0, 3), RD(0, 20); E1(0, 9)); GY(0, 13, E1(0, 10); C1(0, 4), RD(0, 21); E1(0, 11));
            GY(0, 14, E1(0, 12); C1(0, 5), RD(0, 22); E1(0, 13)); GY(0, 15, E1(0, 14); C1(0, 6), E1(0, 15); C1(0, 7));
#define SCOPY() do { MF_DONE(); _Pragma("unroll") for (int i = 0; i < 4; ++i) { S[1][i] = S[0][i]; PIN(S[1][i]); } } while (0)
            if (__builtin_expect((qb | qw) == 0, 0)) { SCOPY(); pf[0][0] = pf[0][1] = bf16x8{}; PIN(pf[0][0]); PIN(pf[0][1]); }
            W4(); asm volatile("s_nop 1"); SB();
            AST(2);
            const int nu = 4 * qb + qw - 2;
#define MFA16Y(c, a, b) asm volatile("v_mfma_f32_16x16x32_bf16 %0, %1, %2, 0" : "+v"(c) : "v"(a), "v"(b))
#pragma unroll
            for (int d = 0; d < 18; ++d) { if (d < 16) asm volatile("" : "=v"(o[d >> 1][d & 1])); else asm volatile("" : "=v"(ls[d & 1])); }
            if (__builtin_expect(nu < 0, 0)) { const bf16x8 z_ = bf16x8{}; asm volatile("s_nop 1" :: "v"(z_));
#pragma unroll
                for (int d = 0; d < 18; ++d) { if (d < 16) MFA16Y(o[d >> 1][d & 1], z_, z_); else MFA16Y(ls[d & 1], z_, z_); } }
            { int n1_ = nu; asm volatile("" : "+s"(n1_)); ONCE2(min(n1_ + 2, 2) >> 1) { US(1, 3); } }
            {
                const int nl = nu >> 2, nB = isb ? nl : 0;
#pragma unroll 1
                while (t < nB) { US(2, 1); US(3, 1); ++t; US(0, 1); US(1, 1); }
#pragma unroll 1
                while (t < nl) { US(2, 0); US(3, 0); ++t; US(0, 0); US(1, 0); }
            }
            AST(3);
            SST(1);
#define PCOPY() do { SCOPY(); pf[0][0] = pf[1][0]; pf[0][1] = pf[1][1]; PIN(pf[0][0]); PIN(pf[0][1]); SB(); asm volatile("s_nop 1"); } while (0)
            {
                const int nr = nu < 0 ? 0 : nu & 3, ne = nu < -1 ? 0 : nu + 1;
                if (__builtin_expect(nr >= 1, 1)) {
                    US(2, 2);
                    if (__builtin_expect(nr >= 2, 1)) {
                        if (isb) { KCHK(); asm volatile("s_setprio 0"); BRQ4(t, 0); BRQ4(t, 1); BRQ4(t, 2); BRQ4(t, 3); asm volatile("s_setprio 1"); }
                        US(3, 2);
                        if (__builtin_expect(nr == 3, 0)) US(0, 2);
                    }
                }
                int np = ne; asm volatile("" : "+s"(np));
                if (__builtin_expect(np & 1, 0)) PCOPY();
                KEEP(ng[0]); KEEP(ng[1]);
                t = qb;
            }
#define RDV(n) fr[n] = lds_rd(avf + (unsigned)((n) * 1024))
#define M1(i) do { e[i] = __builtin_amdgcn_fmed3f(e[i], 0.f, rmp[(i) & 3]); PIN(e[i]); } while (0)
#define E2(i) E1(1, i)
#define CST(i) asm volatile("global_store_dwordx4 %0, %1, %2 offset:%3 sc1\n\ts_nop 1" :: "v"(l16), "v"(pc[i]), "s"(cb + ((i) >> 2) * 262144), "i"((((i) >> 1) & 1) * 1024 + ((i) & 1) * 2048) : "memory")
#define NRM(ISB, i) do { float y_[8];                                              \
                _Pragma("unroll") for (int z_ = 0; z_ < 8; ++z_) { y_[z_] = o[((i) & 6) + (z_ >> 2)][(i) & 1][z_ & 3] * inv[(i) & 1]; PIN(y_[z_]); } \
                pc[i] = u32x4{cvtpk(y_[0], y_[1]), cvtpk(y_[2], y_[3]), cvtpk(y_[4], y_[5]), cvtpk(y_[6], y_[7])};                        \
                PIN(pc[i]); if (!(ISB)) CST(i); SB(); } while (0)
#define XLD(b_) do { _Pragma("unroll") for (int n = 0; n < 8; ++n) asm volatile("global_load_dwordx4 %0, %1, %2 offset:%3 sc1" : "=v"(fr[n]) : "v"(l16 + (n >> 2) * 4096u), "s"(b_), "i"((n & 3) * 1024) : "memory"); \
                asm volatile("s_waitcnt vmcnt(0)" : "+v"(fr[0]), "+v"(fr[1]), "+v"(fr[2]), "+v"(fr[3]), "+v"(fr[4]), "+v"(fr[5]), "+v"(fr[6]), "+v"(fr[7]) :: "memory"); } while (0)
#define MFB(a_, b_, c_) __builtin_amdgcn_mfma_f32_16x16x32_bf16(a_, b_, c_, 0, 0, 0)
#define EXACT() do { __builtin_amdgcn_s_waitcnt(0xC07F); MF_DONE();                                                                                                           \
                float mr_[2] = {-1e30f, -1e30f}; const int nu_ = 4 * qb + qw2;                                                            \
                ls[0] = ls[1] = f32x4{}; _Pragma("unroll") for (int d = 0; d < 16; ++d) o[d >> 1][d & 1] = f32x4{};                       \
                _Pragma("unroll 1") for (int u = 0; u <= nu_; ++u) {                                                                      \
                    const char* kb_ = kvg + (size_t)(u >> 2) * 65536 + (u & 3) * 8192;                                                    \
                    XLD(kb_);                                                                                                             \
                    _Pragma("unroll") for (int i = 0; i < 4; ++i) { f32x4 s_ = f32x4{};                                                   \
                        _Pragma("unroll") for (int d = 0; d < 4; ++d) s_ = MFB(fr[2 * d + (i >> 1)], qf[2 * d + (i & 1)], s_);            \
                        S[1][i] = s_; }                                                                                                   \
                    if (u == nu_) { _Pragma("unroll") for (int r = 0; r < 4; ++r) { S[1][2][r] = -__builtin_inff();                       \
                        if (4 * (ln2 >> 4) + r > (ln2 & 15)) S[1][0][r] = S[1][3][r] = -__builtin_inff(); } }                             \
                    XLD(kb_ + 32768);                                                                                                     \
                    _Pragma("unroll") for (int a = 0; a < 2; ++a) {                                                                       \
                        float mx_ = SV(1, 8 * a); _Pragma("unroll") for (int r = 1; r < 8; ++r) mx_ = fmaxf(mx_, SV(1, 8 * a + r));       \
                        mx_ = fmaxf(quad_max(mx_), mr_[a]); const float al_ = EX(mr_[a] - mx_); mr_[a] = mx_;                             \
                        u32x4 w_; _Pragma("unroll") for (int j = 0; j < 4; ++j) w_[j] = cvtpk(EX(SV(1, 8 * a + 2 * j) - mx_), EX(SV(1, 8 * a + 2 * j + 1) - mx_)); \
                        const bf16x8 p_ = __builtin_bit_cast(bf16x8, w_);                                                                 \
                        ls[a] = MFB(ones, p_, ls[a] * al_);                                                                               \
                        _Pragma("unroll") for (int d = 0; d < 8; ++d) o[d][a] = MFB(fr[d], p_, o[d][a] * al_); } }                        \
                pf[1][0] = pf[1][1] = bf16x8{}; _Pragma("unroll") for (int n = 4; n < 8; ++n) fr[n] = bf16x8{};     \
                PIN(pf[1][0]); PIN(pf[1][1]); MF_DONE(); } while (0)
#define TAIL(ISB) do { float rmp[4];                                                                                         \
                XST(0);                                                                                                                   \
                MX(1, 0, 0); SB();                                                                                                        \
                int ln2 = lane, qw2 = qw; asm volatile("" : "+v"(ln2), "+s"(qw2));   \
                fr[7] = lds_rd(av + (unsigned)((qw2 + 3) & 3) * 8192u + 7168u); SB();                                                     \
                MX(1, 0, 1); SB(); E2(0); SB();                                                                                           \
                GX(1, 1, E2(1), E2(2));                                                                                                   \
                GL(1, E2(3), E2(8));                                                                                                      \
                float fl_ = (float)((ln2 & 15) - 4 * (ln2 >> 4)); PIN(fl_);                                                               \
                unsigned avf = lb16 + (unsigned)(t & 1) * 32768u + (unsigned)qw2 * 8192u; PIN(avf); SB();                                 \
                GX(1, 2, E2(9), E2(10));                                                                             \
                GX(1, 3, E2(11), _Pragma("unroll") for (int r = 0; r < 4; ++r) { rmp[r] = (fl_ + (0.5f - r)) * 1.2089258196146292e24f; PIN(rmp[r]); }); \
                if (__builtin_expect(qw2 == 2, 0)) {                               \
                    if (ISB) TBAR(); else LGKM0_BAR(); }                                                                                  \
                SB();                                                                                                                     \
                RDV(0); RDV(1); RDV(2); RDV(3); SB();                                                                                     \
                GX(1, 4, E2(12), M1(0); M1(1));                                                                                           \
                GX(1, 5, RDV(4); E2(13), M1(2); M1(3));                                                                                   \
                GX(1, 6, RDV(5); E2(14), C1(1, 0); C1(1, 1));                                                                             \
                GX(1, 7, RDV(6); E2(15), C1(1, 4); C1(1, 5));                                                                             \
                RDV(7); M1(12); M1(13); M1(14); M1(15); e[4] = e[5] = e[6] = e[7] = 0.f; C1(1, 2); C1(1, 3); SB();                        \
                C1(1, 6); C1(1, 7); SB();                                                                                                 \
                XST(1);                                                                                                                   \
                if (__builtin_expect(qw2 < 2, 0)) { if (ISB) TBAR(); else LGKM0_BAR(); }                                                  \
                SB();                                                                                                                     \
                XST(2); XST(3);                                                                                                           \
                float inv[2];                                                                                                \
                int hd2 = head; asm volatile("" : "+s"(hd2)); const char* cb = ws + WS_CTX + (unsigned)(hd2 * 524288 + (qb * 8 + qw2 * 2) * 2048); SB(); \
                GL(0, , ); GX(0, 0, , ); GX(0, 1, , ); GX(0, 2, , );                                                                      \
                PIN(ls[0]); PIN(ls[1]);                                                                                                   \
                const bool c_ = __builtin_amdgcn_ballot_w64(!(ls[0][0] + ls[1][0] <= LTHR)) != 0; SB();                                   \
                GX(0, 3, , );                                                                                                             \
                if (__builtin_expect(c_, 0)) EXACT();                                                                                     \
                SB();                                                                                                                     \
                inv[0] = __builtin_amdgcn_rcpf(ls[0][0]); inv[1] = __builtin_amdgcn_rcpf(ls[1][0]); PIN(inv[0]); PIN(inv[1]); SB();       \
                GX(0, 4, , asm volatile("s_nop 0"));                                                                                      \
                NRM(ISB, 0); GX(0, 5, , ); NRM(ISB, 1); GX(0, 6, , ); NRM(ISB, 2); GX(0, 7, , ); NRM(ISB, 3);                             \
                XST(4);                                                                                                                   \
                NRM(ISB, 4); NRM(ISB, 5); NRM(ISB, 6); NRM(ISB, 7);                                                                       \
                if (ISB) { asm volatile("s_setprio 0"); asm volatile("" : "+s"(t)); KCHK(); if (t + 2 < NA) { BRQ4(t, 0); BRQ4(t, 1); BRQ4(t, 3); } BRQ4(t, 2); \
                           CST(0); CST(1); CST(2); CST(3); CST(4); CST(5); CST(6); CST(7); }                                              \
                XST(5);                                                                                                                   \
            } while (0)
#define SPIN() do { PIN(S[1][0]); PIN(S[1][1]); PIN(S[1][2]); PIN(S[1][3]); } while (0)
            { ONCE2(isb ^ 1) { SPIN(); TAIL(0); } }
            { ONCE2(isb) { SPIN(); TAIL(1); } }
            SST(2);
            AST(4);
        }
        if (wave >= 4) {
            int t = qbB + 1;
            const int wv = wave & 3;
            XST(6);
#define WOLD(h_) do { const unsigned ow_ = OWOFF(); const char* wp_ = (const char*)(wo + (size_t)(K64(2 * (h_)) * 64 + 16 * wv) * DM + n0);  \
                _Pragma("unroll") for (int i = 0; i < 8; ++i)                                                                             \
                    GLDS_S(ow_, wp_ + (size_t)((i >> 2) * 64 + 8 * ((i >> 1) & 1)) * (DM * 2) + (i & 1) * dn1,                             \
                           ldsbase + 65536 + (h_) * 32768 + (i >> 2) * 16384 + (2 * wv + ((i >> 1) & 1)) * 2048 + (i & 1) * 1024, 0); } while (0)
            {
                const u32x4 em = u32x4{~0u, ~0u, ~0u, ~0u};
                const char* eb = ws + WS_PART + (size_t)(tile * 8 + wv) * 24576 + kp * 6144;
                asm volatile("global_store_dwordx4 %0, %1, %2 sc1\n\tglobal_store_dwordx4 %0, %1, %2 offset:1024 sc1\n\tglobal_store_dwordx4 %0, %1, %3 sc1\n\t"
                             "global_store_dwordx4 %0, %1, %3 offset:1024 sc1\n\ts_waitcnt vmcnt(12) lgkmcnt(0)"
                             :: "v"(l16), "v"(em), "s"(eb), "s"(eb + 4 * 24576) : "memory"); }
            if (jj == 7) { WOLD(1); EALD(0); EALD(1); }
            asm volatile("s_barrier" ::: "memory");
#define BATCH() do { KCHK(); if (t + 2 < NA) { BRQ4(t, 0); BRQ4(t, 1); BRQ4(t, 3); } BRQ4(t, 2); } while (0)
#define BRQ4S(t, g) do { BRQ(t, 4 * (g)); BRQ(t, 4 * (g) + 1); __builtin_amdgcn_s_sleep(2); BRQ(t, 4 * (g) + 2); BRQ(t, 4 * (g) + 3); __builtin_amdgcn_s_sleep(2); } while (0)
#define BATCHS() do { KCHK(); BRQ4S(t, 2); BRQ4S(t, 0); BRQ4S(t, 1); BRQ4S(t, 3); } while (0)
#define BSTEP(N) do { asm volatile("s_waitcnt vmcnt(%0)\n\ts_barrier" :: "i"(N) : "memory"); ++t; } while (0)
            u32x4 fw[16], wd[16], xq[4], gp[8];
            f32x4 fa[8];
            int ln_ = lane; asm volatile("" : "+v"(ln_));
#define FMM(w_, i0, a_, z_) do { asm volatile("s_setprio 1");                                                              \
                    _Pragma("unroll") for (int i = 0; i < 8; ++i) PIN(w_[(i0) + i]);                                                      \
                    PIN(a_);                                                                                                              \
                    _Pragma("unroll") for (int j = 0; j < 8; ++j) { u32x4 f_;                                                             \
                        _Pragma("unroll") for (int d = 0; d < 4; ++d) f_[d] = __builtin_amdgcn_perm(w_[(i0) + 2 * d + 1][j >> 1], w_[(i0) + 2 * d][j >> 1], (j & 1) ? 0x07060302u : 0x05040100u); \
                        fa[j] = __builtin_amdgcn_mfma_f32_16x16x32_bf16(__builtin_bit_cast(bf16x8, a_), __builtin_bit_cast(bf16x8, f_), (z_) ? f32x4{} : fa[j], 0, 0, 0); } \
                    _Pragma("unroll") for (int j = 0; j < 8; ++j) PIN(fa[j]);                                                             \
                    asm volatile("s_setprio 0"); } while (0)
#define FOUT() f32x4{ln_ >= 32 ? fa[4][0] : fa[0][0], ln_ >= 32 ? fa[5][0] : fa[1][0], ln_ >= 32 ? fa[6][0] : fa[2][0], ln_ >= 32 ? fa[7][0] : fa[3][0]}
            const int c1 = 2 * (i1 - 14 * k1) + (wv & 1);
            const unsigned r4o = (unsigned)(ln_ & 15) * 16u;
            const char* cxp = ws + WS_FLAG + F_CTX + ((qbB * 4 + qw) * 32 + head) * CFS;
            if (nf1 == 0) {
#pragma unroll 1
                while (t < F1T) { BATCHS(); BSTEP(0); }
                const unsigned evo = (unsigned)((16 * (ln_ >> 5) + 4 * ((ln_ >> 4) & 1)) * FF * 2) + r4o;
                const u16* fwp = (wv < 2 ? w_gate : w_up) + (size_t)(k1 * 128) * FF + c1 * 128;
#define FLD(h_, c_) do { _Pragma("unroll") for (int i = 0; i < 8; ++i)                                                                    \
                    asm volatile("global_load_dwordx4 %0, %1, %2" : c_(fw[((h_) & 1) * 8 + i]) : "v"(evo), "s"(fwp + (size_t)(32 * (h_) + 8 * (i >> 2) + (i & 3)) * FF) : "memory"); } while (0)
                BATCH();
                { const int xr_ = (ln_ & 4) ? ri2 : ri1;
#pragma unroll
                  for (int i = 0; i < 4; ++i)
                      asm volatile("global_load_dwordx4 %0, %1, %2 offset:%3 sc1" : "=v"(xq[i]) : "v"((unsigned)((xr_ >> 4) * 2048 + (xr_ & 15) * 16 + (ln_ >> 4) * 256)),
                                   "s"(ws + WS_XF + (size_t)(k1 * 2 + (i >> 1)) * 262144), "i"((i & 1) * 1024) : "memory"); }
                FLD(0, "=v"); FLD(1, "=v"); BSTEP(20);
                BATCH(); asm volatile("s_waitcnt vmcnt(16)" ::: "memory");
                FLAG_PUT(cxp, tag, 1);
                FMM(fw, 0, xq[0], 1); FLD(2, "=v"); FMM(fw, 8, xq[1], 0); FLD(3, "=v"); BSTEP(17);
                BATCH(); asm volatile("s_waitcnt vmcnt(16)" ::: "memory");
                FMM(fw, 0, xq[2], 0); FMM(fw, 8, xq[3], 0); const f32x4 sum = FOUT();
                ST16_SC1((float*)(ws + WS_GP) + ((size_t)k1 * 4 + (wv >> 1) * 2 + ((ln_ >> 4) & 1)) * FF + c1 * 128 + (ln_ & 15) * 8 + (ln_ >> 5) * 4, sum);
                PFW1(7); BSTEP(2);
            }
            const char* pnd = nf1 ? cxp : ws + WS_FLAG + F_GU + (c1 * 16 + (wv >> 1) * 8 + k1) * 8;
            const int tp = nf1 ? t + 1 : t;
#define PUB(N) do { if (t == tp) { asm volatile("s_waitcnt vmcnt(%0)" :: "i"(N) : "memory"); FLAG_PUT(pnd, tag, 1); } } while (0)
            const int f0 = (nf2 ? 0 : i2 >> 3) * 256 + wv * 64;
            const char* gfp = ws + WS_FLAG + F_GU + (f0 >> 7) * 128 + (ln_ & 15) * 8;
            u64 gf = tag + 1;
            char* vs = lds + (NA & 1) * 32768;
#define GFLD() asm volatile("global_load_dwordx2 %0, %1, off sc1" : "+v"(gf) : "v"(gfp) : "memory")
#define WDLD(i, n_) do { const unsigned dvo = (unsigned)((ln_ >> 4) * 8 * DM * 2) + r4o;                                                  \
                    const char* db_ = (const char*)(w_down + (size_t)((nf2 ? 0 : i2 >> 3) * 256 + wv * 64) * DM + (i2 & 7) * 128) + 4096; \
                    asm volatile("s_cmp_lg_u32 %11, 0\n\ts_cbranch_scc1 1f\n\t"                                                          \
                                 "global_load_dwordx4 %0, %8, %9 offset:-4096\n\tglobal_load_dwordx4 %1, %8, %9 offset:-2048\n\tglobal_load_dwordx4 %2, %8, %9\n\t" \
                                 "global_load_dwordx4 %3, %8, %9 offset:2048\n\tglobal_load_dwordx4 %4, %8, %10 offset:-4096\n\tglobal_load_dwordx4 %5, %8, %10 offset:-2048\n\t" \
                                 "global_load_dwordx4 %6, %8, %10\n\tglobal_load_dwordx4 %7, %8, %10 offset:2048\n1:"  \
                                 : "=&v"(wd[i]), "=&v"(wd[i + 1]), "=&v"(wd[i + 2]), "=&v"(wd[i + 3]), "=&v"(wd[i + 4]), "=&v"(wd[i + 5]), "=&v"(wd[i + 6]), "=&v"(wd[i + 7]) \
                                 : "v"(dvo), "s"(db_ + (i) * 8192), "s"(db_ + (i) * 8192 + 8192), "s"(nf2) : "memory", "scc");            \
                    EALD((i) >> 3);                                                                                                       \
                    asm volatile("s_waitcnt vmcnt(%1)\n\ts_cmp_eq_u32 %0, 0\n\ts_cbranch_scc1 1f\n\ts_waitcnt vmcnt(%2)\n1:"                \
                                 :: "s"(nf2), "i"(12 + (n_)), "i"(4 + (n_)) : "memory", "scc");                                           \
                    BSTEP(63); } while (0)
#pragma unroll 1
            while (t < NA - 4) { BATCHS(); PUB(17); BSTEP(0); }
            if (jj < 7) {
                BATCH(); PUB(16); WDLD(0, 0);
                BATCH(); PUB(16); GFLD(); WDLD(8, 0);
            }
            if (jj < 6) {
                BATCH();
                if (nf2 == 0) {
                    FLAG_WAIT(gf, gfp, tag, 63);
#pragma unroll
                    for (int i = 0; i < 8; ++i)
                        asm volatile("global_load_dwordx4 %0, %1, %2 sc1" : "=v"(gp[i]) : "v"((unsigned)((ln_ >> 4) * FF * 4) + r4o), "s"(ws + WS_GP + ((size_t)i * 4 * FF + f0) * 4) : "memory");
                    *(unsigned*)vs = 0;
                    asm volatile("s_waitcnt lgkmcnt(0)" ::: "memory");
                }
                WOLD(NA & 1);
                BSTEP(8);
            } else if (jj == 6) WOLD(0);
            asm volatile("global_load_dwordx2 %0, %1, %2 sc1" : "+v"(cxf) : "v"(CFO()), "s"(CFB) : "memory");
            WOLD((NA & 1) ^ 1);
            asm volatile("s_waitcnt vmcnt(16)" ::: "memory");
            if (jj == 7) FLAG_PUT(cxp, tag, 1);
            if (nf2 == 0) {
                f32x4 g4;
#pragma unroll
                for (int i = 0; i < 8; ++i) { PIN(gp[i]);
#pragma unroll
                    for (int j = 0; j < 4; ++j) { if (i) VADD(g4[j], g4[j], __uint_as_float(gp[i][j])); else g4[j] = __uint_as_float(gp[0][j]); } }
                float hv[2];
#pragma unroll
                for (int k = 0; k < 2; ++k) { auto rr = __builtin_amdgcn_permlane32_swap(__float_as_uint(g4[k]), __float_as_uint(g4[2 + k]), false, false);
                    const float gb = rbf(__uint_as_float(rr[0])), ub = rbf(__uint_as_float(rr[1]));
                    hv[k] = rbf(gb / (1.f + __expf(-gb))) * ub; PIN(hv[k]); }
                char* hl = vs + 8192 + wv * 256;
                *(unsigned*)(hl + ((ln_ >> 4) & 1) * 128 + (ln_ & 15) * 8 + (ln_ >> 5) * 4) = cvtpk(hv[0], hv[1]);
                asm volatile("" ::: "memory");
                xq[0] = *(const u32x4*)(hl + ((ln_ >> 2) & 1) * 128 + (ln_ >> 4) * 16); xq[1] = *(const u32x4*)(hl + ((ln_ >> 2) & 1) * 128 + 64 + (ln_ >> 4) * 16);
                FMM(wd, 0, xq[0], 1); FMM(wd, 8, xq[1], 0);
                *(f32x4*)(vs + 4096 + wv * 1024 + ln_ * 16) = FOUT();
                unsigned n_;
                asm volatile("s_waitcnt lgkmcnt(0)\n\ts_mov_b64 exec, 1\n\tds_add_rtn_u32 %0, %1, %2\n\ts_mov_b64 exec, -1\n\ts_waitcnt lgkmcnt(0)"
                             : "=&v"(n_) : "v"(ldsbase + (NA & 1) * 32768u), "v"(1u) : "memory");
                ypub = __builtin_amdgcn_readfirstlane(n_) ^ 3u;
                if (ypub == 0) {
                    const f32x4* yr = (const f32x4*)(vs + 4096) + ln_;
                    const f32x4 a = (yr[0] + yr[64]) + (yr[128] + yr[192]);
                    ST16_SC1((float*)(ws + WS_YP) + ((size_t)(((ln_ >> 4) & 1) * 8 + (i2 & 7)) * 16 + (i2 >> 3)) * 128 + (ln_ & 15) * 8 + (ln_ >> 5) * 4, a);
                }
            }
            AST(5);
        }
    }
    PH_STAMP(3);
    {
        constexpr int LDW = 1024, STG = 16384;
        int tid = threadIdx.x; asm volatile("" : "+v"(tid));
        const int lane = tid & 63, l15 = lane & 15, q4 = lane >> 4;
        unsigned xvo = (unsigned)lane * 16u; PIN(xvo);
        if (!(kp & 1)) {
#pragma unroll
            for (int i = 0; i < 8; i += 2) { const u32x4 t_ = pc[i]; pc[i] = pc[i + 1]; pc[i + 1] = t_; PIN(pc[i]); PIN(pc[i + 1]); } }
        bf16x8 yr_[4][2][2];
#pragma unroll
        for (int i = 0; i < 8; ++i) yr_[i >> 2][i & 1][(i >> 1) & 1] = __builtin_bit_cast(bf16x8, pc[i]);
        unsigned wa[2][2];
        wa[0][0] = ldsbase + (unsigned)(65536 + ((kp >> 1) ^ 1) * 1024 + (q4 >> 1) * 2048 + (4 * (q4 & 1) + (l15 >> 2)) * 64 + (q4 & 1) * 16 + (lane & 2) * 16 + (lane & 1) * 8);
        wa[0][1] = wa[0][0] ^ 16u; wa[1][0] = wa[0][0] ^ 1024u; wa[1][1] = wa[0][1] ^ 1024u;
        PIN(wa[0][0]); PIN(wa[0][1]); PIN(wa[1][0]); PIN(wa[1][1]);
        const char* cg = ws + WS_CTX + (qb * 8 + qw * 2) * 2048;
        unsigned xok;
#define XLD8 "global_load_dwordx4 %0, %10, %12 sc1\n\tglobal_load_dwordx4 %1, %10, %13 sc1\n\tglobal_load_dwordx4 %2, %10, %12 offset:1024 sc1\n\tglobal_load_dwordx4 %3, %10, %13 offset:1024 sc1\n\t" \
             "global_load_dwordx4 %4, %11, %12 sc1\n\tglobal_load_dwordx4 %5, %11, %13 sc1\n\tglobal_load_dwordx4 %6, %11, %12 offset:1024 sc1\n\tglobal_load_dwordx4 %7, %11, %13 offset:1024 sc1\n\t"
#define XOUT(c_) c_(yr_[2][0][0]), c_(yr_[2][1][0]), c_(yr_[2][0][1]), c_(yr_[2][1][1]), c_(yr_[3][0][0]), c_(yr_[3][1][0]), c_(yr_[3][0][1]), c_(yr_[3][1][1])
#define XIN "v"(xvo), "v"(xvo2), "s"(xp0), "s"(xp1), "s"(tag)
        unsigned xvo2 = xvo + 262144u, wvo = OWOFF() + (unsigned)K64(4) * (64 * LDW * 2), xso = xvo + (unsigned)K64(4) * 262144u, cfo = CFO();
        PIN(xvo2); PIN(wvo); PIN(xso); PIN(cfo);
        const char* cg0 = cg + ((kp ^ 1) & 1) * 2048, * cg1 = cg + (kp & 1) * 2048, * xp0 = cg0 + (unsigned)K64(2) * 262144u, * xp1 = cg1 + (unsigned)K64(2) * 262144u;
        const char* cfb = CFB, * wb0 = (const char*)wob, * wb1 = wb0 + (dn1 - 1024);
        const char* fpp = ws + WS_FLAG + ((wave & 4) ? F_Y + ((i2 & 7) * 16 + ((i2 >> 3) & 15)) * 8 : F_CTX + ((qbA * 4 + wave) * 32 + head) * CFS);
        unsigned fpz = (wave >> 2) * ypub, m0w = ldsbase + wave * 2048;
        asm volatile("" : "+s"(cg0), "+s"(cg1), "+s"(xp0), "+s"(xp1), "+s"(cfb), "+s"(wb0), "+s"(wb1), "+s"(fpp), "+s"(fpz), "+s"(m0w));
        SST(3);
        SB();
        asm volatile("s_waitcnt vmcnt(0)" ::: "memory");
        FLAG_PUTZ(fpp, tag, fpz);
        asm volatile("v_cmp_ne_u64 vcc, %14, %8\n\ts_and_b32 %9, vcc_lo, 14\n\ts_cbranch_scc1 1f\n\t"
                     XLD8 "\n1:\n\ts_barrier" : XOUT("=&v"), "+v"(cxf), "=&s"(xok) : XIN : "memory", "vcc", "scc");
        SST(4);
#define BPRIO() asm volatile("s_bitcmp0_b32 %0, 2\n\ts_cbranch_scc1 1f\n\ts_setprio 1\n1:" :: "s"(wave) : "scc")
        BPRIO();
        f32x4 acc[2][8];
        bf16x4 wl[8], wh[8];
#define OFA(s, k_, j) (wa[(j) >> 2][(j) & 1] + (unsigned)(((s) & 3) * STG + (k_) * 8192 + (((j) >> 1) & 1) * 512))
#define ORL(i, s, k_, j) wl[i] = tr_rd(OFA(s, k_, j))
#define ORH(i, s, k_, j) wh[i] = tr_rd(OFA(s, k_, j) + 4096u)
        ORL(0, 0, 0, 0); ORH(0, 0, 0, 0); ORL(1, 0, 0, 1); ORH(1, 0, 0, 1); ORL(2, 0, 0, 2); ORH(2, 0, 0, 2); ORL(3, 0, 0, 3); ORH(3, 0, 0, 3); SB();
        const int r0 = qb * 128 + qw * 32 + (kp & 1) * 16, ch = kp >> 1;
        const unsigned nrt = min((unsigned)(ri1 - r0) >> 4, (unsigned)(ri2 - r0) >> 4);
        u64 yf = 0;
#define YFP (ws + WS_FLAG + F_Y + (ntc * 16 + (l15 < 14 ? l15 : 0)) * 8)
#define OMF(a, s, k_, j) do { if ((s) == 0 && (k_) == 0) MFA16Z(acc[a][j], w_, yr_[0][a][0]); else MFA16(acc[a][j], w_, yr_[(s) & 3][a][k_]); } while (0)
#define ORQ(h) do { constexpr int x_ = o_x(h), w_ = o_w(h);                                                                               \
            if (x_ >= 0) { asm volatile("global_load_dwordx4 %0, %1, %2 offset:%3 sc1" : "=v"(yr_[(x_ >> 2) & 3][x_ & 1][(x_ >> 1) & 1])   \
                                        : "v"(xso), "s"((x_ & 1) ? cg1 : cg0), "i"(((x_ >> 1) & 1) * 1024) : "memory");         \
                           if ((x_ & 3) == 3) { xso ^= (unsigned)((x_ >> 2) ^ ((x_ >> 2) + 1)) * 262144u; PIN(xso); } }                    \
            if (o_w((h) + 1) >= 0 && !(o_w((h) + 1) & 1)) asm volatile("s_mov_b32 m0, %0" :: "s"(m0w + (((o_w((h) + 1) >> 1) & 7) ^ 4) * STG) : "memory"); \
            if (w_ >= 0) { asm volatile("global_load_lds_dwordx4 %0, %1 offset:%2" :: "v"(wvo), "s"((w_ & 1) ? wb1 : wb0), "i"((w_ & 1) * 1024) : "memory"); \
                           if (w_ & 1) { wvo ^= (unsigned)((w_ >> 1) ^ ((w_ >> 1) + 1)) * (64 * LDW * 2); PIN(wvo); } } } while (0)
#define OPAIR(s, f, A) do { const bf16x8 w_ = CAT8(wl[(f) & 7], wh[(f) & 7]); constexpr int s_ = (s) + (((f) + 4) >> 4), m_ = ((f) + 4) & 15; \
            OMF(0, s, (f) >> 3, (f) & 7); SB(); ORL(m_ & 7, s_, m_ >> 3, m_ & 7); ORQ(32 * (s) + 2 * (f)); A; SB();                       \
            OMF(1, s, (f) >> 3, (f) & 7); SB(); ORH(m_ & 7, s_, m_ >> 3, m_ & 7); ORQ(32 * (s) + 2 * (f) + 1); SB(); if (((f) & 1)) { __builtin_amdgcn_s_waitcnt(0xC47F); SB(); } } while (0)
#define OTG(h_) do { wa[h_][0] ^= 65536u; wa[h_][1] ^= 65536u; PIN(wa[h_][0]); PIN(wa[h_][1]); } while (0)
#define OST(i)
#define OSTEP(s, POST) do {                                                                                                               \
        OPAIR(s, 0, ); OPAIR(s, 1, ); OPAIR(s, 2, ); OPAIR(s, 3, ); OPAIR(s, 4, ); OPAIR(s, 5, ); OPAIR(s, 6, ); OPAIR(s, 7, );           \
        OPAIR(s, 8, ); OPAIR(s, 9, if (((s) & 3) == 3) OTG(0)); OPAIR(s, 10, ); OPAIR(s, 11, );                                           \
        if ((s) >= 3) asm volatile("s_waitcnt vmcnt(%0)\n\ts_barrier" :: "i"(o_wait(s)) : "memory");                                      \
        SB(); POST; OST((s) + 1);                                                                                                         \
        OPAIR(s, 12, ); OPAIR(s, 13, if (((s) & 3) == 3) OTG(1)); OPAIR(s, 14, ); OPAIR(s, 15, );                                         \
    } while (0)
        PH_STAMP(4);
        SST(5);
        OST(0);
        SB();
        OSTEP(0, );
        asm volatile("s_cmp_eq_u32 %3, 0\n\ts_cbranch_scc1 1f\n\tglobal_load_dwordx2 %0, %1, %2 sc1\n1:" : "+v"(cxf) : "v"(cfo), "s"(cfb), "s"(xok) : "memory", "scc");
        OSTEP(1, );
        asm volatile("s_waitcnt vmcnt(8)\n\ts_cmp_eq_u32 %9, 0\n\ts_cbranch_scc1 3f\n\ts_mov_b32 %9, 0x40000\n\ts_waitcnt vmcnt(4)\n"
                     "1:\n\tv_cmp_ne_u64 vcc, %14, %8\n\ts_and_b32 vcc_lo, vcc_lo, 14\n\ts_cbranch_scc0 2f\n\t"
                     "global_load_dwordx2 %8, %15, %16 sc1\n\ts_sleep 4\n\ts_waitcnt vmcnt(0)\n\ts_sub_u32 %9, %9, 1\n\ts_cbranch_scc0 1b\n2:\n\t" XLD8 "s_waitcnt vmcnt(4)\n3:"
                     : XOUT("+v"), "+v"(cxf), "+s"(xok) : XIN, "v"(cfo), "s"(cfb) : "memory", "vcc", "scc");
        const unsigned inc = (unsigned)__builtin_amdgcn_ballot_w64(cxf != tag) & 0x1f0u;
        SB();
        OSTEP(2, if (__builtin_expect(inc != 0, 0)) asm volatile("global_load_dwordx2 %0, %1, %2 sc1" : "+v"(cxf) : "v"(cfo), "s"(cfb) : "memory"); SB());
        asm volatile("s_waitcnt vmcnt(8)" ::: "memory"); SB();
        OSTEP(3, );
        OSTEP(4, if (__builtin_expect(inc != 0, 0)) FLAG_WAITM(cxf, cfb + cfo, tag, 63, 0x1f0u); SB());
        asm volatile("s_setprio 0");
        OSTEP(5, ); OSTEP(6, );
        OSTEP(7, if (__builtin_expect(nrt == 0, 0)) asm volatile("global_load_dwordx2 %0, %1, off sc1" : "+v"(yf) : "v"(YFP) : "memory"); SB());
        OSTEP(8, ); OSTEP(9, ); OSTEP(10, );
        u32x4 yq[7], pv[6], zn[2]; u32x2 ea[4]; unsigned rq;
        if (__builtin_expect(nrt == 0, 0)) {
            FLAG_WAIT(yf, YFP, tag, 63);
            asm volatile("global_load_dwordx4 %0, %8, %10 sc1\n\tglobal_load_dwordx4 %1, %8, %10 offset:512 sc1\n\tglobal_load_dwordx4 %2, %8, %10 offset:1024 sc1\n\t"
                         "global_load_dwordx4 %3, %8, %10 offset:1536 sc1\n\tglobal_load_dwordx4 %4, %8, %10 offset:2048 sc1\n\tglobal_load_dwordx4 %5, %8, %10 offset:2560 sc1\n\t"
                         "global_load_dwordx4 %6, %8, %10 offset:3072 sc1\n\tglobal_load_dword %7, %9, %10 sc1"
                         : "=&v"(yq[0]), "=&v"(yq[1]), "=&v"(yq[2]), "=&v"(yq[3]), "=&v"(yq[4]), "=&v"(yq[5]), "=&v"(yq[6]), "=&v"(rq)
                         : "v"((unsigned)WS_YP + (unsigned)((((lane >> 5) * 8 + ntc) * 16 + (q4 & 1) * 7) * 128 + ch * 64 + 4 * l15) * 4u),
                           "v"((unsigned)WS_R + (unsigned)(lane >= 32 ? ri2 : ri1) * 4u), "s"(ws) : "memory");
        }
        SB();
        OSTEP(11, );
        char* pb = ws + WS_PART + (size_t)(tile * 8 + wave) * 24576;
        const char* tfb = ws + WS_FLAG + F_PARK + (tile * 8 + wave) * 256;
        const unsigned tfo = (unsigned)((lane & 3) ^ kp) * 64u;
        const unsigned ea0 = ldsbase + 131072 + (wave & 3) * 8192 + (wave >> 2) * 4096 + l15 * 128 + (((lane >> 5) * 2 ^ (lane & 7)) << 4) + (q4 & 1) * 8;
        typedef __attribute__((address_space(3))) u32x2 lds_u32x2;
#define OSND(a, j) do { constexpr int x_ = ((a) ^ 1) | (((j) >> 1) & 2 ^ 2);                                             \
            u32x4 p_ = u32x4{cvtpk(acc[a][j][0], acc[a][j][1]), cvtpk(acc[a][j][2], acc[a][j][3]), cvtpk(acc[a][(j) + 1][0], acc[a][(j) + 1][1]), cvtpk(acc[a][(j) + 1][2], acc[a][(j) + 1][3])}; \
            if (x_ == 1) { _Pragma("unroll") for (int k = 0; k < 4; ++k) p_[k] = min(p_[k], 0xfffffffeu); }     \
            asm volatile("global_store_dwordx4 %0, %1, %2 offset:%3 sc1\n\ts_nop 1" :: "v"(xvo), "v"(p_), "s"(pb + (kp ^ x_) * 6144 + (x_ - 1) * 2048), "i"(((j) & 2) * 512) : "memory"); } while (0)
        constexpr int PQ = 35, FQ = 44, GQ = 60;
#define OQ(q) do { constexpr int p_ = (q) + 4; const bf16x8 w_ = CAT8(wl[(q) & 7], wh[(q) & 7]);                                           \
            constexpr int j_ = (q) < 32 ? (q) & 3 : 4 + 2 * (((q) >> 4) & 1) + ((q) & 1), k_ = (q) < 32 ? ((q) >> 2) & 1 : ((q) >> 1) & 1, s_ = (q) < 32 ? ((q) >> 3) & 3 : ((q) >> 2) & 3; \
            constexpr int pj_ = p_ < 32 ? p_ & 3 : 4 + 2 * ((p_ >> 4) & 1) + (p_ & 1), pk_ = p_ < 32 ? (p_ >> 2) & 1 : (p_ >> 1) & 1, ps_ = p_ < 32 ? (p_ >> 3) & 3 : (p_ >> 2) & 3; \
            if ((q) == 16 || (q) == 24) { asm volatile("s_waitcnt vmcnt(%0)" :: "i"((q) == 16 ? 4 : 0) : "memory"); SB(); }               \
            OMF(0, 12 + s_, k_, j_); SB();                                                                                                \
            if (p_ < 64) ORL(p_ & 7, ps_, pk_, pj_);                                                                                      \
            ORQ(384 + 2 * (q)); SB();                                                                                                     \
            OMF(1, 12 + s_, k_, j_); SB();                                                                                                \
            if (p_ < 64) ORH(p_ & 7, ps_, pk_, pj_);                                                                                      \
            if ((q) >= 10 && (q) < 14) ea[((q) - 10) & 3] = *(lds_u32x2*)(uintptr_t)(ea0 ^ (unsigned)((((q) - 10) & 1) * 16 + (((q) - 10) & 2) * 32)); \
            if ((q) == 14 || (q) == 15) zn[(q) & 1] = __builtin_bit_cast(u32x4, lds_rd(ldsbase + 131072 + 2048 + (wave & 3) * 8192 + (wave >> 2) * 4096 + ((q) & 1) * 1024 + xvo)); \
            if ((q) == 32) OSND(0, 0); if ((q) == 33) OSND(1, 0); if ((q) == 34) OSND(0, 2); if ((q) == 35) OSND(1, 2);                   \
            if ((q) == PQ) { asm volatile("s_setprio 0"); BPRIO(); }                                                                                   \
            if ((q) == 50) OSND(0, 4);                                                                                                    \
            if ((q) == FQ) { asm volatile("s_waitcnt vmcnt(%0)" :: "i"(FQ >= 50) : "memory"); FLAG_PUT(tfb + kp * 64, tag, 1); }     \
            if ((q) == GQ) asm volatile("global_load_dwordx2 %0, %1, %2 sc1" : "=v"(cxf) : "v"(tfo), "s"(tfb) : "memory");               \
            SB(); } while (0)
#define OQ8(q) do { OQ(q); OQ((q) + 1); OQ((q) + 2); OQ((q) + 3); OQ((q) + 4); OQ((q) + 5); OQ((q) + 6); OQ((q) + 7); } while (0)
        asm volatile("s_setprio 2");
        OQ8(0); OQ8(8); OQ8(16); OQ8(24); OST(13); OQ8(32); OQ8(40); OQ8(48); OQ8(56);
        MF_DONE();
        OSND(0, 6); S2T(1);
        OST(14);
        PH_STAMP(5);
        SST(6); S2T(2);
        SB();
        if (__builtin_expect(nrt == 0, 0)) {
            f32x4 ys;
#pragma unroll
            for (int i = 0; i < 7; ++i) PIN(yq[i]);
            PIN(rq);
            const float sig = rbf(1.f / (1.f + __expf(-__uint_as_float(rq))));
#pragma unroll
            for (int j = 0; j < 4; ++j) {
                const float s_ = ((__uint_as_float(yq[0][j]) + __uint_as_float(yq[1][j])) + (__uint_as_float(yq[2][j]) + __uint_as_float(yq[3][j])))
                               + ((__uint_as_float(yq[4][j]) + __uint_as_float(yq[5][j])) + __uint_as_float(yq[6][j]));
                auto rr = __builtin_amdgcn_permlane16_swap(__float_as_uint(s_), __float_as_uint(s_), false, false);
                ys[j] = rbf(__uint_as_float(rr[0]) + __uint_as_float(rr[1])) * sig; }
            char* yl = lds + 65536 + wave * 256;
            *(u32x2*)(yl + (lane >> 5) * 128 + (l15 >> 2) * 32 + (l15 & 1) * 16 + (l15 & 2) * 4) = u32x2{cvtpk(ys[0], ys[1]), cvtpk(ys[2], ys[3])};
            asm volatile("" ::: "memory");
            const int m = r0 + l15;
#pragma unroll
            for (int i = 0; i < 2; ++i) { const u32x4 yn = *(const u32x4*)(yl + (m == ri1 ? 0 : 128) + i * 64 + q4 * 16);
#pragma unroll
                for (int k = 0; k < 4; ++k) { const unsigned z_ = cvtpk(HALF(zn[i][k], 0) + HALF(yn[k], 0), HALF(zn[i][k], 1) + HALF(yn[k], 1));
                    zn[i][k] = m == ri1 || m == ri2 ? z_ : zn[i][k]; } }
        }
        float gg[2][8], xx[2][8];
#pragma unroll
        for (int i = 0; i < 2; ++i)
#pragma unroll
            for (int j = 0; j < 8; ++j) { gg[i][j] = HALF(zn[i][j >> 1], j); xx[i][j] = HALF(ea[2 * i + (j >> 2)][(j >> 1) & 1], j); PIN(gg[i][j]); PIN(xx[i][j]); }
        unsigned ovo = (unsigned)((l15 & 7) * DM + 32 * (l15 >> 3) + 8 * q4) * 2u; PIN(ovo);
        const char* ob = (const char*)(outp + (size_t)r0 * DM + ntc * 128 + 64 * ch);
        asm volatile("" : "+s"(ob));
        PH_STAMP(6);
        { unsigned c_, m_;
          asm volatile("s_waitcnt vmcnt(%0)" :: "i"(GQ < 50 ? 2 : 1) : "memory");
          asm volatile("s_mov_b32 %7, 0x40000\n"
                       "1:\n\tv_cmp_ne_u64 vcc, %11, %6\n\ts_and_b32 vcc_lo, vcc_lo, 12\n\ts_cbranch_scc0 2f\n\t"
                       "global_load_dwordx2 %6, %12, %13 sc1\n\ts_waitcnt vmcnt(0)\n\ts_sub_u32 %7, %7, 1\n\ts_cbranch_scc0 1b\n"
                       "2:\n\tglobal_load_dwordx4 %1, %9, %10 offset:-1024 sc1\n\tglobal_load_dwordx4 %2, %9, %10 offset:1024 sc1\n\t"
                       "global_load_dwordx4 %4, %9, %10 sc1\n\tglobal_load_dwordx4 %5, %9, %10 offset:2048 sc1\n\t"
                       "global_load_dwordx4 %0, %9, %10 offset:-3072 sc1\n\tglobal_load_dwordx4 %3, %9, %10 offset:-2048 sc1"
                       : "=&v"(pv[0]), "=&v"(pv[1]), "=&v"(pv[2]), "=&v"(pv[3]), "=&v"(pv[4]), "=&v"(pv[5]), "+v"(cxf), "=&s"(c_), "=&v"(m_)
                       : "v"(xvo), "s"(pb + kp * 6144 + 3072), "s"(tag), "v"(tfo), "s"(tfb) : "memory", "vcc", "scc"); }
        S2T(3);
#pragma unroll
        for (int i = 0; i < 16; i += 2) { float g_[2];
#pragma unroll
            for (int e_ = 0; e_ < 2; ++e_) { float a_ = gg[i >> 3][(i & 7) + e_] * -1.4426950408889634f; PIN(a_); a_ = 1.f + __builtin_amdgcn_exp2f(a_); PIN(a_);
                g_[e_] = gg[i >> 3][(i & 7) + e_] * __builtin_amdgcn_rcpf(a_); PIN(g_[e_]); }
            const unsigned p_ = cvtpk(g_[0], g_[1]); gg[i >> 3][i & 7] = HALF(p_, 0); gg[i >> 3][(i & 7) + 1] = HALF(p_, 1); PIN(gg[i >> 3][i & 7]); PIN(gg[i >> 3][(i & 7) + 1]); }
        S2T(4);
        asm volatile("s_waitcnt vmcnt(0)" : "+v"(pv[0]), "+v"(pv[1]), "+v"(pv[2]), "+v"(pv[3]), "+v"(pv[4]), "+v"(pv[5]) :: "memory");
        S2T(5);
#pragma unroll 1
        for (int n_ = 0; n_ < 0x40000; ++n_) {
            const unsigned mx_ = max(max(max(pv[0][0], pv[0][1]), max(pv[0][2], pv[0][3])), max(max(pv[3][0], pv[3][1]), max(pv[3][2], pv[3][3])));
            if (__builtin_amdgcn_ballot_w64(mx_ == ~0u) == 0) break;
            asm volatile("global_load_dwordx4 %0, %2, %3 sc1\n\tglobal_load_dwordx4 %1, %2, %3 offset:1024 sc1\n\ts_waitcnt vmcnt(0)"
                         : "=&v"(pv[0]), "=&v"(pv[3]) : "v"(xvo), "s"(pb + kp * 6144) : "memory");
        }
        PH_STAMP(7); S2T(6);
        u32x4 ow[2];
#pragma unroll
        for (int i = 0; i < 2; ++i) {
            u32x4& ov = ow[i];
#pragma unroll
            for (int k = 0; k < 4; ++k) { float sv[2];
#pragma unroll
                for (int e_ = 0; e_ < 2; ++e_) sv[e_] = fmaf((acc[1][4 + 2 * i + (k >> 1)][2 * (k & 1) + e_] + HALF(pv[3 * i][k], e_)) + (HALF(pv[3 * i + 1][k], e_) + HALF(pv[3 * i + 2][k], e_)),
                                                             gg[i][2 * k + e_], xx[i][2 * k + e_]);
                ov[k] = cvtpk(sv[0], sv[1]); }
#pragma unroll
            for (int k = 0; k < 2; ++k) { auto rr = __builtin_amdgcn_permlane16_swap(ov[k], ov[2 + k], false, false); ov[k] = rr[0]; ov[2 + k] = rr[1]; }
        }
#pragma unroll
        for (int i = 0; i < 2; ++i) {
            u32x4 ov;
#pragma unroll
            for (int k = 0; k < 4; ++k) ov[k] = i ? __builtin_amdgcn_update_dpp(ow[1][k], ow[0][k], 0x128, 0xf, 0x3, false) : __builtin_amdgcn_update_dpp(ow[0][k], ow[1][k], 0x128, 0xf, 0xc, false);
            asm volatile("global_store_dwordx4 %0, %1, %2 sc1\n\ts_nop 1" :: "v"(ovo), "v"(ov), "s"(ob + i * (8 * DM * 2)) : "memory");
        }
        SST(7); SST_OUT();
        PH_STAMP(8);
        PH_STAMP(9);
    }
}

extern "C" void kernel_launch(void* const* d_in, const int* in_sizes, int n_in, void* d_out, int out_size, void* d_ws, size_t ws_size,
                              hipStream_t stream) {
    const u16* x    = (const u16*)d_in[0];
    const u16* ckv  = (const u16*)d_in[1];
    const u16* wq   = (const u16*)d_in[2];
    const u16* wuk  = (const u16*)d_in[3];
    const u16* wuv  = (const u16*)d_in[4];
    const u16* wo   = (const u16*)d_in[5];
    const u16* wr   = (const u16*)d_in[7];
    const u16* wg   = (const u16*)d_in[8];
    const u16* wu   = (const u16*)d_in[9];
    const u16* wd   = (const u16*)d_in[10];
    (void)in_sizes; (void)n_in; (void)out_size; (void)ws_size;
    (void)hipFuncSetAttribute((const void*)k_all, hipFuncAttributeMaxDynamicSharedMemorySize, 163840);
    k_all<<<256, 512, 163840, stream>>>(x, ckv, wuk, wuv, wr, (char*)d_ws, wq, wo, wg, wu, wd, (u16*)d_out);
}
```
